```python
import torch

def _softmax(x, axis=-1):
    return torch.softmax(x, dim=axis)

def _gelu(x):
    return torch.nn.functional.gelu(x, approximate='tanh')

def _silu(x):
    return torch.nn.functional.silu(x)

def _sigmoid(x):
    return torch.sigmoid(x)

def _layernorm(x, axis=-1, eps=1e-05):
    m = x.mean(dim=axis, keepdim=True)
    v = x.var(dim=axis, keepdim=True, unbiased=False)
    return (x - m) / torch.sqrt(v + eps)

def _scatter_add(tgt, idx, src, axis=0):
    return tgt.clone().index_add_(axis, idx, src)

def _segment_sum(src, seg, n):
    out = torch.zeros((n,) + src.shape[1:], dtype=src.dtype)
    return out.index_add_(0, seg, src)

def _pad(x, pw):
    flat = tuple((v for pair in reversed(pw) for v in pair))
    return torch.nn.functional.pad(x, flat)
kernel_name = 'concat_heads__compressed_kv_cla__memory_aug_recurrent_mem_tr__prompt'

def setup_inputs():

    _w = lambda a: (lambda t: t.to(torch.bfloat16) if t.is_floating_point() else t)(torch.as_tensor(a))

    def _A_setup():
        B, H, Sq, Sk, D, M = (1, 32, 2048, 2048, 128, 576)
        Dc = 4 * D
        return {'x': _w((lambda *s: torch.randn(*s, dtype=torch.bfloat16) * 1.6)(B, Sq, M)), 'c_kv': _w((lambda *s: torch.randn(*s, dtype=torch.bfloat16) * 0.2)(B, Sk, Dc)), 'w_q': _w((lambda *s: torch.randn(*s, dtype=torch.bfloat16) * 3.2)(M, H, D) * M ** (-0.5)), 'w_uk': _w((lambda *s: torch.randn(*s, dtype=torch.bfloat16) * 3.2)(Dc, H, D) * Dc ** (-0.5)), 'w_uv': _w((lambda *s: torch.randn(*s, dtype=torch.bfloat16) * 0.2)(Dc, H, D) * Dc ** (-0.5)), 'w_o': _w((lambda *s: torch.randn(*s, dtype=torch.bfloat16) * 0.2)(H, D, M) * (H * D) ** (-0.5))}
    _A_in = _A_setup()

    def _B_setup():
        B, S, Dm = (1, 2048, 576)
        H, Dh = (32, 128)
        M, seg = (8, 32)
        seg = min(seg, S)
        S = S // seg * seg
        return {'x': _w((lambda *s: torch.randn(*s, dtype=torch.bfloat16) * 0.05)(B, S, Dm)), 'mem0': _w((lambda *s: torch.randn(*s, dtype=torch.bfloat16) * 0.4)(B, M, Dm)), 'Wq': _w((lambda *s: torch.randn(*s, dtype=torch.bfloat16) * 6.4)(Dm, H, Dh) * Dm ** (-0.5)), 'Wk': _w((lambda *s: torch.randn(*s, dtype=torch.bfloat16) * 6.4)(Dm, H, Dh) * Dm ** (-0.5)), 'Wv': _w((lambda *s: torch.randn(*s, dtype=torch.bfloat16) * 0.4)(Dm, H, Dh) * Dm ** (-0.5)), 'Wo': _w((lambda *s: torch.randn(*s, dtype=torch.bfloat16) * 0.4)(H, Dh, Dm) * (H * Dh) ** (-0.5)), 'seg_len': seg}
    _B_in = _B_setup()
    return {'A_x': _A_in['x'], 'A_c_kv': _A_in['c_kv'], 'A_w_q': _A_in['w_q'], 'A_w_uk': _A_in['w_uk'], 'A_w_uv': _A_in['w_uv'], 'A_w_o': _A_in['w_o'], 'B_x': _B_in['x'], 'B_mem0': _B_in['mem0'], 'B_Wq': _B_in['Wq'], 'B_Wk': _B_in['Wk'], 'B_Wv': _B_in['Wv'], 'B_Wo': _B_in['Wo'], 'B_seg_len': _B_in['seg_len']}

def reference(A_x, A_c_kv, A_w_q, A_w_uk, A_w_uv, A_w_o, B_x, B_mem0, B_Wq, B_Wk, B_Wv, B_Wo, B_seg_len):

    def _A_ref(x, c_kv, w_q, w_uk, w_uv, w_o):
        B, Sq, M = (x.shape[0], x.shape[1], x.shape[2])
        Sk = c_kv.shape[1]
        H, D = (w_q.shape[1], w_q.shape[2])
        q = torch.einsum('bqm,mhd->bhqd', x, w_q)
        k = torch.einsum('bkc,chd->bhkd', c_kv, w_uk)
        v = torch.einsum('bkc,chd->bhkd', c_kv, w_uv)
        scores = torch.einsum('bhqd,bhkd->bhqk', q, k) * D ** (-0.5)
        off = Sk - Sq
        qi = torch.arange(Sq)[:, None]
        kj = torch.arange(Sk)[None, :]
        cmask = kj <= qi + off
        scores = torch.where(cmask[None, None], scores, -1000000000.0)
        p = _softmax(scores, axis=-1)
        ctx = torch.einsum('bhqk,bhkd->bhqd', p, v)
        return torch.einsum('bhqd,hdm->bqm', ctx, w_o)

    def _B_ref(x, mem0, Wq, Wk, Wv, Wo, seg_len):
        B, S, Dm = x.shape
        H, Dh = (Wq.shape[1], Wq.shape[2])
        M = mem0.shape[1]
        scale = Dh ** (-0.5)
        n_seg = S // seg_len
        L = M + seg_len
        idx = torch.arange(L)
        mask = (idx[:, None] < M) | (idx[:, None] >= idx[None, :])
        mem = mem0
        outs = []
        for s in range(n_seg):
            xs = x[:, s * seg_len:(s + 1) * seg_len, :].clone()
            h = torch.concatenate([mem, xs], axis=1)
            q = torch.einsum('btd,dhe->bhte', h, Wq)
            k = torch.einsum('btd,dhe->bhte', h, Wk)
            v = torch.einsum('btd,dhe->bhte', h, Wv)
            scores = torch.einsum('bhqe,bhke->bhqk', q, k) * scale
            scores = torch.where(mask[None, None], scores, -1000000000.0)
            p = _softmax(scores, axis=-1)
            o = torch.einsum('bhqk,bhke->bhqe', p, v)
            o = torch.einsum('bhte,hed->btd', o, Wo)
            mem = o[:, :M, :].clone()
            outs.append(o[:, M:, :])
        return torch.concatenate(outs, axis=1)
    a = _A_ref(A_x, A_c_kv, A_w_q, A_w_uk, A_w_uv, A_w_o)
    b = _B_ref(B_x, B_mem0, B_Wq, B_Wk, B_Wv, B_Wo, B_seg_len)
    return torch.concatenate([a, b], axis=-1)
```

```cpp
#include <hip/hip_runtime.h>
#include <hip/hip_bf16.h>
#include <stdint.h>
#include <type_traits>

typedef unsigned short u16;
typedef unsigned int u32;
typedef unsigned long long u64;
typedef __attribute__((ext_vector_type(8))) short bf8;
typedef __attribute__((ext_vector_type(4))) short bf4;
typedef __attribute__((ext_vector_type(4))) float f4;
typedef __attribute__((ext_vector_type(2))) float f2v;
typedef __attribute__((ext_vector_type(2))) __bf16 bf2v;
typedef __attribute__((ext_vector_type(4))) unsigned u32x4;
typedef __attribute__((ext_vector_type(2))) unsigned u32x2;
typedef float f32x16 __attribute__((ext_vector_type(16)));
typedef __attribute__((ext_vector_type(8))) unsigned u32x8;
typedef __attribute__((ext_vector_type(8))) __bf16 bh8;
typedef __attribute__((ext_vector_type(16))) __bf16 bh16;
#define LDSP __attribute__((address_space(3)))

__device__ __forceinline__ u32 pk2(float a, float b) {
  f2v x = {a, b};
  bf2v y = __builtin_convertvector(x, bf2v);
  return __builtin_bit_cast(u32, y);
}
__device__ __forceinline__ u32 umin(u32 a, u32 b) { return a < b ? a : b; }
__device__ __forceinline__ u32 umax(u32 a, u32 b) { return a > b ? a : b; }
__device__ __forceinline__ float rbf(float f) { return __uint_as_float(pk2(f, f) << 16); }
#define MFMA(a, b, c) __builtin_amdgcn_mfma_f32_16x16x32_bf16(a, b, c, 0, 0, 0)
#define MFMA16(a, b, c) __builtin_amdgcn_mfma_f32_16x16x16bf16_1k(a, b, c, 0, 0, 0)
#define SMF(a, b, c, idx) __builtin_amdgcn_smfmac_f32_16x16x64_bf16(__builtin_bit_cast(bh8, a), __builtin_bit_cast(bh16, b), c, idx, 0, 0)
__device__ __forceinline__ u32x8 ilv(bf4 a, bf4 b, bf4 c, bf4 d) {
  const u32x2 x = __builtin_bit_cast(u32x2, a), y = __builtin_bit_cast(u32x2, b), z = __builtin_bit_cast(u32x2, c), w = __builtin_bit_cast(u32x2, d);
  return (u32x8){x[0], x[1], y[0], y[1], z[0], z[1], w[0], w[1]};
}
__device__ __forceinline__ u32x8 cat16(bf8 a, bf8 b) {
  const u32x4 x = __builtin_bit_cast(u32x4, a), y = __builtin_bit_cast(u32x4, b);
  return (u32x8){x[0], x[1], x[2], x[3], y[0], y[1], y[2], y[3]};
}
__device__ __forceinline__ float swadd(float a, float b) {
  auto x = __builtin_amdgcn_permlane32_swap(__float_as_uint(a), __float_as_uint(b), false, false);
  return __uint_as_float(x[0]) + __uint_as_float(x[1]);
}
__device__ __forceinline__ bf4 trrd(const char* p) {
  return __builtin_amdgcn_ds_read_tr16_b64_v4i16((LDSP bf4*)(p));
}
__device__ __forceinline__ bf8 cat8(bf4 a, bf4 b) { return __builtin_shufflevector(a, b, 0, 1, 2, 3, 4, 5, 6, 7); }
__device__ __forceinline__ void st8_sc1(void* p, u64 v) { __hip_atomic_store((u64*)p, v, __ATOMIC_RELAXED, __HIP_MEMORY_SCOPE_AGENT); }
__device__ __forceinline__ u32 ld4_sc1(const u32* p) { return __hip_atomic_load(p, __ATOMIC_RELAXED, __HIP_MEMORY_SCOPE_AGENT); }
__device__ __forceinline__ void st4_sc1(u32* p, u32 v) { __hip_atomic_store(p, v, __ATOMIC_RELAXED, __HIP_MEMORY_SCOPE_AGENT); }
#define VMW0() asm volatile("s_waitcnt vmcnt(0)" ::: "memory")
#define CFENCE() asm volatile("" ::: "memory")
#define VMW0C() do { __builtin_amdgcn_s_waitcnt(0x0F70); asm volatile("" ::: "memory"); } while (0)
#define SPIN_CAP (1 << 22)
#define XPS 4
#define SPS 4
#define WPS 8
__device__ __forceinline__ int tid_now(int wv) {
  int l; asm volatile("v_mbcnt_lo_u32_b32 %0, -1, 0\n\tv_mbcnt_hi_u32_b32 %0, -1, %0" : "=v"(l));
  return wv * 64 + l;
}
#define TIDX tid_now(wv)
#define CLAIM() do { if (TIDX == 0) pre = (int)__hip_atomic_fetch_add(qh, 1u, __ATOMIC_RELAXED, __HIP_MEMORY_SCOPE_AGENT); } while (0)

struct Params {
  const u16 *Ax, *Ac, *Awq, *Awk, *Awv, *Awo, *Bx, *Bm, *BWq, *BWk, *BWv, *BWo;
  u16* out;
  char* ws;
};
#define SY_QHEAD 0
#define SY_CC(rb) (64 + (rb) * 16)
#define SY_CW (64 + 8 * 16)
#define SY_CS(s) (64 + 16 * 16 + (s) * 16)
#define SY_B0 (64 + 80 * 16)
#define SY_SEG(i, h) (SY_B0 + (i) * 32 + (h))
#define SY_INIT(c) (SY_B0 + 64 + (c))
#define SY_VOTE(i) (SY_B0 + 128 + (i))
#define SY_CLAIM(c) (SY_B0 + 192 + (c))
#define SY_XQ(x) (SY_B0 + 256 + (x) * 16)
#define SY_ARR (SY_B0 + 384)
#define SY_NCL (SY_ARR + 16)
#define BLKS 1
#define SY_BLK(mi, cb, g) (SY_NCL + 48 + (((mi) * 16 + (cb)) * 64 + (g)) * BLKS)
#define SY_WORDS (SY_NCL + 48 + 6 * 16 * 64 * BLKS)

#define AL256(x) (((x) + 255) & ~255)
#define O_SYNC_END (SY_WORDS * 4)
#define ACB (2048 * 4096 * 2)
#define O_QA AL256(O_SYNC_END)
#define O_KA (O_QA + ACB)
#define O_VA (O_KA + ACB)
#define O_CA (O_VA + ACB)
#define O_QB (O_CA + ACB)
#define O_KB (O_QB + ACB)
#define O_VB (O_KB + ACB)
#define O_OB (O_VB + ACB)
#define O_WOF (O_OB + ACB)
#define O_XW (O_WOF + 576 * 4096 * 2)
#define X_O 16384
#define X_M (X_O + 3 * 131072)
#define X_ANN (X_M + 3 * 32768)
#define X_CAL (X_ANN + 16384)
#define X_END (X_CAL + 16384)
#define OB_OFF(r, c) (((r) >> 5) * 262144 + ((c) >> 4) * 1024 + ((r) & 31) * 32 + ((c) & 15) * 2)
#define WSP(T, off) ((T*)(p.ws + (off)))
#define XWIN(p) (((uintptr_t)(p).ws + O_XW + 0x1FFFFFu) & ~(uintptr_t)0x1FFFFFu)

#define XS0 52
#define LDS_TOTAL (131072 + 8192 + 16384)
#define LDS_ALL 163840

#define SB0() __builtin_amdgcn_sched_barrier(0)
#define RSRC(ptr, bytes) __builtin_amdgcn_make_buffer_rsrc((void*)(ptr), 0, (bytes), 0x00020000)
__device__ __forceinline__ bf8 ldsc1(__amdgpu_buffer_rsrc_t r, int off) {
  return __builtin_bit_cast(bf8, __builtin_amdgcn_raw_buffer_load_b128(r, off, 0, 16));
}
__device__ __forceinline__ u32x4 pair16(u32 a0, u32 a1, u32 b0, u32 b1) {
  auto r0 = __builtin_amdgcn_permlane16_swap(a0, b0, false, false);
  auto r1 = __builtin_amdgcn_permlane16_swap(a1, b1, false, false);
  return (u32x4){r0[0], r1[0], r0[1], r1[1]};
}
__device__ __forceinline__ int fsw(int k) { return (k & 3) | (((k >> 3) & 1) << 2); }

#define GST(i)
#define GTB true
#define EXPG 0
template <int BM, int BN, int WMG, int WNG, bool SC1, int PD, bool AD = false>
__device__ __forceinline__ void gemm_tile(const u16* __restrict__ A, int lda, const u16* __restrict__ B, int ldb,
                                          u16* __restrict__ C, int ldc, int K, char* smem, const int wv, u32* qh, int& pre, const bool tiled = false) {
  constexpr int kofs = 0;
  constexpr int BRS = ((BN * 2 + 255) / 256) * 256;
  constexpr int ABY = BM * 128, BBY = 64 * BRS, BUF = ABY + BBY;
  constexpr int MT = BM / WMG / 16, NT = BN / WNG / 16;
  constexpr bool SPEC = WMG * WNG == 4;
  constexpr int NTH = SPEC ? 256 : 512;
  constexpr int ACH = AD ? 2 * MT : BM * 8 / NTH, BCH = (8 * BN) / NTH;
  constexpr bool TB = !SC1 && BM == 128 && !SPEC && !AD && GTB;
  constexpr int BCPR = BN / 8;
  const int tid = TIDX, lane = tid & 63, wid = wv;
  const int l15 = lane & 15, lq = lane >> 4;
  const int wr = wid / WNG, wc = wid % WNG;
  const __amdgpu_buffer_rsrc_t rc = RSRC(C, BM * ldc * 2);
  f4 acc[MT][NT];
#pragma unroll
  for (int i = 0; i < MT; i++)
#pragma unroll
    for (int j = 0; j < NT; j++) acc[i][j] = (f4){0, 0, 0, 0};
  bf8 ra[PD][ACH], rb[PD][BCH];
  const __amdgpu_buffer_rsrc_t rsa = RSRC(A, BM * lda * 2), rsb = RSRC(B, K * ldb * 2);
  auto gload = [&](int k0, bf8* xa, bf8* xb) {
    if (!SC1) k0 = (k0 + kofs) & 4095;
    if constexpr (!AD) {
#pragma unroll
    for (int i = 0; i < ACH; i++) {
      int c = (tid & (NTH - 1)) + NTH * i, r = c >> 3, ch = c & 7;
      xa[i] = __builtin_bit_cast(bf8, __builtin_amdgcn_raw_buffer_load_b128(rsa, tiled ? OB_OFF(r, ch * 8) : (r * lda + ch * 8) * 2, tiled ? k0 * 64 : k0 * 2, 0));
    }
    }
#pragma unroll
    for (int i = 0; i < BCH; i++) {
      int c = (tid & (NTH - 1)) + NTH * i, r = c / BCPR, ch = c % BCPR;
      xb[i] = __builtin_bit_cast(bf8, __builtin_amdgcn_raw_buffer_load_b128(rsb, (r * ldb + ch * 8) * 2, (!SC1 && (EXPG & 1)) ? 0 : k0 * ldb * 2, 0));
    }
  };
  auto aload = [&](int k0, bf8* xa) {
    k0 = (k0 + kofs) & 4095;
#pragma unroll
    for (int i = 0; i < ACH; i++)
      xa[i] = __builtin_bit_cast(bf8, __builtin_amdgcn_raw_buffer_load_b128(rsa, ((wr * (MT * 16) + (i % MT) * 16 + l15) * lda + (i / MT) * 32 + lq * 8) * 2, k0 * 2, 0));
  };
  const int q4 = l15 >> 2, p4 = lane & 3;
  int owa, owb[BCH], ora, orb[NT];
  { const int c = tid & (NTH - 1), r = c >> 3, ch = c & 7; owa = r * 128 + ((ch ^ (r & 7)) << 4); asm volatile("" : "+v"(owa)); }
#pragma unroll
  for (int i = 0; i < BCH; i++) {
    const int c = (tid & (NTH - 1)) + NTH * i, r = c / BCPR, ch = c % BCPR;
    owb[i] = r * BRS + (((ch >> 1) ^ fsw(r)) << 5) + ((ch & 1) << 4); asm volatile("" : "+v"(owb[i]));
  }
  ora = (wr * (MT * 16) + l15) * 128 + ((lq ^ (l15 & 7)) << 4); asm volatile("" : "+v"(ora));
#pragma unroll
  for (int j = 0; j < NT; j++) { const int r0 = lq * 8 + q4; orb[j] = r0 * BRS + (((wc * NT + j) ^ fsw(r0)) << 5) + p4 * 8; asm volatile("" : "+v"(orb[j])); }
  auto swrite = [&](int buf, bf8* xa, bf8* xb) {
    int bo = TB ? buf : buf * BUF;
    if constexpr (BM != 128) asm volatile("" : "+s"(bo));
    if constexpr (!AD) {
#pragma unroll
      for (int i = 0; i < ACH; i++) *(bf8*)(smem + (owa + bo) + i * NTH * 16) = xa[i];
    }
#pragma unroll
    for (int i = 0; i < BCH; i++) *(bf8*)(smem + (owb[i] + bo) + ABY) = xb[i];
  };
  auto rd = [&](int buf, int ks, bf8* af, bf8* bfr) {
    int bo = TB ? buf : buf * BUF;
    if constexpr (BM != 128) asm volatile("" : "+s"(bo));
    if constexpr (!AD) {
#pragma unroll
      for (int i = 0; i < MT; i++) af[i] = *(const bf8*)(smem + ((ora ^ (ks * 64)) + bo) + i * 2048);
    }
#pragma unroll
    for (int j = 0; j < NT; j++) {
      const char* q = smem + (orb[j] + bo) + ABY + ks * 32 * BRS;
      bfr[j] = cat8(trrd(q), trrd(q + 4 * BRS));
    }
  };
  auto mm = [&](bf8* af, bf8* bfr) {
#pragma unroll
    for (int j = 0; j < NT; j++)
#pragma unroll
      for (int i = 0; i < MT; i++) acc[i][j] = MFMA(bfr[j], af[i], acc[i][j]);
  };
  auto step = [&](auto cw, int k0, int buf, bf8* xa, bf8* xb) {
    constexpr bool CW = decltype(cw)::value;
    bf8 af0[MT], bf0[NT], af1[MT], bf1[NT];
    if constexpr (CW) {
      rd(buf, 0, af0, bf0);
      if constexpr (BM == 128) rd(buf, 1, af1, bf1);
      SB0();
      mm(AD ? xa : af0, bf0);
      if constexpr (BM != 128) SB0();
    }
    if constexpr (!(SPEC && CW)) if (!SC1 || k0 + 64 < K) {
      swrite(buf ^ 1, xa, xb);
      if (!SC1 || k0 + 64 * (PD + 1) < K) gload(k0 + 64 * (PD + 1), xa, xb);
    }
    SB0();
    if constexpr (CW) {
      if constexpr (BM != 128) { rd(buf, 1, af1, bf1); SB0(); }
      mm(AD ? xa + MT : af1, bf1);
      if constexpr (AD) { SB0(); aload(k0 + 64 * PD, xa); }
    }
    asm volatile("s_waitcnt lgkmcnt(0)" ::: "memory"); __builtin_amdgcn_s_barrier(); asm volatile("" ::: "memory");
  };
  auto pro = [&](auto st) {
    if constexpr (decltype(st)::value) {
      bf8 ta[ACH], tb[BCH];
      gload(0, ta, tb);
#pragma unroll
      for (int u = 0; u < PD; u++) { if constexpr (AD) aload(64 * u, ra[u]); gload(64 * (u + 1), ra[u], rb[u]); }
      swrite(0, ta, tb);
    }
    asm volatile("s_waitcnt lgkmcnt(0)" ::: "memory"); __builtin_amdgcn_s_barrier(); asm volatile("" ::: "memory");
  };
  if constexpr (!SPEC && !TB) pro(std::true_type{});
  GST(0);
  if constexpr (SC1) {
#pragma unroll
    for (int u = 0; u < 9; u++) step(std::true_type{}, 64 * u, u & 1, ra[u % PD], rb[u % PD]);
  } else if constexpr (TB) {
    bf8 af0[MT], bf0[NT];
    {
      bf8 ta[ACH], tb[BCH], ua[ACH], ub[BCH];
      gload(0, ta, tb); gload(64, ua, ub);
#pragma unroll
      for (int u = 0; u < PD; u++) gload(64 * (u + 2), ra[u], rb[u]);
      swrite(0, ta, tb); swrite(BUF, ua, ub);
      asm volatile("s_waitcnt lgkmcnt(0)" ::: "memory"); __builtin_amdgcn_s_barrier(); asm volatile("" ::: "memory");
      rd(0, 0, af0, bf0);
    }
    int b0 = 0, b1 = BUF, b2 = 2 * BUF;
#pragma unroll 1
    for (int k0 = 0; k0 < 4096; k0 += 64 * PD) {
#pragma unroll
      for (int u = 0; u < PD; u++) {
        bf8 af1[MT], bf1[NT];
        rd(b0, 1, af1, bf1);
        SB0();
        mm(af0, bf0);
        swrite(b2, ra[u], rb[u]);
        gload(k0 + 64 * (u + PD + 2), ra[u], rb[u]);
        SB0();
        rd(b1, 0, af0, bf0);
        SB0();
        mm(af1, bf1);
        asm volatile("s_waitcnt lgkmcnt(0)" ::: "memory"); __builtin_amdgcn_s_barrier(); asm volatile("" ::: "memory");
        const int bt = b0; b0 = b1; b1 = b2; b2 = bt;
      }
    }
  } else {
    static_assert(64 % PD == 0 && !(PD & 1), "depth");
    auto loop = [&](auto cw) {
      if constexpr (SPEC) pro(std::bool_constant<!decltype(cw)::value>{});
#pragma unroll 1
      for (int k0 = 0; k0 < 4096; k0 += 64 * PD) {
#pragma unroll
        for (int u = 0; u < PD; u++) step(cw, k0 + 64 * u, u & 1, ra[u], rb[u]);
      }
    };
    if (WMG * WNG == 8 || wid < WMG * WNG) loop(std::true_type{}); else loop(std::false_type{});
  }
  GST(1);
  if (!SC1) CLAIM();
  if (WMG * WNG == 8 || wid < WMG * WNG)
#pragma unroll
  for (int i = 0; i < MT; i++)
#pragma unroll
    for (int j = 0; j < NT; j += 2) {
      int row = wr * (MT * 16) + i * 16 + l15, col = wc * (NT * 16) + j * 16 + (lq & 1) * 16 + (lq >> 1) * 8;
      if (j + 1 == NT) {
        *(u32x2*)(C + (size_t)row * ldc + wc * (NT * 16) + j * 16 + lq * 4) = (u32x2){pk2(acc[i][j][0], acc[i][j][1]), pk2(acc[i][j][2], acc[i][j][3])};
        break;
      }
      u32x4 v = pair16(pk2(acc[i][j][0], acc[i][j][1]), pk2(acc[i][j][2], acc[i][j][3]),
                       pk2(acc[i][(j + 1) % NT][0], acc[i][(j + 1) % NT][1]), pk2(acc[i][(j + 1) % NT][2], acc[i][(j + 1) % NT][3]));
      if (SC1) __builtin_amdgcn_raw_buffer_store_b128(v, rc, (row * ldc + col) * 2, 0, 16);
      else *(u32x4*)(C + (size_t)row * ldc + col) = v;
    }
  GST(2);
  VMW0();
  __syncthreads();
  GST(3);
}

#define EXP_AUX 16
#define C_BUFA 131072
#define C_BUFB (131072 + 16384)
#define PJ_OOB 0x7F000000
__device__ __forceinline__ void proj_slot(const Params& p, int x, int j, char* smem, const int wv) {
  const int tid = TIDX, lane = tid & 63, wid = wv;
  const int l15 = lane & 15, lq = lane >> 4, q4 = l15 >> 2, p4 = lane & 3, wr = wid >> 2, wc = wid & 3;
  const __amdgpu_buffer_rsrc_t rc = RSRC(p.ws, O_WOF);
  const int vB = ((wid * 4 + lq) * 4096 + (((l15 >> 1) ^ (lq | (((wid >> 1) & 1) << 2))) * 16 + (lane & 1) * 8)) * 2, vB1 = vB + 32 * 8192;
  int ao = (wr * 64 + l15) * 128 + ((lq ^ (l15 & 7)) << 4);
  int bo = (int)(uintptr_t)smem + 65536 + (lq * 8 + q4) * 256 + (((wc * 2) ^ (q4 | ((lq & 1) << 2))) << 5) + p4 * 8;
  const int vC = (wid * 4 + (lane >> 5)) * 8192 + (lane & 31) * 16;
  const int vD = ((wr * 16 + l15) * 4096 + wc * 32 + (lq & 1) * 16 + (lq >> 1) * 8) * 2;
  const int cw = C_BUFA + (wr * 16 + l15) * 512 + (((wc * 4 + (lq & 1) * 2 + (lq >> 1)) ^ (l15 & 7)) << 4);
  const int cr = C_BUFA + (wid * 4 + (lane >> 5)) * 512 + (((lane & 31) ^ ((wid & 1) * 4 + (lane >> 5))) << 4);
  const int cb = (x & 1) * 8 + (j >> 2);
  __amdgpu_buffer_rsrc_t rsA, rsB;
  int k2, vA, vA1, sa, sb;
#define PJ_UNIT(u) do { const int mi_ = ((u) >> 1) * 3 + (x >> 1); k2 = mi_ < 4 ? 1152 : 1024; \
    rsA = RSRC(mi_ < 3 ? p.Bx : mi_ == 3 ? p.Ax : p.Ac, 2048 * k2); \
    rsB = RSRC((mi_ == 0 ? p.BWk : mi_ == 1 ? p.BWv : mi_ == 2 ? p.BWq : mi_ == 3 ? p.Awq : mi_ == 4 ? p.Awk : p.Awv) + cb * 256, k2 * 4096 - cb * 512); \
    int ln_ = lane; asm volatile("" : "+v"(ln_)); \
    vA = ((wid >> 1) * 32 + (wid & 1) * 8 + (ln_ >> 3)) * k2 + ((ln_ & 7) ^ ((ln_ >> 3) & 7)) * 16; vA1 = vA + 16 * k2; \
    sa = (((j & 3) + ((u) & 1) * 4) * 256 + (mi_ < 3 ? 256 : 0)) * k2 - 128; sb = 0; } while (0)
#define PJ_STAGE(dst, rs, v0, v1, so) do { const int so_ = (so); \
    __builtin_amdgcn_raw_ptr_buffer_load_lds(rs, (LDSP void*)(smem + (dst) + wid * 1024), 16, v0, so_, 0, 0); \
    __builtin_amdgcn_raw_ptr_buffer_load_lds(rs, (LDSP void*)(smem + (dst) + wid * 1024 + 8192), 16, v1, so_, 0, 0); } while (0)
#define PJ_SA(dst, so) PJ_STAGE(dst, rsA, vA, vA1, so)
#define PJ_SB(dst, so) PJ_STAGE(dst, rsB, vB, vB1, so)
#define PJ_LDA(h) do { _Pragma("unroll") for (int m = 0; m < 4; m++) _Pragma("unroll") for (int k = 0; k < 2; k++) \
    At[m][k] = *(const bf8*)(smem + (ao ^ (k * 64)) + (h) * 16384 + m * 2048); } while (0)
#define PJ_TR(dst, a, off) asm volatile("ds_read_b64_tr_b16 %0, %1 offset:%2" : "=&v"(dst) : "v"(a), "i"(off) : "memory")
#define PJ_LDB(D, h) do { const int b1_ = bo ^ 32; _Pragma("unroll") for (int n = 0; n < 2; n++) _Pragma("unroll") for (int k = 0; k < 2; k++) { \
    bf4 lo_, hi_; PJ_TR(lo_, n ? b1_ : bo, (h) * 16384 + k * 8192); PJ_TR(hi_, n ? b1_ : bo, (h) * 16384 + k * 8192 + 1024); D[n][k] = cat8(lo_, hi_); } } while (0)
#define PJ_MMA(S, ai, bj, D) do { PJ_SCHED; __builtin_amdgcn_s_setprio(1); \
    _Pragma("unroll") for (int k = 0; k < 2; k++) _Pragma("unroll") for (int m = 0; m < 4; m++) _Pragma("unroll") for (int n = 0; n < 2; n++) { \
      if (k == 0 && (S) == 0) asm volatile("v_mfma_f32_16x16x32_bf16 %0, %1, %2, 0" : "+v"(acc[ai][bj][m][n]) : "v"(D[n][k]), "v"(At[m][k])); \
      else asm volatile("v_mfma_f32_16x16x32_bf16 %0, %1, %2, %0" : "+v"(acc[ai][bj][m][n]) : "v"(D[n][k]), "v"(At[m][k])); } \
    __builtin_amdgcn_s_setprio(0); PJ_SCHED; } while (0)
#define PJ_WV(n) asm volatile("s_waitcnt vmcnt(" #n ")" ::: "memory")
#define PJ_WL(n) asm volatile("s_waitcnt lgkmcnt(" #n ")" ::: "memory")
#define PJ_WL0 do { __builtin_amdgcn_s_waitcnt(0xC07F); asm volatile("" ::: "memory"); } while (0)
#define PJ_BAR __builtin_amdgcn_s_barrier()
#define PJ_SCHED __builtin_amdgcn_sched_barrier(0)
#define PJ_NOP do { PJ_SCHED; asm volatile("s_nop 7"); PJ_SCHED; } while (0)
#define PJ_PK(q, bj) pair16(pk2(acc[(q) >> 2][bj][(q) & 3][0][0], acc[(q) >> 2][bj][(q) & 3][0][1]), pk2(acc[(q) >> 2][bj][(q) & 3][0][2], acc[(q) >> 2][bj][(q) & 3][0][3]), \
                            pk2(acc[(q) >> 2][bj][(q) & 3][1][0], acc[(q) >> 2][bj][(q) & 3][1][1]), pk2(acc[(q) >> 2][bj][(q) & 3][1][2], acc[(q) >> 2][bj][(q) & 3][1][3]))
#define PJ_CW(buf, v0, v1) do { *(u32x4*)(smem + cw + (buf) * 16384) = (v0); *(u32x4*)(smem + cw + (buf) * 16384 + 256) = (v1); } while (0)
#define PJ_CR(buf, i) (*(const u32x4*)(smem + (cr ^ ((i) * 32)) + (buf) * 16384 + (i) * 1024))
#define PJ_SO(q) ((q) < dlim ? dco + (q) * 32 * 8192 : PJ_OOB)
#define PJ_ST1(v, vo, so) do { PJ_SCHED; __builtin_amdgcn_raw_buffer_store_b128(v, rc, vo, so, EXP_AUX); asm volatile("s_nop 1"); PJ_SCHED; } while (0)
#define PJ_CS(v, i, q) PJ_ST1(v, vC, PJ_SO(q) + (i) * 16384)
#define PJ_DS(v, bj, q) PJ_ST1(v, vD, PJ_SO(q) + (bj) * 256)
#define PJ_FLAG(q) ((q) < dlim ? (dfl + (q) * BLKS) * 4 : PJ_OOB)
#define PJ_PUB(a, b) do { if (wid == 4) __builtin_amdgcn_raw_buffer_store_b32(8u, rc, lane == 0 ? PJ_FLAG(a) : (lane == 1 ? PJ_FLAG(b) : PJ_OOB), 0, 16); } while (0)
#define PJ_ST()
#define PJ_QA(S) ((S) == 0 ? 2 : (S) == 1 ? 3 : (S) + 3)
#define PJ_STEP(S) do { PJ_ST(); u32x4 c0_, c1_; \
    PJ_LDB(B0, 0); PJ_SCHED; \
    if ((S) == 0) PJ_CW(0, PJ_PK(2, 0), PJ_PK(2, 1)); else if ((S) < 5) PJ_CW(0, pk[((S) - 1) & 3][0], pk[((S) - 1) & 3][1]); \
    PJ_SCHED; PJ_LDA(0); PJ_SA((bsel ^ 32768) + 16384, sa + 128 * k2); \
    PJ_WL(8); PJ_BAR; PJ_WL0; PJ_MMA(S, 0, 0, B0); PJ_BAR; PJ_SCHED; \
    if ((S) == 7) PJ_UNIT(un < 3 ? un : 3); \
    if ((S) < 5) c0_ = PJ_CR(0, 0); \
    if ((S) < 2) c1_ = PJ_CR(1, 1); \
    PJ_SCHED; PJ_LDB(B1, 1); PJ_SB(65536 + bsel, sb); \
    if ((S) == 0) { _Pragma("unroll") for (int i = 1; i < 4; i++) { pk[i][0] = PJ_PK(4 + i, 0); pk[i][1] = PJ_PK(4 + i, 1); } } \
    if ((S) < 5) PJ_CS(c0_, 0, PJ_QA(S)); \
    if ((S) < 2) PJ_CS(c1_, 1, (S) == 0 ? 1 : 4); \
    PJ_BAR; PJ_WL0; PJ_MMA(S, 0, 1, B1); PJ_BAR; \
    if ((S) == 8) { const int mi_ = (u >> 1) * 3 + (x >> 1), g_ = ((j & 3) + (u & 1) * 4) * 8 + (mi_ < 3 ? 8 : 0); \
      dco = (mi_ == 0 ? O_KB : mi_ == 1 ? O_VB : mi_ == 2 ? O_QB : O_QA + (mi_ - 3) * ACB) + cb * 512 + g_ * 32 * 8192; \
      dfl = SY_BLK(mi_, cb, g_); dlim = 8; \
      PJ_NOP; PJ_CW(1, PJ_PK(1, 0), PJ_PK(1, 1)); PJ_SCHED; } \
    if ((S) == 0) { PJ_CW(1, PJ_PK(4, 0), PJ_PK(4, 1)); PJ_SCHED; } \
    PJ_LDA(1); sa += 128; PJ_SA(bsel, sa); \
    if ((S) == 8 || (S) == 0) PJ_WL(8); \
    PJ_BAR; PJ_WL0; PJ_MMA(S, 1, 0, B0); PJ_BAR; PJ_SCHED; \
    if ((S) < 5) c0_ = PJ_CR(0, 1); \
    if ((S) == 8 || (S) == 0) c1_ = PJ_CR(1, 0); \
    PJ_SB(65536 + bsel + 16384, sb + 256); sb += 64 * 8192; \
    if ((S) == 8) { PJ_DS(PJ_PK(0, 0), 0, 0); PJ_DS(PJ_PK(0, 1), 1, 0); pk[0][0] = PJ_PK(3, 0); pk[0][1] = PJ_PK(3, 1); } \
    if ((S) < 5) PJ_CS(c0_, 1, PJ_QA(S)); \
    if ((S) == 8 || (S) == 0) PJ_CS(c1_, 0, (S) == 8 ? 1 : 4); \
      \
    if ((S) == 0) PJ_WV(10); else if ((S) == 1 || (S) == 8) PJ_WV(9); else if ((S) < 5) PJ_WV(8); else PJ_WV(6); \
    PJ_BAR; \
    if ((S) < 6) PJ_PUB((S) == 0 ? 0 : (S) == 1 ? 1 : (S) == 2 ? 3 : (S) + 2, (S) == 1 ? 2 : (S) == 2 ? 4 : 8); \
    PJ_MMA(S, 1, 1, B1); PJ_BAR; \
    bsel ^= 32768; ao ^= 32768; bo ^= 32768; } while (0)
  f4 acc[2][2][4][2];
#pragma unroll
  for (int a = 0; a < 16; a++) { acc[a >> 3][(a >> 2) & 1][a & 3][0] = (f4){0, 0, 0, 0}; acc[a >> 3][(a >> 2) & 1][a & 3][1] = (f4){0, 0, 0, 0}; }
  bf8 At[4][2], B0[2][2], B1[2][2];
  u32x4 pk[4][2];
  pk[0][0] = (u32x4){0u, 0u, 0u, 0u}; pk[0][1] = (u32x4){0u, 0u, 0u, 0u};
  PJ_UNIT(0);
  PJ_SB(65536, 0); PJ_SA(0, sa + 128);
  PJ_SB(65536 + 16384, 256); PJ_SA(16384, sa + 128 + 128 * k2);
  if (wr == 1) PJ_BAR;
  PJ_WV(4); PJ_BAR;
  PJ_SB(65536 + 32768, 64 * 8192); PJ_SA(32768, sa + 256);
  PJ_SB(65536 + 32768 + 16384, 64 * 8192 + 256);
  PJ_WV(6); PJ_BAR;
  sa += 256; sb = 128 * 8192;
  int bsel = 0, dco = 0, dfl = 0, dlim = 0;
#pragma unroll 1
  for (int u = 0, un; u < 4; u = un) {
    un = (u == 0 && (j & 3) == 3) ? 2 : u + 1;
    PJ_STEP(0); PJ_STEP(1); PJ_STEP(2); PJ_STEP(3); PJ_STEP(4); PJ_STEP(5);
    int more = k2 - 1024; asm volatile("" : "+s"(more));
    if (more) PJ_STEP(6);
    PJ_STEP(7); PJ_STEP(8);
  }
  PJ_ST();
  PJ_NOP;
  { const u32x4 c1_ = PJ_CR(1, 1); PJ_CS(c1_, 1, 1); }
  PJ_DS(PJ_PK(2, 0), 0, 2); PJ_DS(PJ_PK(2, 1), 1, 2); PJ_DS(pk[0][0], 0, 3); PJ_DS(pk[0][1], 1, 3);
#pragma unroll
  for (int q = 4; q < 8; q++) { PJ_DS(PJ_PK(q, 0), 0, q); PJ_DS(PJ_PK(q, 1), 1, q); }
  PJ_WV(0);
  if (wr == 0) PJ_BAR;
  PJ_BAR;
  if (wid == 4) __builtin_amdgcn_raw_buffer_store_b32(8u, rc, lane < 8 ? (dfl + lane * BLKS) * 4 : PJ_OOB, 0, 16);
  PJ_ST();
#undef PJ_UNIT
#undef PJ_STAGE
#undef PJ_SA
#undef PJ_SB
#undef PJ_LDA
#undef PJ_LDB
#undef PJ_TR
#undef PJ_PK
#undef PJ_CW
#undef PJ_CR
#undef PJ_CS
#undef PJ_DS
#undef PJ_FLAG
#undef PJ_STEP
#undef PJ_PUB
#undef PJ_MMA
}

namespace fa {
constexpr int LD = 4096;
constexpr float SCALE = 0.08838834764831845f;
constexpr int NW = 8, QBLK = 32, KVBLK = 64, QB = NW * QBLK, D = 128;
constexpr int SHM_V = KVBLK * D * 2, SHM_K = KVBLK * D * 2;
#define SBAR() __builtin_amdgcn_sched_barrier(0)
#define MF32(a, b, c) __builtin_amdgcn_mfma_f32_32x32x16_bf16(a, b, c, 0, 0, 0)
#define FMX3(a, b, c) __builtin_elementwise_maximum(__builtin_elementwise_maximum(a, b), c)
__device__ __forceinline__ void attn_block(const u16* __restrict__ Qp, const u16* __restrict__ Kh, const u16* __restrict__ Vh,
                                           u16* __restrict__ Op, int P0, char* lds, const int wv, u32* qh, int& pre) {
  const int tid = TIDX, wid = wv, lane = tid & 63, r32 = lane & 31, hi = lane >> 5, grp = wid >> 2;
  const int NT = (P0 + QB) / KVBLK;
  const int qlo = P0 + wid * QBLK;
  float m_reg = -1e30f, l_reg = 0.f;
  f32x16 o[4] = {}, s0, s1;
  int ko = r32 * 256 + ((hi * 16) ^ ((r32 & 15) << 4));
  int vo = 4 * SHM_K + (((lane & 3) << 3) | (((lane >> 2) & 3) << 6) | (((lane >> 4) & 1) << 5) | (hi << 8));
  const __amdgpu_buffer_rsrc_t rk = RSRC(Kh, (P0 + QB - 1) * LD * 2 + 256), rv = RSRC(Vh, (P0 + QB - 1) * LD * 2 + 256);
  const int gk = (wid * 4 + (lane >> 4)) * LD * 2 + (((lane & 15) ^ ((wid & 3) * 4 + (lane >> 4))) << 4);
  const int gv = ((wid >> 1) * 8 + ((lane >> 2) & 7)) * LD * 2 + ((wid & 1) * 2 + hi) * 64 + (lane & 3) * 16;
#define DMA(rs, go, dst, so) __builtin_amdgcn_raw_ptr_buffer_load_lds(rs, (LDSP void*)(lds + (dst) + wid * 1024), 16, go, so, 0, 0)
#define ISSUE_K(t) do { const int s_ = (t) * (KVBLK * LD * 2), d_ = ((t) & 3) * SHM_K; DMA(rk, gk, d_, s_); DMA(rk, gk, d_ + 8192, s_ + 32 * LD * 2); } while (0)
#define ISSUE_V(t) do { const int s_ = (t) * (KVBLK * LD * 2), d_ = 4 * SHM_K + ((t) & 3) * SHM_V; DMA(rv, gv, d_, s_); DMA(rv, gv, d_ + 8192, s_ + 32 * LD * 2); } while (0)
#define PHASE(n) do { SBAR(); asm volatile("s_waitcnt vmcnt(" #n ")" ::: "memory"); __builtin_amdgcn_s_barrier(); asm volatile("" ::: "memory"); SBAR(); } while (0)
#define LDK(d0) do { const char* a_ = lds + (ko ^ ((d0) * 32)); \
    kf[(d0) % 3][0] = *(const bf8*)a_; kf[(d0) % 3][1] = *(const bf8*)(a_ + 32 * 256); } while (0)
#define LDV(i) do { const char* a_ = lds + vo + ((i) & 3) * 512 + ((i) >> 2) * 4096; vf[(i) & 3] = cat8(trrd(a_), trrd(a_ + 2048)); } while (0)
#define AST(i)
#define QK(NEXT, td) do { _Pragma("unroll") for (int d0 = 0; d0 < 8; ++d0) { \
      if (d0 + 2 < 8) LDK(d0 + 2); else { NEXT(2 * (d0 - 6)); NEXT(2 * (d0 - 6) + 1); } \
      SBAR(); \
      s0 = MF32(kf[d0 % 3][0], qr[d0], d0 ? s0 : f32x16{}); s1 = MF32(kf[d0 % 3][1], qr[d0], d0 ? s1 : f32x16{}); \
      SBAR(); \
      if ((td) >= 0 && d0 < 4) { const int u_ = (td) + 1 - (d0 >> 1); \
        DMA(d0 < 2 ? rk : rv, d0 < 2 ? gk : gv, (d0 >> 1) * 4 * SHM_K + (u_ & 3) * SHM_K + (d0 & 1) * 8192, (u_ * KVBLK + (d0 & 1) * 32) * LD * 2); } } } while (0)
#define NOV(i)
#define MASK(t) do { if ((t) * KVBLK + KVBLK - 1 > qlo) { \
      const int dq = qlo + r32 - 4 * hi - (t) * KVBLK; \
      _Pragma("unroll") for (int r = 0; r < 16; ++r) { \
        const int c = (r & 3) + 8 * (r >> 2); \
        if (dq < c) s0[r] = -__builtin_inff(); \
        if (dq < c + 32) s1[r] = -__builtin_inff(); } } } while (0)
#define MAXS(i) do { \
      if ((i) == 0) { mx = FMX3(s0[0], s0[1], m_reg); my = FMX3(s0[2], s0[3], s0[4]); } \
      else if ((i) == 1) { mx = FMX3(mx, s0[5], s0[6]); my = FMX3(my, s0[7], s0[8]); } \
      else if ((i) == 2) { mx = FMX3(mx, s0[9], s0[10]); my = FMX3(my, s0[11], s0[12]); } \
      else if ((i) == 3) { mx = FMX3(mx, s0[13], s0[14]); my = FMX3(my, s0[15], s1[0]); } \
      else if ((i) < 7) { mx = FMX3(mx, s1[4 * (i) - 15], s1[4 * (i) - 14]); my = FMX3(my, s1[4 * (i) - 13], s1[4 * (i) - 12]); } \
      else if ((i) == 7) { mx = FMX3(mx, s1[13], s1[14]); mx = FMX3(mx, my, s1[15]); } \
      else if ((i) == 8) { auto rr = __builtin_amdgcn_permlane32_swap(__float_as_uint(mx), __float_as_uint(mx), false, false); \
        mx = __builtin_elementwise_maximum(__uint_as_float(rr[0]), __uint_as_float(rr[1])); } \
      else { alpha = __builtin_amdgcn_exp2f((m_reg - mx) * C2); m_reg = mx; mc = -mx * C2; asm volatile("" : "+v"(alpha), "+v"(mc)); } } while (0)
  constexpr float C2 = 1.4426950408889634f * SCALE;
  bf8 qr[8], kf[3][2], vf[4], pa[4];
  float mx, my, alpha, mc;
#pragma unroll
  for (int d0 = 0; d0 < 8; ++d0) qr[d0] = *(const bf8*)(Qp + (size_t)(wid * QBLK + r32) * LD + d0 * 16 + hi * 8);
  SBAR();
  ISSUE_K(0); ISSUE_K(1); ISSUE_V(0); ISSUE_K(2); ISSUE_V(1); ISSUE_K(3); ISSUE_V(2);
  PHASE(12);
  if (grp) PHASE(8);
  LDK(0); LDK(1);
  QK(NOV, -1);
  MASK(0);
#pragma unroll
  for (int i = 0; i < 10; ++i) MAXS(i);
  PHASE(8);
  auto tile = [&](const int t, auto LASTC) __attribute__((always_inline)) {
    constexpr bool LAST = decltype(LASTC)::value;
    AST(0);
    float x[32], sa = 0.f, sb = 0.f;
    u32 pw[16];
#pragma unroll
    for (int r = 0; r < 19; ++r) {
      SBAR();
      if (r < 16) { x[2 * r] = __builtin_fmaf(r < 8 ? s0[2 * r] : s1[2 * r - 16], C2, mc); x[2 * r + 1] = __builtin_fmaf(r < 8 ? s0[2 * r + 1] : s1[2 * r - 15], C2, mc); }
      if (r >= 1 && r < 17) { x[2 * r - 2] = __builtin_amdgcn_exp2f(x[2 * r - 2]); x[2 * r - 1] = __builtin_amdgcn_exp2f(x[2 * r - 1]); }
      if (r >= 3) {
        sa = r == 3 ? x[0] : sa + x[2 * r - 6]; asm volatile("" : "+v"(sa));
        sb = r == 3 ? x[1] : sb + x[2 * r - 5];
        pw[r - 3] = pk2(x[2 * r - 6], x[2 * r - 5]); }
    }
    SBAR();
    l_reg = l_reg * alpha + (sa + sb);
#pragma unroll
    for (int i = 0; i < 4; ++i) pa[i] = __builtin_bit_cast(bf8, (u32x4){pw[4 * i], pw[4 * i + 1], pw[4 * i + 2], pw[4 * i + 3]});
    asm volatile("" : "+v"(pa[0]), "+v"(pa[1]), "+v"(pa[2]), "+v"(pa[3]), "+v"(l_reg));
    ko ^= (t & 1) ? 3 * SHM_K : SHM_K;
    SBAR();
    AST(3);
    if constexpr (!LAST) { LDK(0); LDK(1); } else { LDV(0); LDV(1); LDV(2); LDV(3); }
    SBAR();
    if (__any(alpha < 1.f)) {
#define RS_PLAIN 2
#pragma unroll
      for (int d = 0; d < RS_PLAIN; ++d)
#pragma unroll
        for (int r = 0; r < 16; ++r) { float v = o[d][r] * alpha; asm volatile("" : "+v"(v)); o[d][r] = v; }
      SBAR();
#pragma unroll
      for (int d = RS_PLAIN; d < 4; ++d) o[d] *= alpha;
    }
    AST(4);
    PHASE(4);
    AST(5);
#define APR 1
    __builtin_amdgcn_s_setprio(APR == 1 ? 1 : 0);
    if constexpr (!LAST) QK(LDV, t + 3);
    AST(6);
#pragma unroll
    for (int i = 0; i < 16; ++i) {
      SBAR();
      o[i & 3] = MF32(vf[i & 3], pa[i >> 2], o[i & 3]);
      SBAR();
      if (i + 4 < 16) LDV(i + 4);
      if constexpr (!LAST) {
        if (i == 1) MASK(t + 1);
        if (i >= 2 && i < 12) MAXS(i - 2);
      }
    }
    __builtin_amdgcn_s_setprio(APR == 2 && !LAST ? 1 : 0);
    vo ^= (t & 1) ? 3 * SHM_V : SHM_V;
    AST(7);
    PHASE(8);
  };
#pragma unroll 1
  for (int t = 0; t < NT - 1; t++) {
    tile(t, std::false_type{});
  }
  tile(NT - 1, std::true_type{});
  CLAIM();
  {
    const float inv = __builtin_amdgcn_rcpf(swadd(l_reg, l_reg));
    char* tw = lds + 4 * SHM_K + 4 * SHM_V + (wid & 3) * 8192;
#pragma unroll
    for (int c = 0; c < 16; ++c) {
      const int g = (c & 3) * 4;
      *(u32x2*)(tw + r32 * 256 + ((c ^ (r32 & 15)) << 4) + hi * 8) =
          (u32x2){pk2(o[c >> 2][g] * inv, o[c >> 2][g + 1] * inv), pk2(o[c >> 2][g + 2] * inv, o[c >> 2][g + 3] * inv)};
    }
    __builtin_amdgcn_fence(__ATOMIC_RELEASE, "wavefront");
    __builtin_amdgcn_wave_barrier();
    const __amdgpu_buffer_rsrc_t ro = RSRC(Op + (size_t)(wid * QBLK) * LD, 32 * LD * 2);
#pragma unroll
    for (int i = 0; i < 8; ++i) { const int row = i * 4 + (lane >> 4), ch = lane & 15;
      const u32x4 v = *(const u32x4*)(tw + row * 256 + ((ch ^ (row & 15)) << 4));
      __builtin_amdgcn_raw_buffer_store_b128(v, ro, row * LD * 2 + ch * 16, 0, 16); }
  }
  if (!grp) { asm volatile("s_waitcnt lgkmcnt(0)" ::: "memory"); __builtin_amdgcn_s_barrier(); }
  VMW0();
  __syncthreads();
}
#undef DMA
#undef ISSUE_K
#undef ISSUE_V
#undef PHASE
#undef LDK
#undef LDV
#undef QK
#undef NOV
#undef MASK
#undef MAXS
}

__device__ __forceinline__ void wof_task(const Params& p, int kc, char* smem, const int wv, u32* qh, int& pre) {
  const int tid = TIDX, lane = tid & 63, wid = wv;
  const int l15 = lane & 15, lq = lane >> 4, q4 = l15 >> 2, p4 = lane & 3;
  const __amdgpu_buffer_rsrc_t rw = RSRC(WSP(u16, O_WOF), 576 * 4096 * 2);
#pragma unroll
  for (int i = 0; i < 9; i++) {
    int id = tid + 512 * i, row = id / 72, ch = id % 72;
    *(bf8*)(smem + row * 1280 + (((ch >> 1) ^ fsw(row)) << 5) + ((ch & 1) << 4)) =
        *(const bf8*)(p.BWo + (size_t)(kc * 64 + row) * 576 + ch * 8);
  }
  CLAIM();
  __syncthreads();
#pragma unroll
  for (int i = 0; i < 9; i++) {
    const int f = wid * 9 + i, nt = f >> 1, k2 = f & 1;
    const int r0 = k2 * 32 + lq * 8 + q4, r1 = r0 + 4;
    bf4 lo = trrd(smem + r0 * 1280 + ((nt ^ fsw(r0)) << 5) + p4 * 8);
    bf4 hi = trrd(smem + r1 * 1280 + ((nt ^ fsw(r1)) << 5) + p4 * 8);
    __builtin_amdgcn_raw_buffer_store_b128(__builtin_bit_cast(u32x4, cat8(lo, hi)), rw, ((nt * 128 + kc * 2 + k2) * 64 + lane) * 16, 0, 16);
  }
  VMW0();
  __syncthreads();
}

__device__ __forceinline__ void small_out_task(const Params& p, int s0, int hf, int nt, char* smem, const int wv, u32* qh, int& pre) {
  const int tid = TIDX, lane = tid & 63, wid = wv;
  const int l15 = lane & 15, lq = lane >> 4;
  const __amdgpu_buffer_rsrc_t rw = RSRC(WSP(u16, O_WOF), 576 * 4096 * 2), ro = RSRC(WSP(u16, O_OB), 2048 * 4096 * 2);
  if (tid == 0) {
    int it = 0;
    while (ld4_sc1(WSP(u32, 0) + SY_CW) < 64u && ++it < SPIN_CAP) __builtin_amdgcn_s_sleep(8);
  }
  __syncthreads();
  bf8 bfr[16];
#pragma unroll
  for (int k = 0; k < 16; k++) bfr[k] = ldsc1(rw, ((nt * 128 + wid * 16 + k) * 64 + lane) * 16);
#pragma unroll 1
  for (int s = s0; s < 64; s += 2) {
    if (tid < 64) {
      int it = 0;
      if (s < 62) while (ld4_sc1(WSP(u32, 0) + SY_CS(s)) == 0u && ++it < SPIN_CAP) __builtin_amdgcn_s_sleep(SPS);
      else {
        while (ld4_sc1(WSP(u32, 0) + SY_CS(61)) == 0u && ++it < SPIN_CAP) __builtin_amdgcn_s_sleep(SPS);
        while (!__all(ld4_sc1(WSP(u32, 0) + SY_SEG(s - 62, tid & 31)) != 0u) && ++it < SPIN_CAP) __builtin_amdgcn_s_sleep(4);
      }
    }
    __syncthreads();
    const int row0 = s * 32 + hf * 16;
    bf8 af[16];
#pragma unroll
    for (int k = 0; k < 16; k++) af[k] = ldsc1(ro, OB_OFF(row0 + l15, (wid * 16 + k) * 32 + lq * 8));
    if (s >= 62) CLAIM();
    f4 acc = (f4){0, 0, 0, 0};
#pragma unroll
    for (int k = 0; k < 16; k++) acc = MFMA(bfr[k], af[k], acc);
    *(f4*)(smem + (wid * 64 + lane) * 16) = acc;
    __syncthreads();
    if (tid < 64) {
      f4 t = (f4){0, 0, 0, 0};
#pragma unroll
      for (int w = 0; w < 8; w++) t += *(const f4*)(smem + (w * 64 + lane) * 16);
      *(u64*)(p.out + (size_t)(row0 + l15) * 1152 + 576 + nt * 16 + lq * 4) = (u64)pk2(t[0], t[1]) | ((u64)pk2(t[2], t[3]) << 32);
    }
  }
  __syncthreads();
}

#define PSTAMP()
#define STAMP(i)
#define ANS 16
#define KSL 1
#define DSH 2
#define K1 24
#define K2 (K1 + 92)
#define F1 0
#define F2 0
#define C_IMG 0
#define C_QT 9216
#define C_KT (C_QT + 3072)
#define C_P (C_KT + 4096)
#define C_PX (C_P + 768)
#define C_KX (C_PX + 2560)
#define C_VX (C_KX + 8192)
#define C_QX (C_VX + 8192)
#define C_WL 49920
__device__ __forceinline__ float ror8(float v) {
  return __uint_as_float(__builtin_amdgcn_mov_dpp(__float_as_uint(v), 0x128, 0xf, 0xf, true));
}
__device__ __forceinline__ float rb1(float f) { return __uint_as_float(pk2(0.f, f)); }
#define FMX(a, b) __builtin_elementwise_maximum(a, b)
__device__ __forceinline__ float tmax(float mx) {
  mx = FMX(mx, ror8(mx));
  { auto t = __builtin_amdgcn_permlane16_swap(__float_as_uint(mx), __float_as_uint(mx), false, false);
    mx = FMX(__uint_as_float(t[0]), __uint_as_float(t[1])); }
  { auto t = __builtin_amdgcn_permlane32_swap(__float_as_uint(mx), __float_as_uint(mx), false, false);
    mx = FMX(__uint_as_float(t[0]), __uint_as_float(t[1])); }
  return mx;
}
__device__ __forceinline__ int wmax(int x) {
  x = max(x, __builtin_amdgcn_mov_dpp(x, 0x128, 0xf, 0xf, true));
  x = max(x, __builtin_amdgcn_mov_dpp(x, 0x124, 0xf, 0xf, true));
  x = max(x, __builtin_amdgcn_mov_dpp(x, 0x4E, 0xf, 0xf, true));
  x = max(x, __builtin_amdgcn_mov_dpp(x, 0xB1, 0xf, 0xf, true));
  { auto t = __builtin_amdgcn_permlane16_swap((u32)x, (u32)x, false, false); x = max((int)t[0], (int)t[1]); }
  { auto t = __builtin_amdgcn_permlane32_swap((u32)x, (u32)x, false, false); x = max((int)t[0], (int)t[1]); }
  return __builtin_amdgcn_readfirstlane(x);
}
typedef __attribute__((ext_vector_type(2))) unsigned long long u64x2;
__device__ __forceinline__ f4 zero4() {
  u64 a, b; asm volatile("v_mov_b64 %0, 0\n\tv_mov_b64 %1, 0\n\ts_nop 1" : "=v"(a), "=v"(b));
  return __builtin_bit_cast(f4, (u64x2){a, b});
}
__device__ __forceinline__ u32x4 ones4() {
  u64 a, b; asm volatile("v_mov_b64 %0, -1\n\tv_mov_b64 %1, -1" : "=v"(a), "=v"(b));
  return __builtin_bit_cast(u32x4, (u64x2){a, b});
}
#define BAR() do { asm volatile("s_waitcnt lgkmcnt(0)" ::: "memory"); __builtin_amdgcn_s_barrier(); asm volatile("" ::: "memory"); } while (0)
#define BARC() do { asm volatile("" ::: "memory"); __builtin_amdgcn_s_waitcnt(0xC07F); __builtin_amdgcn_s_barrier(); asm volatile("" ::: "memory"); } while (0)
#define BARN() do { asm volatile("" ::: "memory"); __builtin_amdgcn_s_barrier(); asm volatile("" ::: "memory"); } while (0)
#define IDX() const int tid = TIDX, lane = tid & 63, l15 = lane & 15, lq = lane >> 4, q4 = l15 >> 2, p4 = lane & 3, hi8 = l15 >> 3; \
    (void)q4; (void)p4; (void)hi8; (void)lq
#define AD_LD() __builtin_amdgcn_raw_buffer_load_b32(rsX, (TIDX & 31) * ANS, X_ANN + (s & 1) * 1024 + xp, 16)
#define AD_WAIT(K, F, CAP) do { int n_ = 0; u32 an = AD_LD(); \
    while (__any((an & 63u) != (u32)s) && ++n_ < (CAP)) { __builtin_amdgcn_s_sleep(1); CFENCE(); an = AD_LD(); } \
    const u32 r_ = (u32)__builtin_amdgcn_readfirstlane((int)an); \
    const u32 dl_ = ((r_ + (u32)wmax((int)(an - r_))) & ~63u) + (u32)(K) * 64u; \
    n_ = 160; \
    while ((int)(((u32)wall_clock64() << 6) - dl_) < 0 && --n_ > 0) {} \
    if (F) __builtin_amdgcn_s_sleep(F); } while (0)
#define STG_AUX 2
#define OSTG_AUX 0
#define STG_D 3
#define STG_POLL 7
#define STS(c, i)

#define DPR(OP, N) OP " %3, %3, %3 row_ror:" #N " row_mask:0xf bank_mask:0xf\n\t" OP " %4, %4, %4 row_ror:" #N " row_mask:0xf bank_mask:0xf\n\t"
#define DPROW(OP) "s_nop 0\n\t" DPR(OP, 8) "s_nop 0\n\t" DPR(OP, 4) "s_nop 0\n\t" DPR(OP, 2) "s_nop 0\n\t" DPR(OP, 1)
#define ANJ 0
#define LSL 2
__device__ __forceinline__ void chain_head(const Params& p, char* smem, const int h, const int wv) {
  STS(h, 1);
  const int wid = wv;
  u32x8 w[2][9], wx[3];
  const __amdgpu_buffer_rsrc_t rsX = RSRC(XWIN(p), X_END);
  const __amdgpu_buffer_rsrc_t rsB = RSRC(WSP(u16, O_QB), 4 * ACB);
  const int cb = h >> 1;
  const int nvt = (wid & 3) == 0 ? 0 : (wid == 5 || wid == 6 ? 2 : 1);
  const int vt = wid < 4 ? wid - 1 : (wid == 7 ? 3 : wid * 2 - 6);
  const int wl = C_WL + (nvt == 1 ? vt * 12288 : 49152 + (nvt == 2 ? wid - 5 : wid >> 2) * 30720);
  int xp;
  {
    IDX();
    u32x4 st[54], nk, nv, nq;
    u32 fl, fv, fk;
    const __amdgpu_buffer_rsrc_t rq = RSRC(p.BWq, 576 * 4096 * 2), rk = RSRC(p.BWk, 576 * 4096 * 2), rv = RSRC(p.BWv, 576 * 4096 * 2);
    const int vw = ((tid >> 4) * 4096 + h * 128 + (tid & 15) * 8) * 2;
    {
      const u32x4 e = ones4();
#pragma unroll
      for (int i = 0; i < 2; i++) {
        const int id = wid + 8 * i;
        __builtin_amdgcn_raw_buffer_store_b128(e, rsX, lane * 16, id < 12 ? X_O + (id >> 2) * 131072 + (((id >> 1) & 1) * 4 + (h >> 3)) * 16384 + (id & 1) * 8192 + (h & 7) * 1024 : PJ_OOB, 16);
      }
      __builtin_amdgcn_raw_buffer_store_b128(e, rsX, tid < 4 ? X_ANN + (tid & 1) * 1024 + (tid >> 1) * 8192 + h * ANS : PJ_OOB, 0, 16);
    }
    const __amdgpu_buffer_rsrc_t rm = RSRC(p.Bm, 8 * 576 * 2);
    const u32x4 m0 = __builtin_amdgcn_raw_buffer_load_b128(rm, tid * 16, 0, 0), m1 = __builtin_amdgcn_raw_buffer_load_b128(rm, tid * 16, 8192, 0);
    auto req = [&](int kc) {
#pragma unroll
      for (int i = 0; i < 6; i++) st[kc * 6 + i] = __builtin_amdgcn_raw_buffer_load_b128(i < 2 ? rq : (i < 4 ? rk : rv), vw, (kc * 64 + (i & 1) * 32) * 8192, STG_AUX);
      SB0();
    };
#pragma unroll
    for (int kc = 0; kc < STG_D; kc++) { req(kc); __builtin_amdgcn_s_barrier(); SB0(); }
    auto cell = [](int row, int slot) { return (((row * 2 + slot) ^ ((((row >> 2) ^ (row >> 4)) & 1) << 2) ^ ((row >> 5) & 1)) << 5); };
    auto put = [&](int unit, int row, const u32x4& v) { *(u32x4*)(smem + unit * 4160 + cell(row, (tid >> 1) & 1) + (tid & 1) * 16) = v; };
    auto frag = [&](int tile) {
      bf4 tt[4];
#pragma unroll
      for (int kk = 0; kk < 4; kk++) {
        const int r0 = (kk >> 1) * 32 + (lq & 1) * 16 + (lq >> 1) * 8 + (kk & 1) * 4 + (q4 & 1) * 2 + (q4 >> 1);
        tt[kk] = trrd(smem + (tile >> 1) * 4160 + cell(r0, tile & 1) + p4 * 8);
      }
      return ilv(tt[0], tt[1], tt[2], tt[3]);
    };
    auto park = [&](int slot, const u32x8& f) {
      char* d = smem + wl + slot * 2048 + lane * 16;
      *(u32x4*)d = (u32x4){f[0], f[1], f[2], f[3]};
      *(u32x4*)(d + 1024) = (u32x4){f[4], f[5], f[6], f[7]};
    };
    auto poll = [&]() {
      fl = ld4_sc1(WSP(u32, 0) + SY_INIT(lane)); fv = ld4_sc1(WSP(u32, 0) + SY_VOTE(lane));
      fk = ld4_sc1(WSP(u32, 0) + SY_BLK(lane < 12 ? lane >> 2 : 0, cb, lane & 3));
    };
#pragma unroll
    for (int kc = 0; kc < 9; kc++) {
      if (kc == STG_POLL) { poll(); SB0(); }
#pragma unroll
      for (int i = 0; i < 6; i++) put((i >> 1) * 4 + ((tid & 15) >> 2), (i & 1) * 32 + (tid >> 4), st[kc * 6 + i]);
      BAR();
      if (kc == 0 && tid == 0) st4_sc1(WSP(u32, 0) + SY_INIT(h), 1u);
      if (kc + STG_D < 9) req(kc + STG_D);
      w[0][kc] = frag(wid * 2);
      w[1][kc] = frag(nvt == 2 ? 16 + vt : wid * 2 + 1);
      const u32x8 tp = frag(nvt == 0 ? 11 + (wid >> 1) : 15 + vt + nvt);
      if (kc >= 4 && kc < 7) { wx[kc - 4] = tp; if (nvt == 0) park(6 + kc, tp); }
      else park(nvt == 0 ? 6 + kc : (kc >= 7 ? kc - 7 : 2 + kc), tp);
      BAR();
      if (kc == 0 || kc == 8) STS(h, kc + 3);
    }
    if (!__all(fl != 0u && fv != 0u && fk >= 8u)) {
      int it = 0;
      do { __builtin_amdgcn_s_sleep(2); poll(); } while (!__all(fl != 0u && fv != 0u && fk >= 8u) && ++it < SPIN_CAP);
    }
    STS(h, 13);
    CFENCE();
    nq = __builtin_amdgcn_raw_buffer_load_b128(rsB, vw, 0, 16);
    nk = __builtin_amdgcn_raw_buffer_load_b128(rsB, vw, ACB, 16);
    nv = __builtin_amdgcn_raw_buffer_load_b128(rsB, vw, 2 * ACB, 16);
    SB0();
#pragma unroll
    for (int i = 0; i < 2; i++) {
      const int id = tid + 512 * i, t = id / 72, hb = id % 72;
      char* d = smem + C_IMG + (hb >> 2) * 512 + (t >> 2) * 256 + (((hb & 3) ^ ((t >> 2) * 2)) << 6) + ((t >> 1) & 1) * 8 + (t & 1) * 2;
      if (i == 0 || tid < 64) {
#pragma unroll
        for (int e = 0; e < 8; e++) *(u16*)(d + (e >> 1) * 16 + (e & 1) * 4) = (u16)((i ? m1 : m0)[e >> 1] >> ((e & 1) * 16));
      }
    }
    xp = __popcll(__ballot(fv == 2u)) > 32 ? 8192 : 0;
    const int r = tid >> 4, ch = tid & 15;
    *(u32x4*)(smem + C_QX + r * 256 + ((ch ^ (r & 15)) << 4)) = nq;
    *(u32x4*)(smem + C_KX + r * 256 + ((ch ^ (r & 15)) << 4)) = nk;
    *(u32x4*)(smem + C_VX + r * 256 + ((ch ^ ((r & 7) << 1)) << 4)) = nv;
  }
  __syncthreads();
  STS(h, 14);
  constexpr float CE = 0.08838834764831845f * 1.4426950408889634f;
  auto run = [&](auto RC) __attribute__((always_inline)) {
  constexpr int R = decltype(RC)::value;
  constexpr int NV = R < 2 ? 0 : (R < 4 ? 1 : 2);
  constexpr bool QW = R == 0 || R == 2;
  int known = 0;
  bf8 pre[4][2], vx[NV ? NV : 1];
  int sidx, mo, mo1, eo, gvo, wvo = 0, so = 0, po = 0, qo = 0, ko = 0, ko1 = 0, fo = 0;
  float mk[4];
#define LDW(dst, slot) do { dst[0] = *(const bf8*)(smem + wvo + (slot) * 2048); dst[1] = *(const bf8*)(smem + wvo + (slot) * 2048 + 1024); } while (0)
  {
    IDX();
    if constexpr (R == 1) {
      fo = (lq * 4096 + h * 128 + l15 * 8) * 2;
      asm volatile("" : "+v"(fo));
    } else if constexpr (R == 0) {
      qo = C_QT + lq * 384 + q4 * 32 + p4 * 8; ko = C_KT + ((((lq * 8 + q4) * 32) ^ ((lq & 1) << 7)) + p4 * 8); ko1 = C_KT + ((((lq * 8 + 4 + q4) * 32) ^ ((lq & 1) << 7)) + p4 * 8);
      po = C_P + l15 * 16 + lq * 4;
      fo = l15 * 256 + ((lq ^ l15) << 4);
#pragma unroll
      for (int r = 0; r < 4; r++) { mk[r] = lq * 4 + r <= l15 ? 0.f : -__builtin_inff(); asm volatile("" : "+v"(mk[r])); }
      asm volatile("" : "+v"(qo), "+v"(ko), "+v"(ko1), "+v"(po), "+v"(fo));
    } else {
      wvo = wl + lane * 16;
      po = C_P + (lq * 4 + q4) * 16 + (p4 & 1) * 8;
      so = ((l15 >> 2) & 1) * 65536 + ((vt + (NV == 2 ? hi8 : 0)) * 8 + ((l15 >> 1) & 1) * 4 + (lq >> 1) * 2 + (l15 & 1)) * 16 + (lq & 1) * 8;
      if (NV == 1 && hi8) so = PJ_OOB;
      asm volatile("" : "+v"(wvo), "+v"(po), "+v"(so));
#pragma unroll
      for (int j = 0; j < 4; j++) LDW(pre[j], NV == 2 ? 6 + j : j);
    }
    eo = QW ? C_QT + wid * 768 + ((l15 >> 2) * 2 + (l15 & 1)) * 32 + lq * 8 + ((l15 >> 1) & 1) * 4
            : C_KT + (wid & 3) * 1024 + ((l15 * 32) ^ (hi8 << 7)) + lq * 8;
    sidx = (l15 & 2) ? 0xEEEE : 0x4444;
    mo = C_IMG + (lq >> 1) * 512 + (p4 >> 1) * (256 + 128) + (lq & 1) * 64 + q4 * 16 + (p4 & 1) * 8; mo1 = mo ^ 128;
    gvo = tid * 16;
    asm volatile("" : "+v"(sidx), "+v"(mo), "+v"(mo1), "+v"(eo), "+v"(gvo));
  }
  f4 a0, a1;
  u32 ts = 0;
  u64 tq = 0;
  int dur = 70 * 64;
  const int ob = (h >> 3) * 16384 + (h & 7) * 1024 + xp;
  int b3 = 0;
#pragma unroll 1
  for (int s = 0;; s++) {
    int more = s; asm volatile("" : "+s"(more));
    const int mb = X_M + b3 * 32768 + xp;
    u32x4 g, gx;
    if (more) {
      BAR();
      g = __builtin_amdgcn_raw_buffer_load_b128(rsX, gvo, mb, 16);
      if constexpr (R == 1) gx = __builtin_amdgcn_raw_buffer_load_b128(rsX, gvo, mb + 16384 - 4096, 16);
    } else asm volatile("" : "=v"(g), "=v"(gx));
    SB0();
    if constexpr (R == 0) {
#pragma unroll
      for (int j = 0; j < 4; j++) { const char* kx_ = smem + C_KX + (fo ^ (j * 64));
        pre[j >> 1][j & 1] = *(const bf8*)kx_; pre[2 + (j >> 1)][j & 1] = *(const bf8*)(kx_ + 4096); }
    } else if constexpr (R > 1) {
      IDX();
#pragma unroll
      for (int d = 0; d < NV; d++) { const char* vb_ = smem + C_VX + (lq * 4 + q4) * 256 + (((vt + d) ^ ((lq & 1) * 4 + q4)) << 5) + p4 * 8;
        vx[d] = cat8(trrd(vb_), trrd(vb_ + 4096)); }
    }
    SB0();
    a0 = zero4(); a1 = zero4();
    int so_ = X_O + b3 * 131072 + ob, an_ = X_ANN + (s & 1) * 1024 + xp + h * ANS;
    int nb = s < 63 ? (s + 1) * 32 * 8192 : PJ_OOB;
    asm volatile("" : "+s"(so_), "+s"(an_), "+s"(nb));
    u32x4 nx[R == 1 ? 16 : 2];
    SB0();
    if (more) {
      VMW0C();
      *(u32x4*)(smem + C_IMG + gvo) = g;
      if constexpr (R == 1) *(u32x4*)(smem + C_IMG + 4096 + gvo) = gx;
      SB0();
#define G_BAD() (umax(umax(umax(g[0], g[1]), umax(g[2], g[3])), R != 1 ? 0u : umax(umax(gx[0], gx[1]), umax(gx[2], gx[3]))) == ~0u)
      if (__builtin_expect(__any(G_BAD()), 0)) {
        int it = 0;
        do {
          CFENCE();
          if (G_BAD()) {
            g = __builtin_amdgcn_raw_buffer_load_b128(rsX, gvo, mb, 16);
            if constexpr (R == 1) gx = __builtin_amdgcn_raw_buffer_load_b128(rsX, gvo, mb + 16384 - 4096, 16);
          }
          VMW0C();
        } while (__any(G_BAD()) && ++it < SPIN_CAP);
        *(u32x4*)(smem + C_IMG + gvo) = g;
        if constexpr (R == 1) *(u32x4*)(smem + C_IMG + 4096 + gvo) = gx;
      }
    }
    SB0();
    STAMP(15);
    BARC();
    STAMP(0);
#define LDM(j) cat8(trrd(smem + mo + (j) * 1024), trrd(smem + mo1 + (j) * 1024))
    bf8 b[5];
    if constexpr (R == 1) { asm volatile("s_memrealtime %0" : "=s"(tq)); SB0(); }
#pragma unroll
    for (int j = 0; j < 4; j++) { b[j] = LDM(j); SB0(); }
#pragma unroll
    for (int j = 0; j < 9; j++) {
      if (j + 4 < 9) b[(j + 4) % 5] = LDM(j + 4);
      SB0();
      a0 = SMF(b[j % 5], w[0][j], a0, sidx);
      if constexpr (NV < 2) a1 = SMF(b[j % 5], w[1][j], a1, sidx);
      else a1 = SMF(b[j % 5], cat16(pre[j & 3][0], pre[j & 3][1]), a1, sidx);
      SB0();
      if constexpr (R == 1) {
        if (j == ANJ) {
          asm volatile("s_waitcnt lgkmcnt(0)" : "+s"(tq));
          ts = (u32)tq;
          __builtin_amdgcn_raw_buffer_store_b32((((ts << 6) + (u32)dur) & ~63u) | (u32)s, rsX, 0, an_, 16);
          SB0();
        }
        if (j > 0) {
#pragma unroll
          for (int i = 2 * j - 2; i < 2 * j; i++) nx[i] = __builtin_amdgcn_raw_buffer_load_b128(rsB, fo, nb + (1 + (i >> 3)) * ACB + (i & 7) * 4 * 8192, 16);
          SB0();
        }
      }
      if constexpr (NV == 2) LDW(pre[j & 3], j < 5 ? 10 + j : j - 5);
    }
    {
      const u32 e0 = pk2(a0[0] + a0[2], a0[1] + a0[3]), e1 = pk2(a1[0] + a1[2], a1[1] + a1[3]);
      if constexpr (QW) asm volatile("ds_write2_b32 %0, %1, %2 offset1:96" :: "v"(eo), "v"(e0), "v"(e1) : "memory");
      else asm volatile("ds_write2_b32 %0, %1, %1 offset1:1\n\tds_write2_b32 %0, %2, %2 offset0:128 offset1:129" :: "v"(eo), "v"(e0), "v"(e1) : "memory");
    }
    SB0();
    if constexpr (R != 1) { a0 = zero4(); a1 = zero4(); }
    if constexpr (NV == 0) {
      f4 a2;
      if constexpr (R == 0) a2 = zero4();
      STAMP(1);
      BARC();
      STAMP(2);
      if constexpr (R == 0) {
        bf8 qa[2], km[4];
#pragma unroll
        for (int i = 0; i < 2; i++) { qa[i] = cat8(trrd(smem + qo + i * 1536), trrd(smem + qo + i * 1536 + 128)); SB0(); }
#pragma unroll
        for (int i = 0; i < 4; i++) { km[i] = cat8(trrd(smem + ko + i * 1024), trrd(smem + ko1 + i * 1024)); SB0(); }
#pragma unroll
        for (int i = 0; i < 2; i++) {
          a0 = SMF(qa[i], cat16(pre[i][0], pre[i][1]), a0, sidx); asm volatile("" : "+v"(a0)); SB0();
          a1 = SMF(qa[i], cat16(pre[2 + i][0], pre[2 + i][1]), a1, sidx); asm volatile("" : "+v"(a1)); SB0();
        }
#pragma unroll
        for (int i = 0; i < 2; i++) { a2 = SMF(qa[i], cat16(km[2 * i], km[2 * i + 1]), a2, sidx); asm volatile("" : "+v"(a2)); SB0(); }
        float e0 = a0[0] + a0[2], e1 = a0[1] + a0[3], e2 = a1[0] + a1[2], e3 = a1[1] + a1[3];
        asm volatile("" : "+v"(e0), "+v"(e1), "+v"(e2), "+v"(e3));
        SB0();
        float e4 = a2[0] + a2[2], e5 = a2[1] + a2[3], x0, x1;
        STAMP(3);
        asm volatile("v_max3_f32 %3, %0, %1, %2\n\tv_max3_f32 %4, %5, %6, %7\n\t" DPROW("v_max_f32_dpp")
                     "v_mul_f32 %3, %8, %3\n\tv_mul_f32 %4, %8, %4\n\t"
                     "v_fma_f32 %0, %0, %8, -%3\n\tv_fma_f32 %5, %5, %8, -%4\n\tv_exp_f32 %0, %0\n\tv_exp_f32 %5, %5\n\t"
                     "v_fma_f32 %1, %1, %8, -%3\n\tv_fma_f32 %6, %6, %8, -%4\n\tv_exp_f32 %1, %1\n\tv_exp_f32 %6, %6\n\t"
                     "v_fma_f32 %2, %2, %8, -%3\n\tv_fma_f32 %7, %7, %8, -%4\n\tv_exp_f32 %2, %2\n\tv_exp_f32 %7, %7\n\t"
                     "v_add_f32 %3, %0, %1\n\tv_add_f32 %4, %5, %6\n\tv_fmac_f32 %3, 0.5, %2\n\tv_fmac_f32 %4, 0.5, %7\n\t" DPROW("v_add_f32_dpp")
                     "v_rcp_f32 %3, %3\n\tv_rcp_f32 %4, %4\n\t"
                     "v_mul_f32 %0, %0, %3\n\tv_mul_f32 %5, %5, %4\n\tv_mul_f32 %1, %1, %3\n\tv_cvt_pk_bf16_f32 %0, %0, %5\n\t"
                     "v_mul_f32 %6, %6, %4\n\tv_mul_f32 %2, %2, %3\n\tv_cvt_pk_bf16_f32 %1, %1, %6\n\tv_mul_f32 %7, %7, %4\n\t"
                     "ds_write2st64_b32 %9, %0, %1 offset1:1\n\tv_cvt_pk_bf16_f32 %2, %2, %7\n\tds_write_b32 %9, %2 offset:512"
                     : "+v"(e0), "+v"(e2), "+v"(e4), "=&v"(x0), "=&v"(x1), "+v"(e1), "+v"(e3), "+v"(e5) : "s"(CE), "v"(po) : "memory");
        SB0();
      } else {
        const int rb = X_O + (b3 == 0 ? 2 : b3 - 1) * 131072 + ob - 4096;
        const u32x4 e = ones4();
        __builtin_amdgcn_raw_buffer_store_b128(e, rsX, gvo, rb, 16);
        __builtin_amdgcn_raw_buffer_store_b128(e, rsX, gvo, rb + 65536, 16);
        if (s == 63 && gvo == 4096) st4_sc1(WSP(u32, 0) + SY_SEG(0, h), 1u);
        PSTAMP();
      }
      STAMP(5);
      BARC();
      STAMP(6);
      if constexpr (R == 1) {
        const u32 tp = (u32)wall_clock64(); dur += ((int)umin((tp - ts) << 6, 200u * 64u) - dur) >> DSH;
        SB0();
        IDX();
        const int pk_ = lq * 256 + ((l15 ^ lq) << 4), pv_ = lq * 256 + ((l15 ^ (lq << 1)) << 4);
#pragma unroll
        for (int i = 0; i < 8; i++) *(u32x4*)(smem + C_KX + (pk_ ^ ((i & 3) * 64)) + i * 1024) = nx[i];
#pragma unroll
        for (int i = 0; i < 8; i++) *(u32x4*)(smem + C_VX + (pv_ ^ ((i & 1) * 128)) + i * 1024) = nx[8 + i];
        SB0();
        STAMP(8);
        BARN();
        if (s == 63) break;
      } else {
        bf8 km[4];
#pragma unroll
        for (int ks = 0; ks < 4; ks++) km[ks] = cat8(trrd(smem + ko + ks * 1024), trrd(smem + ko1 + ks * 1024));
#pragma unroll
        for (int NT = 1; NT <= 2; NT++) {
          f4 sc[2], sm;
          bf8 qf[4];
#pragma unroll
          for (int ks = 0; ks < 4; ks++) qf[ks] = *(const bf8*)(smem + C_QX + (NT - 1) * 4096 + (fo ^ (ks * 64)));
#pragma unroll
          for (int ks = 0; ks < 4; ks++) {
#pragma unroll
            for (int j = 0; j < NT; j++) sc[j] = MFMA(pre[2 * j + (ks >> 1)][ks & 1], qf[ks], ks ? sc[j] : (j == NT - 1 ? ((f4){mk[0], mk[1], mk[2], mk[3]}) : ((f4){0, 0, 0, 0})));
            sm = MFMA(km[ks], qf[ks], ks ? sm : ((f4){0, 0, 0, 0}));
          }
          float mx = FMX3(FMX3(sc[0][0], sc[0][1], sc[0][2]), sc[0][3], FMX(sm[0], sm[1]));
          if (NT == 2) mx = FMX3(FMX3(mx, sc[1][0], sc[1][1]), sc[1][2], sc[1][3]);
          { auto t = __builtin_amdgcn_permlane16_swap(__float_as_uint(mx), __float_as_uint(mx), false, false);
            mx = FMX(__uint_as_float(t[0]), __uint_as_float(t[1])); }
          { auto t = __builtin_amdgcn_permlane32_swap(__float_as_uint(mx), __float_as_uint(mx), false, false);
            mx = FMX(__uint_as_float(t[0]), __uint_as_float(t[1])); }
          mx *= -CE;
#pragma unroll
          for (int j = 0; j < NT; j++)
#pragma unroll
            for (int r = 0; r < 4; r++) sc[j][r] = __builtin_amdgcn_exp2f(__builtin_fmaf(sc[j][r], CE, mx));
          sm[0] = __builtin_amdgcn_exp2f(__builtin_fmaf(sm[0], CE, mx)); sm[1] = __builtin_amdgcn_exp2f(__builtin_fmaf(sm[1], CE, mx));
          float sum = ((sc[0][0] + sc[0][1]) + (sc[0][2] + sc[0][3])) + (sm[0] + sm[1]);
          if (NT == 2) sum += (sc[1][0] + sc[1][1]) + (sc[1][2] + sc[1][3]);
          { auto t = __builtin_amdgcn_permlane16_swap(__float_as_uint(sum), __float_as_uint(sum), false, false);
            sum = __uint_as_float(t[0]) + __uint_as_float(t[1]); }
          sum = swadd(sum, sum);
          const float inv = __builtin_amdgcn_rcpf(sum);
          *(u32x4*)(smem + C_PX + (NT - 1) * 1024 + gvo) = (u32x4){pk2(sc[0][0] * inv, sc[0][1] * inv), pk2(sc[0][2] * inv, sc[0][3] * inv),
                                                                  NT == 2 ? pk2(sc[1][0] * inv, sc[1][1] * inv) : 0u, NT == 2 ? pk2(sc[1][2] * inv, sc[1][3] * inv) : 0u};
          *(u32*)(smem + C_PX + 2048 + (NT - 1) * 256 + (gvo >> 2)) = pk2(sm[0] * inv, sm[1] * inv);
        }
        STAMP(8);
        BAR();
        if (s == 63) break;
        if (known < (s < 61 ? s + 3 : 64)) {
          int n = 0;
          while (true) {
            u32 bf[3];
#pragma unroll
            for (int i = 0; i < 3; i++) bf[i] = ld4_sc1(WSP(u32, 0) + SY_BLK(i, cb, gvo >> 4));
            const u64 miss = __ballot(!(bf[0] >= 8u && bf[1] >= 8u && bf[2] >= 8u));
            known = miss ? __builtin_ctzll(miss) : 64;
            if (known >= (s < 61 ? s + 3 : 64) || ++n >= SPIN_CAP) break;
            __builtin_amdgcn_s_sleep(KSL);
          }
        }
      }
      STAMP(9);
      if constexpr (R == 1) AD_WAIT(K2, F2, 4096);
    } else {
      STAMP(1);
      BARC();
      STAMP(2);
#pragma unroll
      for (int i = 0; i < 9; i++) {
        const int j = i < 5 ? i + 4 : i - 5;
        constexpr int PO = NV == 2 ? 2 : 1;
        SB0();
        if constexpr (NV == 2) a0 = SMF(b[(i + 4) % 5], w[1][j], a0, sidx);
        if (i < 3) a1 = SMF(b[(i + 4) % 5], wx[i], a1, sidx);
        else a1 = SMF(b[(i + 4) % 5], cat16(pre[(i + PO) & 3][0], pre[(i + PO) & 3][1]), a1, sidx);
        SB0();
        if (i < 4) b[(i + 4) % 5] = LDM(i);
        if (i == 3 || i == 4) LDW(pre[(i + PO) & 3], 1 + i);
      }
      u32x4 vm[NV];
      vm[NV - 1] = (u32x4){pk2(a1[0] + a1[2], a1[1] + a1[3]), 0u, 0u, 0u};
      if constexpr (NV == 2) vm[0] = (u32x4){pk2(a0[0] + a0[2], a0[1] + a0[3]), 0u, 0u, 0u};
#pragma unroll
      for (int d = 0; d < NV; d++) asm volatile("" : "+v"(vm[d]));
      const bool hi = so & 128;
      SB0();
      STAMP(5);
      BARN();
      STAMP(6);
      {
        const bf8 px = cat8(trrd(smem + po), trrd(smem + po + 256));
        SB0();
        const bf8 pm = cat8(trrd(smem + po + 512), trrd(smem + po + 256));
        SB0();
        f4 o[NV];
#pragma unroll
        for (int d = 0; d < NV; d++) { o[d] = MFMA(vx[d], px, ((f4){0, 0, 0, 0})); asm volatile("" : "+v"(o[d])); SB0(); }
#pragma unroll
        for (int d = 0; d < NV; d++) { o[d] = MFMA(__builtin_bit_cast(bf8, vm[d]), pm, o[d]); asm volatile("" : "+v"(o[d])); SB0(); }
        u32x2 ov = {pk2(o[0][0], o[0][1]), pk2(o[0][2], o[0][3])};
        if constexpr (NV == 2) {
          const u32x2 ow = {pk2(o[1][0], o[1][1]), pk2(o[1][2], o[1][3])};
          if (hi) ov = ow;
        }
        ov[0] = umin(ov[0], 0xFFFFFFFEu);
        __builtin_amdgcn_raw_buffer_store_b64(ov, rsX, so, so_, 16);
        SB0();
      }
      STAMP(7);
      int xo, lo, pa = 0;
      {
        IDX();
        if constexpr (NV == 1) {
          __builtin_amdgcn_s_sleep(LSL);
#pragma unroll
          for (int i = 0; i < 2; i++) nx[i] = __builtin_amdgcn_raw_buffer_load_b128(rsB, (lq * 4096 + h * 128 + l15 * 8) * 2, nb + (vt * 8 + i * 4) * 8192, 16);
          pa = C_QX + vt * 2048 + lq * 256 + ((l15 ^ lq ^ ((vt & 1) * 8)) << 4);
          asm volatile("" : "+v"(pa));
        }
        xo = OB_OFF((lq & 1) * 16 + l15, (lq >> 1) * 8);
        lo = lane * 4;
        asm volatile("" : "+v"(xo), "+v"(lo));
#pragma unroll
        for (int j = 0; j < 4; j++) LDW(pre[j], NV == 2 ? 6 + j : j);
      }
      SB0();
      BARN();
      STAMP(8);
      {
        f4 o[2][NV];
#pragma unroll
        for (int it = 0; it < 2; it++) {
          const bf8 pf = *(const bf8*)(smem + C_PX + it * 1024 + lo * 4);
          const u32x4 pm = {*(const u32*)(smem + C_PX + 2048 + it * 256 + lo), 0u, 0u, 0u};
#pragma unroll
          for (int d = 0; d < NV; d++) {
            o[it][d] = MFMA(vx[d], pf, ((f4){0, 0, 0, 0}));
            o[it][d] = MFMA(__builtin_bit_cast(bf8, vm[d]), __builtin_bit_cast(bf8, pm), o[it][d]);
          }
        }
#pragma unroll
        for (int d = 0; d < NV; d++)
          __builtin_amdgcn_raw_buffer_store_b128(pair16(pk2(o[0][d][0], o[0][d][1]), pk2(o[0][d][2], o[0][d][3]), pk2(o[1][d][0], o[1][d][1]), pk2(o[1][d][2], o[1][d][3])), rsB,
              xo, 3 * ACB + OB_OFF(s * 32, h * 128 + (vt + d) * 16), 16);
        SB0();
        if (s == 63) break;
        if constexpr (NV == 1) { *(u32x4*)(smem + pa) = nx[0]; *(u32x4*)(smem + (pa ^ 64) + 1024) = nx[1]; }
      }
      STAMP(9);
    }
    b3 = b3 == 2 ? 0 : b3 + 1;
    STAMP(14);
  }
  };
  if (wid == 4) run(std::integral_constant<int, 1>{});
  else if (wid == 0) run(std::integral_constant<int, 0>{});
  else if (wid < 4) run(std::integral_constant<int, 2>{});
  else if (wid == 7) run(std::integral_constant<int, 3>{});
  else run(std::integral_constant<int, 4>{});
  VMW0();
  __syncthreads();
  if (TIDX == 0) st4_sc1(WSP(u32, 0) + SY_SEG(1, h), 1u);
}

__device__ __forceinline__ void chain_out(const Params& p, char* smem, const int c, const int wv) {
  STS(32 + c, 1);
  constexpr int R = 5; (void)R;
  const int wid = wv, tq = c & 1, cg = c >> 1;
  u32x8 w[4][5];
  const __amdgpu_buffer_rsrc_t rsX = RSRC(XWIN(p), X_END);
  int xp, mo, wo, ro;
  {
    IDX();
    mo = wid < 4 ? cg * 512 + tq * 256 + ((wid ^ (tq * 2)) << 6) : 16384 + (cg >> 2) * 512 + tq * 256 + (((cg & 3) ^ (tq * 2)) << 6);
    if (wid > 4 || (wid == 4 && cg >= 8)) mo = PJ_OOB;
    __builtin_amdgcn_raw_buffer_store_b128(ones4(), rsX, lane < 24 ? X_M + (lane >> 3) * 32768 + ((lane >> 2) & 1) * 8192 + (lane & 3) * 16 : PJ_OOB, mo, 16);
    const __amdgpu_buffer_rsrc_t rw = RSRC(p.BWo, 576 * 4096 * 2);
    u32x4 st[4][10];
    const int va = ((lane >> 2) * 576 + cg * 32 + p4 * 8) * 2, vb = cg < 8 ? (lane * 576 + 512 + cg * 8) * 2 : PJ_OOB;
#pragma unroll
    for (int i = 0; i < 4; i++) {
#pragma unroll
      for (int k = 0; k < 8; k++) st[i][k] = __builtin_amdgcn_raw_buffer_load_b128(rw, va, ((i * 8 + wid) * 128 + k * 16) * 1152, OSTG_AUX);
#pragma unroll
      for (int k = 0; k < 2; k++) st[i][8 + k] = __builtin_amdgcn_raw_buffer_load_b128(rw, vb, ((i * 8 + wid) * 128 + k * 64) * 1152, OSTG_AUX);
      SB0();
    }
    char* sc = smem + wid * 10240;
#pragma unroll
    for (int i = 0; i < 4; i++) {
#pragma unroll
      for (int k = 0; k < 8; k++) *(u32x4*)(sc + (k * 16 + (lane >> 2)) * 80 + p4 * 16) = st[i][k];
#pragma unroll
      for (int k = 0; k < 2; k++) *(u32x4*)(sc + (k * 64 + lane) * 80 + 64) = st[i][8 + k];
      if (i == 0) { BAR(); if (tid == 0) st4_sc1(WSP(u32, 0) + SY_INIT(32 + c), 1u); }
      __builtin_amdgcn_fence(__ATOMIC_RELEASE, "wavefront");
      __builtin_amdgcn_wave_barrier();
#pragma unroll
      for (int t = 0; t < 5; t++) {
        bf4 tt[4];
#pragma unroll
        for (int kk = 0; kk < 4; kk++) {
          const int r0 = ((kk >> 1) * 2 + (lq >> 1)) * 32 + (p4 >> 1) * 16 + (q4 >> 1) * 8 + (lq & 1) * 4 + (kk & 1) * 2 + (q4 & 1);
          tt[kk] = trrd(sc + r0 * 80 + t * 16 + (p4 & 1) * 8);
        }
        w[i][t] = ilv(tt[0], tt[1], tt[2], tt[3]);
      }
      asm volatile("s_waitcnt lgkmcnt(0)" ::: "memory");
      __builtin_amdgcn_wave_barrier();
      SB0();
    }
    u32 fl, fv;
    int it = 0;
    do { fl = ld4_sc1(WSP(u32, 0) + SY_INIT(lane)); fv = ld4_sc1(WSP(u32, 0) + SY_VOTE(lane));
         if (__all(fl != 0u && fv != 0u)) break;
         __builtin_amdgcn_s_sleep(2); } while (++it < SPIN_CAP);
    xp = __popcll(__ballot(fv == 2u)) > 32 ? 8192 : 0;
    wo = (lq >> 1) == hi8 ? ((l15 & 7) * 2 + (lq & 1)) * 144 + (wid * 2 + hi8) * 8 : 16 * 144 + lane * 8;
    ro = wid * 2880 + (lane >> 2) * 144 + p4 * 32;
  }
  __syncthreads();
  STS(32 + c, 14);
  int go, sidx, po;
  { IDX(); go = lane * 16; sidx = (l15 & 2) ? 0xEEEE : 0x4444;
    po = p4 == 0 ? (lane >> 4) * 16 + ((lane >> 2) & 1) * 8 + ((lane >> 3) & 1) * 4 : PJ_OOB; }
#define ONOP 7
  int b3 = 0;
#pragma unroll 1
  for (int s = 0; s < 63; s++) {
    f4 d[5];
    int gb[4], pb = mo + X_M + (b3 == 2 ? 0 : b3 + 1) * 32768 + xp;
#pragma unroll
    for (int i = 0; i < 4; i++) { gb[i] = X_O + b3 * 131072 + tq * 65536 + wid * 1024 + xp + i * 16384; asm volatile("" : "+s"(gb[i])); }
    asm volatile("" : "+s"(pb));
    u32x4 g[4];
    u32 bad;
    auto mult = [&]() __attribute__((always_inline)) {
      CFENCE();
#pragma unroll
      for (int i = 0; i < 4; i++) {
        g[i] = __builtin_amdgcn_raw_buffer_load_b128(rsX, go, gb[i], 16);
        SB0();
        if (ONOP >= 0 && i < 3) asm volatile("s_nop %0" :: "i"(ONOP >= 0 ? ONOP : 0));
        SB0();
      }
      bad = 0u;
#pragma unroll
      for (int i = 0; i < 4; i++) {
        SB0();
#pragma unroll
        for (int t = 0; t < 5; t++) {
          d[t] = SMF(g[i], w[i][t], d[t], sidx);
          if (t == 0) bad = umax(umax(bad, g[i][0]), g[i][2]);
          if (i == 3 && t >= 2) { SB0(); *(f2v*)(smem + wo + (t - 2) * 2880) = (f2v){d[t - 2][0] + d[t - 2][2], d[t - 2][1] + d[t - 2][3]}; SB0(); }
        }
      }
      SB0();
#pragma unroll
      for (int t = 3; t < 5; t++) *(f2v*)(smem + wo + t * 2880) = (f2v){d[t][0] + d[t][2], d[t][1] + d[t][3]};
      SB0();
    };
#pragma unroll
    for (int t = 0; t < 5; t++) d[t] = zero4();
    SB0();
    if (wid == 7) AD_WAIT(K1, F1, SPIN_CAP);
    STAMP(15);
    BARN();
    STAMP(0);
    mult();
    if (__builtin_expect(__any(bad == ~0u), 0)) {
      int it = 0;
      do {
#pragma unroll
        for (int t = 0; t < 5; t++) d[t] = zero4();
        mult();
      } while (__any(bad == ~0u) && ++it < SPIN_CAP);
    }
    STAMP(1);
    BARC();
    STAMP(2);
    {
      const f4 t0 = *(const f4*)(smem + ro), t1 = *(const f4*)(smem + ro + 16);
      float a = (t0[0] + t0[2]) + (t1[0] + t1[2]), b = (t0[1] + t0[3]) + (t1[1] + t1[3]);
      a += __uint_as_float(__builtin_amdgcn_mov_dpp(__float_as_uint(a), 0xB1, 0xf, 0xf, true));
      b += __uint_as_float(__builtin_amdgcn_mov_dpp(__float_as_uint(b), 0xB1, 0xf, 0xf, true));
      a += __uint_as_float(__builtin_amdgcn_mov_dpp(__float_as_uint(a), 0x4E, 0xf, 0xf, true));
      b += __uint_as_float(__builtin_amdgcn_mov_dpp(__float_as_uint(b), 0x4E, 0xf, 0xf, true));
      __builtin_amdgcn_raw_buffer_store_b32(umin(pk2(a, b), 0xFFFFFFFEu), rsX, po, pb, 16);
      SB0();
      STAMP(3);
      __builtin_amdgcn_s_sleep(4);
      __builtin_amdgcn_raw_buffer_store_b32(~0u, rsX, po, mo + X_M + b3 * 32768 + xp, 16);
    }
    if (wid == 5 && c == 0 && s > 0 && go == 0) st4_sc1(WSP(u32, 0) + SY_CS(s - 1), 1u);
    VMW0C();
    b3 = b3 == 2 ? 0 : b3 + 1;
  }
  __syncthreads();
}

__device__ __forceinline__ void wait_ge(const u32* w, u32 val, const int wv) {
  if (TIDX == 0) {
    int it = 0;
    while (ld4_sc1(w) < val && ++it < SPIN_CAP) __builtin_amdgcn_s_sleep(WPS);
  }
}
__device__ __forceinline__ void acquire_all(const int wv) {
  if (TIDX == 0) {
    __builtin_amdgcn_fence(__ATOMIC_ACQUIRE, "agent");
    VMW0();
  }
  __syncthreads();
}

#define KROT 0
#define OPD 4
#define PPD 2
#define T_KV 0
#define T_Q 0
#define T_A 192
#define T_W (T_A + 64)
#define RB_BIG (XS0 / 4)
#define NC 3
#define WWM 2
#define WWN 2
#define NWM 4
#define NWN 1
#define WPD 2
#define WAD false
#define NAD false
#define XAFF 0
#define ORD 8
#define T_L (T_W + 256 + NWIDE * 3 + (16 + XS0 / 4 - NWIDE) * 9 + 144)
#define T_CHAIN (1 << 20)
__host__ __device__ __forceinline__ int decode_task(int u, int& a, int& b) {
#define DT_AT(q0, n) if (u < (n) * 32) { a = (q0) - (u >> 5); b = u & 31; return 0; } u -= (n) * 32;
#define DT_W(ty, r0, n) if (u < (n) * 3) { a = (r0) + u / 3; b = u % 3; return ty; } u -= (n) * 3;
#define DT_N(ty, r0, n) if (u < (n) * 9) { a = (r0) + u / 9; b = 3 + u % 9; return ty; } u -= (n) * 9;
#define NWIDE 14
  DT_AT(7, 5) DT_AT(2, 1) DT_W(3, 0, 4) DT_AT(1, 2) DT_W(2, 6, 2) DT_W(2, 4, 2) DT_W(2, 0, 4) DT_W(3, 4, 2)
  DT_N(2, 8, 4) DT_N(3, 6, 2) DT_N(2, 12, 4) DT_N(3, 8, 2) DT_N(3, 10, 3)
  a = XS0 + u / 72; b = u % 72; return 4;
}

__device__ __forceinline__ int next_task(const Params& p, int* s_task, int xq, int& xk, const int wv, const int pt) {
  __syncthreads();
  if (TIDX == 0) {
    int t = -1;
    while (xk < 6) {
      int x = xq + xk; if (x >= 6) x -= 6;
      const int i = (int)__hip_atomic_fetch_add(WSP(u32, 0) + SY_XQ(x), 1u, __ATOMIC_RELAXED, __HIP_MEMORY_SCOPE_AGENT);
      if (i < 32) { t = x * 32 + i; break; }
      xk++;
    }
    if (t < 0 && xk == 6) {
      xk = 7;
      int it = 0;
      while (ld4_sc1(WSP(u32, 0) + SY_NCL) < 64u && ld4_sc1(WSP(u32, 0) + SY_ARR) < 256u && ++it < SPIN_CAP) __builtin_amdgcn_s_sleep(8);
      if (ld4_sc1(WSP(u32, 0) + SY_NCL) < 64u) {
        for (int k = 0; k < 64; k++)
          if (__hip_atomic_exchange(WSP(u32, 0) + SY_CLAIM(k), 1u, __ATOMIC_RELAXED, __HIP_MEMORY_SCOPE_AGENT) == 0u) {
            __hip_atomic_fetch_add(WSP(u32, 0) + SY_NCL, 1u, __ATOMIC_RELAXED, __HIP_MEMORY_SCOPE_AGENT);
            t = T_CHAIN + k; break;
          }
      }
    }
    if (t < 0) t = T_A + (int)__hip_atomic_fetch_add(WSP(u32, 0) + SY_QHEAD, 1u, __ATOMIC_RELAXED, __HIP_MEMORY_SCOPE_AGENT);
    *s_task = t;
  }
  __syncthreads();
  return __builtin_amdgcn_readfirstlane(*s_task);
}

__device__ __forceinline__ int take_task(int* s_task, const int pre, const int wv, const int pt) {
  if (TIDX == 0) {
    *s_task = T_A + pre;
  }
  __syncthreads();
  const int t = __builtin_amdgcn_readfirstlane(*s_task);
  __syncthreads();
  return t;
}
#if defined(__HIP_DEVICE_COMPILE__) && !defined(KEEP_PK)
#define NOPK __attribute__((target("no-packed-fp32-ops")))
#else
#define NOPK
#endif
#define P0V 2
__device__ __forceinline__ void mega_body(const Params& p, char* smem) {
  int* const s_task = (int*)(smem + LDS_TOTAL);
  const int wv = __builtin_amdgcn_readfirstlane(threadIdx.x >> 6);
  if (blockIdx.x == 0 && TIDX == 0) asm volatile("s_nop 0" ::: "memory");
#define tid TIDX
  const int xcc = (int)(__builtin_amdgcn_s_getreg(20 | (3 << 11)) & 7u);
  const int xq = xcc >= 2 ? xcc - 2 : xcc;
  int xk = 0, pre = 0;
  u32* const qh = WSP(u32, 0) + SY_QHEAD;
  if (tid == 0) {
    int t0 = -1;
    if (xcc < 2) {
      const int k = ((blockIdx.x >> 3) & 31) + xcc * 32;
      if (__hip_atomic_exchange(WSP(u32, 0) + SY_CLAIM(k), 1u, __ATOMIC_RELAXED, __HIP_MEMORY_SCOPE_AGENT) == 0u) {
        __hip_atomic_fetch_add(WSP(u32, 0) + SY_NCL, 1u, __ATOMIC_RELAXED, __HIP_MEMORY_SCOPE_AGENT);
        t0 = T_CHAIN + k;
      }
    }
    __hip_atomic_fetch_add(WSP(u32, 0) + SY_ARR, 1u, __ATOMIC_RELAXED, __HIP_MEMORY_SCOPE_AGENT);
    *s_task = t0;
  }
  int t = -1;
  if (xcc < 2) { __syncthreads(); t = __builtin_amdgcn_readfirstlane(*s_task); }
  if (t < 0) t = next_task(p, s_task, xq, xk, wv, t);
  {
    while (t < T_A) {
      const int x = t >> 5, j = t & 31, c = (x - 4) * 32 + j;
      if (x >= 4 && tid < 64) {
        const __amdgpu_buffer_rsrc_t rsX = RSRC(XWIN(p), X_END);
        u32 best[2] = {~0u, ~0u};
#pragma unroll 1
        for (int r = 0; r < 4; r++) {
#pragma unroll
          for (int k = 0; k < 2; k++) {
            const u32 t0 = (u32)__builtin_readcyclecounter();
            __builtin_amdgcn_raw_buffer_store_b32((u32)r, rsX, X_CAL + k * 8192 + c * 128, 0, 16);
            VMW0();
            best[k] = umin(best[k], (u32)__builtin_readcyclecounter() - t0);
          }
        }
        if (tid == 0) st4_sc1(WSP(u32, 0) + SY_VOTE(c), 1u + ((best[1] < best[0] ? 1u : 0u) ^ (u32)(xcc >> 2)));
      }
#pragma unroll 1
      for (int i = 0; i < 2; i++) {
        const int r0 = i * 128, mat = t >> 6, cb = (t >> 2) & 15, co = cb * 256 + (t & 3) * 64;
        gemm_tile<128, 64, 4, 2, true, 4>(p.Bx + r0 * 576, 576, (mat == 0 ? p.BWk : mat == 1 ? p.BWv : p.BWq) + co, 4096,
                                          (mat == 0 ? WSP(u16, O_KB) : mat == 1 ? WSP(u16, O_VB) : WSP(u16, O_QB)) + r0 * 4096 + co, 4096, 576, smem, wv, qh, pre);
        if (tid < 4) __hip_atomic_fetch_add(WSP(u32, 0) + SY_BLK(mat, cb, (r0 >> 5) + tid), 2u, __ATOMIC_RELAXED, __HIP_MEMORY_SCOPE_AGENT);
      }
      proj_slot(p, x, j, smem, wv);
      t = next_task(p, s_task, xq, xk, wv, t);
    }
  }
  if (t >= T_CHAIN) { if (t < T_CHAIN + 32) chain_head(p, smem, t - T_CHAIN, wv); else chain_out(p, smem, t - T_CHAIN - 32, wv); xk = 7; t = next_task(p, s_task, xq, xk, wv, -1); }
  while (t < T_W) {
    wof_task(p, t - T_A, smem, wv, qh, pre);
    if (tid == 0) __hip_atomic_fetch_add(WSP(u32, 0) + SY_CW, 1u, __ATOMIC_RELAXED, __HIP_MEMORY_SCOPE_AGENT);
    t = take_task(s_task, pre, wv, t);
  }
  while (t < T_L) {
    int a, b;
    const int ty = decode_task(t - T_W, a, b);
    if (ty == 0) {
      const int qb = a, h = b;
      if (tid < 64) {
        int it = 0;
        while (true) {
          const u32 f0 = ld4_sc1(WSP(u32, 0) + SY_BLK(3, h >> 1, tid)), f1 = ld4_sc1(WSP(u32, 0) + SY_BLK(4, h >> 1, tid)), f2 = ld4_sc1(WSP(u32, 0) + SY_BLK(5, h >> 1, tid));
          if (__all(tid >= qb * 8 + 8 || ((tid < qb * 8 || f0 >= 8u) && f1 >= 8u && f2 >= 8u)) || ++it >= SPIN_CAP) break;
          __builtin_amdgcn_s_sleep(8);
        }
      }
      acquire_all(wv);
      fa::attn_block(WSP(u16, O_QA) + (size_t)qb * 256 * 4096 + h * 128, WSP(u16, O_KA) + h * 128, WSP(u16, O_VA) + h * 128,
                     WSP(u16, O_CA) + (size_t)qb * 256 * 4096 + h * 128, qb * 256, smem, wv, qh, pre);
      if (tid == 0) __hip_atomic_fetch_add(WSP(u32, 0) + SY_CC(qb), 1u, __ATOMIC_RELAXED, __HIP_MEMORY_SCOPE_AGENT);
    } else if (ty < 4) {
      const u16 *A, *B; u16* C;
      const int co = b < 3 ? b * 192 : (b - 3) * 64;
      if (ty == 2) {
        wait_ge(WSP(u32, 0) + SY_CC(a >> 1), 32u, wv);
        A = WSP(u16, O_CA) + (size_t)a * 128 * 4096; B = p.Awo + co; C = p.out + (size_t)a * 128 * 1152 + co;
      } else {
        wait_ge(WSP(u32, 0) + SY_CS(a * 4 + 3), 1u, wv);
        A = WSP(u16, O_OB) + (size_t)a * 128 * 4096; B = p.BWo + co; C = p.out + (size_t)a * 128 * 1152 + 576 + co;
      }
      acquire_all(wv);
      if (b < 3) gemm_tile<128, 192, WWM, WWN, false, WPD, WAD>(A, 4096, B, 576, C, 1152, 4096, smem, wv, qh, pre, ty == 3);
      else gemm_tile<128, 64, NWM, NWN, false, OPD, NAD>(A, 4096, B, 576, C, 1152, 4096, smem, wv, qh, pre, ty == 3);
    } else {
      small_out_task(p, a, b & 1, b >> 1, smem, wv, qh, pre);
    }
    t = take_task(s_task, pre, wv, t);
  }
}

#undef tid
__global__ NOPK void __launch_bounds__(512, 2) mega(Params p) {
  __shared__ __attribute__((aligned(16))) char smem[LDS_ALL];
  mega_body(p, smem);
}
extern "C" void kernel_launch(void* const* d_in, const int* in_sizes, int n_in, void* d_out, int out_size, void* d_ws,
                              size_t ws_size, hipStream_t stream) {
  Params p;
  p.Ax = (const u16*)d_in[0]; p.Ac = (const u16*)d_in[1]; p.Awq = (const u16*)d_in[2]; p.Awk = (const u16*)d_in[3];
  p.Awv = (const u16*)d_in[4]; p.Awo = (const u16*)d_in[5]; p.Bx = (const u16*)d_in[6]; p.Bm = (const u16*)d_in[7];
  p.BWq = (const u16*)d_in[8]; p.BWk = (const u16*)d_in[9]; p.BWv = (const u16*)d_in[10]; p.BWo = (const u16*)d_in[11];
  p.out = (u16*)d_out;
  p.ws = (char*)d_ws;
  const size_t syncBytes = SY_WORDS * 4;
  (void)hipMemsetAsync(d_ws, 0, syncBytes, stream);
  mega<<<256, 512, 0, stream>>>(p);
}
```

```cpp
#include <hip/hip_runtime.h>
#include <hip/hip_bf16.h>
#include <stdint.h>
#include <type_traits>

typedef unsigned short u16;
typedef unsigned int u32;
typedef unsigned long long u64;
typedef __attribute__((ext_vector_type(8))) short bf8;
typedef __attribute__((ext_vector_type(4))) short bf4;
typedef __attribute__((ext_vector_type(4))) float f4;
typedef __attribute__((ext_vector_type(2))) float f2v;
typedef __attribute__((ext_vector_type(2))) __bf16 bf2v;
typedef __attribute__((ext_vector_type(4))) unsigned u32x4;
typedef __attribute__((ext_vector_type(2))) unsigned u32x2;
typedef float f32x16 __attribute__((ext_vector_type(16)));
typedef __attribute__((ext_vector_type(8))) unsigned u32x8;
typedef __attribute__((ext_vector_type(8))) __bf16 bh8;
typedef __attribute__((ext_vector_type(16))) __bf16 bh16;
#define LDSP __attribute__((address_space(3)))

__device__ __forceinline__ u32 pk2(float a, float b) {
  f2v x = {a, b};
  bf2v y = __builtin_convertvector(x, bf2v);
  return __builtin_bit_cast(u32, y);
}
__device__ __forceinline__ u32 umin(u32 a, u32 b) { return a < b ? a : b; }
__device__ __forceinline__ u32 umax(u32 a, u32 b) { return a > b ? a : b; }
__device__ __forceinline__ float rbf(float f) { return __uint_as_float(pk2(f, f) << 16); }
#define MFMA(a, b, c) __builtin_amdgcn_mfma_f32_16x16x32_bf16(a, b, c, 0, 0, 0)
#define MFMA16(a, b, c) __builtin_amdgcn_mfma_f32_16x16x16bf16_1k(a, b, c, 0, 0, 0)
#define SMF(a, b, c, idx) __builtin_amdgcn_smfmac_f32_16x16x64_bf16(__builtin_bit_cast(bh8, a), __builtin_bit_cast(bh16, b), c, idx, 0, 0)
__device__ __forceinline__ u32x8 ilv(bf4 a, bf4 b, bf4 c, bf4 d) {
  const u32x2 x = __builtin_bit_cast(u32x2, a), y = __builtin_bit_cast(u32x2, b), z = __builtin_bit_cast(u32x2, c), w = __builtin_bit_cast(u32x2, d);
  return (u32x8){x[0], x[1], y[0], y[1], z[0], z[1], w[0], w[1]};
}
__device__ __forceinline__ u32x8 cat16(bf8 a, bf8 b) {
  const u32x4 x = __builtin_bit_cast(u32x4, a), y = __builtin_bit_cast(u32x4, b);
  return (u32x8){x[0], x[1], x[2], x[3], y[0], y[1], y[2], y[3]};
}
__device__ __forceinline__ float swadd(float a, float b) {
  auto x = __builtin_amdgcn_permlane32_swap(__float_as_uint(a), __float_as_uint(b), false, false);
  return __uint_as_float(x[0]) + __uint_as_float(x[1]);
}
__device__ __forceinline__ bf4 trrd(const char* p) {
  return __builtin_amdgcn_ds_read_tr16_b64_v4i16((LDSP bf4*)(p));
}
__device__ __forceinline__ bf8 cat8(bf4 a, bf4 b) { return __builtin_shufflevector(a, b, 0, 1, 2, 3, 4, 5, 6, 7); }
__device__ __forceinline__ void st8_sc1(void* p, u64 v) { __hip_atomic_store((u64*)p, v, __ATOMIC_RELAXED, __HIP_MEMORY_SCOPE_AGENT); }
__device__ __forceinline__ u32 ld4_sc1(const u32* p) { return __hip_atomic_load(p, __ATOMIC_RELAXED, __HIP_MEMORY_SCOPE_AGENT); }
__device__ __forceinline__ void st4_sc1(u32* p, u32 v) { __hip_atomic_store(p, v, __ATOMIC_RELAXED, __HIP_MEMORY_SCOPE_AGENT); }
#define VMW0() asm volatile("s_waitcnt vmcnt(0)" ::: "memory")
#define CFENCE() asm volatile("" ::: "memory")
#define VMW0C() do { __builtin_amdgcn_s_waitcnt(0x0F70); asm volatile("" ::: "memory"); } while (0)
#define SPIN_CAP (1 << 22)
#define XPS 4
#define SPS 4
#define WPS 8
__device__ __forceinline__ int tid_now(int wv) {
  int l; asm volatile("v_mbcnt_lo_u32_b32 %0, -1, 0\n\tv_mbcnt_hi_u32_b32 %0, -1, %0" : "=v"(l));
  return wv * 64 + l;
}
#define TIDX tid_now(wv)
#define CLAIM() do { if (TIDX == 0) pre = (int)__hip_atomic_fetch_add(qh, 1u, __ATOMIC_RELAXED, __HIP_MEMORY_SCOPE_AGENT); } while (0)

struct Params {
  const u16 *Ax, *Ac, *Awq, *Awk, *Awv, *Awo, *Bx, *Bm, *BWq, *BWk, *BWv, *BWo;
  u16* out;
  char* ws;
};
#define SY_QHEAD 0
#define SY_CC(rb) (64 + (rb) * 16)
#define SY_CW (64 + 8 * 16)
#define SY_OQ(x) (64 + 9 * 16 + (x) * 16)
#define SY_CS(s) (64 + 16 * 16 + (s) * 16)
#define SY_B0 (64 + 80 * 16)
#define SY_SEG(i, h) (SY_B0 + (i) * 32 + (h))
#define SY_INIT(c) (SY_B0 + 64 + (c))
#define SY_VOTE(i) (SY_B0 + 128 + (i))
#define SY_CLAIM(c) (SY_B0 + 192 + (c))
#define SY_XQ(x) (SY_B0 + 256 + (x) * 16)
#define SY_ARR (SY_B0 + 384)
#define SY_NCL (SY_ARR + 16)
#define BLKS 1
#define SY_BLK(mi, cb, g) (SY_NCL + 48 + (((mi) * 16 + (cb)) * 64 + (g)) * BLKS)
#define SY_WORDS (SY_NCL + 48 + 6 * 16 * 64 * BLKS)

#define AL256(x) (((x) + 255) & ~255)
#define O_SYNC_END (SY_WORDS * 4)
#define ACB (2048 * 4096 * 2)
#define O_QA AL256(O_SYNC_END)
#define O_KA (O_QA + ACB)
#define O_VA (O_KA + ACB)
#define O_CA (O_VA + ACB)
#define O_QB (O_CA + ACB)
#define O_KB (O_QB + ACB)
#define O_VB (O_KB + ACB)
#define O_OB (O_VB + ACB)
#define O_WOF (O_OB + ACB)
#define WOB (576 * 4096 * 2)
#define O_XW (O_WOF + 2 * WOB)
#define X_O 16384
#define X_M (X_O + 3 * 131072)
#define X_ANN (X_M + 3 * 32768)
#define X_CAL (X_ANN + 16384)
#define X_END (X_CAL + 16384)
#define OB_OFF(r, c) (((r) >> 5) * 262144 + ((c) >> 4) * 1024 + ((r) & 31) * 32 + ((c) & 15) * 2)
#define WSP(T, off) ((T*)(p.ws + (off)))
#define XWIN(p) (((uintptr_t)(p).ws + O_XW + 0x1FFFFFu) & ~(uintptr_t)0x1FFFFFu)

#define XS0 52
#define LDS_TOTAL (131072 + 8192 + 16384)
#define LDS_ALL 163840

#define SB0() __builtin_amdgcn_sched_barrier(0)
#define RSRC(ptr, bytes) __builtin_amdgcn_make_buffer_rsrc((void*)(ptr), 0, (bytes), 0x00020000)
__device__ __forceinline__ bf8 ldsc1(__amdgpu_buffer_rsrc_t r, int off) {
  return __builtin_bit_cast(bf8, __builtin_amdgcn_raw_buffer_load_b128(r, off, 0, 16));
}
__device__ __forceinline__ u32x4 pair16(u32 a0, u32 a1, u32 b0, u32 b1) {
  auto r0 = __builtin_amdgcn_permlane16_swap(a0, b0, false, false);
  auto r1 = __builtin_amdgcn_permlane16_swap(a1, b1, false, false);
  return (u32x4){r0[0], r1[0], r0[1], r1[1]};
}
__device__ __forceinline__ int fsw(int k) { return (k & 3) | (((k >> 3) & 1) << 2); }

#define GST(i)
#define GTB true
#define EXPG 0
template <int BM, int BN, int WMG, int WNG, bool SC1, int PD, bool AD = false>
__device__ __forceinline__ void gemm_tile(const u16* __restrict__ A, int lda, const u16* __restrict__ B, int ldb,
                                          u16* __restrict__ C, int ldc, int K, char* smem, const int wv, u32* qh, int& pre, const bool tiled = false) {
  constexpr int kofs = 0;
  constexpr int BRS = ((BN * 2 + 255) / 256) * 256;
  constexpr int ABY = BM * 128, BBY = 64 * BRS, BUF = ABY + BBY;
  constexpr int MT = BM / WMG / 16, NT = BN / WNG / 16;
  constexpr bool SPEC = WMG * WNG == 4;
  constexpr int NTH = SPEC ? 256 : 512;
  constexpr int ACH = AD ? 2 * MT : BM * 8 / NTH, BCH = (8 * BN) / NTH;
  constexpr bool TB = !SC1 && BM == 128 && !SPEC && !AD && GTB;
  constexpr int BCPR = BN / 8;
  const int tid = TIDX, lane = tid & 63, wid = wv;
  const int l15 = lane & 15, lq = lane >> 4;
  const int wr = wid / WNG, wc = wid % WNG;
  const __amdgpu_buffer_rsrc_t rc = RSRC(C, BM * ldc * 2);
  f4 acc[MT][NT];
#pragma unroll
  for (int i = 0; i < MT; i++)
#pragma unroll
    for (int j = 0; j < NT; j++) acc[i][j] = (f4){0, 0, 0, 0};
  bf8 ra[PD][ACH], rb[PD][BCH];
  const __amdgpu_buffer_rsrc_t rsa = RSRC(A, BM * lda * 2), rsb = RSRC(B, K * ldb * 2);
  auto gload = [&](int k0, bf8* xa, bf8* xb) {
    if (!SC1) k0 = (k0 + kofs) & 4095;
    if constexpr (!AD) {
#pragma unroll
    for (int i = 0; i < ACH; i++) {
      int c = (tid & (NTH - 1)) + NTH * i, r = c >> 3, ch = c & 7;
      xa[i] = __builtin_bit_cast(bf8, __builtin_amdgcn_raw_buffer_load_b128(rsa, tiled ? OB_OFF(r, ch * 8) : (r * lda + ch * 8) * 2, tiled ? k0 * 64 : k0 * 2, 0));
    }
    }
#pragma unroll
    for (int i = 0; i < BCH; i++) {
      int c = (tid & (NTH - 1)) + NTH * i, r = c / BCPR, ch = c % BCPR;
      xb[i] = __builtin_bit_cast(bf8, __builtin_amdgcn_raw_buffer_load_b128(rsb, (r * ldb + ch * 8) * 2, (!SC1 && (EXPG & 1)) ? 0 : k0 * ldb * 2, 0));
    }
  };
  auto aload = [&](int k0, bf8* xa) {
    k0 = (k0 + kofs) & 4095;
#pragma unroll
    for (int i = 0; i < ACH; i++)
      xa[i] = __builtin_bit_cast(bf8, __builtin_amdgcn_raw_buffer_load_b128(rsa, ((wr * (MT * 16) + (i % MT) * 16 + l15) * lda + (i / MT) * 32 + lq * 8) * 2, k0 * 2, 0));
  };
  const int q4 = l15 >> 2, p4 = lane & 3;
  int owa, owb[BCH], ora, orb[NT];
  { const int c = tid & (NTH - 1), r = c >> 3, ch = c & 7; owa = r * 128 + ((ch ^ (r & 7)) << 4); asm volatile("" : "+v"(owa)); }
#pragma unroll
  for (int i = 0; i < BCH; i++) {
    const int c = (tid & (NTH - 1)) + NTH * i, r = c / BCPR, ch = c % BCPR;
    owb[i] = r * BRS + (((ch >> 1) ^ fsw(r)) << 5) + ((ch & 1) << 4); asm volatile("" : "+v"(owb[i]));
  }
  ora = (wr * (MT * 16) + l15) * 128 + ((lq ^ (l15 & 7)) << 4); asm volatile("" : "+v"(ora));
#pragma unroll
  for (int j = 0; j < NT; j++) { const int r0 = lq * 8 + q4; orb[j] = r0 * BRS + (((wc * NT + j) ^ fsw(r0)) << 5) + p4 * 8; asm volatile("" : "+v"(orb[j])); }
  auto swrite = [&](int buf, bf8* xa, bf8* xb) {
    int bo = TB ? buf : buf * BUF;
    if constexpr (BM != 128) asm volatile("" : "+s"(bo));
    if constexpr (!AD) {
#pragma unroll
      for (int i = 0; i < ACH; i++) *(bf8*)(smem + (owa + bo) + i * NTH * 16) = xa[i];
    }
#pragma unroll
    for (int i = 0; i < BCH; i++) *(bf8*)(smem + (owb[i] + bo) + ABY) = xb[i];
  };
  auto rd = [&](int buf, int ks, bf8* af, bf8* bfr) {
    int bo = TB ? buf : buf * BUF;
    if constexpr (BM != 128) asm volatile("" : "+s"(bo));
    if constexpr (!AD) {
#pragma unroll
      for (int i = 0; i < MT; i++) af[i] = *(const bf8*)(smem + ((ora ^ (ks * 64)) + bo) + i * 2048);
    }
#pragma unroll
    for (int j = 0; j < NT; j++) {
      const char* q = smem + (orb[j] + bo) + ABY + ks * 32 * BRS;
      bfr[j] = cat8(trrd(q), trrd(q + 4 * BRS));
    }
  };
  auto mm = [&](bf8* af, bf8* bfr) {
#pragma unroll
    for (int j = 0; j < NT; j++)
#pragma unroll
      for (int i = 0; i < MT; i++) acc[i][j] = MFMA(bfr[j], af[i], acc[i][j]);
  };
  auto step = [&](auto cw, int k0, int buf, bf8* xa, bf8* xb) {
    constexpr bool CW = decltype(cw)::value;
    bf8 af0[MT], bf0[NT], af1[MT], bf1[NT];
    if constexpr (CW) {
      rd(buf, 0, af0, bf0);
      if constexpr (BM == 128) rd(buf, 1, af1, bf1);
      SB0();
      mm(AD ? xa : af0, bf0);
      if constexpr (BM != 128) SB0();
    }
    if constexpr (!(SPEC && CW)) if (!SC1 || k0 + 64 < K) {
      swrite(buf ^ 1, xa, xb);
      if (!SC1 || k0 + 64 * (PD + 1) < K) gload(k0 + 64 * (PD + 1), xa, xb);
    }
    SB0();
    if constexpr (CW) {
      if constexpr (BM != 128) { rd(buf, 1, af1, bf1); SB0(); }
      mm(AD ? xa + MT : af1, bf1);
      if constexpr (AD) { SB0(); aload(k0 + 64 * PD, xa); }
    }
    asm volatile("s_waitcnt lgkmcnt(0)" ::: "memory"); __builtin_amdgcn_s_barrier(); asm volatile("" ::: "memory");
  };
  auto pro = [&](auto st) {
    if constexpr (decltype(st)::value) {
      bf8 ta[ACH], tb[BCH];
      gload(0, ta, tb);
#pragma unroll
      for (int u = 0; u < PD; u++) { if constexpr (AD) aload(64 * u, ra[u]); gload(64 * (u + 1), ra[u], rb[u]); }
      swrite(0, ta, tb);
    }
    asm volatile("s_waitcnt lgkmcnt(0)" ::: "memory"); __builtin_amdgcn_s_barrier(); asm volatile("" ::: "memory");
  };
  if constexpr (!SPEC && !TB) pro(std::true_type{});
  GST(0);
  if constexpr (SC1) {
#pragma unroll
    for (int u = 0; u < 9; u++) step(std::true_type{}, 64 * u, u & 1, ra[u % PD], rb[u % PD]);
  } else if constexpr (TB) {
    bf8 af0[MT], bf0[NT];
    {
      bf8 ta[ACH], tb[BCH], ua[ACH], ub[BCH];
      gload(0, ta, tb); gload(64, ua, ub);
#pragma unroll
      for (int u = 0; u < PD; u++) gload(64 * (u + 2), ra[u], rb[u]);
      swrite(0, ta, tb); swrite(BUF, ua, ub);
      asm volatile("s_waitcnt lgkmcnt(0)" ::: "memory"); __builtin_amdgcn_s_barrier(); asm volatile("" ::: "memory");
      rd(0, 0, af0, bf0);
    }
    int b0 = 0, b1 = BUF, b2 = 2 * BUF;
#pragma unroll 1
    for (int k0 = 0; k0 < 4096; k0 += 64 * PD) {
#pragma unroll
      for (int u = 0; u < PD; u++) {
        bf8 af1[MT], bf1[NT];
        rd(b0, 1, af1, bf1);
        SB0();
        mm(af0, bf0);
        swrite(b2, ra[u], rb[u]);
        gload(k0 + 64 * (u + PD + 2), ra[u], rb[u]);
        SB0();
        rd(b1, 0, af0, bf0);
        SB0();
        mm(af1, bf1);
        asm volatile("s_waitcnt lgkmcnt(0)" ::: "memory"); __builtin_amdgcn_s_barrier(); asm volatile("" ::: "memory");
        const int bt = b0; b0 = b1; b1 = b2; b2 = bt;
      }
    }
  } else {
    static_assert(64 % PD == 0 && !(PD & 1), "depth");
    auto loop = [&](auto cw) {
      if constexpr (SPEC) pro(std::bool_constant<!decltype(cw)::value>{});
#pragma unroll 1
      for (int k0 = 0; k0 < 4096; k0 += 64 * PD) {
#pragma unroll
        for (int u = 0; u < PD; u++) step(cw, k0 + 64 * u, u & 1, ra[u], rb[u]);
      }
    };
    if (WMG * WNG == 8 || wid < WMG * WNG) loop(std::true_type{}); else loop(std::false_type{});
  }
  GST(1);
  if (!SC1) CLAIM();
  if (WMG * WNG == 8 || wid < WMG * WNG)
#pragma unroll
  for (int i = 0; i < MT; i++)
#pragma unroll
    for (int j = 0; j < NT; j += 2) {
      int row = wr * (MT * 16) + i * 16 + l15, col = wc * (NT * 16) + j * 16 + (lq & 1) * 16 + (lq >> 1) * 8;
      if (j + 1 == NT) {
        *(u32x2*)(C + (size_t)row * ldc + wc * (NT * 16) + j * 16 + lq * 4) = (u32x2){pk2(acc[i][j][0], acc[i][j][1]), pk2(acc[i][j][2], acc[i][j][3])};
        break;
      }
      u32x4 v = pair16(pk2(acc[i][j][0], acc[i][j][1]), pk2(acc[i][j][2], acc[i][j][3]),
                       pk2(acc[i][(j + 1) % NT][0], acc[i][(j + 1) % NT][1]), pk2(acc[i][(j + 1) % NT][2], acc[i][(j + 1) % NT][3]));
      if (SC1) __builtin_amdgcn_raw_buffer_store_b128(v, rc, (row * ldc + col) * 2, 0, 16);
      else *(u32x4*)(C + (size_t)row * ldc + col) = v;
    }
  GST(2);
  VMW0();
  __syncthreads();
  GST(3);
}

#define EXP_AUX 16
#define C_BUFA 131072
#define C_BUFB (131072 + 16384)
#define PJ_OOB 0x7F000000
__device__ __forceinline__ void proj_slot(const Params& p, int x, int j, char* smem, const int wv) {
  const int tid = TIDX, lane = tid & 63, wid = wv;
  const int l15 = lane & 15, lq = lane >> 4, q4 = l15 >> 2, p4 = lane & 3, wr = wid >> 2, wc = wid & 3;
  const __amdgpu_buffer_rsrc_t rc = RSRC(p.ws, O_WOF);
  const int vB = ((wid * 4 + lq) * 4096 + (((l15 >> 1) ^ (lq | (((wid >> 1) & 1) << 2))) * 16 + (lane & 1) * 8)) * 2, vB1 = vB + 32 * 8192;
  int ao = (wr * 64 + l15) * 128 + ((lq ^ (l15 & 7)) << 4);
  int bo = (int)(uintptr_t)smem + 65536 + (lq * 8 + q4) * 256 + (((wc * 2) ^ (q4 | ((lq & 1) << 2))) << 5) + p4 * 8;
  const int vC = (wid * 4 + (lane >> 5)) * 8192 + (lane & 31) * 16;
  const int vD = ((wr * 16 + l15) * 4096 + wc * 32 + (lq & 1) * 16 + (lq >> 1) * 8) * 2;
  const int cw = C_BUFA + (wr * 16 + l15) * 512 + (((wc * 4 + (lq & 1) * 2 + (lq >> 1)) ^ (l15 & 7)) << 4);
  const int cr = C_BUFA + (wid * 4 + (lane >> 5)) * 512 + (((lane & 31) ^ ((wid & 1) * 4 + (lane >> 5))) << 4);
  const int cb = (x & 1) * 8 + (j >> 2);
  __amdgpu_buffer_rsrc_t rsA, rsB;
  int k2, vA, vA1, sa, sb;
#define PJ_UNIT(u) do { const int mi_ = ((u) >> 1) * 3 + (x >> 1); k2 = mi_ < 4 ? 1152 : 1024; \
    rsA = RSRC(mi_ < 3 ? p.Bx : mi_ == 3 ? p.Ax : p.Ac, 2048 * k2); \
    rsB = RSRC((mi_ == 0 ? p.BWk : mi_ == 1 ? p.BWv : mi_ == 2 ? p.BWq : mi_ == 3 ? p.Awq : mi_ == 4 ? p.Awk : p.Awv) + cb * 256, k2 * 4096 - cb * 512); \
    int ln_ = lane; asm volatile("" : "+v"(ln_)); \
    vA = ((wid >> 1) * 32 + (wid & 1) * 8 + (ln_ >> 3)) * k2 + ((ln_ & 7) ^ ((ln_ >> 3) & 7)) * 16; vA1 = vA + 16 * k2; \
    sa = (((j & 3) + ((u) & 1) * 4) * 256 + (mi_ < 3 ? 256 : 0)) * k2 - 128; sb = 0; } while (0)
#define PJ_STAGE(dst, rs, v0, v1, so) do { const int so_ = (so); \
    __builtin_amdgcn_raw_ptr_buffer_load_lds(rs, (LDSP void*)(smem + (dst) + wid * 1024), 16, v0, so_, 0, 0); \
    __builtin_amdgcn_raw_ptr_buffer_load_lds(rs, (LDSP void*)(smem + (dst) + wid * 1024 + 8192), 16, v1, so_, 0, 0); } while (0)
#define PJ_SA(dst, so) PJ_STAGE(dst, rsA, vA, vA1, so)
#define PJ_SB(dst, so) PJ_STAGE(dst, rsB, vB, vB1, so)
#define PJ_LDA(h) do { _Pragma("unroll") for (int m = 0; m < 4; m++) _Pragma("unroll") for (int k = 0; k < 2; k++) \
    At[m][k] = *(const bf8*)(smem + (ao ^ (k * 64)) + (h) * 16384 + m * 2048); } while (0)
#define PJ_TR(dst, a, off) asm volatile("ds_read_b64_tr_b16 %0, %1 offset:%2" : "=&v"(dst) : "v"(a), "i"(off) : "memory")
#define PJ_LDB(D, h) do { const int b1_ = bo ^ 32; _Pragma("unroll") for (int n = 0; n < 2; n++) _Pragma("unroll") for (int k = 0; k < 2; k++) { \
    bf4 lo_, hi_; PJ_TR(lo_, n ? b1_ : bo, (h) * 16384 + k * 8192); PJ_TR(hi_, n ? b1_ : bo, (h) * 16384 + k * 8192 + 1024); D[n][k] = cat8(lo_, hi_); } } while (0)
#define PJ_MMA(S, ai, bj, D) do { PJ_SCHED; __builtin_amdgcn_s_setprio(1); \
    _Pragma("unroll") for (int k = 0; k < 2; k++) _Pragma("unroll") for (int m = 0; m < 4; m++) _Pragma("unroll") for (int n = 0; n < 2; n++) { \
      if (k == 0 && (S) == 0) asm volatile("v_mfma_f32_16x16x32_bf16 %0, %1, %2, 0" : "+v"(acc[ai][bj][m][n]) : "v"(D[n][k]), "v"(At[m][k])); \
      else asm volatile("v_mfma_f32_16x16x32_bf16 %0, %1, %2, %0" : "+v"(acc[ai][bj][m][n]) : "v"(D[n][k]), "v"(At[m][k])); } \
    __builtin_amdgcn_s_setprio(0); PJ_SCHED; } while (0)
#define PJ_WV(n) asm volatile("s_waitcnt vmcnt(" #n ")" ::: "memory")
#define PJ_WL(n) asm volatile("s_waitcnt lgkmcnt(" #n ")" ::: "memory")
#define PJ_WL0 do { __builtin_amdgcn_s_waitcnt(0xC07F); asm volatile("" ::: "memory"); } while (0)
#define PJ_BAR __builtin_amdgcn_s_barrier()
#define PJ_SCHED __builtin_amdgcn_sched_barrier(0)
#define PJ_NOP do { PJ_SCHED; asm volatile("s_nop 7"); PJ_SCHED; } while (0)
#define PJ_PK(q, bj) pair16(pk2(acc[(q) >> 2][bj][(q) & 3][0][0], acc[(q) >> 2][bj][(q) & 3][0][1]), pk2(acc[(q) >> 2][bj][(q) & 3][0][2], acc[(q) >> 2][bj][(q) & 3][0][3]), \
                            pk2(acc[(q) >> 2][bj][(q) & 3][1][0], acc[(q) >> 2][bj][(q) & 3][1][1]), pk2(acc[(q) >> 2][bj][(q) & 3][1][2], acc[(q) >> 2][bj][(q) & 3][1][3]))
#define PJ_CW(buf, v0, v1) do { *(u32x4*)(smem + cw + (buf) * 16384) = (v0); *(u32x4*)(smem + cw + (buf) * 16384 + 256) = (v1); } while (0)
#define PJ_CR(buf, i) (*(const u32x4*)(smem + (cr ^ ((i) * 32)) + (buf) * 16384 + (i) * 1024))
#define PJ_SO(q) ((q) < dlim ? dco + (q) * 32 * 8192 : PJ_OOB)
#define PJ_ST1(v, vo, so) do { PJ_SCHED; __builtin_amdgcn_raw_buffer_store_b128(v, rc, vo, so, EXP_AUX); asm volatile("s_nop 1"); PJ_SCHED; } while (0)
#define PJ_CS(v, i, q) PJ_ST1(v, vC, PJ_SO(q) + (i) * 16384)
#define PJ_DS(v, bj, q) PJ_ST1(v, vD, PJ_SO(q) + (bj) * 256)
#define PJ_FLAG(q) ((q) < dlim ? (dfl + (q) * BLKS) * 4 : PJ_OOB)
#define PJ_PUB(a, b) do { if (wid == 4) __builtin_amdgcn_raw_buffer_store_b32(8u, rc, lane == 0 ? PJ_FLAG(a) : (lane == 1 ? PJ_FLAG(b) : PJ_OOB), 0, 16); } while (0)
#define PJ_ST()
#define PJ_QA(S) ((S) == 0 ? 2 : (S) == 1 ? 3 : (S) + 3)
#define PJ_STEP(S) do { PJ_ST(); u32x4 c0_, c1_; \
    PJ_LDB(B0, 0); PJ_SCHED; \
    if ((S) == 0) PJ_CW(0, PJ_PK(2, 0), PJ_PK(2, 1)); else if ((S) < 5) PJ_CW(0, pk[((S) - 1) & 3][0], pk[((S) - 1) & 3][1]); \
    PJ_SCHED; PJ_LDA(0); PJ_SA((bsel ^ 32768) + 16384, sa + 128 * k2); \
    PJ_WL(8); PJ_BAR; PJ_WL0; PJ_MMA(S, 0, 0, B0); PJ_BAR; PJ_SCHED; \
    if ((S) == 7) PJ_UNIT(un < 3 ? un : 3); \
    if ((S) < 5) c0_ = PJ_CR(0, 0); \
    if ((S) < 2) c1_ = PJ_CR(1, 1); \
    PJ_SCHED; PJ_LDB(B1, 1); PJ_SB(65536 + bsel, sb); \
    if ((S) == 0) { _Pragma("unroll") for (int i = 1; i < 4; i++) { pk[i][0] = PJ_PK(4 + i, 0); pk[i][1] = PJ_PK(4 + i, 1); } } \
    if ((S) < 5) PJ_CS(c0_, 0, PJ_QA(S)); \
    if ((S) < 2) PJ_CS(c1_, 1, (S) == 0 ? 1 : 4); \
    PJ_BAR; PJ_WL0; PJ_MMA(S, 0, 1, B1); PJ_BAR; \
    if ((S) == 8) { const int mi_ = (u >> 1) * 3 + (x >> 1), g_ = ((j & 3) + (u & 1) * 4) * 8 + (mi_ < 3 ? 8 : 0); \
      dco = (mi_ == 0 ? O_KB : mi_ == 1 ? O_VB : mi_ == 2 ? O_QB : O_QA + (mi_ - 3) * ACB) + cb * 512 + g_ * 32 * 8192; \
      dfl = SY_BLK(mi_, cb, g_); dlim = 8; \
      PJ_NOP; PJ_CW(1, PJ_PK(1, 0), PJ_PK(1, 1)); PJ_SCHED; } \
    if ((S) == 0) { PJ_CW(1, PJ_PK(4, 0), PJ_PK(4, 1)); PJ_SCHED; } \
    PJ_LDA(1); sa += 128; PJ_SA(bsel, sa); \
    if ((S) == 8 || (S) == 0) PJ_WL(8); \
    PJ_BAR; PJ_WL0; PJ_MMA(S, 1, 0, B0); PJ_BAR; PJ_SCHED; \
    if ((S) < 5) c0_ = PJ_CR(0, 1); \
    if ((S) == 8 || (S) == 0) c1_ = PJ_CR(1, 0); \
    PJ_SB(65536 + bsel + 16384, sb + 256); sb += 64 * 8192; \
    if ((S) == 8) { PJ_DS(PJ_PK(0, 0), 0, 0); PJ_DS(PJ_PK(0, 1), 1, 0); pk[0][0] = PJ_PK(3, 0); pk[0][1] = PJ_PK(3, 1); } \
    if ((S) < 5) PJ_CS(c0_, 1, PJ_QA(S)); \
    if ((S) == 8 || (S) == 0) PJ_CS(c1_, 0, (S) == 8 ? 1 : 4); \
      \
    if ((S) == 0) PJ_WV(10); else if ((S) == 1 || (S) == 8) PJ_WV(9); else if ((S) < 5) PJ_WV(8); else PJ_WV(6); \
    PJ_BAR; \
    if ((S) < 6) PJ_PUB((S) == 0 ? 0 : (S) == 1 ? 1 : (S) == 2 ? 3 : (S) + 2, (S) == 1 ? 2 : (S) == 2 ? 4 : 8); \
    PJ_MMA(S, 1, 1, B1); PJ_BAR; \
    bsel ^= 32768; ao ^= 32768; bo ^= 32768; } while (0)
  f4 acc[2][2][4][2];
#pragma unroll
  for (int a = 0; a < 16; a++) { acc[a >> 3][(a >> 2) & 1][a & 3][0] = (f4){0, 0, 0, 0}; acc[a >> 3][(a >> 2) & 1][a & 3][1] = (f4){0, 0, 0, 0}; }
  bf8 At[4][2], B0[2][2], B1[2][2];
  u32x4 pk[4][2];
  pk[0][0] = (u32x4){0u, 0u, 0u, 0u}; pk[0][1] = (u32x4){0u, 0u, 0u, 0u};
  PJ_UNIT(0);
  PJ_SB(65536, 0); PJ_SA(0, sa + 128);
  PJ_SB(65536 + 16384, 256); PJ_SA(16384, sa + 128 + 128 * k2);
  if (wr == 1) PJ_BAR;
  PJ_WV(4); PJ_BAR;
  PJ_SB(65536 + 32768, 64 * 8192); PJ_SA(32768, sa + 256);
  PJ_SB(65536 + 32768 + 16384, 64 * 8192 + 256);
  PJ_WV(6); PJ_BAR;
  sa += 256; sb = 128 * 8192;
  int bsel = 0, dco = 0, dfl = 0, dlim = 0;
#pragma unroll 1
  for (int u = 0, un; u < 4; u = un) {
    un = (u == 0 && (j & 3) == 3) ? 2 : u + 1;
    PJ_STEP(0); PJ_STEP(1); PJ_STEP(2); PJ_STEP(3); PJ_STEP(4); PJ_STEP(5);
    int more = k2 - 1024; asm volatile("" : "+s"(more));
    if (more) PJ_STEP(6);
    PJ_STEP(7); PJ_STEP(8);
  }
  PJ_ST();
  PJ_NOP;
  { const u32x4 c1_ = PJ_CR(1, 1); PJ_CS(c1_, 1, 1); }
  PJ_DS(PJ_PK(2, 0), 0, 2); PJ_DS(PJ_PK(2, 1), 1, 2); PJ_DS(pk[0][0], 0, 3); PJ_DS(pk[0][1], 1, 3);
#pragma unroll
  for (int q = 4; q < 8; q++) { PJ_DS(PJ_PK(q, 0), 0, q); PJ_DS(PJ_PK(q, 1), 1, q); }
  PJ_WV(0);
  if (wr == 0) PJ_BAR;
  PJ_BAR;
  if (wid == 4) __builtin_amdgcn_raw_buffer_store_b32(8u, rc, lane < 8 ? (dfl + lane * BLKS) * 4 : PJ_OOB, 0, 16);
  PJ_ST();
#undef PJ_UNIT
#undef PJ_STAGE
#undef PJ_SA
#undef PJ_SB
#undef PJ_LDA
#undef PJ_LDB
#undef PJ_TR
#undef PJ_PK
#undef PJ_CW
#undef PJ_CR
#undef PJ_CS
#undef PJ_DS
#undef PJ_FLAG
#undef PJ_STEP
#undef PJ_PUB
#undef PJ_MMA
}

namespace fa {
constexpr int LD = 4096;
constexpr float SCALE = 0.08838834764831845f;
constexpr int NW = 8, QBLK = 32, KVBLK = 64, QB = NW * QBLK, D = 128;
constexpr int SHM_V = KVBLK * D * 2, SHM_K = KVBLK * D * 2;
#define SBAR() __builtin_amdgcn_sched_barrier(0)
#define MF32(a, b, c) __builtin_amdgcn_mfma_f32_32x32x16_bf16(a, b, c, 0, 0, 0)
#define FMX3(a, b, c) __builtin_elementwise_maximum(__builtin_elementwise_maximum(a, b), c)
#define EBAR 14
#define MX0 3
#define ATHR 8.f
#define fadd1(a, b) ((a) + (b))
#define fmul1(a, b) ((a) * (b))
__device__ __forceinline__ void attn_block(const u16* __restrict__ Qp, const u16* __restrict__ Kh, const u16* __restrict__ Vh,
                                           char* __restrict__ Op, int P0, char* lds, const int wv, u32* qh, int& pre) {
  const int tid = TIDX, wid = wv, lane = tid & 63, r32 = lane & 31, hi = lane >> 5, grp = wid >> 2;
  const int NT = (P0 + QB) / KVBLK;
  const int rb = wid ^ 4;
  const int TL = NT - 4 + (rb >> 1);
  const int rem = 2 * (NT - TL) - 2 - grp;
  float l_reg = 0.f;
  f32x16 o[4] = {}, s0, s1, nm = {};
  int ko = r32 * 256 + ((hi * 16) ^ ((r32 & 15) << 4));
  int vo = 4 * SHM_K + (((lane & 3) << 3) | (((lane >> 2) & 3) << 6) | (((lane >> 4) & 1) << 5) | (hi << 8));
  const __amdgpu_buffer_rsrc_t rk = RSRC(Kh, (P0 + QB - 1) * LD * 2 + 256), rv = RSRC(Vh, (P0 + QB - 1) * LD * 2 + 256);
  const int gk = (wid * 4 + (lane >> 4)) * LD * 2 + (((lane & 15) ^ ((wid & 3) * 4 + (lane >> 4))) << 4);
  const int gv = ((wid >> 1) * 8 + ((lane >> 2) & 7)) * LD * 2 + ((wid & 1) * 2 + hi) * 64 + (lane & 3) * 16;
#define DMA(rs, go, dst, so) __builtin_amdgcn_raw_ptr_buffer_load_lds(rs, (LDSP void*)(lds + (dst) + wid * 1024), 16, go, so, 0, 0)
#define ISSUE_K(t) do { const int s_ = (t) * (KVBLK * LD * 2), d_ = ((t) & 3) * SHM_K; DMA(rk, gk, d_, s_); DMA(rk, gk, d_ + 8192, s_ + 32 * LD * 2); } while (0)
#define ISSUE_V(t) do { const int s_ = (t) * (KVBLK * LD * 2), d_ = 4 * SHM_K + ((t) & 3) * SHM_V; DMA(rv, gv, d_, s_); DMA(rv, gv, d_ + 8192, s_ + 32 * LD * 2); } while (0)
#define PHASE(n) do { SBAR(); asm volatile("s_waitcnt vmcnt(" #n ")" ::: "memory"); __builtin_amdgcn_s_barrier(); asm volatile("" ::: "memory"); SBAR(); } while (0)
#define LDK(d0) do { const char* a_ = lds + (ko ^ ((d0) * 32)); \
    kf[(d0) % 3][0] = *(const bf8*)a_; kf[(d0) % 3][1] = *(const bf8*)(a_ + 32 * 256); } while (0)
#define LDV(i) do { const char* a_ = lds + vo + ((i) & 3) * 512 + ((i) >> 2) * 4096; vf[(i) & 3] = cat8(trrd(a_), trrd(a_ + 2048)); } while (0)
#define AST(i)
#define AP(i)
  AP(0);
#define QK(NEXT) do { _Pragma("unroll") for (int d0 = 0; d0 < 8; ++d0) { \
      if (d0 + 2 < 8) LDK(d0 + 2); else { NEXT(2 * (d0 - 6)); NEXT(2 * (d0 - 6) + 1); } \
      SBAR(); \
      if (d0) { s0 = MF32(kf[d0 % 3][0], qr[d0], s0); s1 = MF32(kf[d0 % 3][1], qr[d0], s1); } \
      else { asm volatile("v_mfma_f32_32x32x16_bf16 %0, %1, %2, %3" : "+v"(s0) : "v"(kf[0][0]), "v"(qr[0]), "v"(nm)); \
             asm volatile("v_mfma_f32_32x32x16_bf16 %0, %1, %2, %3" : "+v"(s1) : "v"(kf[0][1]), "v"(qr[0]), "v"(nm)); } \
      SBAR(); } } while (0)
#define NOV(i)
#define MAXS(i) do { \
      if ((i) == 0) { mx = FMX3(s0[0], s0[1], s0[2]); my = FMX3(s1[0], s1[1], s1[2]); } \
      else if ((i) < 7) { mx = FMX3(mx, s0[2 * (i) + 1], s0[2 * (i) + 2]); my = FMX3(my, s1[2 * (i) + 1], s1[2 * (i) + 2]); } \
      else { mx = FMX3(mx, s0[15], s1[15]); mx = __builtin_elementwise_maximum(mx, my); asm volatile("" : "+v"(mx)); } } while (0)
  float mx, my;
  constexpr float C2 = 1.4426950408889634f * SCALE;
  bf8 qr[8], kf[3][2], vf[4], pa[4];
#pragma unroll
  for (int d0 = 0; d0 < 8; ++d0) qr[d0] = *(const bf8*)(Qp + (size_t)(rb * QBLK + r32) * LD + d0 * 16 + hi * 8);
  SBAR();
  ISSUE_K(0); ISSUE_K(1); ISSUE_V(0); ISSUE_K(2); ISSUE_V(1);
  SBAR();
  asm volatile("" : "=v"(s0), "=v"(s1), "+v"(nm), "+v"(o[0]), "+v"(o[1]), "+v"(o[2]), "+v"(o[3]));
  SBAR();
#pragma unroll
  for (int d0 = 0; d0 < 8; ++d0) {
    u32x4 q = __builtin_bit_cast(u32x4, qr[d0]);
#pragma unroll
    for (int i = 0; i < 4; i++) q[i] = pk2(__uint_as_float(q[i] << 16) * C2, __uint_as_float(q[i] & 0xFFFF0000u) * C2);
    qr[d0] = __builtin_bit_cast(bf8, q);
    asm volatile("" : "+v"(qr[d0]));
  }
  AP(8);
  PHASE(8);
  AP(1);
  if (grp) PHASE(6);
  LDK(0); LDK(1);
  QK(NOV);
#pragma unroll
  for (int i = 0; i < 8; ++i) MAXS(i);
  AP(2);
  PHASE(4);
  AP(3);
  auto tile = [&](const int t, auto LASTC) __attribute__((always_inline)) {
    constexpr bool LAST = decltype(LASTC)::value;
    AST(0);
    ISSUE_K(t + 3); ISSUE_V(t + 2);
    SBAR();
    if constexpr (LAST) {
      const int dq = r32 - 4 * hi;
      if (rb & 1) {
#pragma unroll
        for (int r = 0; r < 16; ++r) { if (dq < (r & 3) + 8 * (r >> 2)) s1[r] = -__builtin_inff(); SBAR(); }
      } else {
#pragma unroll
        for (int r = 0; r < 16; ++r) { if (dq < (r & 3) + 8 * (r >> 2)) s0[r] = -__builtin_inff(); s1[r] = -__builtin_inff(); SBAR(); }
      }
#pragma unroll
      for (int i = 0; i < 8; ++i) MAXS(i);
    }
    {
      if (__builtin_expect(t == 0 || __any(mx > ATHR), 0)) {
        auto rr = __builtin_amdgcn_permlane32_swap(__float_as_uint(mx), __float_as_uint(mx), false, false);
        mx = __builtin_elementwise_maximum(__uint_as_float(rr[0]), __uint_as_float(rr[1]));
        if (t) mx = __builtin_elementwise_maximum(mx, 0.f);
        const float alpha = t ? __builtin_amdgcn_exp2f(-mx) : 1.f, nn = nm[0] - mx;
        mx = -mx;
#pragma unroll
        for (int r = 0; r < 16; ++r) { s0[r] = fadd1(s0[r], mx); s1[r] = fadd1(s1[r], mx); nm[r] = nn; }
#pragma unroll
        for (int d = 0; d < 4; ++d)
#pragma unroll
          for (int r = 0; r < 16; ++r) o[d][r] = fmul1(o[d][r], alpha);
        l_reg *= alpha;
        asm volatile("" : "+v"(nm));
      }
    }
    AST(1);
    float x[32], sb = 0.f;
    u32 pw[16];
#pragma unroll
    for (int r = 0; r < 18; ++r) {
      SBAR();
      if (r < 16) { x[2 * r] = __builtin_amdgcn_exp2f(r < 8 ? s0[2 * r] : s1[2 * r - 16]); x[2 * r + 1] = __builtin_amdgcn_exp2f(r < 8 ? s0[2 * r + 1] : s1[2 * r - 15]); }
      if (r >= 2) {
        l_reg = fadd1(l_reg, x[2 * r - 4]);
        sb = r == 2 ? x[1] : fadd1(sb, x[2 * r - 3]);
        pw[r - 2] = pk2(x[2 * r - 4], x[2 * r - 3]); }
    }
    SBAR();
    l_reg = fadd1(l_reg, sb);
#pragma unroll
    for (int i = 0; i < 4; ++i) pa[i] = __builtin_bit_cast(bf8, (u32x4){pw[4 * i], pw[4 * i + 1], pw[4 * i + 2], pw[4 * i + 3]});
    asm volatile("" : "+v"(pa[0]), "+v"(pa[1]), "+v"(pa[2]), "+v"(pa[3]), "+v"(l_reg));
    ko ^= (t & 1) ? 3 * SHM_K : SHM_K;
    SBAR();
    AST(3);
    if constexpr (!LAST) { LDK(0); LDK(1); } else { LDV(0); LDV(1); LDV(2); LDV(3); }
    AST(4);
    PHASE(4);
    AST(5);
    __builtin_amdgcn_s_setprio(1);
    if constexpr (!LAST) QK(LDV);
    AST(6);
#pragma unroll
    for (int i = 0; i < 16; ++i) {
      SBAR();
      o[i & 3] = MF32(vf[i & 3], pa[i >> 2], o[i & 3]);
      SBAR();
      if (i + 4 < 16) LDV(i + 4);
      if constexpr (!LAST) if (i >= MX0 && i < MX0 + 8) MAXS(i - MX0);
      if (i == EBAR && (!LAST || rem)) PHASE(4);
    }
    __builtin_amdgcn_s_setprio(0);
    vo ^= (t & 1) ? 3 * SHM_V : SHM_V;
    AST(7);
  };
#pragma unroll 1
  for (int t = 0; t < TL; t++) {
    tile(t, std::false_type{});
  }
  AP(4);
  tile(TL, std::true_type{});
  AP(5);
  ISSUE_V(TL + 3);
  SBAR();
  {
    const float inv = __builtin_amdgcn_rcpf(swadd(l_reg, l_reg));
    const __amdgpu_buffer_rsrc_t ro = RSRC(Op + rb * 262144, 8192);
#pragma unroll
    for (int i = 0; i < 8; ++i) {
      const int g = (i & 1) * 8;
#define ON(k) fmul1(o[i >> 1][g + (k)], inv)
      auto r0 = __builtin_amdgcn_permlane32_swap(pk2(ON(0), ON(1)), pk2(ON(4), ON(5)), false, false);
      auto r1 = __builtin_amdgcn_permlane32_swap(pk2(ON(2), ON(3)), pk2(ON(6), ON(7)), false, false);
#undef ON
      __builtin_amdgcn_raw_buffer_store_b128((u32x4){r0[0], r1[0], r0[1], r1[1]}, ro, r32 * 32 + hi * 16 + (i & 3) * 1024, (i >> 2) * 4096, 16);
      if (i == 3 && rem > 1) PHASE(6);
    }
  }
  AP(6);
  if (rem > 2) PHASE(10);
  if (rem > 3) { PHASE(8); PHASE(8); }
  CLAIM();
  VMW0();
  AP(7);
  __syncthreads();
}
#undef DMA
#undef ISSUE_K
#undef ISSUE_V
#undef PHASE
#undef LDK
#undef LDV
#undef QK
#undef NOV
#undef MAXS
}

__device__ __forceinline__ void wof_task(const Params& p, int kc, char* smem, const int wv, u32* qh, int& pre) {
  const int tid = TIDX, lane = tid & 63, wid = wv;
  const int l15 = lane & 15, lq = lane >> 4, q4 = l15 >> 2, p4 = lane & 3;
  const __amdgpu_buffer_rsrc_t rw = RSRC(WSP(u16, O_WOF), 2 * WOB);
  bf8 st[2][9];
#pragma unroll
  for (int m = 0; m < 2; m++)
#pragma unroll
    for (int i = 0; i < 9; i++) st[m][i] = *(const bf8*)((m ? p.BWo : p.Awo) + (size_t)kc * 64 * 576 + (tid + 512 * i) * 8);
  CLAIM();
#pragma unroll
  for (int m = 0; m < 2; m++) {
#pragma unroll
    for (int i = 0; i < 9; i++) {
      const int id = tid + 512 * i, row = id / 72, ch = id % 72;
      *(bf8*)(smem + row * 1280 + (((ch >> 1) ^ fsw(row)) << 5) + ((ch & 1) << 4)) = st[m][i];
    }
    asm volatile("s_waitcnt lgkmcnt(0)" ::: "memory"); __builtin_amdgcn_s_barrier(); asm volatile("" ::: "memory");
#pragma unroll
    for (int i = 0; i < 9; i++) {
      const int f = wid * 9 + i, nt = f >> 1, k2 = f & 1;
      const int r0 = k2 * 32 + lq * 8 + q4, r1 = r0 + 4;
      bf4 lo = trrd(smem + r0 * 1280 + ((nt ^ fsw(r0)) << 5) + p4 * 8);
      bf4 hi = trrd(smem + r1 * 1280 + ((nt ^ fsw(r1)) << 5) + p4 * 8);
      __builtin_amdgcn_raw_buffer_store_b128(__builtin_bit_cast(u32x4, cat8(lo, hi)), rw, ((nt * 128 + kc * 2 + k2) * 64 + lane) * 16, m * WOB, 16);
    }
    if (m == 0) { asm volatile("s_waitcnt lgkmcnt(0)" ::: "memory"); __builtin_amdgcn_s_barrier(); asm volatile("" ::: "memory"); }
  }
  VMW0();
  __syncthreads();
}

#define OT_AAUX 0
#define OT_BAUX 0
__device__ __forceinline__ void out_tile(const char* A, const char* W, u16* C, char* smem, const int wv, u32* qh, int& pre) {
  const int tid = TIDX, lane = tid & 63, wid = wv;
  const int l15 = lane & 15, lq = lane >> 4, kh = wid >> 2, wc = wid & 3;
  const __amdgpu_buffer_rsrc_t ra = RSRC(A, 2 * 262144), rw = RSRC(W + wc * 3 * 131072 + kh * 1024, 3 * 131072 - kh * 1024);
  const int va = tid * 16, vw = lane * 16;
  int ao = kh * 2048 + (lq >> 1) * 1024 + l15 * 32 + (lq & 1) * 16;
  f4 acc[4][3];
  bf8 wf[8][3], af[2][4];
#define OT_DMA(i, buf, so) __builtin_amdgcn_raw_ptr_buffer_load_lds(ra, (LDSP void*)(smem + (buf) + (i) * 8192 + wid * 1024), 16, va, (so) + ((i) >> 1) * 262144 + ((i) & 1) * 8192, 0, OT_AAUX)
#define OT_LDW(u, so) do { _Pragma("unroll") for (int j = 0; j < 3; j++) \
    wf[u][j] = __builtin_bit_cast(bf8, __builtin_amdgcn_raw_buffer_load_b128(rw, vw, (so) + j * 131072, OT_BAUX)); } while (0)
#define OT_LDA(d, base, off) do { _Pragma("unroll") for (int m = 0; m < 4; m++) af[d][m] = *(const bf8*)(smem + (base) + (off) + (m >> 1) * 16384 + (m & 1) * 512); } while (0)
#define OT_MF(a, b, c) MFMA(a, b, c)
#define OT_XA(v) (v)
#define OT_XW(v) (v)
#pragma unroll
  for (int i = 0; i < 4; i++) OT_DMA(i, 0, 0);
  SB0();
#pragma unroll
  for (int u = 0; u < 8; u++) { OT_DMA(u & 3, 32768 + (u >> 2) * 32768, 16384 + (u >> 2) * 16384); OT_LDW(u, u * 2048); SB0(); }
#pragma unroll
  for (int m = 0; m < 4; m++)
#pragma unroll
    for (int j = 0; j < 3; j++) acc[m][j] = (f4){0, 0, 0, 0};
  asm volatile("s_waitcnt vmcnt(32)" ::: "memory"); __builtin_amdgcn_s_barrier(); asm volatile("" ::: "memory");
  OT_LDA(0, ao, 0);
  int bs = 0;
#pragma unroll 1
  for (int c = 0; c < 16; c += 2) {
    const int sw = c < 14 ? OT_XW((c + 2) * 8192) : PJ_OOB;
#pragma unroll
    for (int u = 0; u < 8; u++) {
      SB0();
      __builtin_amdgcn_s_waitcnt(0x4F7C);
      if ((u & 3) == 3) { asm volatile("s_waitcnt lgkmcnt(0)" ::: "memory"); __builtin_amdgcn_s_barrier(); asm volatile("" ::: "memory"); }
      if (u == 7) ao ^= 65536;
      OT_LDA((u + 1) & 1, ao, u == 7 ? 0 : ((u + 1) >> 2) * 32768 + ((u + 1) & 3) * 4096);
      SB0();
#define OT_V 1
      const int cn = c + 3 + (u >> 2);
#pragma unroll
      for (int j = 0; j < 3; j++) {
#pragma unroll
        for (int m = 0; m < 4; m++) {
          acc[m][j] = OT_MF(wf[u][j], af[u & 1][m], acc[m][j]);
          if (j == 0 && m == 1) { SB0(); OT_DMA(u & 3, u < 4 ? (bs ^ 65536) + 32768 : bs, cn < 16 ? OT_XA(cn * 16384) : PJ_OOB); SB0(); }
        }
        SB0();
        wf[u][j] = __builtin_bit_cast(bf8, __builtin_amdgcn_raw_buffer_load_b128(rw, vw, sw + u * 2048 + j * 131072, OT_BAUX));
        SB0();
      }
    }
    bs ^= 65536;
  }
  CLAIM();
  VMW0C();
  asm volatile("s_waitcnt lgkmcnt(0)" ::: "memory"); __builtin_amdgcn_s_barrier(); asm volatile("" ::: "memory");
#pragma unroll
  for (int m = 0; m < 2; m++)
#pragma unroll
    for (int j = 0; j < 3; j++) *(f4*)(smem + (m * 3 + j) * 8192 + va) = kh ? acc[m][j] : acc[2 + m][j];
  __syncthreads();
#pragma unroll
  for (int m = 0; m < 2; m++) {
    f4 t[3];
#pragma unroll
    for (int j = 0; j < 3; j++) { const f4 o = *(const f4*)(smem + (m * 3 + j) * 8192 + (va ^ 4096)); t[j] = kh ? o + acc[2 + m][j] : acc[m][j] + o; }
    u16* c = C + (size_t)(kh * 32 + m * 16 + l15) * 1152 + wc * 48;
    *(u32x4*)(c + (lq & 1) * 16 + (lq >> 1) * 8) = pair16(pk2(t[0][0], t[0][1]), pk2(t[0][2], t[0][3]), pk2(t[1][0], t[1][1]), pk2(t[1][2], t[1][3]));
    *(u32x2*)(c + 32 + lq * 4) = (u32x2){pk2(t[2][0], t[2][1]), pk2(t[2][2], t[2][3])};
  }
  __syncthreads();
#undef OT_DMA
#undef OT_LDW
#undef OT_LDA
}

#define CLAIMS() do { const int l_ = TIDX; if (l_ < 64) { \
    const u32 v_ = ld4_sc1(WSP(u32, 0) + SY_OQ(l_ < 6 ? l_ : 0)); \
    if (l_ == 0) pre = (int)__hip_atomic_fetch_add(WSP(u32, 0) + SY_OQ(6), 1u, __ATOMIC_RELAXED, __HIP_MEMORY_SCOPE_AGENT); \
    pre |= (int)((u32)__ballot(l_ < 6 && v_ < ((l_ & 1) ? XS0 / 2 : 32)) << 16); } } while (0)
__device__ __forceinline__ void small_out_task(const Params& p, int s0, int hf, int nt, char* smem, const int wv, int& pre) {
  const int tid = TIDX, lane = tid & 63, wid = wv;
  const int l15 = lane & 15, lq = lane >> 4;
  const __amdgpu_buffer_rsrc_t rw = RSRC(WSP(char, O_WOF) + WOB, WOB), ro = RSRC(WSP(u16, O_OB), 2048 * 4096 * 2);
  if (tid == 0) {
    int it = 0;
    while (ld4_sc1(WSP(u32, 0) + SY_CW) < 64u && ++it < SPIN_CAP) __builtin_amdgcn_s_sleep(8);
  }
  __syncthreads();
  bf8 bfr[16];
#pragma unroll
  for (int k = 0; k < 16; k++) bfr[k] = ldsc1(rw, ((nt * 128 + wid * 16 + k) * 64 + lane) * 16);
#pragma unroll 1
  for (int s = s0; s < 64; s += 2) {
    if (tid < 64) {
      int it = 0;
      if (s < 62) while (ld4_sc1(WSP(u32, 0) + SY_CS(s)) == 0u && ++it < SPIN_CAP) __builtin_amdgcn_s_sleep(SPS);
      else {
        while (ld4_sc1(WSP(u32, 0) + SY_CS(61)) == 0u && ++it < SPIN_CAP) __builtin_amdgcn_s_sleep(SPS);
        while (!__all(ld4_sc1(WSP(u32, 0) + SY_SEG(s - 62, tid & 31)) != 0u) && ++it < SPIN_CAP) __builtin_amdgcn_s_sleep(4);
      }
    }
    __syncthreads();
    const int row0 = s * 32 + hf * 16;
    bf8 af[16];
#pragma unroll
    for (int k = 0; k < 16; k++) af[k] = ldsc1(ro, OB_OFF(row0 + l15, (wid * 16 + k) * 32 + lq * 8));
    if (s >= 62) CLAIMS();
    f4 acc = (f4){0, 0, 0, 0};
#pragma unroll
    for (int k = 0; k < 16; k++) acc = MFMA(bfr[k], af[k], acc);
    *(f4*)(smem + (wid * 64 + lane) * 16) = acc;
    __syncthreads();
    if (tid < 64) {
      f4 t = (f4){0, 0, 0, 0};
#pragma unroll
      for (int w = 0; w < 8; w++) t += *(const f4*)(smem + (w * 64 + lane) * 16);
      *(u64*)(p.out + (size_t)(row0 + l15) * 1152 + 576 + nt * 16 + lq * 4) = (u64)pk2(t[0], t[1]) | ((u64)pk2(t[2], t[3]) << 32);
    }
  }
  __syncthreads();
}

#define PSTAMP()
#define STAMP(i)
#define ANS 16
#define KSL 1
#define DSH 2
#define K1 24
#define K2 (K1 + 92)
#define F1 0
#define F2 0
#define C_IMG 0
#define C_QT 9216
#define C_KT (C_QT + 3072)
#define C_P (C_KT + 4096)
#define C_PX (C_P + 768)
#define C_KX (C_PX + 2560)
#define C_VX (C_KX + 8192)
#define C_QX (C_VX + 8192)
#define C_WL 49920
__device__ __forceinline__ float ror8(float v) {
  return __uint_as_float(__builtin_amdgcn_mov_dpp(__float_as_uint(v), 0x128, 0xf, 0xf, true));
}
__device__ __forceinline__ float rb1(float f) { return __uint_as_float(pk2(0.f, f)); }
#define FMX(a, b) __builtin_elementwise_maximum(a, b)
__device__ __forceinline__ float tmax(float mx) {
  mx = FMX(mx, ror8(mx));
  { auto t = __builtin_amdgcn_permlane16_swap(__float_as_uint(mx), __float_as_uint(mx), false, false);
    mx = FMX(__uint_as_float(t[0]), __uint_as_float(t[1])); }
  { auto t = __builtin_amdgcn_permlane32_swap(__float_as_uint(mx), __float_as_uint(mx), false, false);
    mx = FMX(__uint_as_float(t[0]), __uint_as_float(t[1])); }
  return mx;
}
__device__ __forceinline__ int wmax(int x) {
  x = max(x, __builtin_amdgcn_mov_dpp(x, 0x128, 0xf, 0xf, true));
  x = max(x, __builtin_amdgcn_mov_dpp(x, 0x124, 0xf, 0xf, true));
  x = max(x, __builtin_amdgcn_mov_dpp(x, 0x4E, 0xf, 0xf, true));
  x = max(x, __builtin_amdgcn_mov_dpp(x, 0xB1, 0xf, 0xf, true));
  { auto t = __builtin_amdgcn_permlane16_swap((u32)x, (u32)x, false, false); x = max((int)t[0], (int)t[1]); }
  { auto t = __builtin_amdgcn_permlane32_swap((u32)x, (u32)x, false, false); x = max((int)t[0], (int)t[1]); }
  return __builtin_amdgcn_readfirstlane(x);
}
typedef __attribute__((ext_vector_type(2))) unsigned long long u64x2;
__device__ __forceinline__ f4 zero4() {
  u64 a, b; asm volatile("v_mov_b64 %0, 0\n\tv_mov_b64 %1, 0\n\ts_nop 1" : "=v"(a), "=v"(b));
  return __builtin_bit_cast(f4, (u64x2){a, b});
}
__device__ __forceinline__ u32x4 ones4() {
  u64 a, b; asm volatile("v_mov_b64 %0, -1\n\tv_mov_b64 %1, -1" : "=v"(a), "=v"(b));
  return __builtin_bit_cast(u32x4, (u64x2){a, b});
}
#define BAR() do { asm volatile("s_waitcnt lgkmcnt(0)" ::: "memory"); __builtin_amdgcn_s_barrier(); asm volatile("" ::: "memory"); } while (0)
#define BARC() do { asm volatile("" ::: "memory"); __builtin_amdgcn_s_waitcnt(0xC07F); __builtin_amdgcn_s_barrier(); asm volatile("" ::: "memory"); } while (0)
#define BARN() do { asm volatile("" ::: "memory"); __builtin_amdgcn_s_barrier(); asm volatile("" ::: "memory"); } while (0)
#define IDX() const int tid = TIDX, lane = tid & 63, l15 = lane & 15, lq = lane >> 4, q4 = l15 >> 2, p4 = lane & 3, hi8 = l15 >> 3; \
    (void)q4; (void)p4; (void)hi8; (void)lq
#define AD_LD() __builtin_amdgcn_raw_buffer_load_b32(rsX, (TIDX & 31) * ANS, X_ANN + (s & 1) * 1024 + xp, 16)
#define AD_WAIT(K, F, CAP) do { int n_ = 0; u32 an = AD_LD(); \
    while (__any((an & 63u) != (u32)s) && ++n_ < (CAP)) { __builtin_amdgcn_s_sleep(1); CFENCE(); an = AD_LD(); } \
    const u32 r_ = (u32)__builtin_amdgcn_readfirstlane((int)an); \
    const u32 dl_ = ((r_ + (u32)wmax((int)(an - r_))) & ~63u) + (u32)(K) * 64u; \
    n_ = 160; \
    while ((int)(((u32)wall_clock64() << 6) - dl_) < 0 && --n_ > 0) {} \
    if (F) __builtin_amdgcn_s_sleep(F); } while (0)
#define STG_AUX 2
#define OSTG_AUX 0
#define STG_D 3
#define STG_POLL 7
#define STS(c, i)

#define DPR(OP, N) OP " %3, %3, %3 row_ror:" #N " row_mask:0xf bank_mask:0xf\n\t" OP " %4, %4, %4 row_ror:" #N " row_mask:0xf bank_mask:0xf\n\t"
#define DPROW(OP) "s_nop 0\n\t" DPR(OP, 8) "s_nop 0\n\t" DPR(OP, 4) "s_nop 0\n\t" DPR(OP, 2) "s_nop 0\n\t" DPR(OP, 1)
#define ANJ 0
#define LSL 2
__device__ __forceinline__ void chain_head(const Params& p, char* smem, const int h, const int wv) {
  STS(h, 1);
  const int wid = wv;
  u32x8 w[2][9], wx[3];
  const __amdgpu_buffer_rsrc_t rsX = RSRC(XWIN(p), X_END);
  const __amdgpu_buffer_rsrc_t rsB = RSRC(WSP(u16, O_QB), 4 * ACB);
  const int cb = h >> 1;
  const int nvt = (wid & 3) == 0 ? 0 : (wid == 5 || wid == 6 ? 2 : 1);
  const int vt = wid < 4 ? wid - 1 : (wid == 7 ? 3 : wid * 2 - 6);
  const int wl = C_WL + (nvt == 1 ? vt * 12288 : 49152 + (nvt == 2 ? wid - 5 : wid >> 2) * 30720);
  int xp;
  {
    IDX();
    u32x4 st[54], nk, nv, nq;
    u32 fl, fv, fk;
    const __amdgpu_buffer_rsrc_t rq = RSRC(p.BWq, 576 * 4096 * 2), rk = RSRC(p.BWk, 576 * 4096 * 2), rv = RSRC(p.BWv, 576 * 4096 * 2);
    const int vw = ((tid >> 4) * 4096 + h * 128 + (tid & 15) * 8) * 2;
    {
      const u32x4 e = ones4();
#pragma unroll
      for (int i = 0; i < 2; i++) {
        const int id = wid + 8 * i;
        __builtin_amdgcn_raw_buffer_store_b128(e, rsX, lane * 16, id < 12 ? X_O + (id >> 2) * 131072 + (((id >> 1) & 1) * 4 + (h >> 3)) * 16384 + (id & 1) * 8192 + (h & 7) * 1024 : PJ_OOB, 16);
      }
      __builtin_amdgcn_raw_buffer_store_b128(e, rsX, tid < 4 ? X_ANN + (tid & 1) * 1024 + (tid >> 1) * 8192 + h * ANS : PJ_OOB, 0, 16);
    }
    const __amdgpu_buffer_rsrc_t rm = RSRC(p.Bm, 8 * 576 * 2);
    const u32x4 m0 = __builtin_amdgcn_raw_buffer_load_b128(rm, tid * 16, 0, 0), m1 = __builtin_amdgcn_raw_buffer_load_b128(rm, tid * 16, 8192, 0);
    auto req = [&](int kc) {
#pragma unroll
      for (int i = 0; i < 6; i++) st[kc * 6 + i] = __builtin_amdgcn_raw_buffer_load_b128(i < 2 ? rq : (i < 4 ? rk : rv), vw, (kc * 64 + (i & 1) * 32) * 8192, STG_AUX);
      SB0();
    };
#pragma unroll
    for (int kc = 0; kc < STG_D; kc++) { req(kc); __builtin_amdgcn_s_barrier(); SB0(); }
    auto cell = [](int row, int slot) { return (((row * 2 + slot) ^ ((((row >> 2) ^ (row >> 4)) & 1) << 2) ^ ((row >> 5) & 1)) << 5); };
    auto put = [&](int unit, int row, const u32x4& v) { *(u32x4*)(smem + unit * 4160 + cell(row, (tid >> 1) & 1) + (tid & 1) * 16) = v; };
    auto frag = [&](int tile) {
      bf4 tt[4];
#pragma unroll
      for (int kk = 0; kk < 4; kk++) {
        const int r0 = (kk >> 1) * 32 + (lq & 1) * 16 + (lq >> 1) * 8 + (kk & 1) * 4 + (q4 & 1) * 2 + (q4 >> 1);
        tt[kk] = trrd(smem + (tile >> 1) * 4160 + cell(r0, tile & 1) + p4 * 8);
      }
      return ilv(tt[0], tt[1], tt[2], tt[3]);
    };
    auto park = [&](int slot, const u32x8& f) {
      char* d = smem + wl + slot * 2048 + lane * 16;
      *(u32x4*)d = (u32x4){f[0], f[1], f[2], f[3]};
      *(u32x4*)(d + 1024) = (u32x4){f[4], f[5], f[6], f[7]};
    };
    auto poll = [&]() {
      fl = ld4_sc1(WSP(u32, 0) + SY_INIT(lane)); fv = ld4_sc1(WSP(u32, 0) + SY_VOTE(lane));
      fk = ld4_sc1(WSP(u32, 0) + SY_BLK(lane < 12 ? lane >> 2 : 0, cb, lane & 3));
    };
#pragma unroll
    for (int kc = 0; kc < 9; kc++) {
      if (kc == STG_POLL) { poll(); SB0(); }
#pragma unroll
      for (int i = 0; i < 6; i++) put((i >> 1) * 4 + ((tid & 15) >> 2), (i & 1) * 32 + (tid >> 4), st[kc * 6 + i]);
      BAR();
      if (kc == 0 && tid == 0) st4_sc1(WSP(u32, 0) + SY_INIT(h), 1u);
      if (kc + STG_D < 9) req(kc + STG_D);
      w[0][kc] = frag(wid * 2);
      w[1][kc] = frag(nvt == 2 ? 16 + vt : wid * 2 + 1);
      const u32x8 tp = frag(nvt == 0 ? 11 + (wid >> 1) : 15 + vt + nvt);
      if (kc >= 4 && kc < 7) { wx[kc - 4] = tp; if (nvt == 0) park(6 + kc, tp); }
      else park(nvt == 0 ? 6 + kc : (kc >= 7 ? kc - 7 : 2 + kc), tp);
      BAR();
      if (kc == 0 || kc == 8) STS(h, kc + 3);
    }
    if (!__all(fl != 0u && fv != 0u && fk >= 8u)) {
      int it = 0;
      do { __builtin_amdgcn_s_sleep(2); poll(); } while (!__all(fl != 0u && fv != 0u && fk >= 8u) && ++it < SPIN_CAP);
    }
    STS(h, 13);
    CFENCE();
    nq = __builtin_amdgcn_raw_buffer_load_b128(rsB, vw, 0, 16);
    nk = __builtin_amdgcn_raw_buffer_load_b128(rsB, vw, ACB, 16);
    nv = __builtin_amdgcn_raw_buffer_load_b128(rsB, vw, 2 * ACB, 16);
    SB0();
#pragma unroll
    for (int i = 0; i < 2; i++) {
      const int id = tid + 512 * i, t = id / 72, hb = id % 72;
      char* d = smem + C_IMG + (hb >> 2) * 512 + (t >> 2) * 256 + (((hb & 3) ^ ((t >> 2) * 2)) << 6) + ((t >> 1) & 1) * 8 + (t & 1) * 2;
      if (i == 0 || tid < 64) {
#pragma unroll
        for (int e = 0; e < 8; e++) *(u16*)(d + (e >> 1) * 16 + (e & 1) * 4) = (u16)((i ? m1 : m0)[e >> 1] >> ((e & 1) * 16));
      }
    }
    xp = __builtin_amdgcn_readfirstlane(__popcll(__ballot(fv == 2u)) > 32 ? 8192 : 0);
    const int r = tid >> 4, ch = tid & 15;
    *(u32x4*)(smem + C_QX + r * 256 + ((ch ^ (r & 15)) << 4)) = nq;
    *(u32x4*)(smem + C_KX + r * 256 + ((ch ^ (r & 15)) << 4)) = nk;
    *(u32x4*)(smem + C_VX + r * 256 + ((ch ^ ((r & 7) << 1)) << 4)) = nv;
  }
  __syncthreads();
  STS(h, 14);
  constexpr float CE = 0.08838834764831845f * 1.4426950408889634f;
  auto run = [&](auto RC) __attribute__((always_inline)) {
  constexpr int R = decltype(RC)::value;
  constexpr int NV = R < 2 ? 0 : (R < 4 ? 1 : 2);
  constexpr bool QW = R == 0 || R == 2;
  int known = 0;
  bf8 pre[4][2], vx[NV ? NV : 1];
  int sidx, mo, mo1, eo, gvo, wvo = 0, so = 0, po = 0, qo = 0, ko = 0, ko1 = 0, fo = 0;
  float mk[4];
#define LDW(dst, slot) do { dst[0] = *(const bf8*)(smem + wvo + (slot) * 2048); dst[1] = *(const bf8*)(smem + wvo + (slot) * 2048 + 1024); } while (0)
  {
    IDX();
    if constexpr (R == 1) {
      fo = (lq * 4096 + h * 128 + l15 * 8) * 2;
      asm volatile("" : "+v"(fo));
    } else if constexpr (R == 0) {
      qo = C_QT + lq * 384 + q4 * 32 + p4 * 8; ko = C_KT + ((((lq * 8 + q4) * 32) ^ ((lq & 1) << 7)) + p4 * 8); ko1 = C_KT + ((((lq * 8 + 4 + q4) * 32) ^ ((lq & 1) << 7)) + p4 * 8);
      po = C_P + l15 * 16 + lq * 4;
      fo = l15 * 256 + ((lq ^ l15) << 4);
#pragma unroll
      for (int r = 0; r < 4; r++) { mk[r] = lq * 4 + r <= l15 ? 0.f : -__builtin_inff(); asm volatile("" : "+v"(mk[r])); }
      asm volatile("" : "+v"(qo), "+v"(ko), "+v"(ko1), "+v"(po), "+v"(fo));
    } else {
      wvo = wl + lane * 16;
      po = C_P + (lq * 4 + q4) * 16 + (p4 & 1) * 8;
      so = ((l15 >> 2) & 1) * 65536 + ((vt + (NV == 2 ? hi8 : 0)) * 8 + ((l15 >> 1) & 1) * 4 + (lq >> 1) * 2 + (l15 & 1)) * 16 + (lq & 1) * 8;
      if (NV == 1 && hi8) so = PJ_OOB;
      asm volatile("" : "+v"(wvo), "+v"(po), "+v"(so));
#pragma unroll
      for (int j = 0; j < 4; j++) LDW(pre[j], NV == 2 ? 6 + j : j);
    }
    eo = QW ? C_QT + wid * 768 + ((l15 >> 2) * 2 + (l15 & 1)) * 32 + lq * 8 + ((l15 >> 1) & 1) * 4
            : C_KT + (wid & 3) * 1024 + ((l15 * 32) ^ (hi8 << 7)) + lq * 8;
    sidx = (l15 & 2) ? 0xEEEE : 0x4444;
    mo = C_IMG + (lq >> 1) * 512 + (p4 >> 1) * (256 + 128) + (lq & 1) * 64 + q4 * 16 + (p4 & 1) * 8; mo1 = mo ^ 128;
    gvo = tid * 16;
    asm volatile("" : "+v"(sidx), "+v"(mo), "+v"(mo1), "+v"(eo), "+v"(gvo));
  }
  f4 a0, a1;
  u32 ts = 0;
  u64 tq = 0;
  int dur = 70 * 64;
  const int ob = (h >> 3) * 16384 + (h & 7) * 1024 + xp;
  int b3 = 0;
#pragma unroll 1
  for (int s = 0;; s++) {
    int more = s; asm volatile("" : "+s"(more));
    const int mb = X_M + b3 * 32768 + xp;
    u32x4 g, gx;
    if (more) {
      BAR();
      g = __builtin_amdgcn_raw_buffer_load_b128(rsX, gvo, mb, 16);
      if constexpr (R == 1) gx = __builtin_amdgcn_raw_buffer_load_b128(rsX, gvo, mb + 16384 - 4096, 16);
    } else asm volatile("" : "=v"(g), "=v"(gx));
    SB0();
    if constexpr (R == 0) {
#pragma unroll
      for (int j = 0; j < 4; j++) { const char* kx_ = smem + C_KX + (fo ^ (j * 64));
        pre[j >> 1][j & 1] = *(const bf8*)kx_; pre[2 + (j >> 1)][j & 1] = *(const bf8*)(kx_ + 4096); }
    } else if constexpr (R > 1) {
      IDX();
#pragma unroll
      for (int d = 0; d < NV; d++) { const char* vb_ = smem + C_VX + (lq * 4 + q4) * 256 + (((vt + d) ^ ((lq & 1) * 4 + q4)) << 5) + p4 * 8;
        vx[d] = cat8(trrd(vb_), trrd(vb_ + 4096)); }
    }
    SB0();
    a0 = zero4(); a1 = zero4();
    int so_ = X_O + b3 * 131072 + ob, an_ = X_ANN + (s & 1) * 1024 + xp + h * ANS;
    int nb = s < 63 ? (s + 1) * 32 * 8192 : PJ_OOB;
    asm volatile("" : "+s"(so_), "+s"(an_), "+s"(nb));
    u32x4 nx[R == 1 ? 16 : 2];
    SB0();
    if (more) {
      VMW0C();
      *(u32x4*)(smem + C_IMG + gvo) = g;
      if constexpr (R == 1) *(u32x4*)(smem + C_IMG + 4096 + gvo) = gx;
      SB0();
#define G_BAD() (umax(umax(umax(g[0], g[1]), umax(g[2], g[3])), R != 1 ? 0u : umax(umax(gx[0], gx[1]), umax(gx[2], gx[3]))) == ~0u)
      if (__builtin_expect(__any(G_BAD()), 0)) {
        int it = 0;
        do {
          CFENCE();
          if (G_BAD()) {
            g = __builtin_amdgcn_raw_buffer_load_b128(rsX, gvo, mb, 16);
            if constexpr (R == 1) gx = __builtin_amdgcn_raw_buffer_load_b128(rsX, gvo, mb + 16384 - 4096, 16);
          }
          VMW0C();
        } while (__any(G_BAD()) && ++it < SPIN_CAP);
        *(u32x4*)(smem + C_IMG + gvo) = g;
        if constexpr (R == 1) *(u32x4*)(smem + C_IMG + 4096 + gvo) = gx;
      }
    }
    SB0();
    STAMP(15);
    BARC();
    STAMP(0);
#define LDM(j) cat8(trrd(smem + mo + (j) * 1024), trrd(smem + mo1 + (j) * 1024))
    bf8 b[5];
    if constexpr (R == 1) { asm volatile("s_memrealtime %0" : "=s"(tq)); SB0(); }
#pragma unroll
    for (int j = 0; j < 4; j++) { b[j] = LDM(j); SB0(); }
#pragma unroll
    for (int j = 0; j < 9; j++) {
      if (j + 4 < 9) b[(j + 4) % 5] = LDM(j + 4);
      SB0();
      a0 = SMF(b[j % 5], w[0][j], a0, sidx);
      if constexpr (NV < 2) a1 = SMF(b[j % 5], w[1][j], a1, sidx);
      else a1 = SMF(b[j % 5], cat16(pre[j & 3][0], pre[j & 3][1]), a1, sidx);
      SB0();
      if constexpr (R == 1) {
        if (j == ANJ) {
          asm volatile("s_waitcnt lgkmcnt(0)" : "+s"(tq));
          ts = (u32)tq;
          __builtin_amdgcn_raw_buffer_store_b32((((ts << 6) + (u32)dur) & ~63u) | (u32)s, rsX, 0, an_, 16);
          SB0();
        }
        if (j > 0) {
#pragma unroll
          for (int i = 2 * j - 2; i < 2 * j; i++) nx[i] = __builtin_amdgcn_raw_buffer_load_b128(rsB, fo, nb + (1 + (i >> 3)) * ACB + (i & 7) * 4 * 8192, 16);
          SB0();
        }
      }
      if constexpr (NV == 2) LDW(pre[j & 3], j < 5 ? 10 + j : j - 5);
    }
    {
      const u32 e0 = pk2(a0[0] + a0[2], a0[1] + a0[3]), e1 = pk2(a1[0] + a1[2], a1[1] + a1[3]);
      if constexpr (QW) asm volatile("ds_write2_b32 %0, %1, %2 offset1:96" :: "v"(eo), "v"(e0), "v"(e1) : "memory");
      else asm volatile("ds_write2_b32 %0, %1, %1 offset1:1\n\tds_write2_b32 %0, %2, %2 offset0:128 offset1:129" :: "v"(eo), "v"(e0), "v"(e1) : "memory");
    }
    SB0();
    if constexpr (R != 1) { a0 = zero4(); a1 = zero4(); }
    if constexpr (NV == 0) {
      f4 a2;
      if constexpr (R == 0) a2 = zero4();
      STAMP(1);
      BARC();
      STAMP(2);
      if constexpr (R == 0) {
        bf8 qa[2], km[4];
#pragma unroll
        for (int i = 0; i < 2; i++) { qa[i] = cat8(trrd(smem + qo + i * 1536), trrd(smem + qo + i * 1536 + 128)); SB0(); }
#pragma unroll
        for (int i = 0; i < 4; i++) { km[i] = cat8(trrd(smem + ko + i * 1024), trrd(smem + ko1 + i * 1024)); SB0(); }
#pragma unroll
        for (int i = 0; i < 2; i++) {
          a0 = SMF(qa[i], cat16(pre[i][0], pre[i][1]), a0, sidx); asm volatile("" : "+v"(a0)); SB0();
          a1 = SMF(qa[i], cat16(pre[2 + i][0], pre[2 + i][1]), a1, sidx); asm volatile("" : "+v"(a1)); SB0();
        }
#pragma unroll
        for (int i = 0; i < 2; i++) { a2 = SMF(qa[i], cat16(km[2 * i], km[2 * i + 1]), a2, sidx); asm volatile("" : "+v"(a2)); SB0(); }
        float e0 = a0[0] + a0[2], e1 = a0[1] + a0[3], e2 = a1[0] + a1[2], e3 = a1[1] + a1[3];
        asm volatile("" : "+v"(e0), "+v"(e1), "+v"(e2), "+v"(e3));
        SB0();
        float e4 = a2[0] + a2[2], e5 = a2[1] + a2[3], x0, x1;
        STAMP(3);
        asm volatile("v_max3_f32 %3, %0, %1, %2\n\tv_max3_f32 %4, %5, %6, %7\n\t" DPROW("v_max_f32_dpp")
                     "v_mul_f32 %3, %8, %3\n\tv_mul_f32 %4, %8, %4\n\t"
                     "v_fma_f32 %0, %0, %8, -%3\n\tv_fma_f32 %5, %5, %8, -%4\n\tv_exp_f32 %0, %0\n\tv_exp_f32 %5, %5\n\t"
                     "v_fma_f32 %1, %1, %8, -%3\n\tv_fma_f32 %6, %6, %8, -%4\n\tv_exp_f32 %1, %1\n\tv_exp_f32 %6, %6\n\t"
                     "v_fma_f32 %2, %2, %8, -%3\n\tv_fma_f32 %7, %7, %8, -%4\n\tv_exp_f32 %2, %2\n\tv_exp_f32 %7, %7\n\t"
                     "v_add_f32 %3, %0, %1\n\tv_add_f32 %4, %5, %6\n\tv_fmac_f32 %3, 0.5, %2\n\tv_fmac_f32 %4, 0.5, %7\n\t" DPROW("v_add_f32_dpp")
                     "v_rcp_f32 %3, %3\n\tv_rcp_f32 %4, %4\n\t"
                     "v_mul_f32 %0, %0, %3\n\tv_mul_f32 %5, %5, %4\n\tv_mul_f32 %1, %1, %3\n\tv_cvt_pk_bf16_f32 %0, %0, %5\n\t"
                     "v_mul_f32 %6, %6, %4\n\tv_mul_f32 %2, %2, %3\n\tv_cvt_pk_bf16_f32 %1, %1, %6\n\tv_mul_f32 %7, %7, %4\n\t"
                     "ds_write2st64_b32 %9, %0, %1 offset1:1\n\tv_cvt_pk_bf16_f32 %2, %2, %7\n\tds_write_b32 %9, %2 offset:512"
                     : "+v"(e0), "+v"(e2), "+v"(e4), "=&v"(x0), "=&v"(x1), "+v"(e1), "+v"(e3), "+v"(e5) : "s"(CE), "v"(po) : "memory");
        SB0();
      } else {
        const int rb = X_O + (b3 == 0 ? 2 : b3 - 1) * 131072 + ob - 4096;
        const u32x4 e = ones4();
        __builtin_amdgcn_raw_buffer_store_b128(e, rsX, gvo, rb, 16);
        __builtin_amdgcn_raw_buffer_store_b128(e, rsX, gvo, rb + 65536, 16);
        if (s == 63 && gvo == 4096) st4_sc1(WSP(u32, 0) + SY_SEG(0, h), 1u);
        PSTAMP();
      }
      STAMP(5);
      BARC();
      STAMP(6);
      if constexpr (R == 1) {
        const u32 tp = (u32)wall_clock64(); dur += ((int)umin((tp - ts) << 6, 200u * 64u) - dur) >> DSH;
        SB0();
        IDX();
        const int pk_ = lq * 256 + ((l15 ^ lq) << 4), pv_ = lq * 256 + ((l15 ^ (lq << 1)) << 4);
#pragma unroll
        for (int i = 0; i < 8; i++) *(u32x4*)(smem + C_KX + (pk_ ^ ((i & 3) * 64)) + i * 1024) = nx[i];
#pragma unroll
        for (int i = 0; i < 8; i++) *(u32x4*)(smem + C_VX + (pv_ ^ ((i & 1) * 128)) + i * 1024) = nx[8 + i];
        SB0();
        STAMP(8);
        BARN();
        if (s == 63) break;
      } else {
        bf8 km[4];
#pragma unroll
        for (int ks = 0; ks < 4; ks++) km[ks] = cat8(trrd(smem + ko + ks * 1024), trrd(smem + ko1 + ks * 1024));
#pragma unroll
        for (int NT = 1; NT <= 2; NT++) {
          f4 sc[2], sm;
          bf8 qf[4];
#pragma unroll
          for (int ks = 0; ks < 4; ks++) qf[ks] = *(const bf8*)(smem + C_QX + (NT - 1) * 4096 + (fo ^ (ks * 64)));
#pragma unroll
          for (int ks = 0; ks < 4; ks++) {
#pragma unroll
            for (int j = 0; j < NT; j++) sc[j] = MFMA(pre[2 * j + (ks >> 1)][ks & 1], qf[ks], ks ? sc[j] : (j == NT - 1 ? ((f4){mk[0], mk[1], mk[2], mk[3]}) : ((f4){0, 0, 0, 0})));
            sm = MFMA(km[ks], qf[ks], ks ? sm : ((f4){0, 0, 0, 0}));
          }
          float mx = FMX3(FMX3(sc[0][0], sc[0][1], sc[0][2]), sc[0][3], FMX(sm[0], sm[1]));
          if (NT == 2) mx = FMX3(FMX3(mx, sc[1][0], sc[1][1]), sc[1][2], sc[1][3]);
          { auto t = __builtin_amdgcn_permlane16_swap(__float_as_uint(mx), __float_as_uint(mx), false, false);
            mx = FMX(__uint_as_float(t[0]), __uint_as_float(t[1])); }
          { auto t = __builtin_amdgcn_permlane32_swap(__float_as_uint(mx), __float_as_uint(mx), false, false);
            mx = FMX(__uint_as_float(t[0]), __uint_as_float(t[1])); }
          mx *= -CE;
#pragma unroll
          for (int j = 0; j < NT; j++)
#pragma unroll
            for (int r = 0; r < 4; r++) sc[j][r] = __builtin_amdgcn_exp2f(__builtin_fmaf(sc[j][r], CE, mx));
          sm[0] = __builtin_amdgcn_exp2f(__builtin_fmaf(sm[0], CE, mx)); sm[1] = __builtin_amdgcn_exp2f(__builtin_fmaf(sm[1], CE, mx));
          float sum = ((sc[0][0] + sc[0][1]) + (sc[0][2] + sc[0][3])) + (sm[0] + sm[1]);
          if (NT == 2) sum += (sc[1][0] + sc[1][1]) + (sc[1][2] + sc[1][3]);
          { auto t = __builtin_amdgcn_permlane16_swap(__float_as_uint(sum), __float_as_uint(sum), false, false);
            sum = __uint_as_float(t[0]) + __uint_as_float(t[1]); }
          sum = swadd(sum, sum);
          const float inv = __builtin_amdgcn_rcpf(sum);
          *(u32x4*)(smem + C_PX + (NT - 1) * 1024 + gvo) = (u32x4){pk2(sc[0][0] * inv, sc[0][1] * inv), pk2(sc[0][2] * inv, sc[0][3] * inv),
                                                                  NT == 2 ? pk2(sc[1][0] * inv, sc[1][1] * inv) : 0u, NT == 2 ? pk2(sc[1][2] * inv, sc[1][3] * inv) : 0u};
          *(u32*)(smem + C_PX + 2048 + (NT - 1) * 256 + (gvo >> 2)) = pk2(sm[0] * inv, sm[1] * inv);
        }
        STAMP(8);
        BAR();
        if (s == 63) break;
        if (known < (s < 61 ? s + 3 : 64)) {
          int n = 0;
          while (true) {
            u32 bf[3];
#pragma unroll
            for (int i = 0; i < 3; i++) bf[i] = ld4_sc1(WSP(u32, 0) + SY_BLK(i, cb, gvo >> 4));
            const u64 miss = __ballot(!(bf[0] >= 8u && bf[1] >= 8u && bf[2] >= 8u));
            known = miss ? __builtin_ctzll(miss) : 64;
            if (known >= (s < 61 ? s + 3 : 64) || ++n >= SPIN_CAP) break;
            __builtin_amdgcn_s_sleep(KSL);
          }
        }
      }
      STAMP(9);
      if constexpr (R == 1) AD_WAIT(K2, F2, 4096);
    } else {
      STAMP(1);
      BARC();
      STAMP(2);
#pragma unroll
      for (int i = 0; i < 9; i++) {
        const int j = i < 5 ? i + 4 : i - 5;
        constexpr int PO = NV == 2 ? 2 : 1;
        SB0();
        if constexpr (NV == 2) a0 = SMF(b[(i + 4) % 5], w[1][j], a0, sidx);
        if (i < 3) a1 = SMF(b[(i + 4) % 5], wx[i], a1, sidx);
        else a1 = SMF(b[(i + 4) % 5], cat16(pre[(i + PO) & 3][0], pre[(i + PO) & 3][1]), a1, sidx);
        SB0();
        if (i < 4) b[(i + 4) % 5] = LDM(i);
        if (i == 3 || i == 4) LDW(pre[(i + PO) & 3], 1 + i);
      }
      u32x4 vm[NV];
      vm[NV - 1] = (u32x4){pk2(a1[0] + a1[2], a1[1] + a1[3]), 0u, 0u, 0u};
      if constexpr (NV == 2) vm[0] = (u32x4){pk2(a0[0] + a0[2], a0[1] + a0[3]), 0u, 0u, 0u};
#pragma unroll
      for (int d = 0; d < NV; d++) asm volatile("" : "+v"(vm[d]));
      const bool hi = so & 128;
      SB0();
      STAMP(5);
      BARN();
      STAMP(6);
      {
        const bf8 px = cat8(trrd(smem + po), trrd(smem + po + 256));
        SB0();
        const bf8 pm = cat8(trrd(smem + po + 512), trrd(smem + po + 256));
        SB0();
        f4 o[NV];
#pragma unroll
        for (int d = 0; d < NV; d++) { o[d] = MFMA(vx[d], px, ((f4){0, 0, 0, 0})); asm volatile("" : "+v"(o[d])); SB0(); }
#pragma unroll
        for (int d = 0; d < NV; d++) { o[d] = MFMA(__builtin_bit_cast(bf8, vm[d]), pm, o[d]); asm volatile("" : "+v"(o[d])); SB0(); }
        u32x2 ov = {pk2(o[0][0], o[0][1]), pk2(o[0][2], o[0][3])};
        if constexpr (NV == 2) {
          const u32x2 ow = {pk2(o[1][0], o[1][1]), pk2(o[1][2], o[1][3])};
          if (hi) ov = ow;
        }
        ov[0] = umin(ov[0], 0xFFFFFFFEu);
        __builtin_amdgcn_raw_buffer_store_b64(ov, rsX, so, so_, 16);
        SB0();
      }
      STAMP(7);
      int xo, lo, pa = 0;
      {
        IDX();
        if constexpr (NV == 1) {
          __builtin_amdgcn_s_sleep(LSL);
#pragma unroll
          for (int i = 0; i < 2; i++) nx[i] = __builtin_amdgcn_raw_buffer_load_b128(rsB, (lq * 4096 + h * 128 + l15 * 8) * 2, nb + (vt * 8 + i * 4) * 8192, 16);
          pa = C_QX + vt * 2048 + lq * 256 + ((l15 ^ lq ^ ((vt & 1) * 8)) << 4);
          asm volatile("" : "+v"(pa));
        }
        xo = OB_OFF((lq & 1) * 16 + l15, (lq >> 1) * 8);
        lo = lane * 4;
        asm volatile("" : "+v"(xo), "+v"(lo));
#pragma unroll
        for (int j = 0; j < 4; j++) LDW(pre[j], NV == 2 ? 6 + j : j);
      }
      SB0();
      BARN();
      STAMP(8);
      {
        f4 o[2][NV];
#pragma unroll
        for (int it = 0; it < 2; it++) {
          const bf8 pf = *(const bf8*)(smem + C_PX + it * 1024 + lo * 4);
          const u32x4 pm = {*(const u32*)(smem + C_PX + 2048 + it * 256 + lo), 0u, 0u, 0u};
#pragma unroll
          for (int d = 0; d < NV; d++) {
            o[it][d] = MFMA(vx[d], pf, ((f4){0, 0, 0, 0}));
            o[it][d] = MFMA(__builtin_bit_cast(bf8, vm[d]), __builtin_bit_cast(bf8, pm), o[it][d]);
          }
        }
#pragma unroll
        for (int d = 0; d < NV; d++)
          __builtin_amdgcn_raw_buffer_store_b128(pair16(pk2(o[0][d][0], o[0][d][1]), pk2(o[0][d][2], o[0][d][3]), pk2(o[1][d][0], o[1][d][1]), pk2(o[1][d][2], o[1][d][3])), rsB,
              xo, 3 * ACB + OB_OFF(s * 32, h * 128 + (vt + d) * 16), 16);
        SB0();
        if (s == 63) break;
        if constexpr (NV == 1) { *(u32x4*)(smem + pa) = nx[0]; *(u32x4*)(smem + (pa ^ 64) + 1024) = nx[1]; }
      }
      STAMP(9);
    }
    b3 = b3 == 2 ? 0 : b3 + 1;
    STAMP(14);
  }
  };
  if (wid == 4) run(std::integral_constant<int, 1>{});
  else if (wid == 0) run(std::integral_constant<int, 0>{});
  else if (wid < 4) run(std::integral_constant<int, 2>{});
  else if (wid == 7) run(std::integral_constant<int, 3>{});
  else run(std::integral_constant<int, 4>{});
  VMW0();
  __syncthreads();
  if (TIDX == 0) st4_sc1(WSP(u32, 0) + SY_SEG(1, h), 1u);
}

__device__ __forceinline__ void chain_out(const Params& p, char* smem, const int c, const int wv) {
  STS(32 + c, 1);
  constexpr int R = 5; (void)R;
  const int wid = wv, tq = c & 1, cg = c >> 1;
  u32x8 w[4][5];
  const __amdgpu_buffer_rsrc_t rsX = RSRC(XWIN(p), X_END);
  int xp, mo, wo, ro;
  {
    IDX();
    mo = wid < 4 ? cg * 512 + tq * 256 + ((wid ^ (tq * 2)) << 6) : 16384 + (cg >> 2) * 512 + tq * 256 + (((cg & 3) ^ (tq * 2)) << 6);
    if (wid > 4 || (wid == 4 && cg >= 8)) mo = PJ_OOB;
    mo = __builtin_amdgcn_readfirstlane(mo);
    __builtin_amdgcn_raw_buffer_store_b128(ones4(), rsX, lane < 24 ? X_M + (lane >> 3) * 32768 + ((lane >> 2) & 1) * 8192 + (lane & 3) * 16 : PJ_OOB, mo, 16);
    const __amdgpu_buffer_rsrc_t rw = RSRC(p.BWo, 576 * 4096 * 2);
    u32x4 st[4][10];
    const int va = ((lane >> 2) * 576 + cg * 32 + p4 * 8) * 2, vb = cg < 8 ? (lane * 576 + 512 + cg * 8) * 2 : PJ_OOB;
#pragma unroll
    for (int i = 0; i < 4; i++) {
#pragma unroll
      for (int k = 0; k < 8; k++) st[i][k] = __builtin_amdgcn_raw_buffer_load_b128(rw, va, ((i * 8 + wid) * 128 + k * 16) * 1152, OSTG_AUX);
#pragma unroll
      for (int k = 0; k < 2; k++) st[i][8 + k] = __builtin_amdgcn_raw_buffer_load_b128(rw, vb, ((i * 8 + wid) * 128 + k * 64) * 1152, OSTG_AUX);
      SB0();
    }
    char* sc = smem + wid * 10240;
#pragma unroll
    for (int i = 0; i < 4; i++) {
#pragma unroll
      for (int k = 0; k < 8; k++) *(u32x4*)(sc + (k * 16 + (lane >> 2)) * 80 + p4 * 16) = st[i][k];
#pragma unroll
      for (int k = 0; k < 2; k++) *(u32x4*)(sc + (k * 64 + lane) * 80 + 64) = st[i][8 + k];
      if (i == 0) { BAR(); if (tid == 0) st4_sc1(WSP(u32, 0) + SY_INIT(32 + c), 1u); }
      __builtin_amdgcn_fence(__ATOMIC_RELEASE, "wavefront");
      __builtin_amdgcn_wave_barrier();
#pragma unroll
      for (int t = 0; t < 5; t++) {
        bf4 tt[4];
#pragma unroll
        for (int kk = 0; kk < 4; kk++) {
          const int r0 = ((kk >> 1) * 2 + (lq >> 1)) * 32 + (p4 >> 1) * 16 + (q4 >> 1) * 8 + (lq & 1) * 4 + (kk & 1) * 2 + (q4 & 1);
          tt[kk] = trrd(sc + r0 * 80 + t * 16 + (p4 & 1) * 8);
        }
        w[i][t] = ilv(tt[0], tt[1], tt[2], tt[3]);
      }
      asm volatile("s_waitcnt lgkmcnt(0)" ::: "memory");
      __builtin_amdgcn_wave_barrier();
      SB0();
    }
    u32 fl, fv;
    int it = 0;
    do { fl = ld4_sc1(WSP(u32, 0) + SY_INIT(lane)); fv = ld4_sc1(WSP(u32, 0) + SY_VOTE(lane));
         if (__all(fl != 0u && fv != 0u)) break;
         __builtin_amdgcn_s_sleep(2); } while (++it < SPIN_CAP);
    xp = __builtin_amdgcn_readfirstlane(__popcll(__ballot(fv == 2u)) > 32 ? 8192 : 0);
    wo = (lq >> 1) == hi8 ? ((l15 & 7) * 2 + (lq & 1)) * 144 + (wid * 2 + hi8) * 8 : 16 * 144 + lane * 8;
    ro = wid * 2880 + (lane >> 2) * 144 + p4 * 32;
  }
  __syncthreads();
  STS(32 + c, 14);
  int go, sidx, po;
  { IDX(); go = lane * 16; sidx = (l15 & 2) ? 0xEEEE : 0x4444;
    po = p4 == 0 ? (lane >> 4) * 16 + ((lane >> 2) & 1) * 8 + ((lane >> 3) & 1) * 4 : PJ_OOB; }
#define ONOP 7
  int b3 = 0;
#pragma unroll 1
  for (int s = 0; s < 63; s++) {
    f4 d[5];
    int gb[4], pb = mo + X_M + (b3 == 2 ? 0 : b3 + 1) * 32768 + xp;
#pragma unroll
    for (int i = 0; i < 4; i++) { gb[i] = X_O + b3 * 131072 + tq * 65536 + wid * 1024 + xp + i * 16384; asm volatile("" : "+s"(gb[i])); }
    asm volatile("" : "+s"(pb));
    u32x4 g[4];
    u32 bad;
    auto mult = [&]() __attribute__((always_inline)) {
      CFENCE();
#pragma unroll
      for (int i = 0; i < 4; i++) {
        g[i] = __builtin_amdgcn_raw_buffer_load_b128(rsX, go, gb[i], 16);
        SB0();
        if (ONOP >= 0 && i < 3) asm volatile("s_nop %0" :: "i"(ONOP >= 0 ? ONOP : 0));
        SB0();
      }
      bad = 0u;
#pragma unroll
      for (int i = 0; i < 4; i++) {
        SB0();
#pragma unroll
        for (int t = 0; t < 5; t++) {
          d[t] = SMF(g[i], w[i][t], d[t], sidx);
          if (t == 0) bad = umax(umax(bad, g[i][0]), g[i][2]);
          if (i == 3 && t >= 2) { SB0(); *(f2v*)(smem + wo + (t - 2) * 2880) = (f2v){d[t - 2][0] + d[t - 2][2], d[t - 2][1] + d[t - 2][3]}; SB0(); }
        }
      }
      SB0();
#pragma unroll
      for (int t = 3; t < 5; t++) *(f2v*)(smem + wo + t * 2880) = (f2v){d[t][0] + d[t][2], d[t][1] + d[t][3]};
      SB0();
    };
#pragma unroll
    for (int t = 0; t < 5; t++) d[t] = zero4();
    SB0();
    if (wid == 7) AD_WAIT(K1, F1, SPIN_CAP);
    STAMP(15);
    BARN();
    STAMP(0);
    mult();
    if (__builtin_expect(__any(bad == ~0u), 0)) {
      int it = 0;
      do {
#pragma unroll
        for (int t = 0; t < 5; t++) d[t] = zero4();
        mult();
      } while (__any(bad == ~0u) && ++it < SPIN_CAP);
    }
    STAMP(1);
    BARC();
    STAMP(2);
    {
      const f4 t0 = *(const f4*)(smem + ro), t1 = *(const f4*)(smem + ro + 16);
      float a = (t0[0] + t0[2]) + (t1[0] + t1[2]), b = (t0[1] + t0[3]) + (t1[1] + t1[3]);
      a += __uint_as_float(__builtin_amdgcn_mov_dpp(__float_as_uint(a), 0xB1, 0xf, 0xf, true));
      b += __uint_as_float(__builtin_amdgcn_mov_dpp(__float_as_uint(b), 0xB1, 0xf, 0xf, true));
      a += __uint_as_float(__builtin_amdgcn_mov_dpp(__float_as_uint(a), 0x4E, 0xf, 0xf, true));
      b += __uint_as_float(__builtin_amdgcn_mov_dpp(__float_as_uint(b), 0x4E, 0xf, 0xf, true));
      __builtin_amdgcn_raw_buffer_store_b32(umin(pk2(a, b), 0xFFFFFFFEu), rsX, po, pb, 16);
      SB0();
      STAMP(3);
      __builtin_amdgcn_s_sleep(4);
      __builtin_amdgcn_raw_buffer_store_b32(~0u, rsX, po, mo + X_M + b3 * 32768 + xp, 16);
    }
    if (wid == 5 && c == 0 && s > 0 && go == 0) st4_sc1(WSP(u32, 0) + SY_CS(s - 1), 1u);
    VMW0C();
    b3 = b3 == 2 ? 0 : b3 + 1;
  }
  __syncthreads();
}

__device__ __forceinline__ void wait_ge(const u32* w, u32 val, const int wv) {
  if (TIDX == 0) {
    int it = 0;
    while (ld4_sc1(w) < val && ++it < SPIN_CAP) __builtin_amdgcn_s_sleep(WPS);
  }
}
__device__ __forceinline__ void acquire_all(const int wv) {
  if (TIDX == 0) {
    __builtin_amdgcn_fence(__ATOMIC_ACQUIRE, "agent");
    VMW0();
  }
  __syncthreads();
}

#define KROT 0
#define OPD 4
#define PPD 2
#define T_KV 0
#define T_Q 0
#define T_A 192
#define T_W (T_A + 64)
#define T_S (T_W + 256)
#define T_L (T_S + 144)
#define T_O T_L
#define T_CHAIN (1 << 20)
#define AORD 0x01234567u
#define OORD 0x67014523u

__device__ __forceinline__ int next_task(const Params& p, int* s_task, int xq, int& xk, const int wv, const int pt) {
  __syncthreads();
  if (TIDX == 0) {
    int t = -1;
    while (xk < 6) {
      int x = xq + xk; if (x >= 6) x -= 6;
      const int i = (int)__hip_atomic_fetch_add(WSP(u32, 0) + SY_XQ(x), 1u, __ATOMIC_RELAXED, __HIP_MEMORY_SCOPE_AGENT);
      if (i < 32) { t = x * 32 + i; break; }
      xk++;
    }
    if (t < 0 && xk == 6) {
      xk = 7;
      int it = 0;
      while (ld4_sc1(WSP(u32, 0) + SY_NCL) < 64u && ld4_sc1(WSP(u32, 0) + SY_ARR) < 256u && ++it < SPIN_CAP) __builtin_amdgcn_s_sleep(8);
      if (ld4_sc1(WSP(u32, 0) + SY_NCL) < 64u) {
        for (int k = 0; k < 64; k++)
          if (__hip_atomic_exchange(WSP(u32, 0) + SY_CLAIM(k), 1u, __ATOMIC_RELAXED, __HIP_MEMORY_SCOPE_AGENT) == 0u) {
            __hip_atomic_fetch_add(WSP(u32, 0) + SY_NCL, 1u, __ATOMIC_RELAXED, __HIP_MEMORY_SCOPE_AGENT);
            t = T_CHAIN + k; break;
          }
      }
    }
    if (t < 0) t = T_A + (int)__hip_atomic_fetch_add(WSP(u32, 0) + SY_QHEAD, 1u, __ATOMIC_RELAXED, __HIP_MEMORY_SCOPE_AGENT);
    *s_task = t;
  }
  __syncthreads();
  return __builtin_amdgcn_readfirstlane(*s_task);
}

__device__ __forceinline__ int take_task(int* s_task, const int pre, const int wv, const int pb, const int pl, const int pt) {
  if (TIDX == 0) {
    *s_task = pre;
  }
  __syncthreads();
  const int t = __builtin_amdgcn_readfirstlane(*s_task);
  __syncthreads();
  return t;
}
#if defined(__HIP_DEVICE_COMPILE__) && !defined(KEEP_PK)
#define NOPK __attribute__((target("no-packed-fp32-ops")))
#else
#define NOPK
#endif
#define P0V 2
__device__ __forceinline__ void mega_body(const Params& p, char* smem) {
  int* const s_task = (int*)(smem + LDS_TOTAL);
  const int wv = __builtin_amdgcn_readfirstlane(threadIdx.x >> 6);
  if (blockIdx.x == 0 && TIDX == 0) asm volatile("s_nop 0" ::: "memory");
#define tid TIDX
  const int xcc = (int)(__builtin_amdgcn_s_getreg(20 | (3 << 11)) & 7u);
  const int xq = xcc >= 2 ? xcc - 2 : xcc;
  int xk = 0, pre = 0;
  u32* const qh = WSP(u32, 0) + SY_QHEAD;
  if (tid == 0) {
    int t0 = -1;
    if (xcc < 2) {
      const int k = ((blockIdx.x >> 3) & 31) + xcc * 32;
      if (__hip_atomic_exchange(WSP(u32, 0) + SY_CLAIM(k), 1u, __ATOMIC_RELAXED, __HIP_MEMORY_SCOPE_AGENT) == 0u) {
        __hip_atomic_fetch_add(WSP(u32, 0) + SY_NCL, 1u, __ATOMIC_RELAXED, __HIP_MEMORY_SCOPE_AGENT);
        t0 = T_CHAIN + k;
      }
    }
    __hip_atomic_fetch_add(WSP(u32, 0) + SY_ARR, 1u, __ATOMIC_RELAXED, __HIP_MEMORY_SCOPE_AGENT);
    *s_task = t0;
  }
  int t = -1;
  if (xcc < 2) { __syncthreads(); t = __builtin_amdgcn_readfirstlane(*s_task); }
  if (t < 0) t = next_task(p, s_task, xq, xk, wv, t);
  {
    while (t < T_A) {
      const int x = t >> 5, j = t & 31, c = (x - 4) * 32 + j;
      if (x >= 4 && tid < 64) {
        const __amdgpu_buffer_rsrc_t rsX = RSRC(XWIN(p), X_END);
        u32 best[2] = {~0u, ~0u};
#pragma unroll 1
        for (int r = 0; r < 4; r++) {
#pragma unroll
          for (int k = 0; k < 2; k++) {
            const u32 t0 = (u32)__builtin_readcyclecounter();
            __builtin_amdgcn_raw_buffer_store_b32((u32)r, rsX, X_CAL + k * 8192 + c * 128, 0, 16);
            VMW0();
            best[k] = umin(best[k], (u32)__builtin_readcyclecounter() - t0);
          }
        }
        if (tid == 0) st4_sc1(WSP(u32, 0) + SY_VOTE(c), 1u + ((best[1] < best[0] ? 1u : 0u) ^ (u32)(xcc >> 2)));
      }
#pragma unroll 1
      for (int i = 0; i < 2; i++) {
        const int r0 = i * 128, mat = t >> 6, cb = (t >> 2) & 15, co = cb * 256 + (t & 3) * 64;
        gemm_tile<128, 64, 4, 2, true, 4>(p.Bx + r0 * 576, 576, (mat == 0 ? p.BWk : mat == 1 ? p.BWv : p.BWq) + co, 4096,
                                          (mat == 0 ? WSP(u16, O_KB) : mat == 1 ? WSP(u16, O_VB) : WSP(u16, O_QB)) + r0 * 4096 + co, 4096, 576, smem, wv, qh, pre);
        if (tid < 4) __hip_atomic_fetch_add(WSP(u32, 0) + SY_BLK(mat, cb, (r0 >> 5) + tid), 2u, __ATOMIC_RELAXED, __HIP_MEMORY_SCOPE_AGENT);
      }
      proj_slot(p, x, j, smem, wv);
      t = next_task(p, s_task, xq, xk, wv, t);
    }
  }
  if (t >= T_CHAIN) { if (t < T_CHAIN + 32) chain_head(p, smem, t - T_CHAIN, wv); else chain_out(p, smem, t - T_CHAIN - 32, wv); xk = 7; t = next_task(p, s_task, xq, xk, wv, -1); }
  while (t < T_W) {
    wof_task(p, t - T_A, smem, wv, qh, pre);
    if (tid == 0) __hip_atomic_fetch_add(WSP(u32, 0) + SY_CW, 1u, __ATOMIC_RELAXED, __HIP_MEMORY_SCOPE_AGENT);
    t = T_A + take_task(s_task, pre, wv, T_A, T_S - T_A, t);
  }
  while (t < T_S) {
    const int qb = (AORD >> (((t - T_W) >> 5) * 4)) & 7, h = (t - T_W) & 31;
    if (tid < 64) {
      int it = 0;
      while (true) {
        const u32 f0 = ld4_sc1(WSP(u32, 0) + SY_BLK(3, h >> 1, tid)), f1 = ld4_sc1(WSP(u32, 0) + SY_BLK(4, h >> 1, tid)), f2 = ld4_sc1(WSP(u32, 0) + SY_BLK(5, h >> 1, tid));
        if (__all(tid >= qb * 8 + 8 || ((tid < qb * 8 || f0 >= 8u) && f1 >= 8u && f2 >= 8u)) || ++it >= SPIN_CAP) break;
        __builtin_amdgcn_s_sleep(8);
      }
    }
    acquire_all(wv);
    fa::attn_block(WSP(u16, O_QA) + (size_t)qb * 256 * 4096 + h * 128, WSP(u16, O_KA) + h * 128, WSP(u16, O_VA) + h * 128,
                   WSP(char, O_CA) + OB_OFF(qb * 256, h * 128), qb * 256, smem, wv, qh, pre);
    if (tid == 0) __hip_atomic_fetch_add(WSP(u32, 0) + SY_CC(qb), 1u, __ATOMIC_RELAXED, __HIP_MEMORY_SCOPE_AGENT);
    t = T_A + take_task(s_task, pre, wv, T_A, T_S - T_A, t);
  }
  int rest = 64 | (1 << xq);
#pragma unroll 1
  while (rest) {
    const int x = __builtin_ctz(rest);
    rest &= rest - 1;
    if (x == 6) {
      CLAIMS();
      t = take_task(s_task, pre, wv, T_S, 144, -1);
      while ((t & 0xFFFF) < 144) {
        t &= 0xFFFF;
        small_out_task(p, XS0 + t / 72, t & 1, (t % 72) >> 1, smem, wv, pre);
        t = take_task(s_task, pre, wv, T_S, 144, T_S + t);
      }
      rest = t >> 16;
      continue;
    }
    const int part = x & 1, cg = x >> 1, n = part ? XS0 / 2 : 32;
    u32* const qh = WSP(u32, 0) + SY_OQ(x);
    CLAIM();
    t = take_task(s_task, pre, wv, T_O + x * 32, n, -1);
    while (t < n) {
      const int rb = part ? t : ((OORD >> ((t >> 2) * 4)) & 7) * 4 + (t & 3);
      if (tid == 0) {
        const u32* w = WSP(u32, 0) + (part ? SY_CS(rb * 2 + 1) : SY_CC(rb >> 2));
        int it = 0;
        while ((ld4_sc1(w) < (part ? 1u : 32u) || ld4_sc1(WSP(u32, 0) + SY_CW) < 64u) && ++it < SPIN_CAP) __builtin_amdgcn_s_sleep(WPS);
      }
      acquire_all(wv);
      out_tile(WSP(char, part ? O_OB : O_CA) + rb * 2 * 262144, WSP(char, O_WOF) + part * WOB + cg * 12 * 131072,
               p.out + (size_t)rb * 64 * 1152 + part * 576 + cg * 192, smem, wv, qh, pre);
      t = take_task(s_task, pre, wv, T_O + x * 32, n, T_O + x * 32 + t);
    }
  }
}

#undef tid
__global__ NOPK void __launch_bounds__(512, 2) mega(Params p) {
  __shared__ __attribute__((aligned(16))) char smem[LDS_ALL];
  mega_body(p, smem);
}
extern "C" void kernel_launch(void* const* d_in, const int* in_sizes, int n_in, void* d_out, int out_size, void* d_ws,
                              size_t ws_size, hipStream_t stream) {
  Params p;
  p.Ax = (const u16*)d_in[0]; p.Ac = (const u16*)d_in[1]; p.Awq = (const u16*)d_in[2]; p.Awk = (const u16*)d_in[3];
  p.Awv = (const u16*)d_in[4]; p.Awo = (const u16*)d_in[5]; p.Bx = (const u16*)d_in[6]; p.Bm = (const u16*)d_in[7];
  p.BWq = (const u16*)d_in[8]; p.BWk = (const u16*)d_in[9]; p.BWv = (const u16*)d_in[10]; p.BWo = (const u16*)d_in[11];
  p.out = (u16*)d_out;
  p.ws = (char*)d_ws;
  const size_t syncBytes = SY_WORDS * 4;
  (void)hipMemsetAsync(d_ws, 0, syncBytes, stream);
  mega<<<256, 512, 0, stream>>>(p);
}
```

```cpp
#include <hip/hip_runtime.h>
#include <hip/hip_bf16.h>
#include <stdint.h>
#include <type_traits>

typedef unsigned short u16;
typedef unsigned int u32;
typedef unsigned long long u64;
typedef __attribute__((ext_vector_type(8))) short bf8;
typedef __attribute__((ext_vector_type(4))) short bf4;
typedef __attribute__((ext_vector_type(4))) float f4;
typedef __attribute__((ext_vector_type(2))) float f2v;
typedef __attribute__((ext_vector_type(2))) __bf16 bf2v;
typedef __attribute__((ext_vector_type(4))) unsigned u32x4;
typedef __attribute__((ext_vector_type(2))) unsigned u32x2;
typedef float f32x16 __attribute__((ext_vector_type(16)));
typedef __attribute__((ext_vector_type(8))) unsigned u32x8;
typedef __attribute__((ext_vector_type(8))) __bf16 bh8;
typedef __attribute__((ext_vector_type(16))) __bf16 bh16;
#define LDSP __attribute__((address_space(3)))

__device__ __forceinline__ u32 pk2(float a, float b) {
  f2v x = {a, b};
  bf2v y = __builtin_convertvector(x, bf2v);
  return __builtin_bit_cast(u32, y);
}
__device__ __forceinline__ u32 umin(u32 a, u32 b) { return a < b ? a : b; }
__device__ __forceinline__ u32 umax(u32 a, u32 b) { return a > b ? a : b; }
__device__ __forceinline__ float rbf(float f) { return __uint_as_float(pk2(f, f) << 16); }
#define MFMA(a, b, c) __builtin_amdgcn_mfma_f32_16x16x32_bf16(a, b, c, 0, 0, 0)
#define MFMA16(a, b, c) __builtin_amdgcn_mfma_f32_16x16x16bf16_1k(a, b, c, 0, 0, 0)
#define SMF(a, b, c, idx) __builtin_amdgcn_smfmac_f32_16x16x64_bf16(__builtin_bit_cast(bh8, a), __builtin_bit_cast(bh16, b), c, idx, 0, 0)
__device__ __forceinline__ u32x8 ilv(bf4 a, bf4 b, bf4 c, bf4 d) {
  const u32x2 x = __builtin_bit_cast(u32x2, a), y = __builtin_bit_cast(u32x2, b), z = __builtin_bit_cast(u32x2, c), w = __builtin_bit_cast(u32x2, d);
  return (u32x8){x[0], x[1], y[0], y[1], z[0], z[1], w[0], w[1]};
}
__device__ __forceinline__ u32x8 cat16(bf8 a, bf8 b) {
  const u32x4 x = __builtin_bit_cast(u32x4, a), y = __builtin_bit_cast(u32x4, b);
  return (u32x8){x[0], x[1], x[2], x[3], y[0], y[1], y[2], y[3]};
}
__device__ __forceinline__ float swadd(float a, float b) {
  auto x = __builtin_amdgcn_permlane32_swap(__float_as_uint(a), __float_as_uint(b), false, false);
  return __uint_as_float(x[0]) + __uint_as_float(x[1]);
}
__device__ __forceinline__ bf4 trrd(const char* p) {
  return __builtin_amdgcn_ds_read_tr16_b64_v4i16((LDSP bf4*)(p));
}
__device__ __forceinline__ bf8 cat8(bf4 a, bf4 b) { return __builtin_shufflevector(a, b, 0, 1, 2, 3, 4, 5, 6, 7); }
__device__ __forceinline__ void st8_sc1(void* p, u64 v) { __hip_atomic_store((u64*)p, v, __ATOMIC_RELAXED, __HIP_MEMORY_SCOPE_AGENT); }
__device__ __forceinline__ u32 ld4_sc1(const u32* p) { return __hip_atomic_load(p, __ATOMIC_RELAXED, __HIP_MEMORY_SCOPE_AGENT); }
__device__ __forceinline__ void st4_sc1(u32* p, u32 v) { __hip_atomic_store(p, v, __ATOMIC_RELAXED, __HIP_MEMORY_SCOPE_AGENT); }
#define VMW0() asm volatile("s_waitcnt vmcnt(0)" ::: "memory")
#define CFENCE() asm volatile("" ::: "memory")
#define VMW0C() do { __builtin_amdgcn_s_waitcnt(0x0F70); asm volatile("" ::: "memory"); } while (0)
#define SPIN_CAP (1 << 22)
#define XPS 4
#define SPS 4
#define WPS 8
__device__ __forceinline__ int tid_now(int wv) {
  int l; asm volatile("v_mbcnt_lo_u32_b32 %0, -1, 0\n\tv_mbcnt_hi_u32_b32 %0, -1, %0" : "=v"(l));
  return wv * 64 + l;
}
#define TIDX tid_now(wv)
#define CLAIM() do { if (TIDX == 0) pre = (int)__hip_atomic_fetch_add(qh, 1u, __ATOMIC_RELAXED, __HIP_MEMORY_SCOPE_AGENT); } while (0)

#define TLS(i)
struct Params {
  const u16 *Ax, *Ac, *Awq, *Awk, *Awv, *Awo, *Bx, *Bm, *BWq, *BWk, *BWv, *BWo;
  u16* out;
  char* ws;
};
#define SY_QHEAD 0
#define SY_CC(rb) (64 + (rb) * 16)
#define SY_CW (64 + 8 * 16)
#define SY_OQ(x) (64 + 9 * 16 + (x) * 16)
#define SY_CS(s) (64 + 16 * 16 + (s) * 16)
#define SY_B0 (64 + 80 * 16)
#define SY_SEG(i, h) (SY_B0 + (i) * 32 + (h))
#define SY_INIT(c) (SY_B0 + 64 + (c))
#define SY_VOTE(i) (SY_B0 + 128 + (i))
#define SY_CLAIM(c) (SY_B0 + 192 + (c))
#define SY_XQ(x) (SY_B0 + 256 + (x) * 16)
#define SY_ARR (SY_B0 + 384)
#define SY_NCL (SY_ARR + 16)
#define SY_WX (SY_ARR + 32)
#define BLKS 1
#define SY_BLK(mi, cb, g) (SY_NCL + 48 + (((mi) * 16 + (cb)) * 64 + (g)) * BLKS)
#define SY_WORDS (SY_NCL + 48 + 6 * 16 * 64 * BLKS)

#define AL256(x) (((x) + 255) & ~255)
#define O_SYNC_END (SY_WORDS * 4)
#define ACB (2048 * 4096 * 2)
#define O_QA AL256(O_SYNC_END)
#define O_KA (O_QA + ACB)
#define O_VA (O_KA + ACB)
#define O_CA (O_VA + ACB)
#define O_QB (O_CA + ACB)
#define O_KB (O_QB + ACB)
#define O_VB (O_KB + ACB)
#define O_OB (O_VB + ACB)
#define O_WOF (O_OB + ACB)
#define WOB (576 * 4096 * 2)
#define O_WX (O_WOF + 2 * WOB)
#define O_XW (O_WX + 524288)
#define X_O 16384
#define X_M (X_O + 3 * 131072)
#define X_ANN (X_M + 3 * 32768)
#define X_CAL (X_ANN + 16384)
#define X_END (X_CAL + 16384)
#define OB_OFF(r, c) (((r) >> 5) * 262144 + ((c) >> 4) * 1024 + ((r) & 31) * 32 + ((c) & 15) * 2)
#define FR_OFF(r, c) (((r) >> 5) * 262144 + ((c) >> 3) * 512 + ((r) & 31) * 16 + ((c) & 7) * 2)
#define WSP(T, off) ((T*)(p.ws + (off)))
#define XWIN(p) (((uintptr_t)(p).ws + O_XW + 0x1FFFFFu) & ~(uintptr_t)0x1FFFFFu)

#define XS0 52
#define LDS_TOTAL (131072 + 8192 + 16384)
#define LDS_ALL 163840

#define SB0() __builtin_amdgcn_sched_barrier(0)
#define PJ_OOB 0x7F000000
#define RSRC(ptr, bytes) __builtin_amdgcn_make_buffer_rsrc((void*)(ptr), 0, (bytes), 0x00020000)
__device__ __forceinline__ bf8 ldsc1(__amdgpu_buffer_rsrc_t r, int off) {
  return __builtin_bit_cast(bf8, __builtin_amdgcn_raw_buffer_load_b128(r, off, 0, 16));
}
__device__ __forceinline__ u32x4 pair16(u32 a0, u32 a1, u32 b0, u32 b1) {
  auto r0 = __builtin_amdgcn_permlane16_swap(a0, b0, false, false);
  auto r1 = __builtin_amdgcn_permlane16_swap(a1, b1, false, false);
  return (u32x4){r0[0], r1[0], r0[1], r1[1]};
}
__device__ __forceinline__ int fsw(int k) { return (k & 3) | (((k >> 3) & 1) << 2); }

#define C_PARK 131072
#define WARMU 1
__device__ __forceinline__ void proj_slot(const Params& p, int x, int j, char* smem, const int wv) {
  const int tid = TIDX, lane = tid & 63, wid = wv;
  const int l15 = lane & 15, lq = lane >> 4, q4 = l15 >> 2, p4 = lane & 3, wr = wid >> 2, wc = wid & 3;
  const __amdgpu_buffer_rsrc_t rc = RSRC(p.ws, O_XW);
  const int vB = ((wid * 4 + lq) * 4096 + (((l15 >> 1) ^ (lq | (((wid >> 1) & 1) << 2))) * 16 + (lane & 1) * 8)) * 2, vB1 = vB + 32 * 8192;
  int ao = (wr * 64 + l15) * 128 + ((lq ^ (l15 & 7)) << 4);
  int bo = (int)(uintptr_t)smem + 65536 + (lq * 8 + q4) * 256 + (((wc * 2) ^ (q4 | ((lq & 1) << 2))) << 5) + p4 * 8;
  const int vD = lane * 16;
  const int cb = (x & 1) * 8 + (j >> 2);
  __amdgpu_buffer_rsrc_t rsA, rsB;
  const u16 *pA0 = p.Bx, *pB0 = (x < 2 ? p.BWk : x < 4 ? p.BWv : p.BWq) + cb * 256;
  const u16 *pA1 = x < 2 ? p.Ax : p.Ac, *pB1 = (x < 2 ? p.Awq : x < 4 ? p.Awk : p.Awv) + cb * 256;
  asm volatile("" : "+s"(pA0), "+s"(pB0), "+s"(pA1), "+s"(pB1));
  int k2, vA, vA1, sa, sb, nvA;
#define PJ_K2(u) ((u) < 2 || x < 2 ? 1152 : 1024)
#define PJ_PREP(u) do { int ln_ = vD; asm volatile("" : "+v"(ln_)); \
    nvA = (wid * 8 + (ln_ >> 7)) * PJ_K2(u) + ((ln_ ^ (ln_ >> 3)) & 0x70); \
    asm volatile("" : "+v"(nvA)); } while (0)
#define PJ_UNIT(u) do { k2 = PJ_K2(u); rsA = RSRC((u) < 2 ? pA0 : pA1, 2048 * k2); rsB = RSRC((u) < 2 ? pB0 : pB1, k2 * 4096 - cb * 512); vA = nvA; vA1 = nvA + 64 * k2; \
    sa = ((j & 3) + ((u) & 1) * 4) * 256 * k2 - 128; sb = 0; } while (0)
#define PJ_DEST(u, rt) do { const int mi_ = ((u) >> 1) * 3 + (x >> 1), g_ = ((rt) + ((u) & 1) * 4) * 8; \
    dco = O_QA + (mi_ == 2 ? 4 : (mi_ + 5) & 7) * ACB + cb * 16384 + wc * 2048 + (g_ + wr * 2) * 262144; \
    dfl = SY_BLK(mi_, cb, g_) * 4; } while (0)
#define PJ_STAGE(dst, rs, v0, v1, so) do { const int so_ = (so); \
    __builtin_amdgcn_raw_ptr_buffer_load_lds(rs, (LDSP void*)(smem + (dst) + wid * 1024), 16, v0, so_, 0, 0); \
    __builtin_amdgcn_raw_ptr_buffer_load_lds(rs, (LDSP void*)(smem + (dst) + wid * 1024 + 8192), 16, v1, so_, 0, 0); } while (0)
#define PJ_SA(dst, so) PJ_STAGE(dst, rsA, vA, vA1, so)
#define PJ_SB(dst, so) PJ_STAGE(dst, rsB, vB, vB1, so)
#define PJ_LDA(off) do { _Pragma("unroll") for (int m = 0; m < 4; m++) _Pragma("unroll") for (int k = 0; k < 2; k++) \
    At[m][k] = *(const bf8*)(smem + (ao ^ (k * 64)) + (off) + m * 2048); } while (0)
#define PJ_TR(dst, a, off) asm volatile("ds_read_b64_tr_b16 %0, %1 offset:%2" : "=&v"(dst) : "v"(a), "i"(off) : "memory")
#define PJ_LDB(D, h) do { const int b1_ = bo ^ 32; _Pragma("unroll") for (int n = 0; n < 2; n++) _Pragma("unroll") for (int k = 0; k < 2; k++) { \
    bf4 lo_, hi_; PJ_TR(lo_, n ? b1_ : bo, (h) * 16384 + k * 8192); PJ_TR(hi_, n ? b1_ : bo, (h) * 16384 + k * 8192 + 1024); D[n][k] = cat8(lo_, hi_); } } while (0)
#define PJ_CV(ai, bj, q) ((u32x4){pk2(acc[ai][bj][(q) & 2][(q) & 1][0], acc[ai][bj][(q) & 2][(q) & 1][1]), pk2(acc[ai][bj][(q) & 2][(q) & 1][2], acc[ai][bj][(q) & 2][(q) & 1][3]), \
                                  pk2(acc[ai][bj][((q) & 2) + 1][(q) & 1][0], acc[ai][bj][((q) & 2) + 1][(q) & 1][1]), pk2(acc[ai][bj][((q) & 2) + 1][(q) & 1][2], acc[ai][bj][((q) & 2) + 1][(q) & 1][3])})
#define PJ_SW(v) pair16(v[0], v[1], v[2], v[3])
#define PJ_MMA(Z, ai, bj, D, F) do { PJ_SCHED; __builtin_amdgcn_s_setprio(1); \
    _Pragma("unroll") for (int k = 0; k < 2; k++) _Pragma("unroll") for (int m = 0; m < 4; m++) _Pragma("unroll") for (int n = 0; n < 2; n++) { \
      if (k == 0 && (Z)) asm volatile("v_mfma_f32_16x16x32_bf16 %0, %1, %2, 0" : "+v"(acc[ai][bj][m][n]) : "v"(D[n][k]), "v"(At[m][k])); \
      else asm volatile("v_mfma_f32_16x16x32_bf16 %0, %1, %2, %0" : "+v"(acc[ai][bj][m][n]) : "v"(D[n][k]), "v"(At[m][k])); \
      F(k * 8 + m * 2 + n); } \
    __builtin_amdgcn_s_setprio(0); PJ_SCHED; } while (0)
#define PJ_SWI(a, b) asm volatile("v_permlane16_swap_b32 %0, %1" : "+v"(a), "+v"(b))
#define PJ_CVI(d, s, e) asm volatile("v_cvt_pk_bf16_f32 %0, %1, %2" : "=v"(d) : "v"(s[e]), "v"(s[(e) + 1]))
#define PJ_PACK(P, ai, bj, i) do { const int q_ = (i) >> 2; \
    if (((i) & 3) < 2) { PJ_CVI(P[q_][(i) & 1], acc[ai][bj][q_ & 2][q_ & 1], ((i) & 1) * 2); PJ_CVI(P[q_][2 + ((i) & 1)], acc[ai][bj][(q_ & 2) + 1][q_ & 1], ((i) & 1) * 2); } \
    else PJ_SWI(P[q_][(i) & 1], P[q_][2 + ((i) & 1)]); } while (0)
#define PJ_WV(n) asm volatile("s_waitcnt vmcnt(" #n ")" ::: "memory")
#define PJ_WL(n) asm volatile("s_waitcnt lgkmcnt(" #n ")" ::: "memory")
#define PJ_WL0 do { __builtin_amdgcn_s_waitcnt(0xC07F); asm volatile("" ::: "memory"); } while (0)
#define PJ_BAR __builtin_amdgcn_s_barrier()
#define PJ_SCHED __builtin_amdgcn_sched_barrier(0)
#define PJ_NOP do { PJ_SCHED; asm volatile("s_nop 7"); PJ_SCHED; } while (0)
#define PJ_DS(v, ai, bj, q) do { PJ_SCHED; __builtin_amdgcn_raw_buffer_store_b128(v, rc, vD + ((q) & 1) * 1024, dco + ((ai) * 4 + ((q) >> 1)) * 262144 + (bj) * 8192, 16); \
    asm volatile("s_nop 1"); PJ_SCHED; } while (0)
#define PJ_PARK(q) (*(u32x4*)(smem + C_PARK + wid * 4096 + vD + (q) * 1024))
#define PJ_PUB(ai, n) do { if (wid == 4) __builtin_amdgcn_raw_buffer_store_b32(8u, rc, vD < (n) * 16 ? vD >> 2 : PJ_OOB, dfl + (ai) * 16, 16); } while (0)
#define PJ_ST()
#define PJ_TOUCH(ptr, bytes, vo, so) __builtin_amdgcn_raw_ptr_buffer_load_lds(RSRC(ptr, bytes), (LDSP void*)(smem + C_PARK + wid * 4096), 4, vo, so, 0, 0)
#define PJ_WARM(S) do { int vt_ = vD; asm volatile("" : "+v"(vt_)); vt_ <<= 2; \
    const int g0_ = ((((x & 1) * 8 + (j >> 2)) * 3 + (j & 3) - 1) * 8 + wid) * 4096, g_ = u != WARMU || !(j & 3) ? PJ_OOB : g0_, h_ = g_ + (x >> 1) * 384 * 4096; \
    if (S == 2) { PJ_TOUCH(pA0 + 1024 * 576, 1024 * 1152, vt_, u ? PJ_OOB : g0_ + (x >> 1) * 384 * 4096); PJ_TOUCH(pA1, 2048 * PJ_K2(2), vt_, g_); } \
    if (S == 3) { PJ_TOUCH(pA1, 2048 * PJ_K2(2), vt_, g_ + 384 * 4096); PJ_TOUCH(pB1 - cb * 256, 4096 * PJ_K2(2), vt_, g_); } \
    if (S == 4) { PJ_TOUCH(pB1 - cb * 256, 4096 * PJ_K2(2), vt_, g_ + 384 * 4096); PJ_TOUCH(pB1 - cb * 256, 4096 * PJ_K2(2), vt_, g_ + 768 * 4096); } \
    if (S == 5) { PJ_TOUCH(p.Awo, WOB, vt_, h_); } } while (0)
#define PJ_F0(i)
#define PJ_F1(i) do { if (S == 8) PJ_PACK(pa, 0, 0, i); } while (0)
#define PJ_F2(i) do { if (S == 8) PJ_PACK(pb, 0, 1, i); if (S == 0) PJ_PACK(pb, 1, 1, i); } while (0)
#define PJ_F3(i) do { if (S == 8) PJ_PACK(pa, 1, 0, i); if (S == 5 && (i) == 0) { PJ_SCHED; PJ_PREP(u < 3 ? u + 1 : 3); PJ_SCHED; } } while (0)
#define PJ_STEP(SS) do { constexpr int S = SS; PJ_ST(); \
    PJ_LDB(B0, 0); PJ_SCHED; PJ_LDA(0); PJ_SA((bsel ^ 32768) + 16384, sa + 128 * k2); \
    PJ_WL(8); PJ_BAR; PJ_WL0; PJ_MMA(S == 0, 0, 0, B0, PJ_F0); PJ_BAR; PJ_SCHED; \
    if (S == 7) PJ_UNIT(u < 3 ? u + 1 : 3); \
    PJ_LDB(B1, 1); PJ_SB(65536 + bsel, sb); \
    if (S == 0) { PJ_DS(pb[2], 0, 1, 2); PJ_DS(pb[3], 0, 1, 3); } \
    if (S >= 2 && S < 6) PJ_WARM(S); \
    PJ_BAR; PJ_WL0; PJ_MMA(S == 0, 0, 1, B1, PJ_F1); PJ_BAR; \
    PJ_LDA(16384); sa += 128; PJ_SA(bsel, sa); \
    if (S == 8) { PJ_SCHED; PJ_DEST(u, j & 3); _Pragma("unroll") for (int q = 0; q < 4; q++) PJ_PARK(q) = pa[q]; PJ_SCHED; } \
    PJ_BAR; PJ_WL0; PJ_MMA(S == 0, 1, 0, B0, PJ_F2); PJ_BAR; PJ_SCHED; \
    { u32x4 c0_, c1_; \
      if (S < 2) { c0_ = PJ_PARK(2 * S); c1_ = PJ_PARK(2 * S + 1); PJ_SCHED; } \
      PJ_SB(65536 + bsel + 16384, sb + 256); sb += 64 * 8192; \
      if (S == 8) { PJ_DS(pb[0], 0, 1, 0); PJ_DS(pb[1], 0, 1, 1); } \
      else if (S < 2) { PJ_DS(c0_, 0, 0, (2 * S) & 3); PJ_DS(c1_, 0, 0, (2 * S + 1) & 3); } \
      else if (S < 4) { PJ_DS(pa[(2 * S) & 3], 1, 0, (2 * S) & 3); PJ_DS(pa[(2 * S + 1) & 3], 1, 0, (2 * S + 1) & 3); } \
      else if (S < 6) { PJ_DS(pb[(2 * S) & 3], 1, 1, (2 * S) & 3); PJ_DS(pb[(2 * S + 1) & 3], 1, 1, (2 * S + 1) & 3); } } \
      \
    if (S == 0 || (S >= 2 && S < 5)) PJ_WV(10); else if (S == 5) PJ_WV(9); else if (S == 1 || S == 8) PJ_WV(8); else PJ_WV(6); \
    PJ_BAR; \
    if (S == 2) PJ_PUB(0, 4); \
    if (S == 7) PJ_PUB(1, 4); \
    PJ_MMA(S == 0, 1, 1, B1, PJ_F3); PJ_BAR; \
    bsel ^= 32768; ao ^= 32768; bo ^= 32768; } while (0)
  f4 acc[2][2][4][2];
  bf8 At[4][2], B0[2][2], B1[2][2];
  u32x4 pa[4], pb[4];
  int bsel = 0, dco = PJ_OOB, dfl = PJ_OOB, u = 0;
  auto quick = [&](auto NBC, const int hf, const int ch) __attribute__((always_inline)) {
    constexpr int NB = decltype(NBC)::value, NS = 5 - NB;
#define PJ_FS(t) do { PJ_SA(((t) % NS) * 16384, hf * 128 * 1152 + (t) * 128); \
      _Pragma("unroll") for (int h_ = 0; h_ < NB; h_++) PJ_SB(NS * 16384 + (((t) % NS) * NB + h_) * 16384, (t) * 64 * 8192 + (ch + h_) * 256); PJ_SCHED; } while (0)
#pragma unroll
    for (int t = 0; t < NS - 1; t++) PJ_FS(t);
    bo += NS * 16384 - 65536;
#pragma unroll
    for (int t = 0; t < 9; t++) {
      PJ_ST();
      if (t == 8) PJ_WV(0); else if (NB == 2) PJ_WV(6); else if (t == 7) PJ_WV(4); else PJ_WV(8);
      PJ_BAR; PJ_SCHED;
      if (wr && t + NS - 1 < 9) PJ_FS(t + NS - 1);
      PJ_LDB(B0, 0); PJ_LDA((t % NS) * 16384);
      if (NB == 2) PJ_LDB(B1, 1);
      PJ_SCHED;
      bo += t == 8 ? 65536 - NS * 16384 - (8 % NS) * NB * 16384 : (t % NS == NS - 1 ? -(NS - 1) * NB * 16384 : NB * 16384);
      PJ_WL0;
      PJ_MMA(t == 0, 0, 0, B0, PJ_F0);
      if (NB == 2) PJ_MMA(t == 0, 0, 1, B1, PJ_F0);
      if (!wr && t + NS - 1 < 9) PJ_FS(t + NS - 1);
    }
    PJ_NOP;
#pragma unroll
    for (int q = 0; q < 4 * NB; q++) { const u32x4 v = PJ_CV(0, q >> 2, q & 3); PJ_DS(PJ_SW(v), hf, ch + (q >> 2), q & 3); }
    PJ_WV(0); PJ_BAR;
    if (tid < 4) __hip_atomic_fetch_add(WSP(u32, dfl) + hf * 4 + tid, 4u * NB, __ATOMIC_RELAXED, __HIP_MEMORY_SCOPE_AGENT);
  };
  if (((j + 1) & 3) < 2) {
    PJ_PREP(0); PJ_UNIT(0); PJ_DEST(0, 0);
    quick(std::integral_constant<int, 1>{}, 0, j & 1);
    if ((j & 3) == 0) { quick(std::integral_constant<int, 2>{}, 1, 0); u = 1; }
    dco = PJ_OOB; dfl = PJ_OOB;
  }
#pragma unroll
  for (int a = 0; a < 16; a++) asm volatile("" : "=v"(acc[a >> 3][(a >> 2) & 1][a & 3][0]), "=v"(acc[a >> 3][(a >> 2) & 1][a & 3][1]));
#pragma unroll
  for (int a = 0; a < 4; a++) asm volatile("" : "=v"(pa[a]), "=v"(pb[a]));
  PJ_PREP(u); PJ_UNIT(u);
  const int cs = x < 4 && (j & 3) == 2 ? (x * 8 + (j >> 2)) * 128 : -1;
#pragma unroll
  for (int i = 0; i < 2; i++) pa[i] = __builtin_amdgcn_raw_buffer_load_b128(RSRC(p.BWo, WOB), (tid >> 3) * 1152 + (tid & 7) * 16, cs >= 0 ? (cs + i * 64) * 1152 + 1024 : PJ_OOB, 0);
  PJ_SCHED;
  PJ_SB(65536, 0); PJ_SA(0, sa + 128);
  PJ_SB(65536 + 16384, 256); PJ_SA(16384, sa + 128 + 128 * k2);
  PJ_SCHED;
#pragma unroll
  for (int i = 0; i < 2; i++) __builtin_amdgcn_raw_buffer_store_b128(pa[i], rc, (tid & 7) * 65536 + (tid >> 3) * 16, cs >= 0 ? O_WX + (cs + i * 64) * 16 : PJ_OOB, 16);
  PJ_SCHED;
  if (wr == 1) PJ_BAR;
  PJ_WV(6); PJ_BAR;
  PJ_SB(65536 + 32768, 64 * 8192); PJ_SA(32768, sa + 256);
  PJ_SB(65536 + 32768 + 16384, 64 * 8192 + 256);
  PJ_WV(6); PJ_BAR;
  if (tid == 256 && cs >= 0) __hip_atomic_fetch_add(WSP(u32, 0) + SY_WX, 1u, __ATOMIC_RELAXED, __HIP_MEMORY_SCOPE_AGENT);
  sa += 256; sb = 128 * 8192;
#pragma unroll 1
  for (; u < 4; u++) {
    PJ_STEP(0); PJ_STEP(1); PJ_STEP(2); PJ_STEP(3); PJ_STEP(4); PJ_STEP(5);
    int more = k2 - 1024; asm volatile("" : "+s"(more));
    if (more) PJ_STEP(6);
    PJ_STEP(7); PJ_STEP(8);
  }
  PJ_ST();
  PJ_NOP;
  PJ_DS(pb[2], 0, 1, 2); PJ_DS(pb[3], 0, 1, 3);
#pragma unroll
  for (int q = 0; q < 4; q++) pb[q] = PJ_PARK(q);
#pragma unroll
  for (int q = 0; q < 4; q++) PJ_DS(pa[q], 1, 0, q);
#pragma unroll
  for (int q = 0; q < 4; q++) PJ_DS(pb[q], 0, 0, q);
#pragma unroll
  for (int q = 0; q < 4; q++) { const u32x4 v = PJ_CV(1, 1, q); PJ_DS(PJ_SW(v), 1, 1, q); }
  PJ_WV(0);
  if (wr == 0) PJ_BAR;
  PJ_BAR;
  PJ_PUB(0, 8);
  PJ_ST();
}

namespace fa {
constexpr float SCALE = 0.08838834764831845f;
constexpr int NW = 8, QBLK = 32, KVBLK = 64, QB = NW * QBLK, D = 128;
constexpr int SHM_V = KVBLK * D * 2, SHM_K = KVBLK * D * 2;
#define SBAR() __builtin_amdgcn_sched_barrier(0)
#define MF32(a, b, c) __builtin_amdgcn_mfma_f32_32x32x16_bf16(a, b, c, 0, 0, 0)
#define FMX3(a, b, c) __builtin_elementwise_maximum(__builtin_elementwise_maximum(a, b), c)
#define EBAR 14
#define MX0 3
#define ATHR 8.f
#define fadd1(a, b) ((a) + (b))
#define fmul1(a, b) ((a) * (b))
__device__ __forceinline__ void attn_block(const char* __restrict__ Qp, const char* __restrict__ Kh, const char* __restrict__ Vh,
                                           char* __restrict__ Op, int P0, char* lds, const int wv, u32* qh, int& pre) {
  const int tid = TIDX, wid = wv, lane = tid & 63, r32 = lane & 31, hi = lane >> 5, grp = wid >> 2;
  const int NT = (P0 + QB) / KVBLK;
  const int rb = wid ^ 4;
  const int TL = NT - 4 + (rb >> 1);
  const int rem = 2 * (NT - TL) - 2 - grp;
  float l_reg = 0.f;
  f32x16 o[4] = {}, s0, s1, nm = {};
  int ko = lane * 16;
  int vo = 4 * SHM_K + ((lane >> 4) & 1) * (1024 + 128) + ((lane >> 1) & 1) * 512 + ((hi ^ ((lane >> 1) & 1)) << 6) + ((lane >> 2) & 3) * 16 + (lane & 1) * 8;
  const __amdgpu_buffer_rsrc_t rk = RSRC(Kh, (2 * NT - 1) * 262144 + 8192), rv = RSRC(Vh, (2 * NT - 1) * 262144 + 8192);
  const int gk = tid * 16, gv = gk ^ (((tid >> 5) & 3) << 6);
#define DMA(rs, go, dst, so) __builtin_amdgcn_raw_ptr_buffer_load_lds(rs, (LDSP void*)(lds + (dst) + wid * 1024), 16, go, so, 0, 0)
#define ISSUE_K(t) do { const int s_ = (t) * 524288, d_ = ((t) & 3) * SHM_K; DMA(rk, gk, d_, s_); DMA(rk, gk, d_ + 8192, s_ + 262144); } while (0)
#define ISSUE_V(t) do { const int s_ = (t) * 524288, d_ = 4 * SHM_K + ((t) & 3) * SHM_V; DMA(rv, gv, d_, s_); DMA(rv, gv, d_ + 8192, s_ + 262144); } while (0)
#define PHASE(n) do { SBAR(); asm volatile("s_waitcnt vmcnt(" #n ")" ::: "memory"); __builtin_amdgcn_s_barrier(); asm volatile("" ::: "memory"); SBAR(); } while (0)
#define LDK(d0) do { kf[(d0) % 3][0] = *(const bf8*)(lds + ko + (d0) * 1024); kf[(d0) % 3][1] = *(const bf8*)(lds + ko + (d0) * 1024 + 8192); } while (0)
#define LDV(i) do { const int o_ = ((i) & 3) * 2048 + (((i) >> 2) & 1) * 256 + ((i) >> 3) * 8192; vf[(i) & 3] = cat8(trrd(lds + vo + o_), trrd(lds + vo1 + o_)); } while (0)
#define AST(i)
#define AP(i)
  AP(0);
#define QK(NEXT) do { _Pragma("unroll") for (int d0 = 0; d0 < 8; ++d0) { \
      if (d0 + 2 < 8) LDK(d0 + 2); else { NEXT(2 * (d0 - 6)); NEXT(2 * (d0 - 6) + 1); } \
      SBAR(); \
      if (d0) { s0 = MF32(kf[d0 % 3][0], qr[d0], s0); s1 = MF32(kf[d0 % 3][1], qr[d0], s1); } \
      else { asm volatile("v_mfma_f32_32x32x16_bf16 %0, %1, %2, %3" : "+v"(s0) : "v"(kf[0][0]), "v"(qr[0]), "v"(nm)); \
             asm volatile("v_mfma_f32_32x32x16_bf16 %0, %1, %2, %3" : "+v"(s1) : "v"(kf[0][1]), "v"(qr[0]), "v"(nm)); } \
      SBAR(); } } while (0)
#define NOV(i)
#define MAXS(i) do { \
      if ((i) == 0) { mx = FMX3(s0[0], s0[1], s0[2]); my = FMX3(s1[0], s1[1], s1[2]); } \
      else if ((i) < 7) { mx = FMX3(mx, s0[2 * (i) + 1], s0[2 * (i) + 2]); my = FMX3(my, s1[2 * (i) + 1], s1[2 * (i) + 2]); } \
      else { mx = FMX3(mx, s0[15], s1[15]); mx = __builtin_elementwise_maximum(mx, my); asm volatile("" : "+v"(mx)); } } while (0)
  float mx, my;
  constexpr float C2 = 1.4426950408889634f * SCALE;
  bf8 qr[8], kf[3][2], vf[4], pa[4];
#pragma unroll
  for (int d0 = 0; d0 < 8; ++d0) qr[d0] = *(const bf8*)(Qp + rb * 262144 + d0 * 1024 + lane * 16);
  SBAR();
  ISSUE_K(0); ISSUE_K(1); ISSUE_V(0); ISSUE_K(2); ISSUE_V(1);
  SBAR();
  asm volatile("" : "=v"(s0), "=v"(s1), "+v"(nm), "+v"(o[0]), "+v"(o[1]), "+v"(o[2]), "+v"(o[3]));
  SBAR();
#pragma unroll
  for (int d0 = 0; d0 < 8; ++d0) {
    u32x4 q = __builtin_bit_cast(u32x4, qr[d0]);
#pragma unroll
    for (int i = 0; i < 4; i++) q[i] = pk2(__uint_as_float(q[i] << 16) * C2, __uint_as_float(q[i] & 0xFFFF0000u) * C2);
    qr[d0] = __builtin_bit_cast(bf8, q);
    asm volatile("" : "+v"(qr[d0]));
  }
  AP(8);
  PHASE(8);
  AP(1);
  if (grp) PHASE(6);
  LDK(0); LDK(1);
  QK(NOV);
#pragma unroll
  for (int i = 0; i < 8; ++i) MAXS(i);
  AP(2);
  PHASE(4);
  AP(3);
  auto tile = [&](const int t, auto LASTC) __attribute__((always_inline)) {
    constexpr bool LAST = decltype(LASTC)::value;
    AST(0);
    ISSUE_K(t + 3); ISSUE_V(t + 2);
    SBAR();
    if constexpr (LAST) {
      const int dq = r32 - 4 * hi;
      if (rb & 1) {
#pragma unroll
        for (int r = 0; r < 16; ++r) { if (dq < (r & 3) + 8 * (r >> 2)) s1[r] = -__builtin_inff(); SBAR(); }
      } else {
#pragma unroll
        for (int r = 0; r < 16; ++r) { if (dq < (r & 3) + 8 * (r >> 2)) s0[r] = -__builtin_inff(); s1[r] = -__builtin_inff(); SBAR(); }
      }
#pragma unroll
      for (int i = 0; i < 8; ++i) MAXS(i);
    }
    {
      if (__builtin_expect(t == 0 || __any(mx > ATHR), 0)) {
        auto rr = __builtin_amdgcn_permlane32_swap(__float_as_uint(mx), __float_as_uint(mx), false, false);
        mx = __builtin_elementwise_maximum(__uint_as_float(rr[0]), __uint_as_float(rr[1]));
        if (t) mx = __builtin_elementwise_maximum(mx, 0.f);
        const float alpha = t ? __builtin_amdgcn_exp2f(-mx) : 1.f, nn = nm[0] - mx;
        mx = -mx;
#pragma unroll
        for (int r = 0; r < 16; ++r) { s0[r] = fadd1(s0[r], mx); s1[r] = fadd1(s1[r], mx); nm[r] = nn; }
#pragma unroll
        for (int d = 0; d < 4; ++d)
#pragma unroll
          for (int r = 0; r < 16; ++r) o[d][r] = fmul1(o[d][r], alpha);
        l_reg *= alpha;
        asm volatile("" : "+v"(nm));
      }
    }
    AST(1);
    float x[32], sb = 0.f;
    u32 pw[16];
#pragma unroll
    for (int r = 0; r < 18; ++r) {
      SBAR();
      if (r < 16) { x[2 * r] = __builtin_amdgcn_exp2f(r < 8 ? s0[2 * r] : s1[2 * r - 16]); x[2 * r + 1] = __builtin_amdgcn_exp2f(r < 8 ? s0[2 * r + 1] : s1[2 * r - 15]); }
      if (r >= 2) {
        l_reg = fadd1(l_reg, x[2 * r - 4]);
        sb = r == 2 ? x[1] : fadd1(sb, x[2 * r - 3]);
        pw[r - 2] = pk2(x[2 * r - 4], x[2 * r - 3]); }
    }
    SBAR();
    l_reg = fadd1(l_reg, sb);
#pragma unroll
    for (int i = 0; i < 4; ++i) pa[i] = __builtin_bit_cast(bf8, (u32x4){pw[4 * i], pw[4 * i + 1], pw[4 * i + 2], pw[4 * i + 3]});
    asm volatile("" : "+v"(pa[0]), "+v"(pa[1]), "+v"(pa[2]), "+v"(pa[3]), "+v"(l_reg));
    ko ^= (t & 1) ? 3 * SHM_K : SHM_K;
    const int vo1 = vo ^ 128;
    SBAR();
    AST(3);
    if constexpr (!LAST) { LDK(0); LDK(1); } else { LDV(0); LDV(1); LDV(2); LDV(3); }
    AST(4);
    PHASE(4);
    AST(5);
    __builtin_amdgcn_s_setprio(1);
    if constexpr (!LAST) QK(LDV);
    AST(6);
#pragma unroll
    for (int i = 0; i < 16; ++i) {
      SBAR();
      o[i & 3] = MF32(vf[i & 3], pa[i >> 2], o[i & 3]);
      SBAR();
      if (i + 4 < 16) LDV(i + 4);
      if constexpr (!LAST) if (i >= MX0 && i < MX0 + 8) MAXS(i - MX0);
      if (i == EBAR && (!LAST || rem)) PHASE(4);
    }
    __builtin_amdgcn_s_setprio(0);
    vo ^= (t & 1) ? 3 * SHM_V : SHM_V;
    AST(7);
  };
#pragma unroll 1
  for (int t = 0; t < TL; t++) {
    tile(t, std::false_type{});
  }
  AP(4);
  tile(TL, std::true_type{});
  AP(5);
  ISSUE_V(TL + 3);
  SBAR();
  {
    const float inv = __builtin_amdgcn_rcpf(swadd(l_reg, l_reg));
    const __amdgpu_buffer_rsrc_t ro = RSRC(Op + rb * 262144, 8192);
#pragma unroll
    for (int i = 0; i < 8; ++i) {
      const int g = (i & 1) * 8;
#define ON(k) fmul1(o[i >> 1][g + (k)], inv)
      auto r0 = __builtin_amdgcn_permlane32_swap(pk2(ON(0), ON(1)), pk2(ON(4), ON(5)), false, false);
      auto r1 = __builtin_amdgcn_permlane32_swap(pk2(ON(2), ON(3)), pk2(ON(6), ON(7)), false, false);
#undef ON
      __builtin_amdgcn_raw_buffer_store_b128((u32x4){r0[0], r1[0], r0[1], r1[1]}, ro, r32 * 32 + hi * 16 + (i & 3) * 1024, (i >> 2) * 4096, 16);
      if (i == 3 && rem > 1) PHASE(6);
    }
  }
  AP(6);
  if (rem > 2) PHASE(10);
  if (rem > 3) { PHASE(8); PHASE(8); }
  CLAIM();
  VMW0();
  AP(7);
  __syncthreads();
}
#undef DMA
#undef ISSUE_K
#undef ISSUE_V
#undef PHASE
#undef LDK
#undef LDV
#undef QK
#undef NOV
#undef MAXS
}

__device__ __forceinline__ void wof_task(const Params& p, int kc, char* smem, const int wv, u32* qh, int& pre) {
  const int tid = TIDX, lane = tid & 63, wid = wv;
  const int l15 = lane & 15, lq = lane >> 4, q4 = l15 >> 2, p4 = lane & 3;
  const __amdgpu_buffer_rsrc_t rw = RSRC(WSP(u16, O_WOF), 2 * WOB);
  bf8 st[2][9];
#pragma unroll
  for (int m = 0; m < 2; m++)
#pragma unroll
    for (int i = 0; i < 9; i++) st[m][i] = *(const bf8*)((m ? p.BWo : p.Awo) + (size_t)kc * 64 * 576 + (tid + 512 * i) * 8);
  CLAIM();
#pragma unroll
  for (int m = 0; m < 2; m++) {
#pragma unroll
    for (int i = 0; i < 9; i++) {
      const int id = tid + 512 * i, row = id / 72, ch = id % 72;
      *(bf8*)(smem + row * 1280 + (((ch >> 1) ^ fsw(row)) << 5) + ((ch & 1) << 4)) = st[m][i];
    }
    asm volatile("s_waitcnt lgkmcnt(0)" ::: "memory"); __builtin_amdgcn_s_barrier(); asm volatile("" ::: "memory");
#pragma unroll
    for (int i = 0; i < 9; i++) {
      const int f = wid * 9 + i, nt = f >> 1, k2 = f & 1;
      const int r0 = k2 * 32 + lq * 8 + q4, r1 = r0 + 4;
      bf4 lo = trrd(smem + r0 * 1280 + ((nt ^ fsw(r0)) << 5) + p4 * 8);
      bf4 hi = trrd(smem + r1 * 1280 + ((nt ^ fsw(r1)) << 5) + p4 * 8);
      __builtin_amdgcn_raw_buffer_store_b128(__builtin_bit_cast(u32x4, cat8(lo, hi)), rw, ((nt * 128 + kc * 2 + k2) * 64 + lane) * 16, m * WOB, 16);
    }
    if (m == 0) { asm volatile("s_waitcnt lgkmcnt(0)" ::: "memory"); __builtin_amdgcn_s_barrier(); asm volatile("" ::: "memory"); }
  }
  VMW0();
  __syncthreads();
}

#define OT_AAUX 0
#define OT_BAUX 0
__device__ __forceinline__ void out_tile(const char* A, const char* W, u16* C, char* smem, const int wv, u32* qh, int& pre) {
  const int tid = TIDX, lane = tid & 63, wid = wv;
  const int l15 = lane & 15, lq = lane >> 4, kh = wid >> 2, wc = wid & 3;
  const __amdgpu_buffer_rsrc_t ra = RSRC(A, 2 * 262144), rw = RSRC(W + wc * 3 * 131072 + kh * 1024, 3 * 131072 - kh * 1024);
  const int va = tid * 16, vw = lane * 16;
  int ao = kh * 2048 + (lq >> 1) * 1024 + l15 * 32 + (lq & 1) * 16;
  f4 acc[4][3];
  bf8 wf[8][3], af[2][4];
#define OT_DMA(i, buf, so) __builtin_amdgcn_raw_ptr_buffer_load_lds(ra, (LDSP void*)(smem + (buf) + (i) * 8192 + wid * 1024), 16, va, (so) + ((i) >> 1) * 262144 + ((i) & 1) * 8192, 0, OT_AAUX)
#define OT_LDW(u, so) do { _Pragma("unroll") for (int j = 0; j < 3; j++) \
    wf[u][j] = __builtin_bit_cast(bf8, __builtin_amdgcn_raw_buffer_load_b128(rw, vw, (so) + j * 131072, OT_BAUX)); } while (0)
#define OT_LDA(d, base, off) do { _Pragma("unroll") for (int m = 0; m < 4; m++) af[d][m] = *(const bf8*)(smem + (base) + (off) + (m >> 1) * 16384 + (m & 1) * 512); } while (0)
#define OT_MF(a, b, c) MFMA(a, b, c)
#define OT_XA(v) (v)
#define OT_XW(v) (v)
#pragma unroll
  for (int i = 0; i < 4; i++) OT_DMA(i, 0, 0);
  SB0();
#pragma unroll
  for (int u = 0; u < 8; u++) { OT_DMA(u & 3, 32768 + (u >> 2) * 32768, 16384 + (u >> 2) * 16384); OT_LDW(u, u * 2048); SB0(); }
#pragma unroll
  for (int m = 0; m < 4; m++)
#pragma unroll
    for (int j = 0; j < 3; j++) acc[m][j] = (f4){0, 0, 0, 0};
  asm volatile("s_waitcnt vmcnt(32)" ::: "memory"); __builtin_amdgcn_s_barrier(); asm volatile("" ::: "memory");
  OT_LDA(0, ao, 0);
  int bs = 0;
#pragma unroll 1
  for (int c = 0; c < 16; c += 2) {
    const int sw = c < 14 ? OT_XW((c + 2) * 8192) : PJ_OOB;
#pragma unroll
    for (int u = 0; u < 8; u++) {
      SB0();
      __builtin_amdgcn_s_waitcnt(0x4F7C);
      if ((u & 3) == 3) { asm volatile("s_waitcnt lgkmcnt(0)" ::: "memory"); __builtin_amdgcn_s_barrier(); asm volatile("" ::: "memory"); }
      if (u == 7) ao ^= 65536;
      OT_LDA((u + 1) & 1, ao, u == 7 ? 0 : ((u + 1) >> 2) * 32768 + ((u + 1) & 3) * 4096);
      SB0();
#define OT_V 1
      const int cn = c + 3 + (u >> 2);
#pragma unroll
      for (int j = 0; j < 3; j++) {
#pragma unroll
        for (int m = 0; m < 4; m++) {
          acc[m][j] = OT_MF(wf[u][j], af[u & 1][m], acc[m][j]);
          if (j == 0 && m == 1) { SB0(); OT_DMA(u & 3, u < 4 ? (bs ^ 65536) + 32768 : bs, cn < 16 ? OT_XA(cn * 16384) : PJ_OOB); SB0(); }
        }
        SB0();
        wf[u][j] = __builtin_bit_cast(bf8, __builtin_amdgcn_raw_buffer_load_b128(rw, vw, sw + u * 2048 + j * 131072, OT_BAUX));
        SB0();
      }
    }
    bs ^= 65536;
  }
  CLAIM();
  VMW0C();
  asm volatile("s_waitcnt lgkmcnt(0)" ::: "memory"); __builtin_amdgcn_s_barrier(); asm volatile("" ::: "memory");
#pragma unroll
  for (int m = 0; m < 2; m++)
#pragma unroll
    for (int j = 0; j < 3; j++) *(f4*)(smem + (m * 3 + j) * 8192 + va) = kh ? acc[m][j] : acc[2 + m][j];
  __syncthreads();
#pragma unroll
  for (int m = 0; m < 2; m++) {
    f4 t[3];
#pragma unroll
    for (int j = 0; j < 3; j++) { const f4 o = *(const f4*)(smem + (m * 3 + j) * 8192 + (va ^ 4096)); t[j] = kh ? o + acc[2 + m][j] : acc[m][j] + o; }
    u16* c = C + (size_t)(kh * 32 + m * 16 + l15) * 1152 + wc * 48;
    *(u32x4*)(c + (lq & 1) * 16 + (lq >> 1) * 8) = pair16(pk2(t[0][0], t[0][1]), pk2(t[0][2], t[0][3]), pk2(t[1][0], t[1][1]), pk2(t[1][2], t[1][3]));
    *(u32x2*)(c + 32 + lq * 4) = (u32x2){pk2(t[2][0], t[2][1]), pk2(t[2][2], t[2][3])};
  }
  __syncthreads();
#undef OT_DMA
#undef OT_LDW
#undef OT_LDA
}

#define CLAIMS() do { const int l_ = TIDX; if (l_ < 64) { \
    const u32 v_ = ld4_sc1(WSP(u32, 0) + SY_OQ(l_ < 6 ? l_ : 0)); \
    if (l_ == 0) pre = (int)__hip_atomic_fetch_add(WSP(u32, 0) + SY_OQ(6), 1u, __ATOMIC_RELAXED, __HIP_MEMORY_SCOPE_AGENT); \
    pre |= (int)((u32)__ballot(l_ < 6 && v_ < ((l_ & 1) ? XS0 / 2 : 32)) << 16); } } while (0)
__device__ __forceinline__ void small_out_task(const Params& p, int s0, int hf, int nt, char* smem, const int wv, int& pre) {
  const int tid = TIDX, lane = tid & 63, wid = wv;
  const int l15 = lane & 15, lq = lane >> 4;
  const __amdgpu_buffer_rsrc_t rw = RSRC(WSP(char, O_WOF) + WOB, WOB), ro = RSRC(WSP(u16, O_OB), 2048 * 4096 * 2);
  if (tid == 0) {
    int it = 0;
    while (ld4_sc1(WSP(u32, 0) + SY_CW) < 64u && ++it < SPIN_CAP) __builtin_amdgcn_s_sleep(8);
  }
  __syncthreads();
  bf8 bfr[16];
#pragma unroll
  for (int k = 0; k < 16; k++) bfr[k] = ldsc1(rw, ((nt * 128 + wid * 16 + k) * 64 + lane) * 16);
#pragma unroll 1
  for (int s = s0; s < 64; s += 2) {
    if (tid < 64) {
      int it = 0;
      if (s < 62) while (ld4_sc1(WSP(u32, 0) + SY_CS(s)) == 0u && ++it < SPIN_CAP) __builtin_amdgcn_s_sleep(SPS);
      else {
        while (ld4_sc1(WSP(u32, 0) + SY_CS(61)) == 0u && ++it < SPIN_CAP) __builtin_amdgcn_s_sleep(SPS);
        while (!__all(ld4_sc1(WSP(u32, 0) + SY_SEG(s - 62, tid & 31)) != 0u) && ++it < SPIN_CAP) __builtin_amdgcn_s_sleep(4);
      }
    }
    __syncthreads();
    if (s == 63) TLS(1);
    const int row0 = s * 32 + hf * 16;
    bf8 af[16];
#pragma unroll
    for (int k = 0; k < 16; k++) af[k] = ldsc1(ro, OB_OFF(row0 + l15, (wid * 16 + k) * 32 + lq * 8));
    if (s >= 62) CLAIMS();
    f4 acc = (f4){0, 0, 0, 0};
#pragma unroll
    for (int k = 0; k < 16; k++) acc = MFMA(bfr[k], af[k], acc);
    *(f4*)(smem + (wid * 64 + lane) * 16) = acc;
    __syncthreads();
    if (s == 63) TLS(2);
    if (tid < 64) {
      f4 t = (f4){0, 0, 0, 0};
#pragma unroll
      for (int w = 0; w < 8; w++) t += *(const f4*)(smem + (w * 64 + lane) * 16);
      *(u64*)(p.out + (size_t)(row0 + l15) * 1152 + 576 + nt * 16 + lq * 4) = (u64)pk2(t[0], t[1]) | ((u64)pk2(t[2], t[3]) << 32);
    }
  }
  __syncthreads();
}

#define PSTAMP()
#define STAMP(i)
#define ANS 16
#define KSL 1
#define DSH 2
#define K1 24
#define K3 11
#define DOMAX 90
#define DOCAP 6
#define C_IMG 0
#define C_QT 9216
#define C_KT (C_QT + 3072)
#define C_P (C_KT + 4096)
#define C_PX (C_P + 768)
#define C_KX (C_PX + 2560)
#define C_VX (C_KX + 8192)
#define C_QX (C_VX + 8192)
#define C_XP 161024
#define GPS 6
#define C_WL 49920
__device__ __forceinline__ float ror8(float v) {
  return __uint_as_float(__builtin_amdgcn_mov_dpp(__float_as_uint(v), 0x128, 0xf, 0xf, true));
}
__device__ __forceinline__ float rb1(float f) { return __uint_as_float(pk2(0.f, f)); }
#define FMX(a, b) __builtin_elementwise_maximum(a, b)
__device__ __forceinline__ float tmax(float mx) {
  mx = FMX(mx, ror8(mx));
  { auto t = __builtin_amdgcn_permlane16_swap(__float_as_uint(mx), __float_as_uint(mx), false, false);
    mx = FMX(__uint_as_float(t[0]), __uint_as_float(t[1])); }
  { auto t = __builtin_amdgcn_permlane32_swap(__float_as_uint(mx), __float_as_uint(mx), false, false);
    mx = FMX(__uint_as_float(t[0]), __uint_as_float(t[1])); }
  return mx;
}
__device__ __forceinline__ int hmax(int x) {
  x = max(x, __builtin_amdgcn_mov_dpp(x, 0x128, 0xf, 0xf, true));
  x = max(x, __builtin_amdgcn_mov_dpp(x, 0x124, 0xf, 0xf, true));
  x = max(x, __builtin_amdgcn_mov_dpp(x, 0x4E, 0xf, 0xf, true));
  x = max(x, __builtin_amdgcn_mov_dpp(x, 0xB1, 0xf, 0xf, true));
  { auto t = __builtin_amdgcn_permlane16_swap((u32)x, (u32)x, false, false); x = max((int)t[0], (int)t[1]); }
  return x;
}
typedef __attribute__((ext_vector_type(2))) unsigned long long u64x2;
__device__ __forceinline__ f4 zero4() {
  u64 a, b; asm volatile("v_mov_b64 %0, 0\n\tv_mov_b64 %1, 0\n\ts_nop 1" : "=v"(a), "=v"(b));
  return __builtin_bit_cast(f4, (u64x2){a, b});
}
__device__ __forceinline__ u32x4 ones4() {
  u64 a, b; asm volatile("v_mov_b64 %0, -1\n\tv_mov_b64 %1, -1" : "=v"(a), "=v"(b));
  return __builtin_bit_cast(u32x4, (u64x2){a, b});
}
#define BAR() do { asm volatile("s_waitcnt lgkmcnt(0)" ::: "memory"); __builtin_amdgcn_s_barrier(); asm volatile("" ::: "memory"); } while (0)
#define BARC() do { asm volatile("" ::: "memory"); __builtin_amdgcn_s_waitcnt(0xC07F); __builtin_amdgcn_s_barrier(); asm volatile("" ::: "memory"); } while (0)
#define BARN() do { asm volatile("" ::: "memory"); __builtin_amdgcn_s_barrier(); asm volatile("" ::: "memory"); } while (0)
#define IDX() const int tid = TIDX, lane = tid & 63, l15 = lane & 15, lq = lane >> 4, q4 = l15 >> 2, p4 = lane & 3, hi8 = l15 >> 3; \
    (void)q4; (void)p4; (void)hi8; (void)lq
#define AD_LD() __builtin_amdgcn_raw_buffer_load_b32(rsX, (TIDX & 63) * ANS, X_ANN + (s & 1) * 1024 + xp, 16)
#define AD_WAIT(HEAD, K, CAP) do { int n_ = 0; u32 an = AD_LD(); \
    while ((u32)__ballot((an & 63u) != (u32)s) != 0u && ++n_ < (CAP)) { __builtin_amdgcn_s_sleep(1); CFENCE(); an = AD_LD(); } \
    const u32 r_ = (u32)__builtin_amdgcn_readfirstlane((int)an); \
    const int m_ = hmax((TIDX & 32) ? (int)umin(an, DOMAX * 64u) : (int)(an - r_)); \
    dl = ((r_ + (u32)__builtin_amdgcn_readlane(m_, 0)) & ~63u) + (u32)(K) * 64u + (HEAD ? (u32)__builtin_amdgcn_readlane(m_, 32) : 0u); \
    n_ = 160; \
    while ((int)(((u32)wall_clock64() << 6) - dl) < 0 && --n_ > 0) {} } while (0)
#define STG_AUX 2
#define OSTG_AUX 0
#define STG_D 3
#define STG_POLL 7
#define STS(c, i)

#define DPR(OP, N) OP " %3, %3, %3 row_ror:" #N " row_mask:0xf bank_mask:0xf\n\t" OP " %4, %4, %4 row_ror:" #N " row_mask:0xf bank_mask:0xf\n\t"
#define DPROW(OP) "s_nop 0\n\t" DPR(OP, 8) "s_nop 0\n\t" DPR(OP, 4) "s_nop 0\n\t" DPR(OP, 2) "s_nop 0\n\t" DPR(OP, 1)
#define ANJ 0
#define LSL 2
__device__ __forceinline__ void chain_head(const Params& p, char* smem, const int h, const int wv) {
  STS(h, 1);
  const int wid = wv;
  u32x8 w[2][9], wx[3];
  const __amdgpu_buffer_rsrc_t rsX = RSRC(XWIN(p), X_END);
  const __amdgpu_buffer_rsrc_t rsB = RSRC(WSP(u16, O_QB), 4 * ACB);
  const int cb = h >> 1;
  const int nvt = (wid & 3) == 0 ? 0 : (wid == 5 || wid == 6 ? 2 : 1);
  const int vt = wid < 4 ? wid - 1 : (wid == 7 ? 3 : wid * 2 - 6);
  const int wl = C_WL + (nvt == 1 ? vt * 12288 : 49152 + (nvt == 2 ? wid - 5 : wid >> 2) * 30720);
  int xp;
  {
    IDX();
    u32x4 st[54], nk, nv, nq;
    u32 fl, fv, fk;
    const __amdgpu_buffer_rsrc_t rq = RSRC(p.BWq, 576 * 4096 * 2), rk = RSRC(p.BWk, 576 * 4096 * 2), rv = RSRC(p.BWv, 576 * 4096 * 2);
    const int vw = ((tid >> 4) * 4096 + h * 128 + (tid & 15) * 8) * 2;
    {
      const u32x4 e = ones4();
#pragma unroll
      for (int i = 0; i < 2; i++) {
        const int id = wid + 8 * i;
        __builtin_amdgcn_raw_buffer_store_b128(e, rsX, lane * 16, id < 12 ? X_O + (id >> 2) * 131072 + (((id >> 1) & 1) * 4 + (h >> 3)) * 16384 + (id & 1) * 8192 + (h & 7) * 1024 : PJ_OOB, 16);
      }
      __builtin_amdgcn_raw_buffer_store_b128(e, rsX, tid < 8 ? X_ANN + (tid & 1) * 1024 + ((tid >> 1) & 1) * 8192 + (h + (tid >> 2) * 32) * ANS : PJ_OOB, 0, 16);
    }
    const __amdgpu_buffer_rsrc_t rm = RSRC(p.Bm, 8 * 576 * 2);
    const u32x4 m0 = __builtin_amdgcn_raw_buffer_load_b128(rm, tid * 16, 0, 0), m1 = __builtin_amdgcn_raw_buffer_load_b128(rm, tid * 16, 8192, 0);
    auto req = [&](int kc) {
#pragma unroll
      for (int i = 0; i < 6; i++) st[kc * 6 + i] = __builtin_amdgcn_raw_buffer_load_b128(i < 2 ? rq : (i < 4 ? rk : rv), vw, (kc * 64 + (i & 1) * 32) * 8192, STG_AUX);
      SB0();
    };
#pragma unroll
    for (int kc = 0; kc < STG_D; kc++) { req(kc); __builtin_amdgcn_s_barrier(); SB0(); }
    auto cell = [](int row, int slot) { return (((row * 2 + slot) ^ ((((row >> 2) ^ (row >> 4)) & 1) << 2) ^ ((row >> 5) & 1)) << 5); };
    auto put = [&](int unit, int row, const u32x4& v) { *(u32x4*)(smem + unit * 4160 + cell(row, (tid >> 1) & 1) + (tid & 1) * 16) = v; };
    auto frag = [&](int tile) {
      bf4 tt[4];
#pragma unroll
      for (int kk = 0; kk < 4; kk++) {
        const int r0 = (kk >> 1) * 32 + (lq & 1) * 16 + (lq >> 1) * 8 + (kk & 1) * 4 + (q4 & 1) * 2 + (q4 >> 1);
        tt[kk] = trrd(smem + (tile >> 1) * 4160 + cell(r0, tile & 1) + p4 * 8);
      }
      return ilv(tt[0], tt[1], tt[2], tt[3]);
    };
    auto park = [&](int slot, const u32x8& f) {
      char* d = smem + wl + slot * 2048 + lane * 16;
      *(u32x4*)d = (u32x4){f[0], f[1], f[2], f[3]};
      *(u32x4*)(d + 1024) = (u32x4){f[4], f[5], f[6], f[7]};
    };
    auto poll = [&]() {
      fl = ld4_sc1(WSP(u32, 0) + SY_INIT(lane)); fv = ld4_sc1(WSP(u32, 0) + SY_VOTE(lane & 31));
      fk = ld4_sc1(WSP(u32, 0) + SY_BLK(lane < 12 ? lane >> 2 : 0, cb, lane & 3));
    };
    auto rows = [&]() {
      nq = __builtin_amdgcn_raw_buffer_load_b128(rsB, tid * 16, h * 8192, 16);
      nk = __builtin_amdgcn_raw_buffer_load_b128(rsB, tid * 16, h * 8192 + ACB, 16);
      nv = __builtin_amdgcn_raw_buffer_load_b128(rsB, tid * 16, h * 8192 + 2 * ACB, 16);
    };
    bool early = false;
#pragma unroll
    for (int kc = 0; kc < 9; kc++) {
      if (kc == 10 - STG_D) { early = __all(fk >= 8u); CFENCE(); if (early) rows(); if (wid == 0) poll(); SB0(); }
#pragma unroll
      for (int i = 0; i < 6; i++) put((i >> 1) * 4 + ((tid & 15) >> 2), (i & 1) * 32 + (tid >> 4), st[kc * 6 + i]);
      BAR();
      if (kc == 0 && tid == 0) st4_sc1(WSP(u32, 0) + SY_INIT(h), 1u);
      if (kc == 0) TLS(15);
      if (kc + STG_D == 8) { fk = ld4_sc1(WSP(u32, 0) + SY_BLK(lane < 12 ? lane >> 2 : 0, cb, lane & 3)); SB0(); }
      if (kc + STG_D < 9) req(kc + STG_D);
      w[0][kc] = frag(wid * 2);
      w[1][kc] = frag(nvt == 2 ? 16 + vt : wid * 2 + 1);
      const u32x8 tp = frag(nvt == 0 ? 11 + (wid >> 1) : 15 + vt + nvt);
      if (kc >= 4 && kc < 7) { wx[kc - 4] = tp; if (nvt == 0) park(6 + kc, tp); }
      else park(nvt == 0 ? 6 + kc : (kc >= 7 ? kc - 7 : 2 + kc), tp);
      BAR();
      if (kc == 0 || kc == 8) STS(h, kc + 3);
    }
    TLS(6);
    if (wid == 0) {
      int it = 0;
      while (!__all(fl != 0u && fv != 0u && fk >= 8u) && ++it < SPIN_CAP) { __builtin_amdgcn_s_sleep(GPS); poll(); }
      *(int*)(smem + C_XP) = __popcll(__ballot(fv == 2u)) > 32 ? 8192 : 0;
    }
    BAR();
    if (!early) rows();
    xp = __builtin_amdgcn_readfirstlane(*(const int*)(smem + C_XP));
    TLS(7);
    SB0();
#pragma unroll
    for (int i = 0; i < 2; i++) {
      const int id = tid + 512 * i, t = id / 72, hb = id % 72;
      char* d = smem + C_IMG + (hb >> 2) * 512 + (t >> 2) * 256 + (((hb & 3) ^ ((t >> 2) * 2)) << 6) + ((t >> 1) & 1) * 8 + (t & 1) * 2;
      if (i == 0 || tid < 64) {
#pragma unroll
        for (int e = 0; e < 8; e++) *(u16*)(d + (e >> 1) * 16 + (e & 1) * 4) = (u16)((i ? m1 : m0)[e >> 1] >> ((e & 1) * 16));
      }
    }
    *(u32x4*)(smem + C_QX + tid * 16) = nq;
    *(u32x4*)(smem + C_KX + tid * 16) = nk;
    *(u32x4*)(smem + C_VX + ((tid * 16) ^ ((tid & 32) << 2))) = nv;
  }
  __syncthreads();
  STS(h, 14);
  constexpr float CE = 0.08838834764831845f * 1.4426950408889634f;
  auto run = [&](auto RC) __attribute__((always_inline)) {
  constexpr int R = decltype(RC)::value;
  constexpr int NV = R < 2 ? 0 : (R < 4 ? 1 : 2);
  constexpr bool QW = R == 0 || R == 2;
  int known = 0;
  bf8 pre[4][2], vx[NV ? NV : 1];
  int sidx, mo, mo1, eo, gvo, wvo = 0, so = 0, po = 0, qo = 0, ko = 0, ko1 = 0, fo = 0;
  float mk[4];
#define LDW(dst, slot) do { dst[0] = *(const bf8*)(smem + wvo + (slot) * 2048); dst[1] = *(const bf8*)(smem + wvo + (slot) * 2048 + 1024); } while (0)
  {
    IDX();
    if constexpr (R == 1) {
    } else if constexpr (R == 0) {
      qo = C_QT + lq * 384 + q4 * 32 + p4 * 8; ko = C_KT + ((((lq * 8 + q4) * 32) ^ ((lq & 1) << 7)) + p4 * 8); ko1 = C_KT + ((((lq * 8 + 4 + q4) * 32) ^ ((lq & 1) << 7)) + p4 * 8);
      po = C_P + l15 * 16 + lq * 4;
      fo = lq * 512 + l15 * 16;
#pragma unroll
      for (int r = 0; r < 4; r++) { mk[r] = lq * 4 + r <= l15 ? 0.f : -__builtin_inff(); asm volatile("" : "+v"(mk[r])); }
      asm volatile("" : "+v"(qo), "+v"(ko), "+v"(ko1), "+v"(po), "+v"(fo));
    } else {
      wvo = wl + lane * 16;
      po = C_P + (lq * 4 + q4) * 16 + (p4 & 1) * 8;
      so = ((l15 >> 2) & 1) * 65536 + ((vt + (NV == 2 ? hi8 : 0)) * 8 + ((l15 >> 1) & 1) * 4 + (lq >> 1) * 2 + (l15 & 1)) * 16 + (lq & 1) * 8;
      if (NV == 1 && hi8) so = PJ_OOB;
      asm volatile("" : "+v"(wvo), "+v"(po), "+v"(so));
#pragma unroll
      for (int j = 0; j < 4; j++) LDW(pre[j], NV == 2 ? 6 + j : j);
    }
    eo = QW ? C_QT + wid * 768 + ((l15 >> 2) * 2 + (l15 & 1)) * 32 + lq * 8 + ((l15 >> 1) & 1) * 4
            : C_KT + (wid & 3) * 1024 + ((l15 * 32) ^ (hi8 << 7)) + lq * 8;
    sidx = (l15 & 2) ? 0xEEEE : 0x4444;
    mo = C_IMG + (lq >> 1) * 512 + (p4 >> 1) * (256 + 128) + (lq & 1) * 64 + q4 * 16 + (p4 & 1) * 8; mo1 = mo ^ 128;
    gvo = tid * 16;
    asm volatile("" : "+v"(sidx), "+v"(mo), "+v"(mo1), "+v"(eo), "+v"(gvo));
  }
  f4 a0, a1;
  u32 ts = 0;
  u64 tq = 0;
  int dur = 70 * 64;
  const int ob = (h >> 3) * 16384 + (h & 7) * 1024 + xp;
  int b3 = 0;
#pragma unroll 1
  for (int s = 0;; s++) {
    int more = s; asm volatile("" : "+s"(more));
    const int mb = X_M + b3 * 32768 + xp;
    u32x4 g, gx;
    if (more) {
      BAR();
      g = __builtin_amdgcn_raw_buffer_load_b128(rsX, gvo, mb, 16);
      if constexpr (R == 1) gx = __builtin_amdgcn_raw_buffer_load_b128(rsX, gvo, mb + 16384 - 4096, 16);
    } else asm volatile("" : "=v"(g), "=v"(gx));
    SB0();
    if constexpr (R == 0) {
#pragma unroll
      for (int j = 0; j < 4; j++) { const char* kx_ = smem + C_KX + fo + j * 2048;
        pre[j >> 1][j & 1] = *(const bf8*)kx_; pre[2 + (j >> 1)][j & 1] = *(const bf8*)(kx_ + 256); }
    } else if constexpr (R > 1) {
      IDX();
#pragma unroll
      for (int d = 0; d < NV; d++) { const char* vb_ = smem + C_VX + (vt + d) * 1024 + (p4 >> 1) * 512 + (((lq * 4 + q4) ^ ((p4 >> 1) * 8)) << 4) + (p4 & 1) * 8;
        vx[d] = cat8(trrd(vb_), trrd(vb_ + 256)); }
    }
    SB0();
    a0 = zero4(); a1 = zero4();
    int so_ = X_O + b3 * 131072 + ob, an_ = X_ANN + (s & 1) * 1024 + xp + h * ANS;
    int nb = s < 63 ? (s + 1) * 262144 + h * 8192 : PJ_OOB;
    asm volatile("" : "+s"(so_), "+s"(an_), "+s"(nb));
    u32x4 nx[R == 1 ? 16 : 2];
    SB0();
    if (more) {
      VMW0C();
      *(u32x4*)(smem + C_IMG + gvo) = g;
      if constexpr (R == 1) *(u32x4*)(smem + C_IMG + 4096 + gvo) = gx;
      SB0();
#define G_BAD() (umax(umax(umax(g[0], g[1]), umax(g[2], g[3])), R != 1 ? 0u : umax(umax(gx[0], gx[1]), umax(gx[2], gx[3]))) == ~0u)
      if (__builtin_expect(__any(G_BAD()), 0)) {
        int it = 0;
        do {
          CFENCE();
          if (G_BAD()) {
            g = __builtin_amdgcn_raw_buffer_load_b128(rsX, gvo, mb, 16);
            if constexpr (R == 1) gx = __builtin_amdgcn_raw_buffer_load_b128(rsX, gvo, mb + 16384 - 4096, 16);
          }
          VMW0C();
        } while (__any(G_BAD()) && ++it < SPIN_CAP);
        *(u32x4*)(smem + C_IMG + gvo) = g;
        if constexpr (R == 1) *(u32x4*)(smem + C_IMG + 4096 + gvo) = gx;
      }
    }
    SB0();
    STAMP(15);
    BARC();
    STAMP(0);
#define LDM(j) cat8(trrd(smem + mo + (j) * 1024), trrd(smem + mo1 + (j) * 1024))
    bf8 b[5];
    if constexpr (R == 1) { asm volatile("s_memrealtime %0" : "=s"(tq)); SB0(); }
#pragma unroll
    for (int j = 0; j < 4; j++) { b[j] = LDM(j); SB0(); }
#pragma unroll
    for (int j = 0; j < 9; j++) {
      if (j + 4 < 9) b[(j + 4) % 5] = LDM(j + 4);
      SB0();
      a0 = SMF(b[j % 5], w[0][j], a0, sidx);
      if constexpr (NV < 2) a1 = SMF(b[j % 5], w[1][j], a1, sidx);
      else a1 = SMF(b[j % 5], cat16(pre[j & 3][0], pre[j & 3][1]), a1, sidx);
      SB0();
      if constexpr (R == 1) {
        if (j == ANJ) {
          asm volatile("s_waitcnt lgkmcnt(0)" : "+s"(tq));
          ts = (u32)tq;
          __builtin_amdgcn_raw_buffer_store_b32((((ts << 6) + (u32)dur) & ~63u) | (u32)s, rsX, 0, an_, 16);
          SB0();
        }
        if (j > 0) {
#pragma unroll
          for (int i = 2 * j - 2; i < 2 * j; i++) nx[i] = __builtin_amdgcn_raw_buffer_load_b128(rsB, gvo + (i & 3) * 1024, nb + (1 + (i >> 3)) * ACB + (i & 4) * 1024 - 4096, 16);
          SB0();
        }
      }
      if constexpr (NV == 2) LDW(pre[j & 3], j < 5 ? 10 + j : j - 5);
    }
    {
      const u32 e0 = pk2(a0[0] + a0[2], a0[1] + a0[3]), e1 = pk2(a1[0] + a1[2], a1[1] + a1[3]);
      if constexpr (QW) asm volatile("ds_write2_b32 %0, %1, %2 offset1:96" :: "v"(eo), "v"(e0), "v"(e1) : "memory");
      else asm volatile("ds_write2_b32 %0, %1, %1 offset1:1\n\tds_write2_b32 %0, %2, %2 offset0:128 offset1:129" :: "v"(eo), "v"(e0), "v"(e1) : "memory");
    }
    SB0();
    if constexpr (R != 1) { a0 = zero4(); a1 = zero4(); }
    if constexpr (NV == 0) {
      f4 a2;
      if constexpr (R == 0) a2 = zero4();
      STAMP(1);
      BARC();
      STAMP(2);
      if constexpr (R == 0) {
        bf8 qa[2], km[4];
#pragma unroll
        for (int i = 0; i < 2; i++) { qa[i] = cat8(trrd(smem + qo + i * 1536), trrd(smem + qo + i * 1536 + 128)); SB0(); }
#pragma unroll
        for (int i = 0; i < 4; i++) { km[i] = cat8(trrd(smem + ko + i * 1024), trrd(smem + ko1 + i * 1024)); SB0(); }
#pragma unroll
        for (int i = 0; i < 2; i++) {
          a0 = SMF(qa[i], cat16(pre[i][0], pre[i][1]), a0, sidx); asm volatile("" : "+v"(a0)); SB0();
          a1 = SMF(qa[i], cat16(pre[2 + i][0], pre[2 + i][1]), a1, sidx); asm volatile("" : "+v"(a1)); SB0();
        }
#pragma unroll
        for (int i = 0; i < 2; i++) { a2 = SMF(qa[i], cat16(km[2 * i], km[2 * i + 1]), a2, sidx); asm volatile("" : "+v"(a2)); SB0(); }
        float e0 = a0[0] + a0[2], e1 = a0[1] + a0[3], e2 = a1[0] + a1[2], e3 = a1[1] + a1[3];
        asm volatile("" : "+v"(e0), "+v"(e1), "+v"(e2), "+v"(e3));
        SB0();
        float e4 = a2[0] + a2[2], e5 = a2[1] + a2[3], x0, x1;
        STAMP(3);
        asm volatile("v_max3_f32 %3, %0, %1, %2\n\tv_max3_f32 %4, %5, %6, %7\n\t" DPROW("v_max_f32_dpp")
                     "v_mul_f32 %3, %8, %3\n\tv_mul_f32 %4, %8, %4\n\t"
                     "v_fma_f32 %0, %0, %8, -%3\n\tv_fma_f32 %5, %5, %8, -%4\n\tv_exp_f32 %0, %0\n\tv_exp_f32 %5, %5\n\t"
                     "v_fma_f32 %1, %1, %8, -%3\n\tv_fma_f32 %6, %6, %8, -%4\n\tv_exp_f32 %1, %1\n\tv_exp_f32 %6, %6\n\t"
                     "v_fma_f32 %2, %2, %8, -%3\n\tv_fma_f32 %7, %7, %8, -%4\n\tv_exp_f32 %2, %2\n\tv_exp_f32 %7, %7\n\t"
                     "v_add_f32 %3, %0, %1\n\tv_add_f32 %4, %5, %6\n\tv_fmac_f32 %3, 0.5, %2\n\tv_fmac_f32 %4, 0.5, %7\n\t" DPROW("v_add_f32_dpp")
                     "v_rcp_f32 %3, %3\n\tv_rcp_f32 %4, %4\n\t"
                     "v_mul_f32 %0, %0, %3\n\tv_mul_f32 %5, %5, %4\n\tv_mul_f32 %1, %1, %3\n\tv_cvt_pk_bf16_f32 %0, %0, %5\n\t"
                     "v_mul_f32 %6, %6, %4\n\tv_mul_f32 %2, %2, %3\n\tv_cvt_pk_bf16_f32 %1, %1, %6\n\tv_mul_f32 %7, %7, %4\n\t"
                     "ds_write2st64_b32 %9, %0, %1 offset1:1\n\tv_cvt_pk_bf16_f32 %2, %2, %7\n\tds_write_b32 %9, %2 offset:512"
                     : "+v"(e0), "+v"(e2), "+v"(e4), "=&v"(x0), "=&v"(x1), "+v"(e1), "+v"(e3), "+v"(e5) : "s"(CE), "v"(po) : "memory");
        SB0();
      } else {
        const int rb = X_O + (b3 == 0 ? 2 : b3 - 1) * 131072 + ob - 4096;
        const u32x4 e = ones4();
        __builtin_amdgcn_raw_buffer_store_b128(e, rsX, gvo, rb, 16);
        __builtin_amdgcn_raw_buffer_store_b128(e, rsX, gvo, rb + 65536, 16);
        if (s == 63 && gvo == 4096) st4_sc1(WSP(u32, 0) + SY_SEG(0, h), 1u);
        PSTAMP();
      }
      STAMP(5);
      BARC();
      STAMP(6);
      if constexpr (R == 1) {
        const u32 tp = (u32)wall_clock64(); dur += ((int)umin((tp - ts) << 6, 200u * 64u) - dur) >> DSH;
        SB0();
        const int pv_ = gvo ^ ((gvo >> 2) & 128);
#pragma unroll
        for (int i = 0; i < 8; i++) *(u32x4*)(smem + C_KX - 4096 + gvo + i * 1024) = nx[i];
#pragma unroll
        for (int i = 0; i < 8; i++) *(u32x4*)(smem + C_VX - 4096 + pv_ + i * 1024) = nx[8 + i];
        SB0();
        STAMP(8);
        BARN();
        if (s == 63) break;
      } else {
        bf8 km[4];
#pragma unroll
        for (int ks = 0; ks < 4; ks++) km[ks] = cat8(trrd(smem + ko + ks * 1024), trrd(smem + ko1 + ks * 1024));
#pragma unroll
        for (int NT = 1; NT <= 2; NT++) {
          f4 sc[2], sm;
          bf8 qf[4];
#pragma unroll
          for (int ks = 0; ks < 4; ks++) qf[ks] = *(const bf8*)(smem + C_QX + (NT - 1) * 256 + fo + ks * 2048);
#pragma unroll
          for (int ks = 0; ks < 4; ks++) {
#pragma unroll
            for (int j = 0; j < NT; j++) sc[j] = MFMA(pre[2 * j + (ks >> 1)][ks & 1], qf[ks], ks ? sc[j] : (j == NT - 1 ? ((f4){mk[0], mk[1], mk[2], mk[3]}) : ((f4){0, 0, 0, 0})));
            sm = MFMA(km[ks], qf[ks], ks ? sm : ((f4){0, 0, 0, 0}));
          }
          float mx = FMX3(FMX3(sc[0][0], sc[0][1], sc[0][2]), sc[0][3], FMX(sm[0], sm[1]));
          if (NT == 2) mx = FMX3(FMX3(mx, sc[1][0], sc[1][1]), sc[1][2], sc[1][3]);
          { auto t = __builtin_amdgcn_permlane16_swap(__float_as_uint(mx), __float_as_uint(mx), false, false);
            mx = FMX(__uint_as_float(t[0]), __uint_as_float(t[1])); }
          { auto t = __builtin_amdgcn_permlane32_swap(__float_as_uint(mx), __float_as_uint(mx), false, false);
            mx = FMX(__uint_as_float(t[0]), __uint_as_float(t[1])); }
          mx *= -CE;
#pragma unroll
          for (int j = 0; j < NT; j++)
#pragma unroll
            for (int r = 0; r < 4; r++) sc[j][r] = __builtin_amdgcn_exp2f(__builtin_fmaf(sc[j][r], CE, mx));
          sm[0] = __builtin_amdgcn_exp2f(__builtin_fmaf(sm[0], CE, mx)); sm[1] = __builtin_amdgcn_exp2f(__builtin_fmaf(sm[1], CE, mx));
          float sum = ((sc[0][0] + sc[0][1]) + (sc[0][2] + sc[0][3])) + (sm[0] + sm[1]);
          if (NT == 2) sum += (sc[1][0] + sc[1][1]) + (sc[1][2] + sc[1][3]);
          { auto t = __builtin_amdgcn_permlane16_swap(__float_as_uint(sum), __float_as_uint(sum), false, false);
            sum = __uint_as_float(t[0]) + __uint_as_float(t[1]); }
          sum = swadd(sum, sum);
          const float inv = __builtin_amdgcn_rcpf(sum);
          *(u32x4*)(smem + C_PX + (NT - 1) * 1024 + gvo) = (u32x4){pk2(sc[0][0] * inv, sc[0][1] * inv), pk2(sc[0][2] * inv, sc[0][3] * inv),
                                                                  NT == 2 ? pk2(sc[1][0] * inv, sc[1][1] * inv) : 0u, NT == 2 ? pk2(sc[1][2] * inv, sc[1][3] * inv) : 0u};
          *(u32*)(smem + C_PX + 2048 + (NT - 1) * 256 + (gvo >> 2)) = pk2(sm[0] * inv, sm[1] * inv);
        }
        STAMP(8);
        BAR();
        if (s == 63) break;
        if (known < (s < 61 ? s + 3 : 64)) {
          int n = 0;
          while (true) {
            u32 bf[3];
#pragma unroll
            for (int i = 0; i < 3; i++) bf[i] = ld4_sc1(WSP(u32, 0) + SY_BLK(i, cb, gvo >> 4));
            const u64 miss = __ballot(!(bf[0] >= 8u && bf[1] >= 8u && bf[2] >= 8u));
            known = miss ? __builtin_ctzll(miss) : 64;
            if (known >= (s < 61 ? s + 3 : 64) || ++n >= SPIN_CAP) break;
            __builtin_amdgcn_s_sleep(KSL);
          }
        }
      }
      STAMP(9);
      if constexpr (R == 1) { u32 dl; AD_WAIT(true, K1 + K3, 4096); }
    } else {
      STAMP(1);
      BARC();
      STAMP(2);
#pragma unroll
      for (int i = 0; i < 9; i++) {
        const int j = i < 5 ? i + 4 : i - 5;
        constexpr int PO = NV == 2 ? 2 : 1;
        SB0();
        if constexpr (NV == 2) a0 = SMF(b[(i + 4) % 5], w[1][j], a0, sidx);
        if (i < 3) a1 = SMF(b[(i + 4) % 5], wx[i], a1, sidx);
        else a1 = SMF(b[(i + 4) % 5], cat16(pre[(i + PO) & 3][0], pre[(i + PO) & 3][1]), a1, sidx);
        SB0();
        if (i < 4) b[(i + 4) % 5] = LDM(i);
        if (i == 3 || i == 4) LDW(pre[(i + PO) & 3], 1 + i);
      }
      u32x4 vm[NV];
      vm[NV - 1] = (u32x4){pk2(a1[0] + a1[2], a1[1] + a1[3]), 0u, 0u, 0u};
      if constexpr (NV == 2) vm[0] = (u32x4){pk2(a0[0] + a0[2], a0[1] + a0[3]), 0u, 0u, 0u};
#pragma unroll
      for (int d = 0; d < NV; d++) asm volatile("" : "+v"(vm[d]));
      const bool hi = so & 128;
      SB0();
      STAMP(5);
      BARN();
      STAMP(6);
      {
        const bf8 px = cat8(trrd(smem + po), trrd(smem + po + 256));
        SB0();
        const bf8 pm = cat8(trrd(smem + po + 512), trrd(smem + po + 256));
        SB0();
        f4 o[NV];
#pragma unroll
        for (int d = 0; d < NV; d++) { o[d] = MFMA(vx[d], px, ((f4){0, 0, 0, 0})); asm volatile("" : "+v"(o[d])); SB0(); }
#pragma unroll
        for (int d = 0; d < NV; d++) { o[d] = MFMA(__builtin_bit_cast(bf8, vm[d]), pm, o[d]); asm volatile("" : "+v"(o[d])); SB0(); }
        u32x2 ov = {pk2(o[0][0], o[0][1]), pk2(o[0][2], o[0][3])};
        if constexpr (NV == 2) {
          const u32x2 ow = {pk2(o[1][0], o[1][1]), pk2(o[1][2], o[1][3])};
          if (hi) ov = ow;
        }
        ov[0] = umin(ov[0], 0xFFFFFFFEu);
        __builtin_amdgcn_raw_buffer_store_b64(ov, rsX, so, so_, 16);
        SB0();
      }
      STAMP(7);
      int xo, lo, pa = 0;
      {
        IDX();
        if constexpr (NV == 1) {
          __builtin_amdgcn_s_sleep(LSL);
#pragma unroll
          for (int i = 0; i < 2; i++) nx[i] = __builtin_amdgcn_raw_buffer_load_b128(rsB, lane * 16 + i * 1024, nb + vt * 2048, 16);
          pa = C_QX + vt * 2048 + lane * 16;
          asm volatile("" : "+v"(pa));
        }
        xo = OB_OFF((lq & 1) * 16 + l15, (lq >> 1) * 8);
        lo = lane * 4;
        asm volatile("" : "+v"(xo), "+v"(lo));
#pragma unroll
        for (int j = 0; j < 4; j++) LDW(pre[j], NV == 2 ? 6 + j : j);
      }
      SB0();
      BARN();
      STAMP(8);
      {
        f4 o[2][NV];
#pragma unroll
        for (int it = 0; it < 2; it++) {
          const bf8 pf = *(const bf8*)(smem + C_PX + it * 1024 + lo * 4);
          const u32x4 pm = {*(const u32*)(smem + C_PX + 2048 + it * 256 + lo), 0u, 0u, 0u};
#pragma unroll
          for (int d = 0; d < NV; d++) {
            o[it][d] = MFMA(vx[d], pf, ((f4){0, 0, 0, 0}));
            o[it][d] = MFMA(__builtin_bit_cast(bf8, vm[d]), __builtin_bit_cast(bf8, pm), o[it][d]);
          }
        }
#pragma unroll
        for (int d = 0; d < NV; d++)
          __builtin_amdgcn_raw_buffer_store_b128(pair16(pk2(o[0][d][0], o[0][d][1]), pk2(o[0][d][2], o[0][d][3]), pk2(o[1][d][0], o[1][d][1]), pk2(o[1][d][2], o[1][d][3])), rsB,
              xo, 3 * ACB + OB_OFF(s * 32, h * 128 + (vt + d) * 16), 16);
        SB0();
        if (s == 63) break;
        if constexpr (NV == 1) { *(u32x4*)(smem + pa) = nx[0]; *(u32x4*)(smem + pa + 1024) = nx[1]; }
      }
      STAMP(9);
    }
    b3 = b3 == 2 ? 0 : b3 + 1;
    STAMP(14);
  }
  };
  if (wid == 4) run(std::integral_constant<int, 1>{});
  else if (wid == 0) run(std::integral_constant<int, 0>{});
  else if (wid < 4) run(std::integral_constant<int, 2>{});
  else if (wid == 7) run(std::integral_constant<int, 3>{});
  else run(std::integral_constant<int, 4>{});
  VMW0();
  __syncthreads();
  if (TIDX == 0) st4_sc1(WSP(u32, 0) + SY_SEG(1, h), 1u);
  TLS(0);
}

__device__ __forceinline__ void chain_out(const Params& p, char* smem, const int c, const int wv) {
  STS(32 + c, 1);
  constexpr int R = 5; (void)R;
  const int wid = wv, tq = c & 1, cg = c >> 1;
  u32x8 w[4][5];
  const __amdgpu_buffer_rsrc_t rsX = RSRC(XWIN(p), X_END);
  int xp, mo, wo, ro;
  {
    IDX();
    mo = wid < 4 ? cg * 512 + tq * 256 + ((wid ^ (tq * 2)) << 6) : 16384 + (cg >> 2) * 512 + tq * 256 + (((cg & 3) ^ (tq * 2)) << 6);
    if (wid > 4 || (wid == 4 && cg >= 8)) mo = PJ_OOB;
    mo = __builtin_amdgcn_readfirstlane(mo);
    __builtin_amdgcn_raw_buffer_store_b128(ones4(), rsX, lane < 24 ? X_M + (lane >> 3) * 32768 + ((lane >> 2) & 1) * 8192 + (lane & 3) * 16 : PJ_OOB, mo, 16);
    VMW0();
    BARN();
    if (tid == 0) st4_sc1(WSP(u32, 0) + SY_INIT(32 + c), 1u);
    SB0();
    const __amdgpu_buffer_rsrc_t rw = RSRC(p.BWo, 576 * 4096 * 2);
    u32x4 st[4][8];
    const int va = ((lane >> 2) * 576 + cg * 32 + p4 * 8) * 2;
#pragma unroll
    for (int i = 0; i < 4; i++) {
#pragma unroll
      for (int k = 0; k < 8; k++) st[i][k] = __builtin_amdgcn_raw_buffer_load_b128(rw, va, ((i * 8 + wid) * 128 + k * 16) * 1152, OSTG_AUX);
      SB0();
    }
    char* sc = smem + wid * 10240;
    auto frag = [&](const char* b, int rs) {
      bf4 tt[4];
#pragma unroll
      for (int kk = 0; kk < 4; kk++) {
        const int r0 = ((kk >> 1) * 2 + (lq >> 1)) * 32 + (p4 >> 1) * 16 + (q4 >> 1) * 8 + (lq & 1) * 4 + (kk & 1) * 2 + (q4 & 1);
        tt[kk] = trrd(b + r0 * rs + (p4 & 1) * 8);
      }
      return ilv(tt[0], tt[1], tt[2], tt[3]);
    };
#pragma unroll
    for (int i = 0; i < 4; i++) {
#pragma unroll
      for (int k = 0; k < 8; k++) *(u32x4*)(sc + (k * 16 + (lane >> 2)) * 80 + p4 * 16) = st[i][k];
      __builtin_amdgcn_fence(__ATOMIC_RELEASE, "wavefront");
      __builtin_amdgcn_wave_barrier();
#pragma unroll
      for (int t = 0; t < 4; t++) w[i][t] = frag(sc + t * 16, 80);
      asm volatile("s_waitcnt lgkmcnt(0)" ::: "memory");
      __builtin_amdgcn_wave_barrier();
      SB0();
    }
    TLS(8);
    BARN();
    if (wid == 0) {
      u32 best[2] = {~0u, ~0u};
#pragma unroll 1
      for (int r = 0; r < 3; r++) {
#pragma unroll
        for (int k = 0; k < 2; k++) {
          const u32 t0 = (u32)__builtin_readcyclecounter();
          __builtin_amdgcn_raw_buffer_store_b32((u32)r, rsX, X_CAL + k * 8192 + c * 128, 0, 16);
          VMW0();
          best[k] = umin(best[k], (u32)__builtin_readcyclecounter() - t0);
        }
      }
      if (tid == 0) st4_sc1(WSP(u32, 0) + SY_VOTE(c), best[1] < best[0] ? 2u : 1u);
      TLS(11);
      int it = 0;
      while (c < 16 && ld4_sc1(WSP(u32, 0) + SY_WX) < 32u && ++it < SPIN_CAP) __builtin_amdgcn_s_sleep(GPS);
    }
    BARN();
    {
      const __amdgpu_buffer_rsrc_t rx = RSRC(WSP(char, O_WX), 524288);
#pragma unroll
      for (int k = 0; k < 8; k++) st[0][k] = __builtin_amdgcn_raw_buffer_load_b128(rx, lane * 16, cg * 65536 + (((k >> 1) * 8 + wid) * 128 + (k & 1) * 64) * 16, 16);
#pragma unroll
      for (int k = 0; k < 8; k++) *(u32x4*)(sc + k * 1024 + lane * 16) = st[0][k];
      __builtin_amdgcn_fence(__ATOMIC_RELEASE, "wavefront");
      __builtin_amdgcn_wave_barrier();
#pragma unroll
      for (int i = 0; i < 4; i++) w[i][4] = frag(sc + i * 2048, 16);
    }
    if (wid == 0) {
      u32 fl, fv;
      int it = 0;
      do { fl = ld4_sc1(WSP(u32, 0) + SY_INIT(lane)); fv = ld4_sc1(WSP(u32, 0) + SY_VOTE(lane & 31));
           if (__all(fl != 0u && fv != 0u)) break;
           __builtin_amdgcn_s_sleep(GPS); } while (++it < SPIN_CAP);
      *(int*)(smem + C_XP) = __popcll(__ballot(fv == 2u)) > 32 ? 8192 : 0;
    }
    BAR();
    xp = __builtin_amdgcn_readfirstlane(*(const int*)(smem + C_XP));
    wo = (lq >> 1) == hi8 ? ((l15 & 7) * 2 + (lq & 1)) * 144 + (wid * 2 + hi8) * 8 : 16 * 144 + lane * 8;
    ro = wid * 2880 + (lane >> 2) * 144 + p4 * 32;
  }
  __syncthreads();
  STS(32 + c, 14);
  TLS(9);
  int go, sidx, po;
  { IDX(); go = lane * 16; sidx = (l15 & 2) ? 0xEEEE : 0x4444;
    po = p4 == 0 ? (lane >> 4) * 16 + ((lane >> 2) & 1) * 8 + ((lane >> 3) & 1) * 4 : PJ_OOB; }
#define ONOP 7
  int b3 = 0, dur = 80 * 64;
#pragma unroll 1
  for (int s = 0; s < 63; s++) {
    f4 d[5];
    int gb[4], pb = mo + X_M + (b3 == 2 ? 0 : b3 + 1) * 32768 + xp;
#pragma unroll
    for (int i = 0; i < 4; i++) { gb[i] = X_O + b3 * 131072 + tq * 65536 + wid * 1024 + xp + i * 16384; asm volatile("" : "+s"(gb[i])); }
    asm volatile("" : "+s"(pb));
    u32x4 g[4];
    u32 bad;
    auto mult = [&]() __attribute__((always_inline)) {
      CFENCE();
#pragma unroll
      for (int i = 0; i < 4; i++) {
        g[i] = __builtin_amdgcn_raw_buffer_load_b128(rsX, go, gb[i], 16);
        SB0();
        if (ONOP >= 0 && i < 3) asm volatile("s_nop %0" :: "i"(ONOP >= 0 ? ONOP : 0));
        SB0();
      }
      bad = 0u;
#pragma unroll
      for (int i = 0; i < 4; i++) {
        SB0();
#pragma unroll
        for (int t = 0; t < 5; t++) {
          d[t] = SMF(g[i], w[i][t], d[t], sidx);
          if (t == 0) bad = umax(umax(bad, g[i][0]), g[i][2]);
          if (i == 3 && t >= 2) { SB0(); *(f2v*)(smem + wo + (t - 2) * 2880) = (f2v){d[t - 2][0] + d[t - 2][2], d[t - 2][1] + d[t - 2][3]}; SB0(); }
        }
      }
      SB0();
#pragma unroll
      for (int t = 3; t < 5; t++) *(f2v*)(smem + wo + t * 2880) = (f2v){d[t][0] + d[t][2], d[t][1] + d[t][3]};
      SB0();
    };
#pragma unroll
    for (int t = 0; t < 5; t++) d[t] = zero4();
    SB0();
    u32 dl = 0;
    if (wid == 7) AD_WAIT(false, K1, SPIN_CAP);
    STAMP(15);
    BARN();
    STAMP(0);
    mult();
    if (__builtin_expect(__any(bad == ~0u), 0)) {
      int it = 0;
      do {
#pragma unroll
        for (int t = 0; t < 5; t++) d[t] = zero4();
        mult();
      } while (__any(bad == ~0u) && ++it < SPIN_CAP);
    }
    STAMP(1);
    BARC();
    STAMP(2);
    if (wid == 7) {
      dur += (min((int)(((u32)wall_clock64() << 6) - dl), dur + DOCAP * 64) - dur) >> DSH;
      __builtin_amdgcn_raw_buffer_store_b32((u32)dur, rsX, 0, X_ANN + (~s & 1) * 1024 + xp + (32 + c) * ANS, 16);
    }
    {
      const f4 t0 = *(const f4*)(smem + ro), t1 = *(const f4*)(smem + ro + 16);
      float a = (t0[0] + t0[2]) + (t1[0] + t1[2]), b = (t0[1] + t0[3]) + (t1[1] + t1[3]);
      a += __uint_as_float(__builtin_amdgcn_mov_dpp(__float_as_uint(a), 0xB1, 0xf, 0xf, true));
      b += __uint_as_float(__builtin_amdgcn_mov_dpp(__float_as_uint(b), 0xB1, 0xf, 0xf, true));
      a += __uint_as_float(__builtin_amdgcn_mov_dpp(__float_as_uint(a), 0x4E, 0xf, 0xf, true));
      b += __uint_as_float(__builtin_amdgcn_mov_dpp(__float_as_uint(b), 0x4E, 0xf, 0xf, true));
      __builtin_amdgcn_raw_buffer_store_b32(umin(pk2(a, b), 0xFFFFFFFEu), rsX, po, pb, 16);
      SB0();
      STAMP(3);
      __builtin_amdgcn_s_sleep(4);
      __builtin_amdgcn_raw_buffer_store_b32(~0u, rsX, po, mo + X_M + b3 * 32768 + xp, 16);
    }
    if (wid == 5 && c == 0 && s > 0 && go == 0) st4_sc1(WSP(u32, 0) + SY_CS(s - 1), 1u);
    VMW0C();
    b3 = b3 == 2 ? 0 : b3 + 1;
  }
  __syncthreads();
}

__device__ __forceinline__ void wait_ge(const u32* w, u32 val, const int wv) {
  if (TIDX == 0) {
    int it = 0;
    while (ld4_sc1(w) < val && ++it < SPIN_CAP) __builtin_amdgcn_s_sleep(WPS);
  }
}
__device__ __forceinline__ void acquire_all(const int wv) {
  if (TIDX == 0) {
    __builtin_amdgcn_fence(__ATOMIC_ACQUIRE, "agent");
    VMW0();
  }
  __syncthreads();
}

#define T_KV 0
#define T_Q 0
#define T_A 192
#define T_W (T_A + 64)
#define T_S (T_W + 256)
#define T_L (T_S + 144)
#define T_O T_L
#define T_CHAIN (1 << 20)
#define CODE_KB 84
#define HWARM 112
#define AORD 0x01234567u
#define OORD 0x67014523u

__device__ __forceinline__ int next_task(const Params& p, int* s_task, int xq, int& xk, const int wv, const int pt) {
  __syncthreads();
  if (TIDX == 0) {
    int t = -1;
    while (xk < 6) {
      int x = xq + xk; if (x >= 6) x -= 6;
      const int i = (int)__hip_atomic_fetch_add(WSP(u32, 0) + SY_XQ(x), 1u, __ATOMIC_RELAXED, __HIP_MEMORY_SCOPE_AGENT);
      if (i < 32) { t = x * 32 + i; break; }
      xk++;
    }
    if (t < 0 && xk == 6) {
      xk = 7;
      int it = 0;
      while (ld4_sc1(WSP(u32, 0) + SY_NCL) < 64u && ld4_sc1(WSP(u32, 0) + SY_ARR) < 256u && ++it < SPIN_CAP) __builtin_amdgcn_s_sleep(8);
      if (ld4_sc1(WSP(u32, 0) + SY_NCL) < 64u) {
        for (int k = 0; k < 64; k++)
          if (__hip_atomic_exchange(WSP(u32, 0) + SY_CLAIM(k), 1u, __ATOMIC_RELAXED, __HIP_MEMORY_SCOPE_AGENT) == 0u) {
            __hip_atomic_fetch_add(WSP(u32, 0) + SY_NCL, 1u, __ATOMIC_RELAXED, __HIP_MEMORY_SCOPE_AGENT);
            t = T_CHAIN + k; break;
          }
      }
    }
    if (t < 0) t = T_A + (int)__hip_atomic_fetch_add(WSP(u32, 0) + SY_QHEAD, 1u, __ATOMIC_RELAXED, __HIP_MEMORY_SCOPE_AGENT);
    *s_task = t;
  }
  __syncthreads();
  return __builtin_amdgcn_readfirstlane(*s_task);
}

__device__ __forceinline__ int take_task(int* s_task, const int pre, const int wv, const int pb, const int pl, const int pt) {
  if (TIDX == 0) {
    *s_task = pre;
  }
  __syncthreads();
  const int t = __builtin_amdgcn_readfirstlane(*s_task);
  __syncthreads();
  return t;
}
#if defined(__HIP_DEVICE_COMPILE__) && !defined(KEEP_PK)
#define NOPK __attribute__((target("no-packed-fp32-ops")))
#else
#define NOPK
#endif
#define P0V 2
__device__ __forceinline__ void mega_body(const Params& p, char* smem) {
  u64 pc0; asm volatile("s_getpc_b64 %0" : "=s"(pc0));
  int* const s_task = (int*)(smem + LDS_TOTAL);
  const int wv = __builtin_amdgcn_readfirstlane(threadIdx.x >> 6);
  if (blockIdx.x == 0 && TIDX == 0) asm volatile("s_nop 0" ::: "memory");
#define tid TIDX
  const int xcc = (int)(__builtin_amdgcn_s_getreg(20 | (3 << 11)) & 7u);
  const int xq = xcc >= 2 ? xcc - 2 : xcc;
  int xk = 0, pre = 0;
  u32* const qh = WSP(u32, 0) + SY_QHEAD;
  if (wv) {
    const int g = ((blockIdx.x >> 3) & 31) * 7 + wv - 1, vo = (tid & 63) * 64;
#define WARM(ptr, bytes, so) __builtin_amdgcn_raw_ptr_buffer_load_lds(RSRC(ptr, bytes), (LDSP void*)(smem + LDS_ALL - 2048 + wv * 256), 4, vo, so, 0, 0)
    WARM(pc0, CODE_KB * 1024, g * 4096);
    if (xcc > 1) {
      const u16* w = xq < 2 ? p.BWk : xq < 4 ? p.BWv : p.BWq;
      WARM(p.Bx, 256 * 1152, (223 - g) * 4096);
#pragma unroll
      for (int i = 0; i < 3; i++) WARM(w, WOB, (g + i * 224) * 8192 + (xq & 1) * 4096);
    } else if (xcc == 0) {
      WARM(p.BWq, HWARM * 8192, g * 4096); WARM(p.BWk, HWARM * 8192, g * 4096); WARM(p.BWv, HWARM * 8192, g * 4096);
    }
  }
  if (tid == 0) {
    int t0 = -1;
    if (xcc < 2) {
      const int k = ((blockIdx.x >> 3) & 31) + xcc * 32;
      if (__hip_atomic_exchange(WSP(u32, 0) + SY_CLAIM(k), 1u, __ATOMIC_RELAXED, __HIP_MEMORY_SCOPE_AGENT) == 0u) {
        __hip_atomic_fetch_add(WSP(u32, 0) + SY_NCL, 1u, __ATOMIC_RELAXED, __HIP_MEMORY_SCOPE_AGENT);
        t0 = T_CHAIN + k;
      }
    } else {
      const int i = (int)__hip_atomic_fetch_add(WSP(u32, 0) + SY_XQ(xq), 1u, __ATOMIC_RELAXED, __HIP_MEMORY_SCOPE_AGENT);
      if (i < 32) t0 = xq * 32 + i;
    }
    __hip_atomic_fetch_add(WSP(u32, 0) + SY_ARR, 1u, __ATOMIC_RELAXED, __HIP_MEMORY_SCOPE_AGENT);
    *s_task = t0;
  }
  BAR();
  int t = __builtin_amdgcn_readfirstlane(*s_task);
  if (t < 0) t = next_task(p, s_task, xq, xk, wv, t);
  {
    while (t < T_A) {
      const int x = t >> 5, j = (t & 7) * 4 + ((0x9C >> ((t >> 2) & 6)) & 3);
      TLS(10);
      proj_slot(p, x, j, smem, wv);
      t = next_task(p, s_task, xq, xk, wv, t);
    }
  }
  if (t >= T_CHAIN) { if (t < T_CHAIN + 32) chain_head(p, smem, t - T_CHAIN, wv); else chain_out(p, smem, t - T_CHAIN - 32, wv); xk = 7; t = next_task(p, s_task, xq, xk, wv, -1); }
  while (t < T_W) {
    wof_task(p, t - T_A, smem, wv, qh, pre);
    if (tid == 0) __hip_atomic_fetch_add(WSP(u32, 0) + SY_CW, 1u, __ATOMIC_RELAXED, __HIP_MEMORY_SCOPE_AGENT);
    t = T_A + take_task(s_task, pre, wv, T_A, T_S - T_A, t);
  }
  while (t < T_S) {
    const int qb = (AORD >> (((t - T_W) >> 5) * 4)) & 7, h = (t - T_W) & 31;
    if (tid < 64) {
      int it = 0;
      while (true) {
        const u32 f0 = ld4_sc1(WSP(u32, 0) + SY_BLK(3, h >> 1, tid)), f1 = ld4_sc1(WSP(u32, 0) + SY_BLK(4, h >> 1, tid)), f2 = ld4_sc1(WSP(u32, 0) + SY_BLK(5, h >> 1, tid));
        if (__all(tid >= qb * 8 + 8 || ((tid < qb * 8 || f0 >= 8u) && f1 >= 8u && f2 >= 8u)) || ++it >= SPIN_CAP) break;
        __builtin_amdgcn_s_sleep(8);
      }
    }
    acquire_all(wv);
    fa::attn_block(WSP(char, O_QA) + FR_OFF(qb * 256, h * 128), WSP(char, O_KA) + h * 8192, WSP(char, O_VA) + h * 8192,
                   WSP(char, O_CA) + OB_OFF(qb * 256, h * 128), qb * 256, smem, wv, qh, pre);
    if (tid == 0) __hip_atomic_fetch_add(WSP(u32, 0) + SY_CC(qb), 1u, __ATOMIC_RELAXED, __HIP_MEMORY_SCOPE_AGENT);
    t = T_A + take_task(s_task, pre, wv, T_A, T_S - T_A, t);
  }
  int rest = 64 | (1 << xq);
#pragma unroll 1
  while (rest) {
    const int x = __builtin_ctz(rest);
    rest &= rest - 1;
    if (x == 6) {
      CLAIMS();
      t = take_task(s_task, pre, wv, T_S, 144, -1);
      while ((t & 0xFFFF) < 144) {
        t &= 0xFFFF;
        small_out_task(p, XS0 + t / 72, t & 1, (t % 72) >> 1, smem, wv, pre);
        t = take_task(s_task, pre, wv, T_S, 144, T_S + t);
      }
      rest = t >> 16;
      continue;
    }
    const int part = x & 1, cg = x >> 1, n = part ? XS0 / 2 : 32;
    u32* const qh = WSP(u32, 0) + SY_OQ(x);
    CLAIM();
    t = take_task(s_task, pre, wv, T_O + x * 32, n, -1);
    while (t < n) {
      const int rb = part ? t : ((OORD >> ((t >> 2) * 4)) & 7) * 4 + (t & 3);
      if (tid == 0) {
        const u32* w = WSP(u32, 0) + (part ? SY_CS(rb * 2 + 1) : SY_CC(rb >> 2));
        int it = 0;
        while ((ld4_sc1(w) < (part ? 1u : 32u) || ld4_sc1(WSP(u32, 0) + SY_CW) < 64u) && ++it < SPIN_CAP) __builtin_amdgcn_s_sleep(WPS);
      }
      acquire_all(wv);
      out_tile(WSP(char, part ? O_OB : O_CA) + rb * 2 * 262144, WSP(char, O_WOF) + part * WOB + cg * 12 * 131072,
               p.out + (size_t)rb * 64 * 1152 + part * 576 + cg * 192, smem, wv, qh, pre);
      t = take_task(s_task, pre, wv, T_O + x * 32, n, T_O + x * 32 + t);
    }
  }
  if (xcc == 0) TLS(3); else if (xcc == 1) TLS(4); else TLS(5);
}

#undef tid
__global__ NOPK void __launch_bounds__(512, 2) mega(Params p) {
  __shared__ __attribute__((aligned(16))) char smem[LDS_ALL];
  mega_body(p, smem);
}
extern "C" void kernel_launch(void* const* d_in, const int* in_sizes, int n_in, void* d_out, int out_size, void* d_ws,
                              size_t ws_size, hipStream_t stream) {
  Params p;
  p.Ax = (const u16*)d_in[0]; p.Ac = (const u16*)d_in[1]; p.Awq = (const u16*)d_in[2]; p.Awk = (const u16*)d_in[3];
  p.Awv = (const u16*)d_in[4]; p.Awo = (const u16*)d_in[5]; p.Bx = (const u16*)d_in[6]; p.Bm = (const u16*)d_in[7];
  p.BWq = (const u16*)d_in[8]; p.BWk = (const u16*)d_in[9]; p.BWv = (const u16*)d_in[10]; p.BWo = (const u16*)d_in[11];
  p.out = (u16*)d_out;
  p.ws = (char*)d_ws;
  const size_t syncBytes = SY_WORDS * 4;
  (void)hipMemsetAsync(d_ws, 0, syncBytes, stream);
  mega<<<256, 512, 0, stream>>>(p);
}
```

```cpp
#include <hip/hip_runtime.h>
#include <hip/hip_bf16.h>
#include <stdint.h>
#include <type_traits>

typedef unsigned short u16;
typedef unsigned int u32;
typedef unsigned long long u64;
typedef __attribute__((ext_vector_type(8))) short bf8;
typedef __attribute__((ext_vector_type(4))) short bf4;
typedef __attribute__((ext_vector_type(4))) float f4;
typedef __attribute__((ext_vector_type(2))) float f2v;
typedef __attribute__((ext_vector_type(2))) __bf16 bf2v;
typedef __attribute__((ext_vector_type(4))) unsigned u32x4;
typedef __attribute__((ext_vector_type(2))) unsigned u32x2;
typedef float f32x16 __attribute__((ext_vector_type(16)));
typedef __attribute__((ext_vector_type(8))) unsigned u32x8;
typedef __attribute__((ext_vector_type(8))) __bf16 bh8;
typedef __attribute__((ext_vector_type(16))) __bf16 bh16;
#define LDSP __attribute__((address_space(3)))

__device__ __forceinline__ u32 pk2(float a, float b) {
  f2v x = {a, b};
  bf2v y = __builtin_convertvector(x, bf2v);
  return __builtin_bit_cast(u32, y);
}
__device__ __forceinline__ u32 umin(u32 a, u32 b) { return a < b ? a : b; }
__device__ __forceinline__ u32 umax(u32 a, u32 b) { return a > b ? a : b; }
__device__ __forceinline__ float rbf(float f) { return __uint_as_float(pk2(f, f) << 16); }
#define MFMA(a, b, c) __builtin_amdgcn_mfma_f32_16x16x32_bf16(a, b, c, 0, 0, 0)
#define MFMA16(a, b, c) __builtin_amdgcn_mfma_f32_16x16x16bf16_1k(a, b, c, 0, 0, 0)
#define SMF(a, b, c, idx) __builtin_amdgcn_smfmac_f32_16x16x64_bf16(__builtin_bit_cast(bh8, a), __builtin_bit_cast(bh16, b), c, idx, 0, 0)
__device__ __forceinline__ u32x8 ilv(bf4 a, bf4 b, bf4 c, bf4 d) {
  const u32x2 x = __builtin_bit_cast(u32x2, a), y = __builtin_bit_cast(u32x2, b), z = __builtin_bit_cast(u32x2, c), w = __builtin_bit_cast(u32x2, d);
  return (u32x8){x[0], x[1], y[0], y[1], z[0], z[1], w[0], w[1]};
}
__device__ __forceinline__ u32x8 cat16(bf8 a, bf8 b) {
  const u32x4 x = __builtin_bit_cast(u32x4, a), y = __builtin_bit_cast(u32x4, b);
  return (u32x8){x[0], x[1], x[2], x[3], y[0], y[1], y[2], y[3]};
}
__device__ __forceinline__ float swadd(float a, float b) {
  auto x = __builtin_amdgcn_permlane32_swap(__float_as_uint(a), __float_as_uint(b), false, false);
  return __uint_as_float(x[0]) + __uint_as_float(x[1]);
}
__device__ __forceinline__ bf4 trrd(const char* p) {
  return __builtin_amdgcn_ds_read_tr16_b64_v4i16((LDSP bf4*)(p));
}
__device__ __forceinline__ bf8 cat8(bf4 a, bf4 b) { return __builtin_shufflevector(a, b, 0, 1, 2, 3, 4, 5, 6, 7); }
__device__ __forceinline__ void st8_sc1(void* p, u64 v) { __hip_atomic_store((u64*)p, v, __ATOMIC_RELAXED, __HIP_MEMORY_SCOPE_AGENT); }
__device__ __forceinline__ u32 ld4_sc1(const u32* p) { return __hip_atomic_load(p, __ATOMIC_RELAXED, __HIP_MEMORY_SCOPE_AGENT); }
__device__ __forceinline__ void st4_sc1(u32* p, u32 v) { __hip_atomic_store(p, v, __ATOMIC_RELAXED, __HIP_MEMORY_SCOPE_AGENT); }
#define VMW0() asm volatile("s_waitcnt vmcnt(0)" ::: "memory")
#define CFENCE() asm volatile("" ::: "memory")
#define VMW0C() do { __builtin_amdgcn_s_waitcnt(0x0F70); asm volatile("" ::: "memory"); } while (0)
#define SPIN_CAP (1 << 22)
#define XPS 4
#define SPS 4
#define WPS 8
__device__ __forceinline__ int tid_now(int wv) {
  int l; asm volatile("v_mbcnt_lo_u32_b32 %0, -1, 0\n\tv_mbcnt_hi_u32_b32 %0, -1, %0" : "=v"(l));
  return wv * 64 + l;
}
#define TIDX tid_now(wv)
#define CLAIM() do { if (TIDX == 0) pre = (int)__hip_atomic_fetch_add(qh, 1u, __ATOMIC_RELAXED, __HIP_MEMORY_SCOPE_AGENT); } while (0)

#define TLS(i)
struct Params {
  const u16 *Ax, *Ac, *Awq, *Awk, *Awv, *Awo, *Bx, *Bm, *BWq, *BWk, *BWv, *BWo;
  u16* out;
  char* ws;
};
#define SY_QHEAD 0
#define SY_CC(rb) (64 + (rb) * 16)
#define SY_CW (64 + 8 * 16)
#define SY_OQ(x) (64 + 9 * 16 + (x) * 16)
#define SY_CS(s) (64 + 16 * 16 + (s) * 16)
#define SY_B0 (64 + 80 * 16)
#define SY_INIT(c) (SY_B0 + 64 + (c))
#define SY_VOTE(i) (SY_B0 + 128 + (i))
#define SY_CLAIM(c) (SY_B0 + 192 + (c))
#define SY_XQ(x) (SY_B0 + 256 + (x) * 16)
#define SY_ARR (SY_B0 + 384)
#define SY_NCL (SY_ARR + 16)
#define SY_WX (SY_ARR + 32)
#define BLKS 1
#define SY_BLK(mi, cb, g) (SY_NCL + 48 + (((mi) * 16 + (cb)) * 64 + (g)) * BLKS)
#define SY_WORDS (SY_NCL + 48 + 6 * 16 * 64 * BLKS)

#define AL256(x) (((x) + 255) & ~255)
#define O_SYNC_END (SY_WORDS * 4)
#define ACB (2048 * 4096 * 2)
#define O_QA AL256(O_SYNC_END)
#define O_KA (O_QA + ACB)
#define O_VA (O_KA + ACB)
#define O_CA (O_VA + ACB)
#define O_QB (O_CA + ACB)
#define O_KB (O_QB + ACB)
#define O_VB (O_KB + ACB)
#define O_OB (O_VB + ACB)
#define O_WOF (O_OB + ACB)
#define WOB (576 * 4096 * 2)
#define O_WX (O_WOF + 2 * WOB)
#define O_XW (O_WX + 524288)
#define X_O 16384
#define X_M (X_O + 3 * 131072)
#define X_ANN (X_M + 3 * 32768)
#define X_CAL (X_ANN + 16384)
#define X_END (X_CAL + 16384)
#define OB_OFF(r, c) (((r) >> 5) * 262144 + ((c) >> 4) * 1024 + ((r) & 31) * 32 + ((c) & 15) * 2)
#define FR_OFF(r, c) (((r) >> 5) * 262144 + ((c) >> 3) * 512 + ((r) & 31) * 16 + ((c) & 7) * 2)
#define WSP(T, off) ((T*)(p.ws + (off)))
#define XWIN(p) (((uintptr_t)(p).ws + O_XW + 0x1FFFFFu) & ~(uintptr_t)0x1FFFFFu)

#define XS0 52
#define LDS_TOTAL (131072 + 8192 + 16384)
#define LDS_ALL 163840

#define SB0() __builtin_amdgcn_sched_barrier(0)
#define PJ_OOB 0x7F000000
#define RSRC(ptr, bytes) __builtin_amdgcn_make_buffer_rsrc((void*)(ptr), 0, (bytes), 0x00020000)
__device__ __forceinline__ bf8 ldsc1(__amdgpu_buffer_rsrc_t r, int off) {
  return __builtin_bit_cast(bf8, __builtin_amdgcn_raw_buffer_load_b128(r, off, 0, 16));
}
__device__ __forceinline__ u32x4 pair16(u32 a0, u32 a1, u32 b0, u32 b1) {
  auto r0 = __builtin_amdgcn_permlane16_swap(a0, b0, false, false);
  auto r1 = __builtin_amdgcn_permlane16_swap(a1, b1, false, false);
  return (u32x4){r0[0], r1[0], r0[1], r1[1]};
}
__device__ __forceinline__ int fsw(int k) { return (k & 3) | (((k >> 3) & 1) << 2); }

#define C_PARK 131072
#define PJ_PAD 0
__device__ __forceinline__ void proj_slot(const Params& p, int x, int j, char* smem, const int wv) {
  const int tid = TIDX, lane = tid & 63, wid = wv;
  const int l15 = lane & 15, lq = lane >> 4, q4 = l15 >> 2, p4 = lane & 3, wr = wid >> 2, wc = wid & 3;
  const __amdgpu_buffer_rsrc_t rc = RSRC(p.ws, O_XW);
  const int vB = ((wid * 4 + lq) * 4096 + (((l15 >> 1) ^ (lq | (((wid >> 1) & 1) << 2))) * 16 + (lane & 1) * 8)) * 2, vB1 = vB + 32 * 8192;
  int ao = (wr * 64 + l15) * 128 + ((lq ^ (l15 & 7)) << 4);
  int bo = (int)(uintptr_t)smem + 65536 + (lq * 8 + q4) * 256 + (((wc * 2) ^ (q4 | ((lq & 1) << 2))) << 5) + p4 * 8;
  const int vD = lane * 16;
  const int cb = (x & 1) * 8 + (j >> 2);
  __amdgpu_buffer_rsrc_t rsA, rsB;
  const u16 *pA0 = p.Bx, *pB0 = (x < 2 ? p.BWk : x < 4 ? p.BWv : p.BWq) + cb * 256;
  const u16 *pA1 = x < 2 ? p.Ax : p.Ac, *pB1 = (x < 2 ? p.Awq : x < 4 ? p.Awk : p.Awv) + cb * 256;
  asm volatile("" : "+s"(pA0), "+s"(pB0), "+s"(pA1), "+s"(pB1));
  int k2, vA, vA1, sa, sb, nvA;
#define PJ_K2(u) ((u) < 2 || x < 2 ? 1152 : 1024)
#define PJ_PREP(u) do { int ln_ = vD; asm volatile("" : "+v"(ln_)); \
    nvA = (wid * 8 + (ln_ >> 7)) * PJ_K2(u) + ((ln_ ^ (ln_ >> 3)) & 0x70); \
    asm volatile("" : "+v"(nvA)); } while (0)
#define PJ_UNIT(u) do { k2 = PJ_K2(u); rsA = RSRC((u) < 2 ? pA0 : pA1, 2048 * k2); rsB = RSRC((u) < 2 ? pB0 : pB1, k2 * 4096 - cb * 512); vA = nvA; vA1 = nvA + 64 * k2; \
    sa = ((j & 3) + ((u) & 1) * 4) * 256 * k2 - 128; sb = 0; } while (0)
#define PJ_DEST(u, rt) do { const int mi_ = ((u) >> 1) * 3 + (x >> 1), g_ = ((rt) + ((u) & 1) * 4) * 8; \
    dco = O_QA + (mi_ == 2 ? 4 : (mi_ + 5) & 7) * ACB + cb * 16384 + wc * 2048 + (g_ + wr * 2) * 262144; \
    dfl = SY_BLK(mi_, cb, g_) * 4; } while (0)
#define PJ_STAGE(dst, rs, v0, v1, so) do { const int so_ = (so); \
    __builtin_amdgcn_raw_ptr_buffer_load_lds(rs, (LDSP void*)(smem + (dst) + wid * 1024), 16, v0, so_, 0, 0); \
    __builtin_amdgcn_raw_ptr_buffer_load_lds(rs, (LDSP void*)(smem + (dst) + wid * 1024 + 8192), 16, v1, so_, 0, 0); } while (0)
#define PJ_SA(dst, so) PJ_STAGE(dst, rsA, vA, vA1, so)
#define PJ_SB(dst, so) PJ_STAGE(dst, rsB, vB, vB1, so)
#define PJ_LDA(off) do { _Pragma("unroll") for (int m = 0; m < 4; m++) _Pragma("unroll") for (int k = 0; k < 2; k++) \
    At[m][k] = *(const bf8*)(smem + (ao ^ (k * 64)) + (off) + m * 2048); } while (0)
#define PJ_TR(dst, a, off) asm volatile("ds_read_b64_tr_b16 %0, %1 offset:%2" : "=&v"(dst) : "v"(a), "i"(off) : "memory")
#define PJ_LDB(D, h) do { const int b1_ = bo ^ 32; _Pragma("unroll") for (int n = 0; n < 2; n++) _Pragma("unroll") for (int k = 0; k < 2; k++) { \
    bf4 lo_, hi_; PJ_TR(lo_, n ? b1_ : bo, (h) * 16384 + k * 8192); PJ_TR(hi_, n ? b1_ : bo, (h) * 16384 + k * 8192 + 1024); D[n][k] = cat8(lo_, hi_); } } while (0)
#define PJ_CV(ai, bj, q) ((u32x4){pk2(acc[ai][bj][(q) & 2][(q) & 1][0], acc[ai][bj][(q) & 2][(q) & 1][1]), pk2(acc[ai][bj][(q) & 2][(q) & 1][2], acc[ai][bj][(q) & 2][(q) & 1][3]), \
                                  pk2(acc[ai][bj][((q) & 2) + 1][(q) & 1][0], acc[ai][bj][((q) & 2) + 1][(q) & 1][1]), pk2(acc[ai][bj][((q) & 2) + 1][(q) & 1][2], acc[ai][bj][((q) & 2) + 1][(q) & 1][3])})
#define PJ_SW(v) pair16(v[0], v[1], v[2], v[3])
#define PJ_MMA(Z, ai, bj, D, F) do { PJ_SCHED; __builtin_amdgcn_s_setprio(1); \
    _Pragma("unroll") for (int k = 0; k < 2; k++) _Pragma("unroll") for (int m = 0; m < 4; m++) _Pragma("unroll") for (int n = 0; n < 2; n++) { \
      if (k == 0 && (Z)) asm volatile("v_mfma_f32_16x16x32_bf16 %0, %1, %2, 0" : "+v"(acc[ai][bj][m][n]) : "v"(D[n][k]), "v"(At[m][k])); \
      else asm volatile("v_mfma_f32_16x16x32_bf16 %0, %1, %2, %0" : "+v"(acc[ai][bj][m][n]) : "v"(D[n][k]), "v"(At[m][k])); \
      F(k * 8 + m * 2 + n); } \
    __builtin_amdgcn_s_setprio(0); if (PJ_PAD) __builtin_amdgcn_s_sleep(PJ_PAD); PJ_SCHED; } while (0)
#define PJ_SWI(a, b) asm volatile("v_permlane16_swap_b32 %0, %1" : "+v"(a), "+v"(b))
#define PJ_CVI(d, s, e) asm volatile("v_cvt_pk_bf16_f32 %0, %1, %2" : "=v"(d) : "v"(s[e]), "v"(s[(e) + 1]))
#define PJ_PACK(P, ai, bj, i) do { const int q_ = (i) >> 2; \
    if (((i) & 3) < 2) { PJ_CVI(P[q_][(i) & 1], acc[ai][bj][q_ & 2][q_ & 1], ((i) & 1) * 2); PJ_CVI(P[q_][2 + ((i) & 1)], acc[ai][bj][(q_ & 2) + 1][q_ & 1], ((i) & 1) * 2); } \
    else PJ_SWI(P[q_][(i) & 1], P[q_][2 + ((i) & 1)]); } while (0)
#define PJ_WV(n) asm volatile("s_waitcnt vmcnt(" #n ")" ::: "memory")
#define PJ_WL(n) asm volatile("s_waitcnt lgkmcnt(" #n ")" ::: "memory")
#define PJ_WL0 do { __builtin_amdgcn_s_waitcnt(0xC07F); asm volatile("" ::: "memory"); } while (0)
#define PJ_BAR __builtin_amdgcn_s_barrier()
#define PJ_SCHED __builtin_amdgcn_sched_barrier(0)
#define PJ_NOP do { PJ_SCHED; asm volatile("s_nop 7"); PJ_SCHED; } while (0)
#define PJ_DS(v, ai, bj, q) do { PJ_SCHED; __builtin_amdgcn_raw_buffer_store_b128(v, rc, vD + ((q) & 1) * 1024, dco + ((ai) * 4 + ((q) >> 1)) * 262144 + (bj) * 8192, 16); \
    asm volatile("s_nop 1"); PJ_SCHED; } while (0)
#define PJ_PARK(q) (*(u32x4*)(smem + C_PARK + wid * 4096 + vD + (q) * 1024))
#define PJ_PUB(ai, n) do { if (wid == 4) __builtin_amdgcn_raw_buffer_store_b32(8u, rc, vD < (n) * 16 ? vD >> 2 : PJ_OOB, dfl + (ai) * 16, 16); } while (0)
#define PJ_ST()
#define PJ_TOUCH(ptr, bytes, vo, so) __builtin_amdgcn_raw_ptr_buffer_load_lds(RSRC(ptr, bytes), (LDSP void*)(smem + C_PARK + wid * 4096), 4, vo, so, 0, 0)
#define PJ_WARM(S) do { int vt_ = vD; asm volatile("" : "+v"(vt_)); vt_ <<= 2; \
    const int g_ = u > 1 || !(j & 3) ? PJ_OOB : ((((x & 1) * 8 + (j >> 2)) * 3 + (j & 3) - 1) * 8 + wid) * 4096, h_ = g_ + (x >> 1) * 384 * 4096; \
    const u16* w_ = pB1 - cb * 256; \
    if (S == 2) PJ_TOUCH(u ? w_ : pA0 + 1024 * 576, u ? 4096 * PJ_K2(2) : 1024 * 1152, vt_, u ? g_ + 384 * 4096 : h_); \
    if (S == 3) PJ_TOUCH(u ? w_ : pA1, (u ? 4096 : 2048) * PJ_K2(2), vt_, u ? g_ + 768 * 4096 : g_); \
    if (S == 4) PJ_TOUCH(u ? p.Awo : pA1, u ? WOB : 2048 * PJ_K2(2), vt_, u ? h_ : g_ + 384 * 4096); \
    if (S == 5) PJ_TOUCH(w_, 4096 * PJ_K2(2), vt_, u ? PJ_OOB : g_); } while (0)
#define PJ_F0(i)
#define PJ_F1(i) do { if (S == 8) PJ_PACK(pa, 0, 0, i); } while (0)
#define PJ_F2(i) do { if (S == 8) PJ_PACK(pb, 0, 1, i); if (S == 0) PJ_PACK(pb, 1, 1, i); } while (0)
#define PJ_F3(i) do { if (S == 8) PJ_PACK(pa, 1, 0, i); if (S == 5 && (i) == 0) { PJ_SCHED; PJ_PREP(u < 3 ? u + 1 : 3); PJ_SCHED; } } while (0)
#define PJ_STEP(SS) do { constexpr int S = SS; PJ_ST(); \
    PJ_LDB(B0, 0); PJ_SCHED; PJ_LDA(0); PJ_SA((bsel ^ 32768) + 16384, sa + 128 * k2); \
    PJ_WL(8); PJ_BAR; PJ_WL0; PJ_MMA(S == 0, 0, 0, B0, PJ_F0); PJ_BAR; PJ_SCHED; \
    if (S == 7) PJ_UNIT(u < 3 ? u + 1 : 3); \
    PJ_LDB(B1, 1); PJ_SB(65536 + bsel, sb); \
    if (S == 0) { PJ_DS(pb[2], 0, 1, 2); PJ_DS(pb[3], 0, 1, 3); } \
    if (S >= 2 && S < 6) PJ_WARM(S); \
    PJ_BAR; PJ_WL0; PJ_MMA(S == 0, 0, 1, B1, PJ_F1); PJ_BAR; \
    PJ_LDA(16384); sa += 128; PJ_SA(bsel, sa); \
    if (S == 8) { PJ_SCHED; PJ_DEST(u, j & 3); _Pragma("unroll") for (int q = 0; q < 4; q++) PJ_PARK(q) = pa[q]; PJ_SCHED; } \
    PJ_BAR; PJ_WL0; PJ_MMA(S == 0, 1, 0, B0, PJ_F2); PJ_BAR; PJ_SCHED; \
    { u32x4 c0_, c1_; \
      if (S < 2) { c0_ = PJ_PARK(2 * S); c1_ = PJ_PARK(2 * S + 1); PJ_SCHED; } \
      PJ_SB(65536 + bsel + 16384, sb + 256); sb += 64 * 8192; \
      if (S == 8) { PJ_DS(pb[0], 0, 1, 0); PJ_DS(pb[1], 0, 1, 1); } \
      else if (S < 2) { PJ_DS(c0_, 0, 0, (2 * S) & 3); PJ_DS(c1_, 0, 0, (2 * S + 1) & 3); } \
      else if (S < 4) { PJ_DS(pa[(2 * S) & 3], 1, 0, (2 * S) & 3); PJ_DS(pa[(2 * S + 1) & 3], 1, 0, (2 * S + 1) & 3); } \
      else if (S < 6) { PJ_DS(pb[(2 * S) & 3], 1, 1, (2 * S) & 3); PJ_DS(pb[(2 * S + 1) & 3], 1, 1, (2 * S + 1) & 3); } } \
      \
    if (S == 0) PJ_WV(10); else if (S >= 2 && S < 6) PJ_WV(9); else if (S == 1 || S == 8) PJ_WV(8); else PJ_WV(6); \
    PJ_BAR; \
    if (S == 2) PJ_PUB(0, 4); \
    if (S == 7) PJ_PUB(1, 4); \
    PJ_MMA(S == 0, 1, 1, B1, PJ_F3); PJ_BAR; \
    bsel ^= 32768; ao ^= 32768; bo ^= 32768; } while (0)
  f4 acc[2][2][4][2];
  bf8 At[4][2], B0[2][2], B1[2][2];
  u32x4 pa[4], pb[4];
  int bsel = 0, dco = PJ_OOB, dfl = PJ_OOB, u = 0;
  auto quick = [&](auto NBC, const int hf, const int ch) __attribute__((always_inline)) {
    constexpr int NB = decltype(NBC)::value, NS = 5 - NB;
#define PJ_FS(t) do { PJ_SA(((t) % NS) * 16384, hf * 128 * 1152 + (t) * 128); \
      _Pragma("unroll") for (int h_ = 0; h_ < NB; h_++) PJ_SB(NS * 16384 + (((t) % NS) * NB + h_) * 16384, (t) * 64 * 8192 + (ch + h_) * 256); PJ_SCHED; } while (0)
#pragma unroll
    for (int t = 0; t < NS - 1; t++) PJ_FS(t);
    bo += NS * 16384 - 65536;
#pragma unroll
    for (int t = 0; t < 9; t++) {
      PJ_ST();
      if (t == 8) PJ_WV(0); else if (NB == 2) PJ_WV(6); else if (t == 7) PJ_WV(4); else PJ_WV(8);
      PJ_BAR; PJ_SCHED;
      if (wr && t + NS - 1 < 9) PJ_FS(t + NS - 1);
      PJ_LDB(B0, 0); PJ_LDA((t % NS) * 16384);
      if (NB == 2) PJ_LDB(B1, 1);
      PJ_SCHED;
      bo += t == 8 ? 65536 - NS * 16384 - (8 % NS) * NB * 16384 : (t % NS == NS - 1 ? -(NS - 1) * NB * 16384 : NB * 16384);
      PJ_WL0;
      PJ_MMA(t == 0, 0, 0, B0, PJ_F0);
      if (NB == 2) PJ_MMA(t == 0, 0, 1, B1, PJ_F0);
      if (!wr && t + NS - 1 < 9) PJ_FS(t + NS - 1);
    }
    PJ_NOP;
#pragma unroll
    for (int q = 0; q < 4 * NB; q++) { const u32x4 v = PJ_CV(0, q >> 2, q & 3); PJ_DS(PJ_SW(v), hf, ch + (q >> 2), q & 3); }
    PJ_WV(0); PJ_BAR;
    if (tid < 4) __hip_atomic_fetch_add(WSP(u32, dfl) + hf * 4 + tid, 4u * NB, __ATOMIC_RELAXED, __HIP_MEMORY_SCOPE_AGENT);
  };
  if (((j + 1) & 3) < 2) {
    PJ_PREP(0); PJ_UNIT(0); PJ_DEST(0, 0);
    quick(std::integral_constant<int, 1>{}, 0, j & 1);
    if ((j & 3) == 0) { quick(std::integral_constant<int, 2>{}, 1, 0); u = 1; }
    dco = PJ_OOB; dfl = PJ_OOB;
  }
#pragma unroll
  for (int a = 0; a < 16; a++) asm volatile("" : "=v"(acc[a >> 3][(a >> 2) & 1][a & 3][0]), "=v"(acc[a >> 3][(a >> 2) & 1][a & 3][1]));
#pragma unroll
  for (int a = 0; a < 4; a++) asm volatile("" : "=v"(pa[a]), "=v"(pb[a]));
  PJ_PREP(u); PJ_UNIT(u);
  const int cs = x < 4 && (j & 3) == 2 ? (x * 8 + (j >> 2)) * 128 : -1;
#pragma unroll
  for (int i = 0; i < 2; i++) pa[i] = __builtin_amdgcn_raw_buffer_load_b128(RSRC(p.BWo, WOB), (tid >> 3) * 1152 + (tid & 7) * 16, cs >= 0 ? (cs + i * 64) * 1152 + 1024 : PJ_OOB, 0);
  PJ_SCHED;
  PJ_SB(65536, 0); PJ_SA(0, sa + 128);
  PJ_SB(65536 + 16384, 256); PJ_SA(16384, sa + 128 + 128 * k2);
  PJ_SCHED;
#pragma unroll
  for (int i = 0; i < 2; i++) __builtin_amdgcn_raw_buffer_store_b128(pa[i], rc, (tid & 7) * 65536 + (tid >> 3) * 16, cs >= 0 ? O_WX + (cs + i * 64) * 16 : PJ_OOB, 16);
  PJ_SCHED;
  if (wr == 1) PJ_BAR;
  PJ_WV(6); PJ_BAR;
  PJ_SB(65536 + 32768, 64 * 8192); PJ_SA(32768, sa + 256);
  PJ_SB(65536 + 32768 + 16384, 64 * 8192 + 256);
  PJ_WV(6); PJ_BAR;
  if (tid == 256 && cs >= 0) __hip_atomic_fetch_add(WSP(u32, 0) + SY_WX, 1u, __ATOMIC_RELAXED, __HIP_MEMORY_SCOPE_AGENT);
  sa += 256; sb = 128 * 8192;
#pragma unroll 1
  for (; u < 4; u++) {
    PJ_STEP(0); PJ_STEP(1); PJ_STEP(2); PJ_STEP(3); PJ_STEP(4); PJ_STEP(5);
    int more = k2 - 1024; asm volatile("" : "+s"(more));
    if (more) PJ_STEP(6);
    PJ_STEP(7); PJ_STEP(8);
  }
  PJ_ST();
  PJ_NOP;
  PJ_DS(pb[2], 0, 1, 2); PJ_DS(pb[3], 0, 1, 3);
#pragma unroll
  for (int q = 0; q < 4; q++) pb[q] = PJ_PARK(q);
#pragma unroll
  for (int q = 0; q < 4; q++) PJ_DS(pa[q], 1, 0, q);
#pragma unroll
  for (int q = 0; q < 4; q++) PJ_DS(pb[q], 0, 0, q);
#pragma unroll
  for (int q = 0; q < 4; q++) { const u32x4 v = PJ_CV(1, 1, q); PJ_DS(PJ_SW(v), 1, 1, q); }
  PJ_WV(0);
  if (wr == 0) PJ_BAR;
  PJ_BAR;
  PJ_PUB(0, 8);
  PJ_ST();
}

namespace fa {
constexpr float SCALE = 0.08838834764831845f;
constexpr int NW = 8, QBLK = 32, KVBLK = 64, QB = NW * QBLK, D = 128;
constexpr int SHM_V = KVBLK * D * 2, SHM_K = KVBLK * D * 2;
#define SBAR() __builtin_amdgcn_sched_barrier(0)
#define MF32(a, b, c) __builtin_amdgcn_mfma_f32_32x32x16_bf16(a, b, c, 0, 0, 0)
#define FMX3(a, b, c) __builtin_elementwise_maximum(__builtin_elementwise_maximum(a, b), c)
#define AT_PAD 0
#define EBAR 14
#define MX0 3
#define ATHR 8.f
#define fadd1(a, b) ((a) + (b))
#define fmul1(a, b) ((a) * (b))
__device__ __forceinline__ void attn_block(const char* __restrict__ Qp, const char* __restrict__ Kh, const char* __restrict__ Vh,
                                           char* __restrict__ Op, int P0, char* lds, const int wv, u32* qh, int& pre) {
  const int tid = TIDX, wid = wv, lane = tid & 63, r32 = lane & 31, hi = lane >> 5, grp = wid >> 2;
  const int NT = (P0 + QB) / KVBLK;
  const int rb = wid ^ 4;
  const int TL = NT - 4 + (rb >> 1);
  const int rem = 2 * (NT - TL) - 2 - grp;
  float l_reg = 0.f;
  f32x16 o[4] = {}, s0, s1, nm = {};
  int ko = lane * 16;
  int vo = 4 * SHM_K + ((lane >> 4) & 1) * (1024 + 128) + ((lane >> 1) & 1) * 512 + ((hi ^ ((lane >> 1) & 1)) << 6) + ((lane >> 2) & 3) * 16 + (lane & 1) * 8;
  const __amdgpu_buffer_rsrc_t rk = RSRC(Kh, (2 * NT - 1) * 262144 + 8192), rv = RSRC(Vh, (2 * NT - 1) * 262144 + 8192);
  const int gk = tid * 16, gv = gk ^ (((tid >> 5) & 3) << 6);
#define DMA(rs, go, dst, so) __builtin_amdgcn_raw_ptr_buffer_load_lds(rs, (LDSP void*)(lds + (dst) + wid * 1024), 16, go, so, 0, 0)
#define ISSUE_K(t) do { const int s_ = (t) * 524288, d_ = ((t) & 3) * SHM_K; DMA(rk, gk, d_, s_); DMA(rk, gk, d_ + 8192, s_ + 262144); } while (0)
#define ISSUE_V(t) do { const int s_ = (t) * 524288, d_ = 4 * SHM_K + ((t) & 3) * SHM_V; DMA(rv, gv, d_, s_); DMA(rv, gv, d_ + 8192, s_ + 262144); } while (0)
#define PHASE(n) do { SBAR(); asm volatile("s_waitcnt vmcnt(" #n ")" ::: "memory"); __builtin_amdgcn_s_barrier(); asm volatile("" ::: "memory"); SBAR(); } while (0)
#define LDK(d0) do { kf[(d0) % 3][0] = *(const bf8*)(lds + ko + (d0) * 1024); kf[(d0) % 3][1] = *(const bf8*)(lds + ko + (d0) * 1024 + 8192); } while (0)
#define LDV(i) do { const int o_ = ((i) & 3) * 2048 + (((i) >> 2) & 1) * 256 + ((i) >> 3) * 8192; vf[(i) & 3] = cat8(trrd(lds + vo + o_), trrd(lds + vo1 + o_)); } while (0)
#define AST(i)
#define AP(i)
  AP(0);
#define QK(NEXT) do { _Pragma("unroll") for (int d0 = 0; d0 < 8; ++d0) { \
      if (d0 + 2 < 8) LDK(d0 + 2); else { NEXT(2 * (d0 - 6)); NEXT(2 * (d0 - 6) + 1); } \
      SBAR(); \
      if (d0) { s0 = MF32(kf[d0 % 3][0], qr[d0], s0); s1 = MF32(kf[d0 % 3][1], qr[d0], s1); } \
      else { asm volatile("v_mfma_f32_32x32x16_bf16 %0, %1, %2, %3" : "+v"(s0) : "v"(kf[0][0]), "v"(qr[0]), "v"(nm)); \
             asm volatile("v_mfma_f32_32x32x16_bf16 %0, %1, %2, %3" : "+v"(s1) : "v"(kf[0][1]), "v"(qr[0]), "v"(nm)); } \
      SBAR(); } } while (0)
#define NOV(i)
#define MAXS(i) do { \
      if ((i) == 0) { mx = FMX3(s0[0], s0[1], s0[2]); my = FMX3(s1[0], s1[1], s1[2]); } \
      else if ((i) < 7) { mx = FMX3(mx, s0[2 * (i) + 1], s0[2 * (i) + 2]); my = FMX3(my, s1[2 * (i) + 1], s1[2 * (i) + 2]); } \
      else { mx = FMX3(mx, s0[15], s1[15]); mx = __builtin_elementwise_maximum(mx, my); asm volatile("" : "+v"(mx)); } } while (0)
  float mx, my;
  constexpr float C2 = 1.4426950408889634f * SCALE;
  bf8 qr[8], kf[3][2], vf[4], pa[4];
#pragma unroll
  for (int d0 = 0; d0 < 8; ++d0) qr[d0] = *(const bf8*)(Qp + rb * 262144 + d0 * 1024 + lane * 16);
  SBAR();
  ISSUE_K(0); ISSUE_K(1); ISSUE_V(0); ISSUE_K(2); ISSUE_V(1);
  SBAR();
  asm volatile("" : "=v"(s0), "=v"(s1), "+v"(nm), "+v"(o[0]), "+v"(o[1]), "+v"(o[2]), "+v"(o[3]));
  SBAR();
#pragma unroll
  for (int d0 = 0; d0 < 8; ++d0) {
    u32x4 q = __builtin_bit_cast(u32x4, qr[d0]);
#pragma unroll
    for (int i = 0; i < 4; i++) q[i] = pk2(__uint_as_float(q[i] << 16) * C2, __uint_as_float(q[i] & 0xFFFF0000u) * C2);
    qr[d0] = __builtin_bit_cast(bf8, q);
    asm volatile("" : "+v"(qr[d0]));
  }
  AP(8);
  PHASE(8);
  AP(1);
  if (grp) PHASE(6);
  LDK(0); LDK(1);
  QK(NOV);
#pragma unroll
  for (int i = 0; i < 8; ++i) MAXS(i);
  AP(2);
  PHASE(4);
  AP(3);
  auto tile = [&](const int t, auto LASTC) __attribute__((always_inline)) {
    constexpr bool LAST = decltype(LASTC)::value;
    AST(0);
    ISSUE_K(t + 3); ISSUE_V(t + 2);
    SBAR();
    if constexpr (LAST) {
      const int dq = r32 - 4 * hi;
      if (rb & 1) {
#pragma unroll
        for (int r = 0; r < 16; ++r) { if (dq < (r & 3) + 8 * (r >> 2)) s1[r] = -__builtin_inff(); SBAR(); }
      } else {
#pragma unroll
        for (int r = 0; r < 16; ++r) { if (dq < (r & 3) + 8 * (r >> 2)) s0[r] = -__builtin_inff(); s1[r] = -__builtin_inff(); SBAR(); }
      }
#pragma unroll
      for (int i = 0; i < 8; ++i) MAXS(i);
    }
    {
      if (__builtin_expect(t == 0 || __any(mx > ATHR), 0)) {
        auto rr = __builtin_amdgcn_permlane32_swap(__float_as_uint(mx), __float_as_uint(mx), false, false);
        mx = __builtin_elementwise_maximum(__uint_as_float(rr[0]), __uint_as_float(rr[1]));
        if (t) mx = __builtin_elementwise_maximum(mx, 0.f);
        const float alpha = t ? __builtin_amdgcn_exp2f(-mx) : 1.f, nn = nm[0] - mx;
        mx = -mx;
#pragma unroll
        for (int r = 0; r < 16; ++r) { s0[r] = fadd1(s0[r], mx); s1[r] = fadd1(s1[r], mx); nm[r] = nn; }
#pragma unroll
        for (int d = 0; d < 4; ++d)
#pragma unroll
          for (int r = 0; r < 16; ++r) o[d][r] = fmul1(o[d][r], alpha);
        l_reg *= alpha;
        asm volatile("" : "+v"(nm));
      }
    }
    AST(1);
    float x[32], sb = 0.f;
    u32 pw[16];
#pragma unroll
    for (int r = 0; r < 18; ++r) {
      SBAR();
      if (r < 16) { x[2 * r] = __builtin_amdgcn_exp2f(r < 8 ? s0[2 * r] : s1[2 * r - 16]); x[2 * r + 1] = __builtin_amdgcn_exp2f(r < 8 ? s0[2 * r + 1] : s1[2 * r - 15]); }
      if (r >= 2) {
        l_reg = fadd1(l_reg, x[2 * r - 4]);
        sb = r == 2 ? x[1] : fadd1(sb, x[2 * r - 3]);
        pw[r - 2] = pk2(x[2 * r - 4], x[2 * r - 3]); }
    }
    SBAR();
    l_reg = fadd1(l_reg, sb);
#pragma unroll
    for (int i = 0; i < 4; ++i) pa[i] = __builtin_bit_cast(bf8, (u32x4){pw[4 * i], pw[4 * i + 1], pw[4 * i + 2], pw[4 * i + 3]});
    asm volatile("" : "+v"(pa[0]), "+v"(pa[1]), "+v"(pa[2]), "+v"(pa[3]), "+v"(l_reg));
    ko ^= (t & 1) ? 3 * SHM_K : SHM_K;
    const int vo1 = vo ^ 128;
    SBAR();
    AST(3);
    if constexpr (!LAST) { LDK(0); LDK(1); } else { LDV(0); LDV(1); LDV(2); LDV(3); }
    AST(4);
    PHASE(4);
    AST(5);
    __builtin_amdgcn_s_setprio(1);
    if constexpr (!LAST) QK(LDV);
    AST(6);
#pragma unroll
    for (int i = 0; i < 16; ++i) {
      SBAR();
      o[i & 3] = MF32(vf[i & 3], pa[i >> 2], o[i & 3]);
      SBAR();
      if (i + 4 < 16) LDV(i + 4);
      if constexpr (!LAST) if (i >= MX0 && i < MX0 + 8) MAXS(i - MX0);
      if (AT_PAD && i == EBAR) __builtin_amdgcn_s_sleep(AT_PAD);
      if (i == EBAR && (!LAST || rem)) PHASE(4);
    }
    __builtin_amdgcn_s_setprio(0);
    vo ^= (t & 1) ? 3 * SHM_V : SHM_V;
    AST(7);
  };
#pragma unroll 1
  for (int t = 0; t < TL; t++) {
    tile(t, std::false_type{});
  }
  AP(4);
  tile(TL, std::true_type{});
  AP(5);
  ISSUE_V(TL + 3);
  SBAR();
  {
    const float inv = __builtin_amdgcn_rcpf(swadd(l_reg, l_reg));
    const __amdgpu_buffer_rsrc_t ro = RSRC(Op + rb * 262144, 8192);
#pragma unroll
    for (int i = 0; i < 8; ++i) {
      const int g = (i & 1) * 8;
#define ON(k) fmul1(o[i >> 1][g + (k)], inv)
      auto r0 = __builtin_amdgcn_permlane32_swap(pk2(ON(0), ON(1)), pk2(ON(4), ON(5)), false, false);
      auto r1 = __builtin_amdgcn_permlane32_swap(pk2(ON(2), ON(3)), pk2(ON(6), ON(7)), false, false);
#undef ON
      __builtin_amdgcn_raw_buffer_store_b128((u32x4){r0[0], r1[0], r0[1], r1[1]}, ro, r32 * 32 + hi * 16 + (i & 3) * 1024, (i >> 2) * 4096, 16);
      if (i == 3 && rem > 1) PHASE(6);
    }
  }
  AP(6);
  if (rem > 2) PHASE(10);
  if (rem > 3) { PHASE(8); PHASE(8); }
  CLAIM();
  VMW0();
  AP(7);
  __syncthreads();
}
#undef DMA
#undef ISSUE_K
#undef ISSUE_V
#undef PHASE
#undef LDK
#undef LDV
#undef QK
#undef NOV
#undef MAXS
}

__device__ __forceinline__ void wof_task(const Params& p, int kc, char* smem, const int wv, u32* qh, int& pre) {
  const int tid = TIDX, lane = tid & 63, wid = wv;
  const int l15 = lane & 15, lq = lane >> 4, q4 = l15 >> 2, p4 = lane & 3;
  const __amdgpu_buffer_rsrc_t rw = RSRC(WSP(u16, O_WOF), 2 * WOB);
  bf8 st[2][9];
#pragma unroll
  for (int m = 0; m < 2; m++)
#pragma unroll
    for (int i = 0; i < 9; i++) st[m][i] = *(const bf8*)((m ? p.BWo : p.Awo) + (size_t)kc * 64 * 576 + (tid + 512 * i) * 8);
  CLAIM();
#pragma unroll
  for (int m = 0; m < 2; m++) {
#pragma unroll
    for (int i = 0; i < 9; i++) {
      const int id = tid + 512 * i, row = id / 72, ch = id % 72;
      *(bf8*)(smem + row * 1280 + (((ch >> 1) ^ fsw(row)) << 5) + ((ch & 1) << 4)) = st[m][i];
    }
    asm volatile("s_waitcnt lgkmcnt(0)" ::: "memory"); __builtin_amdgcn_s_barrier(); asm volatile("" ::: "memory");
#pragma unroll
    for (int i = 0; i < 9; i++) {
      const int f = wid * 9 + i, nt = f >> 1, k2 = f & 1;
      const int r0 = k2 * 32 + lq * 8 + q4, r1 = r0 + 4;
      bf4 lo = trrd(smem + r0 * 1280 + ((nt ^ fsw(r0)) << 5) + p4 * 8);
      bf4 hi = trrd(smem + r1 * 1280 + ((nt ^ fsw(r1)) << 5) + p4 * 8);
      __builtin_amdgcn_raw_buffer_store_b128(__builtin_bit_cast(u32x4, cat8(lo, hi)), rw, ((nt * 128 + kc * 2 + k2) * 64 + lane) * 16, m * WOB, 16);
    }
    if (m == 0) { asm volatile("s_waitcnt lgkmcnt(0)" ::: "memory"); __builtin_amdgcn_s_barrier(); asm volatile("" ::: "memory"); }
  }
  VMW0();
  __syncthreads();
}

#define OT_AAUX 0
#define OT_BAUX 0
__device__ __forceinline__ void out_tile(const char* A, const char* W, u16* C, char* smem, const int wv, u32* qh, int& pre) {
  const int tid = TIDX, lane = tid & 63, wid = wv;
  const int l15 = lane & 15, lq = lane >> 4, kh = wid >> 2, wc = wid & 3;
  const __amdgpu_buffer_rsrc_t ra = RSRC(A, 2 * 262144), rw = RSRC(W + wc * 3 * 131072 + kh * 1024, 3 * 131072 - kh * 1024);
  const int va = tid * 16, vw = lane * 16;
  int ao = kh * 2048 + (lq >> 1) * 1024 + l15 * 32 + (lq & 1) * 16;
  f4 acc[4][3];
  bf8 wf[8][3], af[2][4];
#define OT_DMA(i, buf, so) __builtin_amdgcn_raw_ptr_buffer_load_lds(ra, (LDSP void*)(smem + (buf) + (i) * 8192 + wid * 1024), 16, va, (so) + ((i) >> 1) * 262144 + ((i) & 1) * 8192, 0, OT_AAUX)
#define OT_LDW(u, so) do { _Pragma("unroll") for (int j = 0; j < 3; j++) \
    wf[u][j] = __builtin_bit_cast(bf8, __builtin_amdgcn_raw_buffer_load_b128(rw, vw, (so) + j * 131072, OT_BAUX)); } while (0)
#define OT_LDA(d, base, off) do { _Pragma("unroll") for (int m = 0; m < 4; m++) af[d][m] = *(const bf8*)(smem + (base) + (off) + (m >> 1) * 16384 + (m & 1) * 512); } while (0)
#define OT_MF(a, b, c) MFMA(a, b, c)
#define OT_XA(v) (v)
#define OT_XW(v) (v)
#pragma unroll
  for (int i = 0; i < 4; i++) OT_DMA(i, 0, 0);
  SB0();
#pragma unroll
  for (int u = 0; u < 8; u++) { OT_DMA(u & 3, 32768 + (u >> 2) * 32768, 16384 + (u >> 2) * 16384); OT_LDW(u, u * 2048); SB0(); }
#pragma unroll
  for (int m = 0; m < 4; m++)
#pragma unroll
    for (int j = 0; j < 3; j++) acc[m][j] = (f4){0, 0, 0, 0};
  asm volatile("s_waitcnt vmcnt(32)" ::: "memory"); __builtin_amdgcn_s_barrier(); asm volatile("" ::: "memory");
  OT_LDA(0, ao, 0);
  int bs = 0;
#pragma unroll 1
  for (int c = 0; c < 16; c += 2) {
    const int sw = c < 14 ? OT_XW((c + 2) * 8192) : PJ_OOB;
#pragma unroll
    for (int u = 0; u < 8; u++) {
      SB0();
      __builtin_amdgcn_s_waitcnt(0x4F7C);
      if ((u & 3) == 3) { asm volatile("s_waitcnt lgkmcnt(0)" ::: "memory"); __builtin_amdgcn_s_barrier(); asm volatile("" ::: "memory"); }
      if (u == 7) ao ^= 65536;
      OT_LDA((u + 1) & 1, ao, u == 7 ? 0 : ((u + 1) >> 2) * 32768 + ((u + 1) & 3) * 4096);
      SB0();
#define OT_V 1
      const int cn = c + 3 + (u >> 2);
#pragma unroll
      for (int j = 0; j < 3; j++) {
#pragma unroll
        for (int m = 0; m < 4; m++) {
          acc[m][j] = OT_MF(wf[u][j], af[u & 1][m], acc[m][j]);
          if (j == 0 && m == 1) { SB0(); OT_DMA(u & 3, u < 4 ? (bs ^ 65536) + 32768 : bs, cn < 16 ? OT_XA(cn * 16384) : PJ_OOB); SB0(); }
        }
        SB0();
        wf[u][j] = __builtin_bit_cast(bf8, __builtin_amdgcn_raw_buffer_load_b128(rw, vw, sw + u * 2048 + j * 131072, OT_BAUX));
        SB0();
      }
    }
    bs ^= 65536;
  }
  CLAIM();
  VMW0C();
  asm volatile("s_waitcnt lgkmcnt(0)" ::: "memory"); __builtin_amdgcn_s_barrier(); asm volatile("" ::: "memory");
#pragma unroll
  for (int m = 0; m < 2; m++)
#pragma unroll
    for (int j = 0; j < 3; j++) *(f4*)(smem + (m * 3 + j) * 8192 + va) = kh ? acc[m][j] : acc[2 + m][j];
  __syncthreads();
#pragma unroll
  for (int m = 0; m < 2; m++) {
    f4 t[3];
#pragma unroll
    for (int j = 0; j < 3; j++) { const f4 o = *(const f4*)(smem + (m * 3 + j) * 8192 + (va ^ 4096)); t[j] = kh ? o + acc[2 + m][j] : acc[m][j] + o; }
    u16* c = C + (size_t)(kh * 32 + m * 16 + l15) * 1152 + wc * 48;
    *(u32x4*)(c + (lq & 1) * 16 + (lq >> 1) * 8) = pair16(pk2(t[0][0], t[0][1]), pk2(t[0][2], t[0][3]), pk2(t[1][0], t[1][1]), pk2(t[1][2], t[1][3]));
    *(u32x2*)(c + 32 + lq * 4) = (u32x2){pk2(t[2][0], t[2][1]), pk2(t[2][2], t[2][3])};
  }
  __syncthreads();
#undef OT_DMA
#undef OT_LDW
#undef OT_LDA
}

#define CLAIMS() do { const int l_ = TIDX; if (l_ < 64) { \
    const u32 v_ = ld4_sc1(WSP(u32, 0) + SY_OQ(l_ < 6 ? l_ : 0)); \
    if (l_ == 0) pre = (int)__hip_atomic_fetch_add(WSP(u32, 0) + SY_OQ(6), 1u, __ATOMIC_RELAXED, __HIP_MEMORY_SCOPE_AGENT); \
    pre |= (int)((u32)__ballot(l_ < 6 && v_ < ((l_ & 1) ? XS0 / 2 : 32)) << 16); } } while (0)
__device__ __forceinline__ void small_out_task(const Params& p, int rq, int cp, char* smem, const int wv, int& pre) {
  const int tid = TIDX, lane = tid & 63, wid = wv;
  const int l15 = lane & 15, lq = lane >> 4;
  const __amdgpu_buffer_rsrc_t rw = RSRC(WSP(char, O_WOF) + WOB, WOB), ro = RSRC(WSP(u16, O_OB), 2048 * 4096 * 2);
  if (tid == 0) {
    int it = 0;
    while (ld4_sc1(WSP(u32, 0) + SY_CW) < 64u && ++it < SPIN_CAP) __builtin_amdgcn_s_sleep(8);
  }
  __syncthreads();
  bf8 wf[8][4];
#pragma unroll
  for (int k = 0; k < 8; k++)
#pragma unroll
    for (int j = 0; j < 4; j++)
      wf[k][j] = __builtin_bit_cast(bf8, __builtin_amdgcn_raw_buffer_load_b128(rw, (l15 >> 3) * 1024 + (lq * 16 + (l15 & 7)) * 16 + (j & 1) * 128,
                                                                              ((cp * 2 + (j >> 1)) * 128 + wid * 16 + k * 2) * 1024, 16));
  const int va = OB_OFF(rq * 8 + (l15 & 7), wid * 512 + (l15 >> 3) * 32 + lq * 8);
  const int wo = (lq >> 1) == (l15 >> 3) ? (wid * 2 + (lq >> 1)) * 272 + (l15 & 7) * 32 + (lq & 1) * 16 : 4 * 4352 + lane * 16;
  const int rd = (lane >> 4) * 4352 + (lane & 7) * 544 + wid * 32 + ((lane >> 3) & 1) * 16;
#pragma unroll 1
  for (int s = XS0; s < 64; s++) {
    if (tid < 64 && s < 62) {
      int it = 0;
      while (ld4_sc1(WSP(u32, 0) + SY_CS(s)) == 0u && ++it < SPIN_CAP) __builtin_amdgcn_s_sleep(SPS);
    }
    asm volatile("s_waitcnt lgkmcnt(0)" ::: "memory"); __builtin_amdgcn_s_barrier(); asm volatile("" ::: "memory");
    if (s == 63) TLS(1);
    u32x4 af[8];
    int it = 0;
    while (true) {
      CFENCE();
#pragma unroll
      for (int k = 0; k < 8; k++) af[k] = __builtin_amdgcn_raw_buffer_load_b128(ro, va, s * 262144 + k * 4096, 16);
      if (s < 62) break;
      u32 bad = 0u;
#pragma unroll
      for (int k = 0; k < 8; k++) bad = umax(umax(bad, af[k][0]), af[k][2]);
      if (!__any(bad == ~0u) || ++it >= SPIN_CAP) break;
      __builtin_amdgcn_s_sleep(2);
    }
    if (s == 60) CLAIMS();
    f4 acc[4];
#pragma unroll
    for (int k = 0; k < 8; k++)
#pragma unroll
      for (int j = 0; j < 4; j++) acc[j] = MFMA(wf[k][j], __builtin_bit_cast(bf8, af[k]), k ? acc[j] : ((f4){0, 0, 0, 0}));
#pragma unroll
    for (int j = 0; j < 4; j++) *(f4*)(smem + wo + ((lq >> 1) == (l15 >> 3) ? j * 4352 : 0)) = acc[j];
    asm volatile("s_waitcnt lgkmcnt(0)" ::: "memory"); __builtin_amdgcn_s_barrier(); asm volatile("" ::: "memory");
    if (s == 63) TLS(2);
    f4 t = *(const f4*)(smem + rd) + *(const f4*)(smem + rd + 272);
#pragma unroll
    for (int r = 0; r < 4; r++) {
      t[r] += __uint_as_float(__builtin_amdgcn_mov_dpp(__float_as_uint(t[r]), 0xB1, 0xf, 0xf, true));
      t[r] += __uint_as_float(__builtin_amdgcn_mov_dpp(__float_as_uint(t[r]), 0x4E, 0xf, 0xf, true));
      t[r] += __uint_as_float(__builtin_amdgcn_mov_dpp(__float_as_uint(t[r]), 0x141, 0xf, 0xf, true));
    }
    if ((lane & 7) == 0) *(u32x2*)(p.out + (size_t)(s * 32 + rq * 8 + wid) * 1152 + 576 + cp * 32 + (lane >> 3) * 4) = (u32x2){pk2(t[0], t[1]), pk2(t[2], t[3])};
  }
}

#define PSTAMP()
#define STAMP(i)
#define ANS 16
#define KSL 1
#define DSH 2
#define K1 24
#define K3 12
#define DOMAX 90
#define DO0 80
#define DH0 76
#define DOCAP 6
#define C_IMG 0
#define C_QT 9216
#define C_KT (C_QT + 3072)
#define C_P (C_KT + 4096)
#define C_PX (C_P + 768)
#define C_KX (C_PX + 2560)
#define C_VX (C_KX + 8192)
#define C_QX (C_VX + 8192)
#define C_XP 161024
#define GPS 6
#define C_WL 49920
__device__ __forceinline__ float ror8(float v) {
  return __uint_as_float(__builtin_amdgcn_mov_dpp(__float_as_uint(v), 0x128, 0xf, 0xf, true));
}
__device__ __forceinline__ float rb1(float f) { return __uint_as_float(pk2(0.f, f)); }
#define FMX(a, b) __builtin_elementwise_maximum(a, b)
__device__ __forceinline__ float tmax(float mx) {
  mx = FMX(mx, ror8(mx));
  { auto t = __builtin_amdgcn_permlane16_swap(__float_as_uint(mx), __float_as_uint(mx), false, false);
    mx = FMX(__uint_as_float(t[0]), __uint_as_float(t[1])); }
  { auto t = __builtin_amdgcn_permlane32_swap(__float_as_uint(mx), __float_as_uint(mx), false, false);
    mx = FMX(__uint_as_float(t[0]), __uint_as_float(t[1])); }
  return mx;
}
__device__ __forceinline__ int hmax(int x) {
  x = max(x, __builtin_amdgcn_mov_dpp(x, 0x128, 0xf, 0xf, true));
  x = max(x, __builtin_amdgcn_mov_dpp(x, 0x124, 0xf, 0xf, true));
  x = max(x, __builtin_amdgcn_mov_dpp(x, 0x4E, 0xf, 0xf, true));
  x = max(x, __builtin_amdgcn_mov_dpp(x, 0xB1, 0xf, 0xf, true));
  { auto t = __builtin_amdgcn_permlane16_swap((u32)x, (u32)x, false, false); x = max((int)t[0], (int)t[1]); }
  return x;
}
typedef __attribute__((ext_vector_type(2))) unsigned long long u64x2;
__device__ __forceinline__ f4 zero4() {
  u64 a, b; asm volatile("v_mov_b64 %0, 0\n\tv_mov_b64 %1, 0\n\ts_nop 1" : "=v"(a), "=v"(b));
  return __builtin_bit_cast(f4, (u64x2){a, b});
}
__device__ __forceinline__ u32x4 ones4() {
  u64 a, b; asm volatile("v_mov_b64 %0, -1\n\tv_mov_b64 %1, -1" : "=v"(a), "=v"(b));
  return __builtin_bit_cast(u32x4, (u64x2){a, b});
}
#define BAR() do { asm volatile("s_waitcnt lgkmcnt(0)" ::: "memory"); __builtin_amdgcn_s_barrier(); asm volatile("" ::: "memory"); } while (0)
#define BARC() do { asm volatile("" ::: "memory"); __builtin_amdgcn_s_waitcnt(0xC07F); __builtin_amdgcn_s_barrier(); asm volatile("" ::: "memory"); } while (0)
#define BARN() do { asm volatile("" ::: "memory"); __builtin_amdgcn_s_barrier(); asm volatile("" ::: "memory"); } while (0)
#define IDX() const int tid = TIDX, lane = tid & 63, l15 = lane & 15, lq = lane >> 4, q4 = l15 >> 2, p4 = lane & 3, hi8 = l15 >> 3; \
    (void)q4; (void)p4; (void)hi8; (void)lq
#define AD_LD() __builtin_amdgcn_raw_buffer_load_b32(rsX, (TIDX & 63) * ANS, X_ANN + (s & 1) * 1024 + xp, 16)
#define AD_WAIT(HEAD, K, CAP) do { int n_ = 0; u32 an = HEAD && HL2 < 16 ? an0 : AD_LD(); \
    while ((u32)__ballot((an & 63u) != (u32)s) != 0u && ++n_ < (CAP)) { __builtin_amdgcn_s_sleep(1); CFENCE(); an = AD_LD(); } \
    if (!HEAD && c == 0 && s > 0 && n_ < (CAP)) st4_sc1(WSP(u32, 0) + SY_CS(s - 1), 1u); \
    const u32 r_ = (u32)__builtin_amdgcn_readfirstlane((int)an); \
    const int m_ = hmax((TIDX & 32) ? (int)umin(an, DOMAX * 64u) : (int)(an - r_)); \
    dl = ((r_ + (u32)__builtin_amdgcn_readlane(m_, 0)) & ~63u) + (u32)(K) * 64u + (HEAD ? (u32)__builtin_amdgcn_readlane(m_, 32) : 0u); \
    n_ = 160; \
    while ((int)(((u32)wall_clock64() << 6) - dl) < 0 && --n_ > 0) {} } while (0)
#define STG_AUX 2
#define OSTG_AUX 0
#define STG_D 3
#define STG_POLL 7
#define STS(c, i)

#define DPR(OP, N) OP " %3, %3, %3 row_ror:" #N " row_mask:0xf bank_mask:0xf\n\t" OP " %4, %4, %4 row_ror:" #N " row_mask:0xf bank_mask:0xf\n\t"
#define DPROW(OP) "s_nop 0\n\t" DPR(OP, 8) "s_nop 0\n\t" DPR(OP, 4) "s_nop 0\n\t" DPR(OP, 2) "s_nop 0\n\t" DPR(OP, 1)
#define HL1 16
#define HL2 16
#define HSL 4
#define ANJ 0
#define LSL 2
__device__ __forceinline__ void chain_head(const Params& p, char* smem, const int h, const int wv) {
  STS(h, 1);
  TLS(12);
  const int wid = wv;
  u32x8 w[2][9], wx[3];
  const __amdgpu_buffer_rsrc_t rsX = RSRC(XWIN(p), X_END);
  const __amdgpu_buffer_rsrc_t rsB = RSRC(WSP(u16, O_QB), 4 * ACB);
  const int cb = h >> 1;
  const int nvt = (wid & 3) == 0 ? 0 : (wid == 5 || wid == 6 ? 2 : 1);
  const int vt = wid < 4 ? wid - 1 : (wid == 7 ? 3 : wid * 2 - 6);
  const int wl = C_WL + (nvt == 1 ? vt * 12288 : 49152 + (nvt == 2 ? wid - 5 : wid >> 2) * 30720);
  int xp;
  {
    IDX();
    u32x4 st[54], nk, nv, nq;
    u32 fl, fv, fk;
    const __amdgpu_buffer_rsrc_t rq = RSRC(p.BWq, 576 * 4096 * 2), rk = RSRC(p.BWk, 576 * 4096 * 2), rv = RSRC(p.BWv, 576 * 4096 * 2);
    const int vw = ((tid >> 4) * 4096 + h * 128 + (tid & 15) * 8) * 2;
    {
      const u32x4 e = ones4();
#pragma unroll
      for (int i = 0; i < 2; i++) {
        const int id = wid + 8 * i;
        __builtin_amdgcn_raw_buffer_store_b128(e, rsX, lane * 16, id < 12 ? X_O + (id >> 2) * 131072 + (((id >> 1) & 1) * 4 + (h >> 3)) * 16384 + (id & 1) * 8192 + (h & 7) * 1024 : PJ_OOB, 16);
      }
      __builtin_amdgcn_raw_buffer_store_b128(e, rsX, tid < 4 ? X_ANN + (tid & 1) * 1024 + (tid >> 1) * 8192 + h * ANS : PJ_OOB, 0, 16);
    }
    auto req = [&](int kc) {
#pragma unroll
      for (int i = 0; i < 6; i++) st[kc * 6 + i] = __builtin_amdgcn_raw_buffer_load_b128(i < 2 ? rq : (i < 4 ? rk : rv), vw, (kc * 64 + (i & 1) * 32) * 8192, STG_AUX);
      SB0();
    };
#pragma unroll
    for (int kc = 0; kc < STG_D; kc++) { if (kc == 0) TLS(13); req(kc); __builtin_amdgcn_s_barrier(); SB0(); }
    TLS(14);
    auto cell = [](int row, int slot) { return (((row * 2 + slot) ^ ((((row >> 2) ^ (row >> 4)) & 1) << 2) ^ ((row >> 5) & 1)) << 5); };
    auto put = [&](int unit, int row, const u32x4& v) { *(u32x4*)(smem + unit * 4160 + cell(row, (tid >> 1) & 1) + (tid & 1) * 16) = v; };
    auto frag = [&](int tile) {
      bf4 tt[4];
#pragma unroll
      for (int kk = 0; kk < 4; kk++) {
        const int r0 = (kk >> 1) * 32 + (lq & 1) * 16 + (lq >> 1) * 8 + (kk & 1) * 4 + (q4 & 1) * 2 + (q4 >> 1);
        tt[kk] = trrd(smem + (tile >> 1) * 4160 + cell(r0, tile & 1) + p4 * 8);
      }
      return ilv(tt[0], tt[1], tt[2], tt[3]);
    };
    auto park = [&](int slot, const u32x8& f) {
      char* d = smem + wl + slot * 2048 + lane * 16;
      *(u32x4*)d = (u32x4){f[0], f[1], f[2], f[3]};
      *(u32x4*)(d + 1024) = (u32x4){f[4], f[5], f[6], f[7]};
    };
    auto poll = [&]() {
      fl = ld4_sc1(WSP(u32, 0) + SY_INIT(lane)); fv = ld4_sc1(WSP(u32, 0) + SY_VOTE(lane & 31));
      fk = ld4_sc1(WSP(u32, 0) + SY_BLK(lane < 12 ? lane >> 2 : 0, cb, lane & 3));
    };
    auto rows = [&]() {
      nq = __builtin_amdgcn_raw_buffer_load_b128(rsB, tid * 16, h * 8192, 16);
      nk = __builtin_amdgcn_raw_buffer_load_b128(rsB, tid * 16, h * 8192 + ACB, 16);
      nv = __builtin_amdgcn_raw_buffer_load_b128(rsB, tid * 16, h * 8192 + 2 * ACB, 16);
    };
    bool early = false;
#pragma unroll
    for (int kc = 0; kc < 9; kc++) {
      if (kc == 10 - STG_D) { early = __all(fk >= 8u); CFENCE(); if (early) rows(); if (wid == 0) poll(); SB0(); }
#pragma unroll
      for (int i = 0; i < 6; i++) put((i >> 1) * 4 + ((tid & 15) >> 2), (i & 1) * 32 + (tid >> 4), st[kc * 6 + i]);
      BAR();
      if (kc == 0 && tid == 0) st4_sc1(WSP(u32, 0) + SY_INIT(h), 1u);
      if (kc == 0) TLS(15);
      if (kc + STG_D == 8) { fk = ld4_sc1(WSP(u32, 0) + SY_BLK(lane < 12 ? lane >> 2 : 0, cb, lane & 3)); SB0(); }
      if (kc + STG_D < 9) req(kc + STG_D);
      w[0][kc] = frag(wid * 2);
      w[1][kc] = frag(nvt == 2 ? 16 + vt : wid * 2 + 1);
      const u32x8 tp = frag(nvt == 0 ? 11 + (wid >> 1) : 15 + vt + nvt);
      if (kc >= 4 && kc < 7) { wx[kc - 4] = tp; if (nvt == 0) park(6 + kc, tp); }
      else park(nvt == 0 ? 6 + kc : (kc >= 7 ? kc - 7 : 2 + kc), tp);
      BAR();
      if (kc == 0 || kc == 8) STS(h, kc + 3);
    }
    TLS(6);
    if (wid == 0) {
      int it = 0;
      while (!__all(fl != 0u && fv != 0u && fk >= 8u) && ++it < SPIN_CAP) { __builtin_amdgcn_s_sleep(GPS); poll(); }
      *(int*)(smem + C_XP) = __popcll(__ballot(fv == 2u)) > 32 ? 8192 : 0;
    }
    BAR();
    if (!early) rows();
    xp = __builtin_amdgcn_readfirstlane(*(const int*)(smem + C_XP));
    TLS(7);
    SB0();
    *(u32x4*)(smem + C_QX + tid * 16) = nq;
    *(u32x4*)(smem + C_KX + tid * 16) = nk;
    *(u32x4*)(smem + C_VX + ((tid * 16) ^ ((tid & 32) << 2))) = nv;
  }
  STS(h, 14);
  constexpr float CE = 0.08838834764831845f * 1.4426950408889634f;
  auto run = [&](auto RC) __attribute__((always_inline)) {
  constexpr int R = decltype(RC)::value;
  constexpr int NV = R < 2 ? 0 : (R < 4 ? 1 : 2);
  constexpr bool QW = R == 0 || R == 2;
  int known = 0;
  bf8 pre[4][2], vx[NV ? NV : 1];
  int sidx, mo, mo1, eo, gvo, wvo = 0, so = 0, po = 0, qo = 0, ko = 0, ko1 = 0, fo = 0;
  float mk[4];
#define LDW(dst, slot) do { dst[0] = *(const bf8*)(smem + wvo + (slot) * 2048); dst[1] = *(const bf8*)(smem + wvo + (slot) * 2048 + 1024); } while (0)
  {
    IDX();
    if constexpr (R == 1) {
    } else if constexpr (R == 0) {
      qo = C_QT + lq * 384 + q4 * 32 + p4 * 8; ko = C_KT + ((((lq * 8 + q4) * 32) ^ ((lq & 1) << 7)) + p4 * 8); ko1 = C_KT + ((((lq * 8 + 4 + q4) * 32) ^ ((lq & 1) << 7)) + p4 * 8);
      po = C_P + l15 * 16 + lq * 4;
      fo = lq * 512 + l15 * 16;
#pragma unroll
      for (int r = 0; r < 4; r++) { mk[r] = lq * 4 + r <= l15 ? 0.f : -__builtin_inff(); asm volatile("" : "+v"(mk[r])); }
      asm volatile("" : "+v"(qo), "+v"(ko), "+v"(ko1), "+v"(po), "+v"(fo));
    } else {
      wvo = wl + lane * 16;
      po = C_P + (lq * 4 + q4) * 16 + (p4 & 1) * 8;
      so = ((l15 >> 2) & 1) * 65536 + ((vt + (NV == 2 ? hi8 : 0)) * 8 + ((l15 >> 1) & 1) * 4 + (lq >> 1) * 2 + (l15 & 1)) * 16 + (lq & 1) * 8;
      if (NV == 1 && hi8) so = PJ_OOB;
      asm volatile("" : "+v"(wvo), "+v"(po), "+v"(so));
#pragma unroll
      for (int j = 0; j < 4; j++) LDW(pre[j], NV == 2 ? 6 + j : j);
    }
    eo = QW ? C_QT + wid * 768 + ((l15 >> 2) * 2 + (l15 & 1)) * 32 + lq * 8 + ((l15 >> 1) & 1) * 4
            : C_KT + (wid & 3) * 1024 + ((l15 * 32) ^ (hi8 << 7)) + lq * 8;
    sidx = (l15 & 2) ? 0xEEEE : 0x4444;
    mo = C_IMG + (lq >> 1) * 512 + (p4 >> 1) * (256 + 128) + (lq & 1) * 64 + q4 * 16 + (p4 & 1) * 8; mo1 = mo ^ 128;
    gvo = tid * 16;
    asm volatile("" : "+v"(sidx), "+v"(mo), "+v"(mo1), "+v"(eo), "+v"(gvo));
  }
  f4 a0, a1;
  u32 ts = 0;
  u64 tq = 0;
  int dur = DH0 * 64;
  const int ob = (h >> 3) * 16384 + (h & 7) * 1024 + xp;
  int b3 = 0;
#pragma unroll 1
  for (int s = 0;; s++) {
    const int mb = X_M + b3 * 32768 + xp;
    u32x4 g, gx;
    BAR();
    g = __builtin_amdgcn_raw_buffer_load_b128(rsX, gvo, mb, 16);
    if constexpr (R == 1) gx = __builtin_amdgcn_raw_buffer_load_b128(rsX, gvo, mb + 16384 - 4096, 16);
    SB0();
    if constexpr (R == 0) {
#pragma unroll
      for (int j = 0; j < 4; j++) { const char* kx_ = smem + C_KX + fo + j * 2048;
        pre[j >> 1][j & 1] = *(const bf8*)kx_; pre[2 + (j >> 1)][j & 1] = *(const bf8*)(kx_ + 256); }
    } else if constexpr (R > 1) {
      IDX();
#pragma unroll
      for (int d = 0; d < NV; d++) { const char* vb_ = smem + C_VX + (vt + d) * 1024 + (p4 >> 1) * 512 + (((lq * 4 + q4) ^ ((p4 >> 1) * 8)) << 4) + (p4 & 1) * 8;
        vx[d] = cat8(trrd(vb_), trrd(vb_ + 256)); }
    }
    SB0();
    a0 = zero4(); a1 = zero4();
    int so_ = X_O + b3 * 131072 + ob, an_ = X_ANN + (s & 1) * 1024 + xp + h * ANS;
    int nb = s < 63 ? (s + 1) * 262144 + h * 8192 : PJ_OOB;
    asm volatile("" : "+s"(so_), "+s"(an_), "+s"(nb));
    u32x4 nx[R == 1 ? 16 : 2];
    u32 an0 = 0;
    SB0();
    {
      VMW0C();
      *(u32x4*)(smem + C_IMG + gvo) = g;
      if constexpr (R == 1) *(u32x4*)(smem + C_IMG + 4096 + gvo) = gx;
      SB0();
#define G_BAD() (umax(umax(umax(g[0], g[1]), umax(g[2], g[3])), R != 1 ? 0u : umax(umax(gx[0], gx[1]), umax(gx[2], gx[3]))) == ~0u)
      if (__builtin_expect(__any(G_BAD()), 0)) {
        int it = 0;
        do {
          CFENCE();
          if (G_BAD()) {
            g = __builtin_amdgcn_raw_buffer_load_b128(rsX, gvo, mb, 16);
            if constexpr (R == 1) gx = __builtin_amdgcn_raw_buffer_load_b128(rsX, gvo, mb + 16384 - 4096, 16);
          }
          VMW0C();
        } while (__any(G_BAD()) && ++it < SPIN_CAP);
        *(u32x4*)(smem + C_IMG + gvo) = g;
        if constexpr (R == 1) *(u32x4*)(smem + C_IMG + 4096 + gvo) = gx;
      }
    }
    SB0();
    STAMP(15);
    BARC();
    STAMP(0);
#define LDM(j) cat8(trrd(smem + mo + (j) * 1024), trrd(smem + mo1 + (j) * 1024))
#define NXL(i) nx[i] = __builtin_amdgcn_raw_buffer_load_b128(rsB, gvo + ((i) & 3) * 1024, nb + (1 + ((i) >> 3)) * ACB + ((i) & 4) * 1024 - 4096, 16)
    bf8 b[5];
    if constexpr (R == 1) { asm volatile("s_memrealtime %0" : "=s"(tq)); SB0(); }
#pragma unroll
    for (int j = 0; j < 4; j++) { b[j] = LDM(j); SB0(); }
#pragma unroll
    for (int j = 0; j < 9; j++) {
      if (j + 4 < 9) b[(j + 4) % 5] = LDM(j + 4);
      SB0();
      a0 = SMF(b[j % 5], w[0][j], a0, sidx);
      if constexpr (NV < 2) a1 = SMF(b[j % 5], w[1][j], a1, sidx);
      else a1 = SMF(b[j % 5], cat16(pre[j & 3][0], pre[j & 3][1]), a1, sidx);
      SB0();
      if constexpr (R == 1) {
        if (j == ANJ) {
          asm volatile("s_waitcnt lgkmcnt(0)" : "+s"(tq));
          ts = (u32)tq;
          __builtin_amdgcn_raw_buffer_store_b32((((ts << 6) + (u32)dur) & ~63u) | (u32)s, rsX, 0, an_, 16);
          SB0();
        }
        if (j > 0 && 2 * j <= HL1) {
#pragma unroll
          for (int i = 2 * j - 2; i < 2 * j; i++) NXL(i);
          SB0();
        }
      }
      if constexpr (NV == 2) LDW(pre[j & 3], j < 5 ? 10 + j : j - 5);
    }
    {
      const u32 e0 = pk2(a0[0] + a0[2], a0[1] + a0[3]), e1 = pk2(a1[0] + a1[2], a1[1] + a1[3]);
      if constexpr (QW) asm volatile("ds_write2_b32 %0, %1, %2 offset1:96" :: "v"(eo), "v"(e0), "v"(e1) : "memory");
      else asm volatile("ds_write2_b32 %0, %1, %1 offset1:1\n\tds_write2_b32 %0, %2, %2 offset0:128 offset1:129" :: "v"(eo), "v"(e0), "v"(e1) : "memory");
    }
    SB0();
    if constexpr (R != 1) { a0 = zero4(); a1 = zero4(); }
    if constexpr (NV == 0) {
      f4 a2;
      if constexpr (R == 0) a2 = zero4();
      STAMP(1);
      BARC();
      STAMP(2);
      if constexpr (R == 0) {
        bf8 qa[2], km[4];
#pragma unroll
        for (int i = 0; i < 2; i++) { qa[i] = cat8(trrd(smem + qo + i * 1536), trrd(smem + qo + i * 1536 + 128)); SB0(); }
#pragma unroll
        for (int i = 0; i < 4; i++) { km[i] = cat8(trrd(smem + ko + i * 1024), trrd(smem + ko1 + i * 1024)); SB0(); }
#pragma unroll
        for (int i = 0; i < 2; i++) {
          a0 = SMF(qa[i], cat16(pre[i][0], pre[i][1]), a0, sidx); asm volatile("" : "+v"(a0)); SB0();
          a1 = SMF(qa[i], cat16(pre[2 + i][0], pre[2 + i][1]), a1, sidx); asm volatile("" : "+v"(a1)); SB0();
        }
#pragma unroll
        for (int i = 0; i < 2; i++) { a2 = SMF(qa[i], cat16(km[2 * i], km[2 * i + 1]), a2, sidx); asm volatile("" : "+v"(a2)); SB0(); }
        float e0 = a0[0] + a0[2], e1 = a0[1] + a0[3], e2 = a1[0] + a1[2], e3 = a1[1] + a1[3];
        asm volatile("" : "+v"(e0), "+v"(e1), "+v"(e2), "+v"(e3));
        SB0();
        float e4 = a2[0] + a2[2], e5 = a2[1] + a2[3], x0, x1;
        STAMP(3);
        asm volatile("v_max3_f32 %3, %0, %1, %2\n\tv_max3_f32 %4, %5, %6, %7\n\t" DPROW("v_max_f32_dpp")
                     "v_mul_f32 %3, %8, %3\n\tv_mul_f32 %4, %8, %4\n\t"
                     "v_fma_f32 %0, %0, %8, -%3\n\tv_fma_f32 %5, %5, %8, -%4\n\tv_exp_f32 %0, %0\n\tv_exp_f32 %5, %5\n\t"
                     "v_fma_f32 %1, %1, %8, -%3\n\tv_fma_f32 %6, %6, %8, -%4\n\tv_exp_f32 %1, %1\n\tv_exp_f32 %6, %6\n\t"
                     "v_fma_f32 %2, %2, %8, -%3\n\tv_fma_f32 %7, %7, %8, -%4\n\tv_exp_f32 %2, %2\n\tv_exp_f32 %7, %7\n\t"
                     "v_add_f32 %3, %0, %1\n\tv_add_f32 %4, %5, %6\n\tv_fmac_f32 %3, 0.5, %2\n\tv_fmac_f32 %4, 0.5, %7\n\t" DPROW("v_add_f32_dpp")
                     "v_rcp_f32 %3, %3\n\tv_rcp_f32 %4, %4\n\t"
                     "v_mul_f32 %0, %0, %3\n\tv_mul_f32 %5, %5, %4\n\tv_mul_f32 %1, %1, %3\n\tv_cvt_pk_bf16_f32 %0, %0, %5\n\t"
                     "v_mul_f32 %6, %6, %4\n\tv_mul_f32 %2, %2, %3\n\tv_cvt_pk_bf16_f32 %1, %1, %6\n\tv_mul_f32 %7, %7, %4\n\t"
                     "ds_write2st64_b32 %9, %0, %1 offset1:1\n\tv_cvt_pk_bf16_f32 %2, %2, %7\n\tds_write_b32 %9, %2 offset:512"
                     : "+v"(e0), "+v"(e2), "+v"(e4), "=&v"(x0), "=&v"(x1), "+v"(e1), "+v"(e3), "+v"(e5) : "s"(CE), "v"(po) : "memory");
        SB0();
      } else {
#pragma unroll
        for (int i = HL1; i < HL2; i++) NXL(i);
        SB0();
        const int rb = X_O + (b3 == 0 ? 2 : b3 - 1) * 131072 + ob - 4096;
        const u32x4 e = ones4();
        __builtin_amdgcn_raw_buffer_store_b128(e, rsX, gvo, rb, 16);
        __builtin_amdgcn_raw_buffer_store_b128(e, rsX, gvo, rb + 65536, 16);
        PSTAMP();
      }
      STAMP(5);
      BARC();
      STAMP(6);
      if constexpr (R == 1) {
        const u32 tp = (u32)wall_clock64(); dur += ((int)umin((tp - ts) << 6, 200u * 64u) - dur) >> DSH;
        SB0();
        if (HL2 < 16) {
          an0 = AD_LD();
          __builtin_amdgcn_s_sleep(HSL);
#pragma unroll
          for (int i = HL2; i < 16; i++) NXL(i);
          SB0();
          BARN();
        }
        const int pv_ = gvo ^ ((gvo >> 2) & 128);
#pragma unroll
        for (int i = 0; i < 8; i++) *(u32x4*)(smem + C_KX - 4096 + gvo + i * 1024) = nx[i];
#pragma unroll
        for (int i = 0; i < 8; i++) *(u32x4*)(smem + C_VX - 4096 + pv_ + i * 1024) = nx[8 + i];
        SB0();
        STAMP(8);
        if (HL2 == 16) BARN();
        if (s == 63) break;
      } else {
        bf8 km[4];
#pragma unroll
        for (int ks = 0; ks < 4; ks++) km[ks] = cat8(trrd(smem + ko + ks * 1024), trrd(smem + ko1 + ks * 1024));
#pragma unroll
        for (int NT = 1; NT <= 2; NT++) {
          f4 sc[2], sm;
          bf8 qf[4];
#pragma unroll
          for (int ks = 0; ks < 4; ks++) qf[ks] = *(const bf8*)(smem + C_QX + (NT - 1) * 256 + fo + ks * 2048);
#pragma unroll
          for (int ks = 0; ks < 4; ks++) {
#pragma unroll
            for (int j = 0; j < NT; j++) sc[j] = MFMA(pre[2 * j + (ks >> 1)][ks & 1], qf[ks], ks ? sc[j] : (j == NT - 1 ? ((f4){mk[0], mk[1], mk[2], mk[3]}) : ((f4){0, 0, 0, 0})));
            sm = MFMA(km[ks], qf[ks], ks ? sm : ((f4){0, 0, 0, 0}));
          }
          float mx = FMX3(FMX3(sc[0][0], sc[0][1], sc[0][2]), sc[0][3], FMX(sm[0], sm[1]));
          if (NT == 2) mx = FMX3(FMX3(mx, sc[1][0], sc[1][1]), sc[1][2], sc[1][3]);
          { auto t = __builtin_amdgcn_permlane16_swap(__float_as_uint(mx), __float_as_uint(mx), false, false);
            mx = FMX(__uint_as_float(t[0]), __uint_as_float(t[1])); }
          { auto t = __builtin_amdgcn_permlane32_swap(__float_as_uint(mx), __float_as_uint(mx), false, false);
            mx = FMX(__uint_as_float(t[0]), __uint_as_float(t[1])); }
          mx *= -CE;
#pragma unroll
          for (int j = 0; j < NT; j++)
#pragma unroll
            for (int r = 0; r < 4; r++) sc[j][r] = __builtin_amdgcn_exp2f(__builtin_fmaf(sc[j][r], CE, mx));
          sm[0] = __builtin_amdgcn_exp2f(__builtin_fmaf(sm[0], CE, mx)); sm[1] = __builtin_amdgcn_exp2f(__builtin_fmaf(sm[1], CE, mx));
          float sum = ((sc[0][0] + sc[0][1]) + (sc[0][2] + sc[0][3])) + (sm[0] + sm[1]);
          if (NT == 2) sum += (sc[1][0] + sc[1][1]) + (sc[1][2] + sc[1][3]);
          { auto t = __builtin_amdgcn_permlane16_swap(__float_as_uint(sum), __float_as_uint(sum), false, false);
            sum = __uint_as_float(t[0]) + __uint_as_float(t[1]); }
          sum = swadd(sum, sum);
          const float inv = __builtin_amdgcn_rcpf(sum);
          *(u32x4*)(smem + C_PX + (NT - 1) * 1024 + gvo) = (u32x4){pk2(sc[0][0] * inv, sc[0][1] * inv), pk2(sc[0][2] * inv, sc[0][3] * inv),
                                                                  NT == 2 ? pk2(sc[1][0] * inv, sc[1][1] * inv) : 0u, NT == 2 ? pk2(sc[1][2] * inv, sc[1][3] * inv) : 0u};
          *(u32*)(smem + C_PX + 2048 + (NT - 1) * 256 + (gvo >> 2)) = pk2(sm[0] * inv, sm[1] * inv);
        }
        STAMP(8);
        BAR();
        if (s == 63) break;
        if (known < (s < 61 ? s + 3 : 64)) {
          int n = 0;
          while (true) {
            u32 bf[3];
#pragma unroll
            for (int i = 0; i < 3; i++) bf[i] = ld4_sc1(WSP(u32, 0) + SY_BLK(i, cb, gvo >> 4));
            const u64 miss = __ballot(!(bf[0] >= 8u && bf[1] >= 8u && bf[2] >= 8u));
            known = miss ? __builtin_ctzll(miss) : 64;
            if (known >= (s < 61 ? s + 3 : 64) || ++n >= SPIN_CAP) break;
            __builtin_amdgcn_s_sleep(KSL);
          }
        }
      }
      STAMP(9);
      if constexpr (R == 1) { u32 dl; const int c = 1; AD_WAIT(true, K1 + K3, 4096); }
    } else {
      STAMP(1);
      BARC();
      STAMP(2);
#pragma unroll
      for (int i = 0; i < 9; i++) {
        const int j = i < 5 ? i + 4 : i - 5;
        constexpr int PO = NV == 2 ? 2 : 1;
        SB0();
        if constexpr (NV == 2) a0 = SMF(b[(i + 4) % 5], w[1][j], a0, sidx);
        if (i < 3) a1 = SMF(b[(i + 4) % 5], wx[i], a1, sidx);
        else a1 = SMF(b[(i + 4) % 5], cat16(pre[(i + PO) & 3][0], pre[(i + PO) & 3][1]), a1, sidx);
        SB0();
        if (i < 4) b[(i + 4) % 5] = LDM(i);
        if (i == 3 || i == 4) LDW(pre[(i + PO) & 3], 1 + i);
      }
      u32x4 vm[NV];
      vm[NV - 1] = (u32x4){pk2(a1[0] + a1[2], a1[1] + a1[3]), 0u, 0u, 0u};
      if constexpr (NV == 2) vm[0] = (u32x4){pk2(a0[0] + a0[2], a0[1] + a0[3]), 0u, 0u, 0u};
#pragma unroll
      for (int d = 0; d < NV; d++) asm volatile("" : "+v"(vm[d]));
      const bool hi = so & 128;
      SB0();
      STAMP(5);
      BARN();
      STAMP(6);
      {
        const bf8 px = cat8(trrd(smem + po), trrd(smem + po + 256));
        SB0();
        const bf8 pm = cat8(trrd(smem + po + 512), trrd(smem + po + 256));
        SB0();
        f4 o[NV];
#pragma unroll
        for (int d = 0; d < NV; d++) { o[d] = MFMA(vx[d], px, ((f4){0, 0, 0, 0})); asm volatile("" : "+v"(o[d])); SB0(); }
#pragma unroll
        for (int d = 0; d < NV; d++) { o[d] = MFMA(__builtin_bit_cast(bf8, vm[d]), pm, o[d]); asm volatile("" : "+v"(o[d])); SB0(); }
        u32x2 ov = {pk2(o[0][0], o[0][1]), pk2(o[0][2], o[0][3])};
        if constexpr (NV == 2) {
          const u32x2 ow = {pk2(o[1][0], o[1][1]), pk2(o[1][2], o[1][3])};
          if (hi) ov = ow;
        }
        ov[0] = umin(ov[0], 0xFFFFFFFEu);
        __builtin_amdgcn_raw_buffer_store_b64(ov, rsX, so, so_, 16);
        SB0();
      }
      STAMP(7);
      int xo, lo, pa = 0;
      {
        IDX();
        if constexpr (NV == 1) {
          __builtin_amdgcn_s_sleep(LSL);
#pragma unroll
          for (int i = 0; i < 2; i++) nx[i] = __builtin_amdgcn_raw_buffer_load_b128(rsB, lane * 16 + i * 1024, nb + vt * 2048, 16);
          pa = C_QX + vt * 2048 + lane * 16;
          asm volatile("" : "+v"(pa));
        }
        xo = OB_OFF((lq & 1) * 16 + l15, (lq >> 1) * 8);
        lo = lane * 4;
        asm volatile("" : "+v"(xo), "+v"(lo));
#pragma unroll
        for (int j = 0; j < 4; j++) LDW(pre[j], NV == 2 ? 6 + j : j);
      }
      SB0();
      BARN();
      STAMP(8);
      {
        f4 o[2][NV];
#pragma unroll
        for (int it = 0; it < 2; it++) {
          const bf8 pf = *(const bf8*)(smem + C_PX + it * 1024 + lo * 4);
          const u32x4 pm = {*(const u32*)(smem + C_PX + 2048 + it * 256 + lo), 0u, 0u, 0u};
#pragma unroll
          for (int d = 0; d < NV; d++) {
            o[it][d] = MFMA(vx[d], pf, ((f4){0, 0, 0, 0}));
            o[it][d] = MFMA(__builtin_bit_cast(bf8, vm[d]), __builtin_bit_cast(bf8, pm), o[it][d]);
          }
        }
#pragma unroll
        for (int d = 0; d < NV; d++)
          __builtin_amdgcn_raw_buffer_store_b128(pair16(umin(pk2(o[0][d][0], o[0][d][1]), 0xFFFFFFFEu), pk2(o[0][d][2], o[0][d][3]),
                                                        umin(pk2(o[1][d][0], o[1][d][1]), 0xFFFFFFFEu), pk2(o[1][d][2], o[1][d][3])), rsB,
              xo, 3 * ACB + OB_OFF(s * 32, h * 128 + (vt + d) * 16), 16);
        SB0();
        if (s == 63) break;
        if constexpr (NV == 1) { *(u32x4*)(smem + pa) = nx[0]; *(u32x4*)(smem + pa + 1024) = nx[1]; }
      }
      STAMP(9);
    }
    b3 = b3 == 2 ? 0 : b3 + 1;
    STAMP(14);
  }
  };
  if (wid == 4) run(std::integral_constant<int, 1>{});
  else if (wid == 0) run(std::integral_constant<int, 0>{});
  else if (wid < 4) run(std::integral_constant<int, 2>{});
  else if (wid == 7) run(std::integral_constant<int, 3>{});
  else run(std::integral_constant<int, 4>{});
  TLS(0);
}

__device__ __forceinline__ void chain_out(const Params& p, char* smem, const int c, const int wv) {
  STS(32 + c, 1);
  constexpr int R = 5; (void)R;
  const int wid = wv, tq = c & 1, cg = c >> 1;
  u32x8 w[4][5];
  const __amdgpu_buffer_rsrc_t rsX = RSRC(XWIN(p), X_END);
  int xp, mo, wo, ro;
  {
    IDX();
    mo = wid < 4 ? cg * 512 + tq * 256 + ((wid ^ (tq * 2)) << 6) : 16384 + (cg >> 2) * 512 + tq * 256 + (((cg & 3) ^ (tq * 2)) << 6);
    if (wid > 4 || (wid == 4 && cg >= 8)) mo = PJ_OOB;
    mo = __builtin_amdgcn_readfirstlane(mo);
    __builtin_amdgcn_raw_buffer_store_b128(ones4(), rsX, lane < 16 ? X_M + 32768 + (lane >> 3) * 32768 + ((lane >> 2) & 1) * 8192 + (lane & 3) * 16 : PJ_OOB, mo, 16);
    {
      const u16* m = p.Bm + (tq * 4 + ((lane >> 2) & 1) * 2) * 576 + (wid < 4 ? cg * 32 + wid * 8 : 512 + (cg & 7) * 8) + (lane >> 3);
      __builtin_amdgcn_raw_buffer_store_b32(umin((u32)m[0] | ((u32)m[576] << 16), 0xFFFFFFFEu), rsX,
          p4 < 2 ? X_M + p4 * 8192 + (lane >> 4) * 16 + ((lane >> 2) & 1) * 8 + ((lane >> 3) & 1) * 4 : PJ_OOB, mo, 16);
      __builtin_amdgcn_raw_buffer_store_b32(DO0 * 64u, rsX, tid < 4 ? X_ANN + (tid & 1) * 1024 + (tid >> 1) * 8192 + (32 + c) * ANS : PJ_OOB, 0, 16);
    }
#pragma unroll
    for (int i = 0; i < 2; i++) __builtin_amdgcn_raw_buffer_store_b128(ones4(), RSRC(WSP(char, O_OB), ACB), tid * 16, OB_OFF((62 + i) * 32, c * 128), 16);
    VMW0();
    BARN();
    if (tid == 0) st4_sc1(WSP(u32, 0) + SY_INIT(32 + c), 1u);
    SB0();
    const __amdgpu_buffer_rsrc_t rw = RSRC(p.BWo, 576 * 4096 * 2);
    u32x4 st[4][8];
    const int va = ((lane >> 2) * 576 + cg * 32 + p4 * 8) * 2;
#pragma unroll
    for (int i = 0; i < 4; i++) {
#pragma unroll
      for (int k = 0; k < 8; k++) st[i][k] = __builtin_amdgcn_raw_buffer_load_b128(rw, va, ((i * 8 + wid) * 128 + k * 16) * 1152, OSTG_AUX);
      SB0();
    }
    char* sc = smem + wid * 10240;
    auto frag = [&](const char* b, int rs) {
      bf4 tt[4];
#pragma unroll
      for (int kk = 0; kk < 4; kk++) {
        const int r0 = ((kk >> 1) * 2 + (lq >> 1)) * 32 + (p4 >> 1) * 16 + (q4 >> 1) * 8 + (lq & 1) * 4 + (kk & 1) * 2 + (q4 & 1);
        tt[kk] = trrd(b + r0 * rs + (p4 & 1) * 8);
      }
      return ilv(tt[0], tt[1], tt[2], tt[3]);
    };
#pragma unroll
    for (int i = 0; i < 4; i++) {
#pragma unroll
      for (int k = 0; k < 8; k++) *(u32x4*)(sc + (k * 16 + (lane >> 2)) * 80 + p4 * 16) = st[i][k];
      __builtin_amdgcn_fence(__ATOMIC_RELEASE, "wavefront");
      __builtin_amdgcn_wave_barrier();
#pragma unroll
      for (int t = 0; t < 4; t++) w[i][t] = frag(sc + t * 16, 80);
      asm volatile("s_waitcnt lgkmcnt(0)" ::: "memory");
      __builtin_amdgcn_wave_barrier();
      SB0();
    }
    TLS(8);
    BARN();
    if (wid == 0) {
      u32 best[2] = {~0u, ~0u};
#pragma unroll 1
      for (int r = 0; r < 3; r++) {
#pragma unroll
        for (int k = 0; k < 2; k++) {
          const u32 t0 = (u32)__builtin_readcyclecounter();
          __builtin_amdgcn_raw_buffer_store_b32((u32)r, rsX, X_CAL + k * 8192 + c * 128, 0, 16);
          VMW0();
          best[k] = umin(best[k], (u32)__builtin_readcyclecounter() - t0);
        }
      }
      if (tid == 0) st4_sc1(WSP(u32, 0) + SY_VOTE(c), best[1] < best[0] ? 2u : 1u);
      TLS(11);
      int it = 0;
      while (c < 16 && ld4_sc1(WSP(u32, 0) + SY_WX) < 32u && ++it < SPIN_CAP) __builtin_amdgcn_s_sleep(GPS);
    }
    BARN();
    {
      const __amdgpu_buffer_rsrc_t rx = RSRC(WSP(char, O_WX), 524288);
#pragma unroll
      for (int k = 0; k < 8; k++) st[0][k] = __builtin_amdgcn_raw_buffer_load_b128(rx, lane * 16, cg * 65536 + (((k >> 1) * 8 + wid) * 128 + (k & 1) * 64) * 16, 16);
#pragma unroll
      for (int k = 0; k < 8; k++) *(u32x4*)(sc + k * 1024 + lane * 16) = st[0][k];
      __builtin_amdgcn_fence(__ATOMIC_RELEASE, "wavefront");
      __builtin_amdgcn_wave_barrier();
#pragma unroll
      for (int i = 0; i < 4; i++) w[i][4] = frag(sc + i * 2048, 16);
    }
    if (wid == 0) {
      u32 fl, fv;
      int it = 0;
      do { fl = ld4_sc1(WSP(u32, 0) + SY_INIT(lane)); fv = ld4_sc1(WSP(u32, 0) + SY_VOTE(lane & 31));
           if (__all(fl != 0u && fv != 0u)) break;
           __builtin_amdgcn_s_sleep(GPS); } while (++it < SPIN_CAP);
      *(int*)(smem + C_XP) = __popcll(__ballot(fv == 2u)) > 32 ? 8192 : 0;
    }
    BAR();
    xp = __builtin_amdgcn_readfirstlane(*(const int*)(smem + C_XP));
    wo = (lq >> 1) == hi8 ? ((l15 & 7) * 2 + (lq & 1)) * 144 + (wid * 2 + hi8) * 8 : 16 * 144 + lane * 8;
    ro = wid * 2880 + (lane >> 2) * 144 + p4 * 32;
  }
  __syncthreads();
  STS(32 + c, 14);
  TLS(9);
  int go, sidx, po;
  { IDX(); go = lane * 16; sidx = (l15 & 2) ? 0xEEEE : 0x4444;
    po = p4 == 0 ? (lane >> 4) * 16 + ((lane >> 2) & 1) * 8 + ((lane >> 3) & 1) * 4 : PJ_OOB; }
#define ONOP 7
  int b3 = 0, dur = DO0 * 64;
#pragma unroll 1
  for (int s = 0; s < 63; s++) {
    f4 d[5];
    int gb[4], pb = mo + X_M + (b3 == 2 ? 0 : b3 + 1) * 32768 + xp;
#pragma unroll
    for (int i = 0; i < 4; i++) { gb[i] = X_O + b3 * 131072 + tq * 65536 + wid * 1024 + xp + i * 16384; asm volatile("" : "+s"(gb[i])); }
    asm volatile("" : "+s"(pb));
    u32x4 g[4];
    u32 bad;
    auto mult = [&]() __attribute__((always_inline)) {
      CFENCE();
#pragma unroll
      for (int i = 0; i < 4; i++) {
        g[i] = __builtin_amdgcn_raw_buffer_load_b128(rsX, go, gb[i], 16);
        SB0();
        if (ONOP >= 0 && i < 3) asm volatile("s_nop %0" :: "i"(ONOP >= 0 ? ONOP : 0));
        SB0();
      }
      bad = 0u;
#pragma unroll
      for (int i = 0; i < 4; i++) {
        SB0();
#pragma unroll
        for (int t = 0; t < 5; t++) {
          d[t] = SMF(g[i], w[i][t], d[t], sidx);
          if (t == 0) bad = umax(umax(bad, g[i][0]), g[i][2]);
          if (i == 3 && t >= 2) { SB0(); *(f2v*)(smem + wo + (t - 2) * 2880) = (f2v){d[t - 2][0] + d[t - 2][2], d[t - 2][1] + d[t - 2][3]}; SB0(); }
        }
      }
      SB0();
#pragma unroll
      for (int t = 3; t < 5; t++) *(f2v*)(smem + wo + t * 2880) = (f2v){d[t][0] + d[t][2], d[t][1] + d[t][3]};
      SB0();
    };
#pragma unroll
    for (int t = 0; t < 5; t++) d[t] = zero4();
    SB0();
    u32 dl = 0, an0 = 0;
    if (wid == 7) AD_WAIT(false, K1, SPIN_CAP);
    STAMP(15);
    BARN();
    STAMP(0);
    mult();
    if (__builtin_expect(__any(bad == ~0u), 0)) {
      int it = 0;
      do {
#pragma unroll
        for (int t = 0; t < 5; t++) d[t] = zero4();
        mult();
      } while (__any(bad == ~0u) && ++it < SPIN_CAP);
    }
    STAMP(1);
    BARC();
    STAMP(2);
    if (wid == 7) {
      dur += (min((int)(((u32)wall_clock64() << 6) - dl), dur + DOCAP * 64) - dur) >> DSH;
      __builtin_amdgcn_raw_buffer_store_b32((u32)dur, rsX, 0, X_ANN + (~s & 1) * 1024 + xp + (32 + c) * ANS, 16);
    }
    {
      const f4 t0 = *(const f4*)(smem + ro), t1 = *(const f4*)(smem + ro + 16);
      float a = (t0[0] + t0[2]) + (t1[0] + t1[2]), b = (t0[1] + t0[3]) + (t1[1] + t1[3]);
      a += __uint_as_float(__builtin_amdgcn_mov_dpp(__float_as_uint(a), 0xB1, 0xf, 0xf, true));
      b += __uint_as_float(__builtin_amdgcn_mov_dpp(__float_as_uint(b), 0xB1, 0xf, 0xf, true));
      a += __uint_as_float(__builtin_amdgcn_mov_dpp(__float_as_uint(a), 0x4E, 0xf, 0xf, true));
      b += __uint_as_float(__builtin_amdgcn_mov_dpp(__float_as_uint(b), 0x4E, 0xf, 0xf, true));
      __builtin_amdgcn_raw_buffer_store_b32(umin(pk2(a, b), 0xFFFFFFFEu), rsX, po, pb, 16);
      SB0();
      STAMP(3);
      __builtin_amdgcn_s_sleep(4);
      __builtin_amdgcn_raw_buffer_store_b32(~0u, rsX, po, mo + X_M + b3 * 32768 + xp, 16);
    }
    VMW0C();
    b3 = b3 == 2 ? 0 : b3 + 1;
  }
  __syncthreads();
}

__device__ __forceinline__ void wait_ge(const u32* w, u32 val, const int wv) {
  if (TIDX == 0) {
    int it = 0;
    while (ld4_sc1(w) < val && ++it < SPIN_CAP) __builtin_amdgcn_s_sleep(WPS);
  }
}
__device__ __forceinline__ void acquire_all(const int wv) {
  if (TIDX == 0) {
    __builtin_amdgcn_fence(__ATOMIC_ACQUIRE, "agent");
    VMW0();
  }
  __syncthreads();
}

#define T_KV 0
#define T_Q 0
#define T_A 192
#define T_W (T_A + 64)
#define T_S (T_W + 256)
#define T_L (T_S + 72)
#define T_O T_L
#define T_CHAIN (1 << 20)
#define CODE_KB 84
#define WWARM 0
#define HWARM 112
#define AORD 0x01234567u
#define OORD 0x67014523u

__device__ __forceinline__ int next_task(const Params& p, int* s_task, int xq, int& xk, const int wv, const int pt) {
  __syncthreads();
  if (TIDX == 0) {
    int t = -1;
    while (xk < 6) {
      int x = xq + xk; if (x >= 6) x -= 6;
      const int i = (int)__hip_atomic_fetch_add(WSP(u32, 0) + SY_XQ(x), 1u, __ATOMIC_RELAXED, __HIP_MEMORY_SCOPE_AGENT);
      if (i < 32) { t = x * 32 + i; break; }
      xk++;
    }
    if (t < 0 && xk == 6) {
      xk = 7;
      int it = 0;
      while (ld4_sc1(WSP(u32, 0) + SY_NCL) < 64u && ld4_sc1(WSP(u32, 0) + SY_ARR) < 256u && ++it < SPIN_CAP) __builtin_amdgcn_s_sleep(8);
      if (ld4_sc1(WSP(u32, 0) + SY_NCL) < 64u) {
        for (int k = 0; k < 64; k++)
          if (__hip_atomic_exchange(WSP(u32, 0) + SY_CLAIM(k), 1u, __ATOMIC_RELAXED, __HIP_MEMORY_SCOPE_AGENT) == 0u) {
            __hip_atomic_fetch_add(WSP(u32, 0) + SY_NCL, 1u, __ATOMIC_RELAXED, __HIP_MEMORY_SCOPE_AGENT);
            t = T_CHAIN + k; break;
          }
      }
    }
    if (t < 0) t = T_A + (int)__hip_atomic_fetch_add(WSP(u32, 0) + SY_QHEAD, 1u, __ATOMIC_RELAXED, __HIP_MEMORY_SCOPE_AGENT);
    *s_task = t;
  }
  __syncthreads();
  return __builtin_amdgcn_readfirstlane(*s_task);
}

__device__ __forceinline__ int take_task(int* s_task, const int pre, const int wv, const int pb, const int pl, const int pt) {
  if (TIDX == 0) {
    *s_task = pre;
  }
  __syncthreads();
  const int t = __builtin_amdgcn_readfirstlane(*s_task);
  __syncthreads();
  return t;
}
#if defined(__HIP_DEVICE_COMPILE__) && !defined(KEEP_PK)
#define NOPK __attribute__((target("no-packed-fp32-ops")))
#else
#define NOPK
#endif
#define P0V 2
__device__ __forceinline__ void mega_body(const Params& p, char* smem) {
  u64 pc0; asm volatile("s_getpc_b64 %0" : "=s"(pc0));
  int* const s_task = (int*)(smem + LDS_TOTAL);
  const int wv = __builtin_amdgcn_readfirstlane(threadIdx.x >> 6);
  if (blockIdx.x == 0 && TIDX == 0) asm volatile("s_nop 0" ::: "memory");
#define tid TIDX
  const int xcc = (int)(__builtin_amdgcn_s_getreg(20 | (3 << 11)) & 7u);
  const int xq = xcc >= 2 ? xcc - 2 : xcc;
  int xk = 0, pre = 0;
  u32* const qh = WSP(u32, 0) + SY_QHEAD;
  if (wv) {
    const int g = ((blockIdx.x >> 3) & 31) * 7 + wv - 1, vo = (tid & 63) * 64;
#define WARM(ptr, bytes, so) __builtin_amdgcn_raw_ptr_buffer_load_lds(RSRC(ptr, bytes), (LDSP void*)(smem + LDS_ALL - 2048 + wv * 256), 4, vo, so, 0, 0)
    WARM(pc0, CODE_KB * 1024, g * 4096);
    if (xcc > 1) {
      const u16* w = xq < 2 ? p.BWk : xq < 4 ? p.BWv : p.BWq;
      WARM(p.Bx, 256 * 1152, (223 - g) * 4096);
#pragma unroll
      for (int i = 0; i < WWARM; i++) WARM(w, WOB, (g + i * 224) * 8192 + (xq & 1) * 4096);
    } else if (xcc == 0) {
      WARM(p.BWq, HWARM * 8192, g * 4096); WARM(p.BWk, HWARM * 8192, g * 4096); WARM(p.BWv, HWARM * 8192, g * 4096);
    }
  }
  if (tid == 0) {
    int t0 = -1;
    if (xcc < 2) {
      const int k = ((blockIdx.x >> 3) & 31) + xcc * 32;
      if (__hip_atomic_exchange(WSP(u32, 0) + SY_CLAIM(k), 1u, __ATOMIC_RELAXED, __HIP_MEMORY_SCOPE_AGENT) == 0u) {
        __hip_atomic_fetch_add(WSP(u32, 0) + SY_NCL, 1u, __ATOMIC_RELAXED, __HIP_MEMORY_SCOPE_AGENT);
        t0 = T_CHAIN + k;
      }
    } else {
      const int i = (int)__hip_atomic_fetch_add(WSP(u32, 0) + SY_XQ(xq), 1u, __ATOMIC_RELAXED, __HIP_MEMORY_SCOPE_AGENT);
      if (i < 32) t0 = xq * 32 + i;
    }
    __hip_atomic_fetch_add(WSP(u32, 0) + SY_ARR, 1u, __ATOMIC_RELAXED, __HIP_MEMORY_SCOPE_AGENT);
    *s_task = t0;
  }
  BAR();
  int t = __builtin_amdgcn_readfirstlane(*s_task);
  if (t < 0) t = next_task(p, s_task, xq, xk, wv, t);
  {
    while (t < T_A) {
      const int x = t >> 5, j = (t & 7) * 4 + ((0x9C >> ((t >> 2) & 6)) & 3);
      TLS(10);
      proj_slot(p, x, j, smem, wv);
      t = next_task(p, s_task, xq, xk, wv, t);
    }
  }
  if (t >= T_CHAIN) { if (t < T_CHAIN + 32) chain_head(p, smem, t - T_CHAIN, wv); else chain_out(p, smem, t - T_CHAIN - 32, wv);
    const int l = tid & 7;
    const u32 v = ld4_sc1(WSP(u32, 0) + (l < 7 ? SY_OQ(l) : SY_QHEAD));
    if (tid == 0) *s_task = 0;
    __syncthreads();
    if (v < (l == 7 ? T_S - T_A : l == 6 ? 72 : (l & 1) ? XS0 / 2 : 32)) *s_task = 1;
    __syncthreads();
    if (__builtin_amdgcn_readfirstlane(*s_task) == 0) {
      if (xcc == 0) TLS(3); else TLS(4);
      return;
    }
    xk = 7; t = next_task(p, s_task, xq, xk, wv, -1);
  }
  while (t < T_W) {
    wof_task(p, t - T_A, smem, wv, qh, pre);
    if (tid == 0) __hip_atomic_fetch_add(WSP(u32, 0) + SY_CW, 1u, __ATOMIC_RELAXED, __HIP_MEMORY_SCOPE_AGENT);
    t = T_A + take_task(s_task, pre, wv, T_A, T_S - T_A, t);
  }
  while (t < T_S) {
    const int qb = (AORD >> (((t - T_W) >> 5) * 4)) & 7, h = (t - T_W) & 31;
    if (tid < 64) {
      int it = 0;
      while (true) {
        const u32 f0 = ld4_sc1(WSP(u32, 0) + SY_BLK(3, h >> 1, tid)), f1 = ld4_sc1(WSP(u32, 0) + SY_BLK(4, h >> 1, tid)), f2 = ld4_sc1(WSP(u32, 0) + SY_BLK(5, h >> 1, tid));
        if (__all(tid >= qb * 8 + 8 || ((tid < qb * 8 || f0 >= 8u) && f1 >= 8u && f2 >= 8u)) || ++it >= SPIN_CAP) break;
        __builtin_amdgcn_s_sleep(8);
      }
    }
    acquire_all(wv);
    fa::attn_block(WSP(char, O_QA) + FR_OFF(qb * 256, h * 128), WSP(char, O_KA) + h * 8192, WSP(char, O_VA) + h * 8192,
                   WSP(char, O_CA) + OB_OFF(qb * 256, h * 128), qb * 256, smem, wv, qh, pre);
    if (tid == 0) __hip_atomic_fetch_add(WSP(u32, 0) + SY_CC(qb), 1u, __ATOMIC_RELAXED, __HIP_MEMORY_SCOPE_AGENT);
    t = T_A + take_task(s_task, pre, wv, T_A, T_S - T_A, t);
  }
  int rest = 64 | (1 << xq);
#pragma unroll 1
  while (rest) {
    const int x = __builtin_ctz(rest);
    rest &= rest - 1;
    if (x == 6) {
      CLAIMS();
      t = take_task(s_task, pre, wv, T_S, 72, -1);
      while ((t & 0xFFFF) < 72) {
        t &= 0xFFFF;
        small_out_task(p, t & 3, t >> 2, smem, wv, pre);
        t = take_task(s_task, pre, wv, T_S, 72, T_S + t);
      }
      rest = t >> 16;
      continue;
    }
    const int part = x & 1, cg = x >> 1, n = part ? XS0 / 2 : 32;
    u32* const qh = WSP(u32, 0) + SY_OQ(x);
    CLAIM();
    t = take_task(s_task, pre, wv, T_O + x * 32, n, -1);
    while (t < n) {
      const int rb = part ? t : ((OORD >> ((t >> 2) * 4)) & 7) * 4 + (t & 3);
      if (tid == 0) {
        const u32* w = WSP(u32, 0) + (part ? SY_CS(rb * 2 + 1) : SY_CC(rb >> 2));
        int it = 0;
        while ((ld4_sc1(w) < (part ? 1u : 32u) || ld4_sc1(WSP(u32, 0) + SY_CW) < 64u) && ++it < SPIN_CAP) __builtin_amdgcn_s_sleep(WPS);
      }
      acquire_all(wv);
      out_tile(WSP(char, part ? O_OB : O_CA) + rb * 2 * 262144, WSP(char, O_WOF) + part * WOB + cg * 12 * 131072,
               p.out + (size_t)rb * 64 * 1152 + part * 576 + cg * 192, smem, wv, qh, pre);
      t = take_task(s_task, pre, wv, T_O + x * 32, n, T_O + x * 32 + t);
    }
  }
  if (xcc == 0) TLS(3); else if (xcc == 1) TLS(4); else TLS(5);
}

#undef tid
__global__ NOPK void __launch_bounds__(512, 2) mega(Params p) {
  __shared__ __attribute__((aligned(16))) char smem[LDS_ALL];
  mega_body(p, smem);
}
extern "C" void kernel_launch(void* const* d_in, const int* in_sizes, int n_in, void* d_out, int out_size, void* d_ws,
                              size_t ws_size, hipStream_t stream) {
  Params p;
  p.Ax = (const u16*)d_in[0]; p.Ac = (const u16*)d_in[1]; p.Awq = (const u16*)d_in[2]; p.Awk = (const u16*)d_in[3];
  p.Awv = (const u16*)d_in[4]; p.Awo = (const u16*)d_in[5]; p.Bx = (const u16*)d_in[6]; p.Bm = (const u16*)d_in[7];
  p.BWq = (const u16*)d_in[8]; p.BWk = (const u16*)d_in[9]; p.BWv = (const u16*)d_in[10]; p.BWo = (const u16*)d_in[11];
  p.out = (u16*)d_out;
  p.ws = (char*)d_ws;
  const size_t syncBytes = SY_WORDS * 4;
  (void)hipMemsetAsync(d_ws, 0, syncBytes, stream);
  mega<<<256, 512, 0, stream>>>(p);
}
```

```cpp
#include <hip/hip_runtime.h>
#include <hip/hip_bf16.h>
#include <stdint.h>
#include <type_traits>

typedef unsigned short u16;
typedef unsigned int u32;
typedef unsigned long long u64;
typedef __attribute__((ext_vector_type(8))) short bf8;
typedef __attribute__((ext_vector_type(4))) short bf4;
typedef __attribute__((ext_vector_type(4))) float f4;
typedef __attribute__((ext_vector_type(2))) float f2v;
typedef __attribute__((ext_vector_type(2))) __bf16 bf2v;
typedef __attribute__((ext_vector_type(4))) unsigned u32x4;
typedef __attribute__((ext_vector_type(2))) unsigned u32x2;
typedef float f32x16 __attribute__((ext_vector_type(16)));
typedef __attribute__((ext_vector_type(8))) unsigned u32x8;
typedef __attribute__((ext_vector_type(8))) __bf16 bh8;
typedef __attribute__((ext_vector_type(16))) __bf16 bh16;
#define LDSP __attribute__((address_space(3)))

__device__ __forceinline__ u32 pk2(float a, float b) {
  f2v x = {a, b};
  bf2v y = __builtin_convertvector(x, bf2v);
  return __builtin_bit_cast(u32, y);
}
__device__ __forceinline__ u32 umin(u32 a, u32 b) { return a < b ? a : b; }
__device__ __forceinline__ u32 umax(u32 a, u32 b) { return a > b ? a : b; }
__device__ __forceinline__ float rbf(float f) { return __uint_as_float(pk2(f, f) << 16); }
#define MFMA(a, b, c) __builtin_amdgcn_mfma_f32_16x16x32_bf16(a, b, c, 0, 0, 0)
#define MFMA16(a, b, c) __builtin_amdgcn_mfma_f32_16x16x16bf16_1k(a, b, c, 0, 0, 0)
#define SMF(a, b, c, idx) __builtin_amdgcn_smfmac_f32_16x16x64_bf16(__builtin_bit_cast(bh8, a), __builtin_bit_cast(bh16, b), c, idx, 0, 0)
__device__ __forceinline__ u32x8 ilv(bf4 a, bf4 b, bf4 c, bf4 d) {
  const u32x2 x = __builtin_bit_cast(u32x2, a), y = __builtin_bit_cast(u32x2, b), z = __builtin_bit_cast(u32x2, c), w = __builtin_bit_cast(u32x2, d);
  return (u32x8){x[0], x[1], y[0], y[1], z[0], z[1], w[0], w[1]};
}
__device__ __forceinline__ u32x8 cat16(bf8 a, bf8 b) {
  const u32x4 x = __builtin_bit_cast(u32x4, a), y = __builtin_bit_cast(u32x4, b);
  return (u32x8){x[0], x[1], x[2], x[3], y[0], y[1], y[2], y[3]};
}
__device__ __forceinline__ float swadd(float a, float b) {
  auto x = __builtin_amdgcn_permlane32_swap(__float_as_uint(a), __float_as_uint(b), false, false);
  return __uint_as_float(x[0]) + __uint_as_float(x[1]);
}
__device__ __forceinline__ bf4 trrd(const char* p) {
  return __builtin_amdgcn_ds_read_tr16_b64_v4i16((LDSP bf4*)(p));
}
__device__ __forceinline__ bf8 cat8(bf4 a, bf4 b) { return __builtin_shufflevector(a, b, 0, 1, 2, 3, 4, 5, 6, 7); }
__device__ __forceinline__ void st8_sc1(void* p, u64 v) { __hip_atomic_store((u64*)p, v, __ATOMIC_RELAXED, __HIP_MEMORY_SCOPE_AGENT); }
__device__ __forceinline__ u32 ld4_sc1(const u32* p) { return __hip_atomic_load(p, __ATOMIC_RELAXED, __HIP_MEMORY_SCOPE_AGENT); }
__device__ __forceinline__ void st4_sc1(u32* p, u32 v) { __hip_atomic_store(p, v, __ATOMIC_RELAXED, __HIP_MEMORY_SCOPE_AGENT); }
#define VMW0() asm volatile("s_waitcnt vmcnt(0)" ::: "memory")
#define CFENCE() asm volatile("" ::: "memory")
#define VMW0C() do { __builtin_amdgcn_s_waitcnt(0x0F70); asm volatile("" ::: "memory"); } while (0)
#define SPIN_CAP (1 << 22)
#define XPS 4
#define SPS 4
#define WPS 8
__device__ __forceinline__ int tid_now(int wv) {
  int l; asm volatile("v_mbcnt_lo_u32_b32 %0, -1, 0\n\tv_mbcnt_hi_u32_b32 %0, -1, %0" : "=v"(l));
  return wv * 64 + l;
}
#define TIDX tid_now(wv)
#define CLAIM() do { if (TIDX == 0) pre = (int)__hip_atomic_fetch_add(qh, 1u, __ATOMIC_RELAXED, __HIP_MEMORY_SCOPE_AGENT); } while (0)

#define TLS(i)
struct Params {
  const u16 *Ax, *Ac, *Awq, *Awk, *Awv, *Awo, *Bx, *Bm, *BWq, *BWk, *BWv, *BWo;
  u16* out;
  char* ws;
};
#define SY_QHEAD 0
#define SY_CC(rb) (64 + (rb) * 16)
#define SY_CW (64 + 8 * 16)
#define SY_OQ(x) (64 + 9 * 16 + (x) * 16)
#define SY_CS(s) (64 + 16 * 16 + (s) * 16)
#define SY_B0 (64 + 80 * 16)
#define SY_INIT(c) (SY_B0 + 64 + (c))
#define SY_VOTE(i) (SY_B0 + 128 + (i))
#define SY_CLAIM(c) (SY_B0 + 192 + (c))
#define SY_XQ(x) (SY_B0 + 256 + (x) * 16)
#define SY_ARR (SY_B0 + 384)
#define SY_NCL (SY_ARR + 16)
#define SY_WX (SY_ARR + 32)
#define BLKS 1
#define SY_BLK(mi, cb, g) (SY_NCL + 48 + (((mi) * 16 + (cb)) * 64 + (g)) * BLKS)
#define SY_WORDS (SY_NCL + 48 + 6 * 16 * 64 * BLKS)

#define AL256(x) (((x) + 255) & ~255)
#define O_SYNC_END (SY_WORDS * 4)
#define ACB (2048 * 4096 * 2)
#define O_QA AL256(O_SYNC_END)
#define O_KA (O_QA + ACB)
#define O_VA (O_KA + ACB)
#define O_CA (O_VA + ACB)
#define O_QB (O_CA + ACB)
#define O_KB (O_QB + ACB)
#define O_VB (O_KB + ACB)
#define O_OB (O_VB + ACB)
#define O_WOF (O_OB + ACB)
#define WOB (576 * 4096 * 2)
#define O_WX (O_WOF + 2 * WOB)
#define O_XW (O_WX + 524288)
#define X_O 16384
#define X_M (X_O + 3 * 131072)
#define X_ANN (X_M + 3 * 32768)
#define X_CAL (X_ANN + 16384)
#define X_END (X_CAL + 16384)
#define OB_OFF(r, c) (((r) >> 5) * 262144 + ((c) >> 4) * 1024 + ((r) & 31) * 32 + ((c) & 15) * 2)
#define FR_OFF(r, c) (((r) >> 5) * 262144 + ((c) >> 3) * 512 + ((r) & 31) * 16 + ((c) & 7) * 2)
#define WSP(T, off) ((T*)(p.ws + (off)))
#define XWIN(p) (((uintptr_t)(p).ws + O_XW + 0x1FFFFFu) & ~(uintptr_t)0x1FFFFFu)

#define XS0 52
#define LDS_TOTAL (131072 + 8192 + 16384)
#define LDS_ALL 163840

#define SB0() __builtin_amdgcn_sched_barrier(0)
#define PJ_OOB 0x7F000000
#define RSRC(ptr, bytes) __builtin_amdgcn_make_buffer_rsrc((void*)(ptr), 0, (bytes), 0x00020000)
__device__ __forceinline__ bf8 ldsc1(__amdgpu_buffer_rsrc_t r, int off) {
  return __builtin_bit_cast(bf8, __builtin_amdgcn_raw_buffer_load_b128(r, off, 0, 16));
}
__device__ __forceinline__ u32x4 pair16(u32 a0, u32 a1, u32 b0, u32 b1) {
  auto r0 = __builtin_amdgcn_permlane16_swap(a0, b0, false, false);
  auto r1 = __builtin_amdgcn_permlane16_swap(a1, b1, false, false);
  return (u32x4){r0[0], r1[0], r0[1], r1[1]};
}
__device__ __forceinline__ int fsw(int k) { return (k & 3) | (((k >> 3) & 1) << 2); }

#define C_PARK 131072
#define PJ_PAD 0
__device__ __forceinline__ void proj_slot(const Params& p, int x, int j, char* smem, const int wv) {
  const int tid = TIDX, lane = tid & 63, wid = wv;
  const int l15 = lane & 15, lq = lane >> 4, q4 = l15 >> 2, p4 = lane & 3, wr = wid >> 2, wc = wid & 3;
  const __amdgpu_buffer_rsrc_t rc = RSRC(p.ws, O_XW);
  const int vB = ((wid * 4 + lq) * 4096 + (((l15 >> 1) ^ (lq | (((wid >> 1) & 1) << 2))) * 16 + (lane & 1) * 8)) * 2, vB1 = vB + 32 * 8192;
  int ao = (wr * 64 + l15) * 128 + ((lq ^ (l15 & 7)) << 4);
  int bo = (int)(uintptr_t)smem + 65536 + (lq * 8 + q4) * 256 + (((wc * 2) ^ (q4 | ((lq & 1) << 2))) << 5) + p4 * 8;
  const int vD = lane * 16;
  const int cb = (x & 1) * 8 + (j >> 2);
  __amdgpu_buffer_rsrc_t rsA, rsB;
  const u16 *pA0 = p.Bx, *pB0 = (x < 2 ? p.BWk : x < 4 ? p.BWv : p.BWq) + cb * 256;
  const u16 *pA1 = x < 2 ? p.Ax : p.Ac, *pB1 = (x < 2 ? p.Awq : x < 4 ? p.Awk : p.Awv) + cb * 256;
  asm volatile("" : "+s"(pA0), "+s"(pB0), "+s"(pA1), "+s"(pB1));
  int k2, vA, vA1, sa, sb, nvA;
#define PJ_K2(u) ((u) < 2 || x < 2 ? 1152 : 1024)
#define PJ_PREP(u) do { int ln_ = vD; asm volatile("" : "+v"(ln_)); \
    nvA = (wid * 8 + (ln_ >> 7)) * PJ_K2(u) + ((ln_ ^ (ln_ >> 3)) & 0x70); \
    asm volatile("" : "+v"(nvA)); } while (0)
#define PJ_UNIT(u) do { k2 = PJ_K2(u); rsA = RSRC((u) < 2 ? pA0 : pA1, 2048 * k2); rsB = RSRC((u) < 2 ? pB0 : pB1, k2 * 4096 - cb * 512); vA = nvA; vA1 = nvA + 64 * k2; \
    sa = ((j & 3) + ((u) & 1) * 4) * 256 * k2 - 128; sb = 0; } while (0)
#define PJ_DEST(u, rt) do { const int mi_ = ((u) >> 1) * 3 + (x >> 1), g_ = ((rt) + ((u) & 1) * 4) * 8; \
    dco = O_QA + (mi_ == 2 ? 4 : (mi_ + 5) & 7) * ACB + cb * 16384 + wc * 2048 + (g_ + wr * 2) * 262144; \
    dfl = SY_BLK(mi_, cb, g_) * 4; } while (0)
#define PJ_STAGE(dst, rs, v0, v1, so) do { const int so_ = (so); \
    __builtin_amdgcn_raw_ptr_buffer_load_lds(rs, (LDSP void*)(smem + (dst) + wid * 1024), 16, v0, so_, 0, 0); \
    __builtin_amdgcn_raw_ptr_buffer_load_lds(rs, (LDSP void*)(smem + (dst) + wid * 1024 + 8192), 16, v1, so_, 0, 0); } while (0)
#define PJ_SA(dst, so) PJ_STAGE(dst, rsA, vA, vA1, so)
#define PJ_SB(dst, so) PJ_STAGE(dst, rsB, vB, vB1, so)
#define PJ_LDA(off) do { _Pragma("unroll") for (int m = 0; m < 4; m++) _Pragma("unroll") for (int k = 0; k < 2; k++) \
    At[m][k] = *(const bf8*)(smem + (ao ^ (k * 64)) + (off) + m * 2048); } while (0)
#define PJ_TR(dst, a, off) asm volatile("ds_read_b64_tr_b16 %0, %1 offset:%2" : "=&v"(dst) : "v"(a), "i"(off) : "memory")
#define PJ_LDB(D, h) do { const int b1_ = bo ^ 32; _Pragma("unroll") for (int n = 0; n < 2; n++) _Pragma("unroll") for (int k = 0; k < 2; k++) { \
    bf4 lo_, hi_; PJ_TR(lo_, n ? b1_ : bo, (h) * 16384 + k * 8192); PJ_TR(hi_, n ? b1_ : bo, (h) * 16384 + k * 8192 + 1024); D[n][k] = cat8(lo_, hi_); } } while (0)
#define PJ_CV(ai, bj, q) ((u32x4){pk2(acc[ai][bj][(q) & 2][(q) & 1][0], acc[ai][bj][(q) & 2][(q) & 1][1]), pk2(acc[ai][bj][(q) & 2][(q) & 1][2], acc[ai][bj][(q) & 2][(q) & 1][3]), \
                                  pk2(acc[ai][bj][((q) & 2) + 1][(q) & 1][0], acc[ai][bj][((q) & 2) + 1][(q) & 1][1]), pk2(acc[ai][bj][((q) & 2) + 1][(q) & 1][2], acc[ai][bj][((q) & 2) + 1][(q) & 1][3])})
#define PJ_SW(v) pair16(v[0], v[1], v[2], v[3])
#define PJ_MMA(Z, ai, bj, D, F) do { PJ_SCHED; __builtin_amdgcn_s_setprio(1); \
    _Pragma("unroll") for (int k = 0; k < 2; k++) _Pragma("unroll") for (int m = 0; m < 4; m++) _Pragma("unroll") for (int n = 0; n < 2; n++) { \
      if (k == 0 && (Z)) asm volatile("v_mfma_f32_16x16x32_bf16 %0, %1, %2, 0" : "+v"(acc[ai][bj][m][n]) : "v"(D[n][k]), "v"(At[m][k])); \
      else asm volatile("v_mfma_f32_16x16x32_bf16 %0, %1, %2, %0" : "+v"(acc[ai][bj][m][n]) : "v"(D[n][k]), "v"(At[m][k])); \
      F(k * 8 + m * 2 + n); } \
    __builtin_amdgcn_s_setprio(0); if (PJ_PAD) __builtin_amdgcn_s_sleep(PJ_PAD); PJ_SCHED; } while (0)
#define PJ_SWI(a, b) asm volatile("v_permlane16_swap_b32 %0, %1" : "+v"(a), "+v"(b))
#define PJ_CVI(d, s, e) asm volatile("v_cvt_pk_bf16_f32 %0, %1, %2" : "=v"(d) : "v"(s[e]), "v"(s[(e) + 1]))
#define PJ_PACK(P, ai, bj, i) do { const int q_ = (i) >> 2; \
    if (((i) & 3) < 2) { PJ_CVI(P[q_][(i) & 1], acc[ai][bj][q_ & 2][q_ & 1], ((i) & 1) * 2); PJ_CVI(P[q_][2 + ((i) & 1)], acc[ai][bj][(q_ & 2) + 1][q_ & 1], ((i) & 1) * 2); } \
    else PJ_SWI(P[q_][(i) & 1], P[q_][2 + ((i) & 1)]); } while (0)
#define PJ_WV(n) asm volatile("s_waitcnt vmcnt(" #n ")" ::: "memory")
#define PJ_WL(n) asm volatile("s_waitcnt lgkmcnt(" #n ")" ::: "memory")
#define PJ_WL0 do { __builtin_amdgcn_s_waitcnt(0xC07F); asm volatile("" ::: "memory"); } while (0)
#define PJ_BAR __builtin_amdgcn_s_barrier()
#define PJ_SCHED __builtin_amdgcn_sched_barrier(0)
#define PJ_NOP do { PJ_SCHED; asm volatile("s_nop 7"); PJ_SCHED; } while (0)
#define PJ_DS(v, ai, bj, q) do { PJ_SCHED; __builtin_amdgcn_raw_buffer_store_b128(v, rc, vD + ((q) & 1) * 1024, dco + ((ai) * 4 + ((q) >> 1)) * 262144 + (bj) * 8192, 16); \
    asm volatile("s_nop 1"); PJ_SCHED; } while (0)
#define PJ_PARK(q) (*(u32x4*)(smem + C_PARK + wid * 4096 + vD + (q) * 1024))
#define PJ_PUB(ai, n) do { if (wid == 4) __builtin_amdgcn_raw_buffer_store_b32(8u, rc, vD < (n) * 16 ? vD >> 2 : PJ_OOB, dfl + (ai) * 16, 16); } while (0)
#define PJ_ST()
#define PJ_TOUCH(ptr, bytes, vo, so) __builtin_amdgcn_raw_ptr_buffer_load_lds(RSRC(ptr, bytes), (LDSP void*)(smem + C_PARK + wid * 4096), 4, vo, so, 0, 0)
#define PJ_WARM(S) do { int vt_ = vD; asm volatile("" : "+v"(vt_)); vt_ <<= 2; \
    const int g_ = u > 1 || !(j & 3) ? PJ_OOB : ((((x & 1) * 8 + (j >> 2)) * 3 + (j & 3) - 1) * 8 + wid) * 4096, h_ = g_ + (x >> 1) * 384 * 4096; \
    const u16* w_ = pB1 - cb * 256; \
    if (S == 2) PJ_TOUCH(u ? w_ : pA0 + 1024 * 576, u ? 4096 * PJ_K2(2) : 1024 * 1152, vt_, u ? g_ + 384 * 4096 : h_); \
    if (S == 3) PJ_TOUCH(u ? w_ : pA1, (u ? 4096 : 2048) * PJ_K2(2), vt_, u ? g_ + 768 * 4096 : g_); \
    if (S == 4) PJ_TOUCH(u ? p.Awo : pA1, u ? WOB : 2048 * PJ_K2(2), vt_, u ? h_ : g_ + 384 * 4096); \
    if (S == 5) PJ_TOUCH(w_, 4096 * PJ_K2(2), vt_, u ? PJ_OOB : g_); } while (0)
#define PJ_F0(i)
#define PJ_F1(i) do { if (S == 8) PJ_PACK(pa, 0, 0, i); } while (0)
#define PJ_F2(i) do { if (S == 8) PJ_PACK(pb, 0, 1, i); if (S == 0) PJ_PACK(pb, 1, 1, i); } while (0)
#define PJ_F3(i) do { if (S == 8) PJ_PACK(pa, 1, 0, i); if (S == 5 && (i) == 0) { PJ_SCHED; PJ_PREP(u < 3 ? u + 1 : 3); PJ_SCHED; } } while (0)
#define PJ_STEP(SS) do { constexpr int S = SS; PJ_ST(); \
    PJ_LDB(B0, 0); PJ_SCHED; PJ_LDA(0); PJ_SA((bsel ^ 32768) + 16384, sa + 128 * k2); \
    PJ_WL(8); PJ_BAR; PJ_WL0; PJ_MMA(S == 0, 0, 0, B0, PJ_F0); PJ_BAR; PJ_SCHED; \
    if (S == 7) PJ_UNIT(u < 3 ? u + 1 : 3); \
    PJ_LDB(B1, 1); PJ_SB(65536 + bsel, sb); \
    if (S == 0) { PJ_DS(pb[2], 0, 1, 2); PJ_DS(pb[3], 0, 1, 3); } \
    if (S >= 2 && S < 6) PJ_WARM(S); \
    PJ_BAR; PJ_WL0; PJ_MMA(S == 0, 0, 1, B1, PJ_F1); PJ_BAR; \
    PJ_LDA(16384); sa += 128; PJ_SA(bsel, sa); \
    if (S == 8) { PJ_SCHED; PJ_DEST(u, j & 3); _Pragma("unroll") for (int q = 0; q < 4; q++) PJ_PARK(q) = pa[q]; PJ_SCHED; } \
    PJ_BAR; PJ_WL0; PJ_MMA(S == 0, 1, 0, B0, PJ_F2); PJ_BAR; PJ_SCHED; \
    { u32x4 c0_, c1_; \
      if (S < 2) { c0_ = PJ_PARK(2 * S); c1_ = PJ_PARK(2 * S + 1); PJ_SCHED; } \
      PJ_SB(65536 + bsel + 16384, sb + 256); sb += 64 * 8192; \
      if (S == 8) { PJ_DS(pb[0], 0, 1, 0); PJ_DS(pb[1], 0, 1, 1); } \
      else if (S < 2) { PJ_DS(c0_, 0, 0, (2 * S) & 3); PJ_DS(c1_, 0, 0, (2 * S + 1) & 3); } \
      else if (S < 4) { PJ_DS(pa[(2 * S) & 3], 1, 0, (2 * S) & 3); PJ_DS(pa[(2 * S + 1) & 3], 1, 0, (2 * S + 1) & 3); } \
      else if (S < 6) { PJ_DS(pb[(2 * S) & 3], 1, 1, (2 * S) & 3); PJ_DS(pb[(2 * S + 1) & 3], 1, 1, (2 * S + 1) & 3); } } \
      \
    if (S == 0) PJ_WV(10); else if (S >= 2 && S < 6) PJ_WV(9); else if (S == 1 || S == 8) PJ_WV(8); else PJ_WV(6); \
    PJ_BAR; \
    if (S == 2) PJ_PUB(0, 4); \
    if (S == 7) PJ_PUB(1, 4); \
    PJ_MMA(S == 0, 1, 1, B1, PJ_F3); PJ_BAR; \
    bsel ^= 32768; ao ^= 32768; bo ^= 32768; } while (0)
  f4 acc[2][2][4][2];
  bf8 At[4][2], B0[2][2], B1[2][2];
  u32x4 pa[4], pb[4];
  int bsel = 0, dco = PJ_OOB, dfl = PJ_OOB, u = 0;
  auto quick = [&](auto NBC, const int hf, const int ch) __attribute__((always_inline)) {
    constexpr int NB = decltype(NBC)::value, NS = 5 - NB;
#define PJ_FS(t) do { PJ_SA(((t) % NS) * 16384, hf * 128 * 1152 + (t) * 128); \
      _Pragma("unroll") for (int h_ = 0; h_ < NB; h_++) PJ_SB(NS * 16384 + (((t) % NS) * NB + h_) * 16384, (t) * 64 * 8192 + (ch + h_) * 256); PJ_SCHED; } while (0)
#pragma unroll
    for (int t = 0; t < NS - 1; t++) PJ_FS(t);
    bo += NS * 16384 - 65536;
#pragma unroll
    for (int t = 0; t < 9; t++) {
      PJ_ST();
      if (t == 8) PJ_WV(0); else if (NB == 2) PJ_WV(6); else if (t == 7) PJ_WV(4); else PJ_WV(8);
      PJ_BAR; PJ_SCHED;
      if (wr && t + NS - 1 < 9) PJ_FS(t + NS - 1);
      PJ_LDB(B0, 0); PJ_LDA((t % NS) * 16384);
      if (NB == 2) PJ_LDB(B1, 1);
      PJ_SCHED;
      bo += t == 8 ? 65536 - NS * 16384 - (8 % NS) * NB * 16384 : (t % NS == NS - 1 ? -(NS - 1) * NB * 16384 : NB * 16384);
      PJ_WL0;
      PJ_MMA(t == 0, 0, 0, B0, PJ_F0);
      if (NB == 2) PJ_MMA(t == 0, 0, 1, B1, PJ_F0);
      if (!wr && t + NS - 1 < 9) PJ_FS(t + NS - 1);
    }
    PJ_NOP;
#pragma unroll
    for (int q = 0; q < 4 * NB; q++) { const u32x4 v = PJ_CV(0, q >> 2, q & 3); PJ_DS(PJ_SW(v), hf, ch + (q >> 2), q & 3); }
    PJ_WV(0); PJ_BAR;
    if (tid < 4) __hip_atomic_fetch_add(WSP(u32, dfl) + hf * 4 + tid, 4u * NB, __ATOMIC_RELAXED, __HIP_MEMORY_SCOPE_AGENT);
  };
  if (((j + 1) & 3) < 2) {
    PJ_PREP(0); PJ_UNIT(0); PJ_DEST(0, 0);
    quick(std::integral_constant<int, 1>{}, 0, j & 1);
    if ((j & 3) == 0) { quick(std::integral_constant<int, 2>{}, 1, 0); u = 1; }
    dco = PJ_OOB; dfl = PJ_OOB;
  }
#pragma unroll
  for (int a = 0; a < 16; a++) asm volatile("" : "=v"(acc[a >> 3][(a >> 2) & 1][a & 3][0]), "=v"(acc[a >> 3][(a >> 2) & 1][a & 3][1]));
#pragma unroll
  for (int a = 0; a < 4; a++) asm volatile("" : "=v"(pa[a]), "=v"(pb[a]));
  PJ_PREP(u); PJ_UNIT(u);
  const int cs = x < 4 && (j & 3) == 2 ? (x * 8 + (j >> 2)) * 128 : -1;
#pragma unroll
  for (int i = 0; i < 2; i++) pa[i] = __builtin_amdgcn_raw_buffer_load_b128(RSRC(p.BWo, WOB), (tid >> 3) * 1152 + (tid & 7) * 16, cs >= 0 ? (cs + i * 64) * 1152 + 1024 : PJ_OOB, 0);
  PJ_SCHED;
  PJ_SB(65536, 0); PJ_SA(0, sa + 128);
  PJ_SB(65536 + 16384, 256); PJ_SA(16384, sa + 128 + 128 * k2);
  PJ_SCHED;
#pragma unroll
  for (int i = 0; i < 2; i++) __builtin_amdgcn_raw_buffer_store_b128(pa[i], rc, (tid & 7) * 65536 + (tid >> 3) * 16, cs >= 0 ? O_WX + (cs + i * 64) * 16 : PJ_OOB, 16);
  PJ_SCHED;
  if (wr == 1) PJ_BAR;
  PJ_WV(6); PJ_BAR;
  PJ_SB(65536 + 32768, 64 * 8192); PJ_SA(32768, sa + 256);
  PJ_SB(65536 + 32768 + 16384, 64 * 8192 + 256);
  PJ_WV(6); PJ_BAR;
  if (tid == 256 && cs >= 0) __hip_atomic_fetch_add(WSP(u32, 0) + SY_WX, 1u, __ATOMIC_RELAXED, __HIP_MEMORY_SCOPE_AGENT);
  sa += 256; sb = 128 * 8192;
#pragma unroll 1
  for (; u < 4; u++) {
    PJ_STEP(0); PJ_STEP(1); PJ_STEP(2); PJ_STEP(3); PJ_STEP(4); PJ_STEP(5);
    int more = k2 - 1024; asm volatile("" : "+s"(more));
    if (more) PJ_STEP(6);
    PJ_STEP(7); PJ_STEP(8);
  }
  PJ_ST();
  PJ_NOP;
  PJ_DS(pb[2], 0, 1, 2); PJ_DS(pb[3], 0, 1, 3);
#pragma unroll
  for (int q = 0; q < 4; q++) pb[q] = PJ_PARK(q);
#pragma unroll
  for (int q = 0; q < 4; q++) PJ_DS(pa[q], 1, 0, q);
#pragma unroll
  for (int q = 0; q < 4; q++) PJ_DS(pb[q], 0, 0, q);
#pragma unroll
  for (int q = 0; q < 4; q++) { const u32x4 v = PJ_CV(1, 1, q); PJ_DS(PJ_SW(v), 1, 1, q); }
  PJ_WV(0);
  if (wr == 0) PJ_BAR;
  PJ_BAR;
  PJ_PUB(0, 8);
  PJ_ST();
}

namespace fa {
constexpr float SCALE = 0.08838834764831845f;
constexpr int NW = 8, QBLK = 32, KVBLK = 64, QB = NW * QBLK, D = 128;
constexpr int SHM_V = KVBLK * D * 2, SHM_K = KVBLK * D * 2;
#define SBAR() __builtin_amdgcn_sched_barrier(0)
#define MF32(a, b, c) __builtin_amdgcn_mfma_f32_32x32x16_bf16(a, b, c, 0, 0, 0)
#define FMX3(a, b, c) __builtin_elementwise_maximum(__builtin_elementwise_maximum(a, b), c)
#define AT_PAD 0
#define EBAR 14
#define MX0 3
#define ATHR 8.f
#define fadd1(a, b) ((a) + (b))
#define fmul1(a, b) ((a) * (b))
__device__ __forceinline__ void attn_block(const char* __restrict__ Qp, const char* __restrict__ Kh, const char* __restrict__ Vh,
                                           char* __restrict__ Op, int P0, char* lds, const int wv, u32* qh, int& pre) {
  const int tid = TIDX, wid = wv, lane = tid & 63, r32 = lane & 31, hi = lane >> 5, grp = wid >> 2;
  const int NT = (P0 + QB) / KVBLK;
  const int rb = wid ^ 4;
  const int TL = NT - 4 + (rb >> 1);
  const int rem = 2 * (NT - TL) - 2 - grp;
  float l_reg = 0.f;
  f32x16 o[4] = {}, s0, s1, nm = {};
  int ko = lane * 16;
  int vo = 4 * SHM_K + ((lane >> 4) & 1) * (1024 + 128) + ((lane >> 1) & 1) * 512 + ((hi ^ ((lane >> 1) & 1)) << 6) + ((lane >> 2) & 3) * 16 + (lane & 1) * 8;
  const __amdgpu_buffer_rsrc_t rk = RSRC(Kh, (2 * NT - 1) * 262144 + 8192), rv = RSRC(Vh, (2 * NT - 1) * 262144 + 8192);
  const int gk = tid * 16, gv = gk ^ (((tid >> 5) & 3) << 6);
#define DMA(rs, go, dst, so) __builtin_amdgcn_raw_ptr_buffer_load_lds(rs, (LDSP void*)(lds + (dst) + wid * 1024), 16, go, so, 0, 0)
#define ISSUE_K(t) do { const int s_ = (t) * 524288, d_ = ((t) & 3) * SHM_K; DMA(rk, gk, d_, s_); DMA(rk, gk, d_ + 8192, s_ + 262144); } while (0)
#define ISSUE_V(t) do { const int s_ = (t) * 524288, d_ = 4 * SHM_K + ((t) & 3) * SHM_V; DMA(rv, gv, d_, s_); DMA(rv, gv, d_ + 8192, s_ + 262144); } while (0)
#define PHASE(n) do { SBAR(); asm volatile("s_waitcnt vmcnt(" #n ")" ::: "memory"); __builtin_amdgcn_s_barrier(); asm volatile("" ::: "memory"); SBAR(); } while (0)
#define LDK(d0) do { kf[(d0) % 3][0] = *(const bf8*)(lds + ko + (d0) * 1024); kf[(d0) % 3][1] = *(const bf8*)(lds + ko + (d0) * 1024 + 8192); } while (0)
#define LDV(i) do { const int o_ = ((i) & 3) * 2048 + (((i) >> 2) & 1) * 256 + ((i) >> 3) * 8192; vf[(i) & 3] = cat8(trrd(lds + vo + o_), trrd(lds + vo1 + o_)); } while (0)
#define AST(i)
#define AP(i)
  AP(0);
#define QK(NEXT) do { _Pragma("unroll") for (int d0 = 0; d0 < 8; ++d0) { \
      if (d0 + 2 < 8) LDK(d0 + 2); else { NEXT(2 * (d0 - 6)); NEXT(2 * (d0 - 6) + 1); } \
      SBAR(); \
      if (d0) { s0 = MF32(kf[d0 % 3][0], qr[d0], s0); s1 = MF32(kf[d0 % 3][1], qr[d0], s1); } \
      else { asm volatile("v_mfma_f32_32x32x16_bf16 %0, %1, %2, %3" : "+v"(s0) : "v"(kf[0][0]), "v"(qr[0]), "v"(nm)); \
             asm volatile("v_mfma_f32_32x32x16_bf16 %0, %1, %2, %3" : "+v"(s1) : "v"(kf[0][1]), "v"(qr[0]), "v"(nm)); } \
      SBAR(); } } while (0)
#define NOV(i)
#define MAXS(i) do { \
      if ((i) == 0) { mx = FMX3(s0[0], s0[1], s0[2]); my = FMX3(s1[0], s1[1], s1[2]); } \
      else if ((i) < 7) { mx = FMX3(mx, s0[2 * (i) + 1], s0[2 * (i) + 2]); my = FMX3(my, s1[2 * (i) + 1], s1[2 * (i) + 2]); } \
      else { mx = FMX3(mx, s0[15], s1[15]); mx = __builtin_elementwise_maximum(mx, my); asm volatile("" : "+v"(mx)); } } while (0)
  float mx, my;
  constexpr float C2 = 1.4426950408889634f * SCALE;
  bf8 qr[8], kf[3][2], vf[4], pa[4];
#pragma unroll
  for (int d0 = 0; d0 < 8; ++d0) qr[d0] = *(const bf8*)(Qp + rb * 262144 + d0 * 1024 + lane * 16);
  SBAR();
  ISSUE_K(0); ISSUE_K(1); ISSUE_V(0); ISSUE_K(2); ISSUE_V(1);
  SBAR();
  asm volatile("" : "=v"(s0), "=v"(s1), "+v"(nm), "+v"(o[0]), "+v"(o[1]), "+v"(o[2]), "+v"(o[3]));
  SBAR();
#pragma unroll
  for (int d0 = 0; d0 < 8; ++d0) {
    u32x4 q = __builtin_bit_cast(u32x4, qr[d0]);
#pragma unroll
    for (int i = 0; i < 4; i++) q[i] = pk2(__uint_as_float(q[i] << 16) * C2, __uint_as_float(q[i] & 0xFFFF0000u) * C2);
    qr[d0] = __builtin_bit_cast(bf8, q);
    asm volatile("" : "+v"(qr[d0]));
  }
  AP(8);
  PHASE(8);
  AP(1);
  if (grp) PHASE(6);
  LDK(0); LDK(1);
  QK(NOV);
#pragma unroll
  for (int i = 0; i < 8; ++i) MAXS(i);
  AP(2);
  PHASE(4);
  AP(3);
  auto tile = [&](const int t, auto LASTC) __attribute__((always_inline)) {
    constexpr bool LAST = decltype(LASTC)::value;
    AST(0);
    ISSUE_K(t + 3); ISSUE_V(t + 2);
    SBAR();
    if constexpr (LAST) {
      const int dq = r32 - 4 * hi;
      if (rb & 1) {
#pragma unroll
        for (int r = 0; r < 16; ++r) { if (dq < (r & 3) + 8 * (r >> 2)) s1[r] = -__builtin_inff(); SBAR(); }
      } else {
#pragma unroll
        for (int r = 0; r < 16; ++r) { if (dq < (r & 3) + 8 * (r >> 2)) s0[r] = -__builtin_inff(); s1[r] = -__builtin_inff(); SBAR(); }
      }
#pragma unroll
      for (int i = 0; i < 8; ++i) MAXS(i);
    }
    {
      if (__builtin_expect(t == 0 || __any(mx > ATHR), 0)) {
        auto rr = __builtin_amdgcn_permlane32_swap(__float_as_uint(mx), __float_as_uint(mx), false, false);
        mx = __builtin_elementwise_maximum(__uint_as_float(rr[0]), __uint_as_float(rr[1]));
        if (t) mx = __builtin_elementwise_maximum(mx, 0.f);
        const float alpha = t ? __builtin_amdgcn_exp2f(-mx) : 1.f, nn = nm[0] - mx;
        mx = -mx;
#pragma unroll
        for (int r = 0; r < 16; ++r) { s0[r] = fadd1(s0[r], mx); s1[r] = fadd1(s1[r], mx); nm[r] = nn; }
#pragma unroll
        for (int d = 0; d < 4; ++d)
#pragma unroll
          for (int r = 0; r < 16; ++r) o[d][r] = fmul1(o[d][r], alpha);
        l_reg *= alpha;
        asm volatile("" : "+v"(nm));
      }
    }
    AST(1);
    float x[32], sb = 0.f;
    u32 pw[16];
#pragma unroll
    for (int r = 0; r < 18; ++r) {
      SBAR();
      if (r < 16) { x[2 * r] = __builtin_amdgcn_exp2f(r < 8 ? s0[2 * r] : s1[2 * r - 16]); x[2 * r + 1] = __builtin_amdgcn_exp2f(r < 8 ? s0[2 * r + 1] : s1[2 * r - 15]); }
      if (r >= 2) {
        l_reg = fadd1(l_reg, x[2 * r - 4]);
        sb = r == 2 ? x[1] : fadd1(sb, x[2 * r - 3]);
        pw[r - 2] = pk2(x[2 * r - 4], x[2 * r - 3]); }
    }
    SBAR();
    l_reg = fadd1(l_reg, sb);
#pragma unroll
    for (int i = 0; i < 4; ++i) pa[i] = __builtin_bit_cast(bf8, (u32x4){pw[4 * i], pw[4 * i + 1], pw[4 * i + 2], pw[4 * i + 3]});
    asm volatile("" : "+v"(pa[0]), "+v"(pa[1]), "+v"(pa[2]), "+v"(pa[3]), "+v"(l_reg));
    ko ^= (t & 1) ? 3 * SHM_K : SHM_K;
    const int vo1 = vo ^ 128;
    SBAR();
    AST(3);
    if constexpr (!LAST) { LDK(0); LDK(1); } else { LDV(0); LDV(1); LDV(2); LDV(3); }
    AST(4);
    PHASE(4);
    AST(5);
    __builtin_amdgcn_s_setprio(1);
    if constexpr (!LAST) QK(LDV);
    AST(6);
#pragma unroll
    for (int i = 0; i < 16; ++i) {
      SBAR();
      o[i & 3] = MF32(vf[i & 3], pa[i >> 2], o[i & 3]);
      SBAR();
      if (i + 4 < 16) LDV(i + 4);
      if constexpr (!LAST) if (i >= MX0 && i < MX0 + 8) MAXS(i - MX0);
      if (AT_PAD && i == EBAR) __builtin_amdgcn_s_sleep(AT_PAD);
      if (i == EBAR && (!LAST || rem)) PHASE(4);
    }
    __builtin_amdgcn_s_setprio(0);
    vo ^= (t & 1) ? 3 * SHM_V : SHM_V;
    AST(7);
  };
#pragma unroll 1
  for (int t = 0; t < TL; t++) {
    tile(t, std::false_type{});
  }
  AP(4);
  tile(TL, std::true_type{});
  AP(5);
  ISSUE_V(TL + 3);
  SBAR();
  {
    const float inv = __builtin_amdgcn_rcpf(swadd(l_reg, l_reg));
    const __amdgpu_buffer_rsrc_t ro = RSRC(Op + rb * 262144, 8192);
#pragma unroll
    for (int i = 0; i < 8; ++i) {
      const int g = (i & 1) * 8;
#define ON(k) fmul1(o[i >> 1][g + (k)], inv)
      auto r0 = __builtin_amdgcn_permlane32_swap(pk2(ON(0), ON(1)), pk2(ON(4), ON(5)), false, false);
      auto r1 = __builtin_amdgcn_permlane32_swap(pk2(ON(2), ON(3)), pk2(ON(6), ON(7)), false, false);
#undef ON
      __builtin_amdgcn_raw_buffer_store_b128((u32x4){r0[0], r1[0], r0[1], r1[1]}, ro, r32 * 32 + hi * 16 + (i & 3) * 1024, (i >> 2) * 4096, 16);
      if (i == 3 && rem > 1) PHASE(6);
    }
  }
  AP(6);
  if (rem > 2) PHASE(10);
  if (rem > 3) { PHASE(8); PHASE(8); }
  CLAIM();
  VMW0();
  AP(7);
  __syncthreads();
}
#undef DMA
#undef ISSUE_K
#undef ISSUE_V
#undef PHASE
#undef LDK
#undef LDV
#undef QK
#undef NOV
#undef MAXS
}

__device__ __forceinline__ void wof_task(const Params& p, int kc, char* smem, const int wv, u32* qh, int& pre) {
  const int tid = TIDX, lane = tid & 63, wid = wv;
  const int l15 = lane & 15, lq = lane >> 4, q4 = l15 >> 2, p4 = lane & 3;
  const __amdgpu_buffer_rsrc_t rw = RSRC(WSP(u16, O_WOF), 2 * WOB);
  bf8 st[2][9];
#pragma unroll
  for (int m = 0; m < 2; m++)
#pragma unroll
    for (int i = 0; i < 9; i++) st[m][i] = *(const bf8*)((m ? p.BWo : p.Awo) + (size_t)kc * 64 * 576 + (tid + 512 * i) * 8);
  CLAIM();
#pragma unroll
  for (int m = 0; m < 2; m++) {
#pragma unroll
    for (int i = 0; i < 9; i++) {
      const int id = tid + 512 * i, row = id / 72, ch = id % 72;
      *(bf8*)(smem + row * 1280 + (((ch >> 1) ^ fsw(row)) << 5) + ((ch & 1) << 4)) = st[m][i];
    }
    asm volatile("s_waitcnt lgkmcnt(0)" ::: "memory"); __builtin_amdgcn_s_barrier(); asm volatile("" ::: "memory");
#pragma unroll
    for (int i = 0; i < 9; i++) {
      const int f = wid * 9 + i, nt = f >> 1, k2 = f & 1;
      const int r0 = k2 * 32 + lq * 8 + q4, r1 = r0 + 4;
      bf4 lo = trrd(smem + r0 * 1280 + ((nt ^ fsw(r0)) << 5) + p4 * 8);
      bf4 hi = trrd(smem + r1 * 1280 + ((nt ^ fsw(r1)) << 5) + p4 * 8);
      __builtin_amdgcn_raw_buffer_store_b128(__builtin_bit_cast(u32x4, cat8(lo, hi)), rw, ((nt * 128 + kc * 2 + k2) * 64 + lane) * 16, m * WOB, 16);
    }
    if (m == 0) { asm volatile("s_waitcnt lgkmcnt(0)" ::: "memory"); __builtin_amdgcn_s_barrier(); asm volatile("" ::: "memory"); }
  }
  VMW0();
  __syncthreads();
}

#define OT_AAUX 0
#define OT_BAUX 0
__device__ __forceinline__ void out_tile(const char* A, const char* W, u16* C, char* smem, const int wv, u32* qh, int& pre) {
  const int tid = TIDX, lane = tid & 63, wid = wv;
  const int l15 = lane & 15, lq = lane >> 4, kh = wid >> 2, wc = wid & 3;
  const __amdgpu_buffer_rsrc_t ra = RSRC(A, 2 * 262144), rw = RSRC(W + wc * 3 * 131072 + kh * 1024, 3 * 131072 - kh * 1024);
  const int va = tid * 16, vw = lane * 16;
  int ao = kh * 2048 + (lq >> 1) * 1024 + l15 * 32 + (lq & 1) * 16;
  f4 acc[4][3];
  bf8 wf[8][3], af[2][4];
#define OT_DMA(i, buf, so) __builtin_amdgcn_raw_ptr_buffer_load_lds(ra, (LDSP void*)(smem + (buf) + (i) * 8192 + wid * 1024), 16, va, (so) + ((i) >> 1) * 262144 + ((i) & 1) * 8192, 0, OT_AAUX)
#define OT_LDW(u, so) do { _Pragma("unroll") for (int j = 0; j < 3; j++) \
    wf[u][j] = __builtin_bit_cast(bf8, __builtin_amdgcn_raw_buffer_load_b128(rw, vw, (so) + j * 131072, OT_BAUX)); } while (0)
#define OT_LDA(d, base, off) do { _Pragma("unroll") for (int m = 0; m < 4; m++) af[d][m] = *(const bf8*)(smem + (base) + (off) + (m >> 1) * 16384 + (m & 1) * 512); } while (0)
#define OT_MF(a, b, c) MFMA(a, b, c)
#define OT_XA(v) (v)
#define OT_XW(v) (v)
#pragma unroll
  for (int i = 0; i < 4; i++) OT_DMA(i, 0, 0);
  SB0();
#pragma unroll
  for (int u = 0; u < 8; u++) { OT_DMA(u & 3, 32768 + (u >> 2) * 32768, 16384 + (u >> 2) * 16384); OT_LDW(u, u * 2048); SB0(); }
#pragma unroll
  for (int m = 0; m < 4; m++)
#pragma unroll
    for (int j = 0; j < 3; j++) acc[m][j] = (f4){0, 0, 0, 0};
  asm volatile("s_waitcnt vmcnt(32)" ::: "memory"); __builtin_amdgcn_s_barrier(); asm volatile("" ::: "memory");
  OT_LDA(0, ao, 0);
  int bs = 0;
#pragma unroll 1
  for (int c = 0; c < 16; c += 2) {
    const int sw = c < 14 ? OT_XW((c + 2) * 8192) : PJ_OOB;
#pragma unroll
    for (int u = 0; u < 8; u++) {
      SB0();
      __builtin_amdgcn_s_waitcnt(0x4F7C);
      if ((u & 3) == 3) { asm volatile("s_waitcnt lgkmcnt(0)" ::: "memory"); __builtin_amdgcn_s_barrier(); asm volatile("" ::: "memory"); }
      if (u == 7) ao ^= 65536;
      OT_LDA((u + 1) & 1, ao, u == 7 ? 0 : ((u + 1) >> 2) * 32768 + ((u + 1) & 3) * 4096);
      SB0();
#define OT_V 1
      const int cn = c + 3 + (u >> 2);
#pragma unroll
      for (int j = 0; j < 3; j++) {
#pragma unroll
        for (int m = 0; m < 4; m++) {
          acc[m][j] = OT_MF(wf[u][j], af[u & 1][m], acc[m][j]);
          if (j == 0 && m == 1) { SB0(); OT_DMA(u & 3, u < 4 ? (bs ^ 65536) + 32768 : bs, cn < 16 ? OT_XA(cn * 16384) : PJ_OOB); SB0(); }
        }
        SB0();
        wf[u][j] = __builtin_bit_cast(bf8, __builtin_amdgcn_raw_buffer_load_b128(rw, vw, sw + u * 2048 + j * 131072, OT_BAUX));
        SB0();
      }
    }
    bs ^= 65536;
  }
  CLAIM();
  VMW0C();
  asm volatile("s_waitcnt lgkmcnt(0)" ::: "memory"); __builtin_amdgcn_s_barrier(); asm volatile("" ::: "memory");
#pragma unroll
  for (int m = 0; m < 2; m++)
#pragma unroll
    for (int j = 0; j < 3; j++) *(f4*)(smem + (m * 3 + j) * 8192 + va) = kh ? acc[m][j] : acc[2 + m][j];
  __syncthreads();
#pragma unroll
  for (int m = 0; m < 2; m++) {
    f4 t[3];
#pragma unroll
    for (int j = 0; j < 3; j++) { const f4 o = *(const f4*)(smem + (m * 3 + j) * 8192 + (va ^ 4096)); t[j] = kh ? o + acc[2 + m][j] : acc[m][j] + o; }
    u16* c = C + (size_t)(kh * 32 + m * 16 + l15) * 1152 + wc * 48;
    *(u32x4*)(c + (lq & 1) * 16 + (lq >> 1) * 8) = pair16(pk2(t[0][0], t[0][1]), pk2(t[0][2], t[0][3]), pk2(t[1][0], t[1][1]), pk2(t[1][2], t[1][3]));
    *(u32x2*)(c + 32 + lq * 4) = (u32x2){pk2(t[2][0], t[2][1]), pk2(t[2][2], t[2][3])};
  }
  __syncthreads();
#undef OT_DMA
#undef OT_LDW
#undef OT_LDA
}

#define CLAIMS() do { const int l_ = TIDX; if (l_ < 64) { \
    const u32 v_ = ld4_sc1(WSP(u32, 0) + SY_OQ(l_ < 6 ? l_ : 0)); \
    if (l_ == 0) pre = (int)__hip_atomic_fetch_add(WSP(u32, 0) + SY_OQ(6), 1u, __ATOMIC_RELAXED, __HIP_MEMORY_SCOPE_AGENT); \
    pre |= (int)((u32)__ballot(l_ < 6 && v_ < ((l_ & 1) ? XS0 / 2 : 32)) << 16); } } while (0)
__device__ __forceinline__ void small_out_task(const Params& p, int rq, int cp, char* smem, const int wv, int& pre) {
  const int tid = TIDX, lane = tid & 63, wid = wv;
  const int l15 = lane & 15, lq = lane >> 4;
  const __amdgpu_buffer_rsrc_t rw = RSRC(WSP(char, O_WOF) + WOB, WOB), ro = RSRC(WSP(u16, O_OB), 2048 * 4096 * 2);
  if (tid == 0) {
    int it = 0;
    while (ld4_sc1(WSP(u32, 0) + SY_CW) < 64u && ++it < SPIN_CAP) __builtin_amdgcn_s_sleep(8);
  }
  __syncthreads();
  bf8 wf[8][4];
#pragma unroll
  for (int k = 0; k < 8; k++)
#pragma unroll
    for (int j = 0; j < 4; j++)
      wf[k][j] = __builtin_bit_cast(bf8, __builtin_amdgcn_raw_buffer_load_b128(rw, (l15 >> 3) * 1024 + (lq * 16 + (l15 & 7)) * 16 + (j & 1) * 128,
                                                                              ((cp * 2 + (j >> 1)) * 128 + wid * 16 + k * 2) * 1024, 16));
  const int va = OB_OFF(rq * 8 + (l15 & 7), wid * 512 + (l15 >> 3) * 32 + lq * 8);
  const int wo = (lq >> 1) == (l15 >> 3) ? (wid * 2 + (lq >> 1)) * 272 + (l15 & 7) * 32 + (lq & 1) * 16 : 4 * 4352 + lane * 16;
  const int rd = (lane >> 4) * 4352 + (lane & 7) * 544 + wid * 32 + ((lane >> 3) & 1) * 16;
#pragma unroll 1
  for (int s = XS0; s < 64; s++) {
    if (tid < 64 && s < 62) {
      int it = 0;
      while (ld4_sc1(WSP(u32, 0) + SY_CS(s)) == 0u && ++it < SPIN_CAP) __builtin_amdgcn_s_sleep(SPS);
    }
    asm volatile("s_waitcnt lgkmcnt(0)" ::: "memory"); __builtin_amdgcn_s_barrier(); asm volatile("" ::: "memory");
    if (s == 63) TLS(1);
    u32x4 af[8];
    int it = 0;
    while (true) {
      CFENCE();
#pragma unroll
      for (int k = 0; k < 8; k++) af[k] = __builtin_amdgcn_raw_buffer_load_b128(ro, va, s * 262144 + k * 4096, 16);
      if (s < 62) break;
      u32 bad = 0u;
#pragma unroll
      for (int k = 0; k < 8; k++) bad = umax(umax(bad, af[k][0]), af[k][2]);
      if (!__any(bad == ~0u) || ++it >= SPIN_CAP) break;
      __builtin_amdgcn_s_sleep(2);
    }
    if (s == 60) CLAIMS();
    f4 acc[4];
#pragma unroll
    for (int k = 0; k < 8; k++)
#pragma unroll
      for (int j = 0; j < 4; j++) acc[j] = MFMA(wf[k][j], __builtin_bit_cast(bf8, af[k]), k ? acc[j] : ((f4){0, 0, 0, 0}));
#pragma unroll
    for (int j = 0; j < 4; j++) *(f4*)(smem + wo + ((lq >> 1) == (l15 >> 3) ? j * 4352 : 0)) = acc[j];
    asm volatile("s_waitcnt lgkmcnt(0)" ::: "memory"); __builtin_amdgcn_s_barrier(); asm volatile("" ::: "memory");
    if (s == 63) TLS(2);
    f4 t = *(const f4*)(smem + rd) + *(const f4*)(smem + rd + 272);
#pragma unroll
    for (int r = 0; r < 4; r++) {
      t[r] += __uint_as_float(__builtin_amdgcn_mov_dpp(__float_as_uint(t[r]), 0xB1, 0xf, 0xf, true));
      t[r] += __uint_as_float(__builtin_amdgcn_mov_dpp(__float_as_uint(t[r]), 0x4E, 0xf, 0xf, true));
      t[r] += __uint_as_float(__builtin_amdgcn_mov_dpp(__float_as_uint(t[r]), 0x141, 0xf, 0xf, true));
    }
    if ((lane & 7) == 0) *(u32x2*)(p.out + (size_t)(s * 32 + rq * 8 + wid) * 1152 + 576 + cp * 32 + (lane >> 3) * 4) = (u32x2){pk2(t[0], t[1]), pk2(t[2], t[3])};
  }
}

#define PSTAMP()
#define STAMP(i)
#define YEND()
#define ANS 16
#define KSL 1
#define DSH 2
#define K1 22
#define K3 12
#define DOMAX 90
#define DO0 80
#define DH0 54
#define DOCAP 6
#define C_IMG 0
#define C_QT 9216
#define C_KT (C_QT + 3072)
#define C_P (C_KT + 4096)
#define C_PX (C_P + 768)
#define C_KX (C_PX + 2560)
#define C_VX (C_KX + 8192)
#define C_QX (C_VX + 8192)
#define C_XP 161024
#define GPS 6
#define C_WL 49920
__device__ __forceinline__ float ror8(float v) {
  return __uint_as_float(__builtin_amdgcn_mov_dpp(__float_as_uint(v), 0x128, 0xf, 0xf, true));
}
__device__ __forceinline__ float rb1(float f) { return __uint_as_float(pk2(0.f, f)); }
#define FMX(a, b) __builtin_elementwise_maximum(a, b)
__device__ __forceinline__ float tmax(float mx) {
  mx = FMX(mx, ror8(mx));
  { auto t = __builtin_amdgcn_permlane16_swap(__float_as_uint(mx), __float_as_uint(mx), false, false);
    mx = FMX(__uint_as_float(t[0]), __uint_as_float(t[1])); }
  { auto t = __builtin_amdgcn_permlane32_swap(__float_as_uint(mx), __float_as_uint(mx), false, false);
    mx = FMX(__uint_as_float(t[0]), __uint_as_float(t[1])); }
  return mx;
}
__device__ __forceinline__ int hmax(int x) {
  x = max(x, __builtin_amdgcn_mov_dpp(x, 0x128, 0xf, 0xf, true));
  x = max(x, __builtin_amdgcn_mov_dpp(x, 0x124, 0xf, 0xf, true));
  x = max(x, __builtin_amdgcn_mov_dpp(x, 0x4E, 0xf, 0xf, true));
  x = max(x, __builtin_amdgcn_mov_dpp(x, 0xB1, 0xf, 0xf, true));
  { auto t = __builtin_amdgcn_permlane16_swap((u32)x, (u32)x, false, false); x = max((int)t[0], (int)t[1]); }
  return x;
}
typedef __attribute__((ext_vector_type(2))) unsigned long long u64x2;
__device__ __forceinline__ f4 zero4() {
  u64 a, b; asm volatile("v_mov_b64 %0, 0\n\tv_mov_b64 %1, 0\n\ts_nop 1" : "=v"(a), "=v"(b));
  return __builtin_bit_cast(f4, (u64x2){a, b});
}
__device__ __forceinline__ u32x4 ones4() {
  u64 a, b; asm volatile("v_mov_b64 %0, -1\n\tv_mov_b64 %1, -1" : "=v"(a), "=v"(b));
  return __builtin_bit_cast(u32x4, (u64x2){a, b});
}
#define BAR() do { asm volatile("s_waitcnt lgkmcnt(0)" ::: "memory"); __builtin_amdgcn_s_barrier(); asm volatile("" ::: "memory"); } while (0)
#define BARC() do { asm volatile("" ::: "memory"); __builtin_amdgcn_s_waitcnt(0xC07F); __builtin_amdgcn_s_barrier(); asm volatile("" ::: "memory"); } while (0)
#define BARN() do { asm volatile("" ::: "memory"); __builtin_amdgcn_s_barrier(); asm volatile("" ::: "memory"); } while (0)
#define IDX() const int tid = TIDX, lane = tid & 63, l15 = lane & 15, lq = lane >> 4, q4 = l15 >> 2, p4 = lane & 3, hi8 = l15 >> 3; \
    (void)q4; (void)p4; (void)hi8; (void)lq
#define AD_LD() __builtin_amdgcn_raw_buffer_load_b32(rsX, (TIDX & 63) * ANS, X_ANN + (s & 1) * 1024 + xp, 16)
#define AD_WAIT(HEAD, K, CAP) do { int n_ = 0; u32 an = AD_LD(); \
    while ((u32)__ballot((an & 63u) != (u32)s) != 0u && ++n_ < (CAP)) { __builtin_amdgcn_s_sleep(1); CFENCE(); an = AD_LD(); } \
    if (!HEAD && c == 0 && s > 0 && n_ < (CAP)) st4_sc1(WSP(u32, 0) + SY_CS(s - 1), 1u); \
    const u32 r_ = (u32)__builtin_amdgcn_readfirstlane((int)an); \
    const int m_ = hmax((TIDX & 32) ? (int)umin(an, DOMAX * 64u) : (int)(an - r_)); \
    dl = ((r_ + (u32)__builtin_amdgcn_readlane(m_, 0)) & ~63u) + (u32)(K) * 64u + (HEAD ? (u32)__builtin_amdgcn_readlane(m_, 32) : 0u); \
    n_ = 160; \
    while ((int)(((u32)wall_clock64() << 6) - dl) < 0 && --n_ > 0) {} } while (0)
#define STG_AUX 2
#define OSTG_AUX 0
#define STG_D 3
#define STG_POLL 7
#define STS(c, i)

#define DPR(OP, N) OP " %3, %3, %3 row_ror:" #N " row_mask:0xf bank_mask:0xf\n\t" OP " %4, %4, %4 row_ror:" #N " row_mask:0xf bank_mask:0xf\n\t"
#define DPROW(OP) "s_nop 0\n\t" DPR(OP, 8) "s_nop 0\n\t" DPR(OP, 4) "s_nop 0\n\t" DPR(OP, 2) "s_nop 0\n\t" DPR(OP, 1)
#define NXA 0
#define NXS 3
#define BJ 2
#define KSP 0
#define SP1 2
#define SP2 0
#define KP 3
#define GX 1
#define GXO (GX == 1 ? 4096 : 1024)
#define ZE 0x0D
#define KD 0
#define QSL 0
#define DJ 9
#define VSL 0
#define VSL1 4
#define KV0 1
#define PVP 0
#define IPR 3
#define DSLOT 0x9876E1CBAD0ull
#define F_K (44288 - C_KT)
#define F_Q (47872 - 768 - C_QT)
#define PRIO(n) __builtin_amdgcn_s_setprio(n)
#define LSL 2
__device__ __forceinline__ void chain_head(const Params& p, char* smem, const int h, const int wv) {
  STS(h, 1);
  TLS(12);
  const int wid = wv;
  u32x8 w[2][9], wx[3];
  const __amdgpu_buffer_rsrc_t rsX = RSRC(XWIN(p), X_END);
  const __amdgpu_buffer_rsrc_t rsB = RSRC(WSP(u16, O_QB), 4 * ACB);
  const int cb = h >> 1;
  const int nvt = (wid & 3) == 0 ? 0 : (wid < 3 ? 2 : 1);
  const int vt = wid < 3 ? wid * 2 + 2 : (wid == 3 ? 0 : wid - 4);
  const int wl = C_WL + (nvt == 1 ? vt * 12288 : 49152 + (nvt == 2 ? wid - 1 : wid >> 2) * 30720);
  int xp;
  {
    IDX();
    u32x4 st[54], nk, nv, nq;
    u32 fl, fv, fk;
    const __amdgpu_buffer_rsrc_t rq = RSRC(p.BWq, 576 * 4096 * 2), rk = RSRC(p.BWk, 576 * 4096 * 2), rv = RSRC(p.BWv, 576 * 4096 * 2);
    const int vw = ((tid >> 4) * 4096 + h * 128 + (tid & 15) * 8) * 2;
    {
      const u32x4 e = ones4();
#pragma unroll
      for (int i = 0; i < 2; i++) {
        const int id = wid + 8 * i;
        __builtin_amdgcn_raw_buffer_store_b128(e, rsX, (lane >> 5) * 32768 + (lane & 31) * 16,
                                               id < 12 ? X_O + (id >> 1) * 65536 + (h >> 4) * 16384 + (id & 1) * 8192 + (h & 15) * 512 : PJ_OOB, 16);
      }
      __builtin_amdgcn_raw_buffer_store_b128(e, rsX, tid < 4 ? X_ANN + (tid & 1) * 1024 + (tid >> 1) * 8192 + h * ANS : PJ_OOB, 0, 16);
    }
    auto req = [&](int kc) {
#pragma unroll
      for (int i = 0; i < 6; i++) st[kc * 6 + i] = __builtin_amdgcn_raw_buffer_load_b128(i < 2 ? rq : (i < 4 ? rk : rv), vw, (kc * 64 + (i & 1) * 32) * 8192, STG_AUX);
      SB0();
    };
#pragma unroll
    for (int kc = 0; kc < STG_D; kc++) { if (kc == 0) TLS(13); req(kc); __builtin_amdgcn_s_barrier(); SB0(); }
    TLS(14);
    auto cell = [](int row, int slot) { return (((row * 2 + slot) ^ ((((row >> 2) ^ (row >> 4)) & 1) << 2) ^ ((row >> 5) & 1)) << 5); };
    auto put = [&](int unit, int row, const u32x4& v) { *(u32x4*)(smem + unit * 4160 + cell(row, (tid >> 1) & 1) + (tid & 1) * 16) = v; };
    auto frag = [&](int tile) {
      bf4 tt[4];
#pragma unroll
      for (int kk = 0; kk < 4; kk++) {
        const int r0 = (kk >> 1) * 32 + (lq >> 1) * 16 + (q4 >> 1) * 8 + (lq & 1) * 4 + (kk & 1) * 2 + (q4 & 1);
        tt[kk] = trrd(smem + (tile >> 1) * 4160 + cell(r0, tile & 1) + p4 * 8);
      }
      return ilv(tt[0], tt[1], tt[2], tt[3]);
    };
    auto park = [&](int slot, const u32x8& f) {
      char* d = smem + wl + slot * 2048 + lane * 16;
      *(u32x4*)d = (u32x4){f[0], f[1], f[2], f[3]};
      *(u32x4*)(d + 1024) = (u32x4){f[4], f[5], f[6], f[7]};
    };
    auto poll = [&]() {
      fl = ld4_sc1(WSP(u32, 0) + SY_INIT(lane)); fv = ld4_sc1(WSP(u32, 0) + SY_VOTE(lane & 31));
      fk = ld4_sc1(WSP(u32, 0) + SY_BLK(lane < 12 ? lane >> 2 : 0, cb, lane & 3));
    };
    auto rows = [&]() {
      nq = __builtin_amdgcn_raw_buffer_load_b128(rsB, tid * 16, h * 8192, 16);
      nk = __builtin_amdgcn_raw_buffer_load_b128(rsB, tid * 16, h * 8192 + ACB, 16);
      nv = __builtin_amdgcn_raw_buffer_load_b128(rsB, tid * 16, h * 8192 + 2 * ACB, 16);
    };
    bool early = false;
#pragma unroll
    for (int kc = 0; kc < 9; kc++) {
      if (kc == 10 - STG_D) { early = __all(fk >= 8u); CFENCE(); if (early) rows(); if (wid == 0) poll(); SB0(); }
#pragma unroll
      for (int i = 0; i < 6; i++) put((i >> 1) * 4 + ((tid & 15) >> 2), (i & 1) * 32 + (tid >> 4), st[kc * 6 + i]);
      BAR();
      if (kc == 0 && tid == 0) st4_sc1(WSP(u32, 0) + SY_INIT(h), 1u);
      if (kc == 0) TLS(15);
      if (kc + STG_D == 8) { fk = ld4_sc1(WSP(u32, 0) + SY_BLK(lane < 12 ? lane >> 2 : 0, cb, lane & 3)); SB0(); }
      if (kc + STG_D < 9) req(kc + STG_D);
      w[0][kc] = frag(wid * 2);
      w[1][kc] = frag(nvt == 2 && kc < 4 ? 16 + vt : wid * 2 + 1);
      const u32x8 tp = frag(nvt == 0 ? 21 + (wid >> 1) : (nvt == 2 && kc < 4 ? wid * 2 + 1 : 16 + vt));
      if (kc >= 4 && kc < 7) { wx[kc - 4] = tp; if (nvt == 0) park(6 + kc, tp); }
      else park(nvt == 0 ? 6 + kc : (kc >= 7 ? kc - 7 : 2 + kc), tp);
      BAR();
      if (kc == 0 || kc == 8) STS(h, kc + 3);
    }
    TLS(6);
    if (wid == 0) {
      int it = 0;
      while (!__all(fl != 0u && fv != 0u && fk >= 8u) && ++it < SPIN_CAP) { __builtin_amdgcn_s_sleep(GPS); poll(); }
      *(int*)(smem + C_XP) = __popcll(__ballot(fv == 2u)) > 32 ? 8192 : 0;
    }
    BAR();
    if (!early) rows();
    xp = __builtin_amdgcn_readfirstlane(*(const int*)(smem + C_XP));
    TLS(7);
    SB0();
    *(u32x4*)(smem + C_QX + tid * 16) = nq;
    *(u32x4*)(smem + C_KX + tid * 16) = nk;
    *(u32x4*)(smem + C_VX + ((tid * 16) ^ ((tid & 32) << 2))) = nv;
  }
  STS(h, 14);
  constexpr float CE = 0.08838834764831845f * 1.4426950408889634f;
  auto run = [&](auto RC) __attribute__((always_inline)) {
  constexpr int R = decltype(RC)::value;
  constexpr int NV = R < 2 ? 0 : (R < 4 ? 1 : 2);
  constexpr bool QW = !(R & 1);
  int known = 0;
  bf8 pre[4][2], vx[NV ? NV : 1];
  int sidx, mo, io, eo, gvo, wvo = 0, so = 0, po = 0, qo = 0, ko = 0, ko1 = 0, fo = 0, fq = 0, fk = 0;
  float mk[4], mm = 0.f, cv = CE;
#define LDW(dst, slot) do { dst[0] = *(const bf8*)(smem + wvo + (slot) * 2048); dst[1] = *(const bf8*)(smem + wvo + (slot) * 2048 + 1024); } while (0)
  {
    IDX();
    if constexpr (R == 1) {
      po = (tid * 16) ^ ((tid & 32) << 2);
      so = (lane >> 5) * 32768 + (lane & 31) * 16;
      asm volatile("" : "+v"(po), "+v"(so));
    } else if constexpr (R == 0) {
      qo = C_QT + lq * 384 + q4 * 32 + p4 * 8; ko = C_KT + ((((lq * 8 + q4) * 32) ^ ((lq & 1) << 7)) + p4 * 8); ko1 = C_KT + ((((lq * 8 + 4 + q4) * 32) ^ ((lq & 1) << 7)) + p4 * 8);
      po = C_P + l15 * 16 + lq * 4;
      fo = lq * 512 + l15 * 16;
#pragma unroll
      for (int r = 0; r < 4; r++) { mk[r] = lq * 4 + r <= l15 ? 0.f : -__builtin_inff(); asm volatile("" : "+v"(mk[r])); }
      mm = (l15 & 2) ? -__builtin_inff() : 0.f;
      asm volatile("" : "+v"(mm), "+v"(cv));
      fq = C_QT + F_Q + (lq ? lq : 1) * 768 + lq * 8; fk = C_KT + F_K + lq * 1032;
      asm volatile("" : "+v"(qo), "+v"(ko), "+v"(ko1), "+v"(po), "+v"(fo), "+v"(fq), "+v"(fk));
      *(u32*)(smem + fq) = 0u; *(u32*)(smem + fk) = 0u;
    } else {
      wvo = wl + lane * 16;
      po = C_P + (lq * 4 + q4) * 16 + (p4 & 1) * 8;
      so = ((l15 >> 2) & 1) * 65536 + (NV - 1) * 32768 + (((vt & 3) + (NV == 2 ? hi8 : 0)) * 8 + ((l15 >> 1) & 1) * 4 + (lq >> 1) * 2 + (l15 & 1)) * 16 + (lq & 1) * 8;
      if (NV == 1 && hi8) so = PJ_OOB;
      asm volatile("" : "+v"(wvo), "+v"(po), "+v"(so));
      if constexpr (NV == 2) { qo = po ^ 32; asm volatile("" : "+v"(qo)); }
#pragma unroll
      for (int j = 0; j < 4; j++) LDW(pre[j], NV == 2 ? 2 + j : j);
    }
    eo = QW ? C_QT + wid * 768 + ((l15 >> 2) * 2 + (l15 & 1)) * 32 + lq * 8 + ((l15 >> 1) & 1) * 4
            : C_KT + (wid & 3) * 1024 + ((l15 * 32) ^ (hi8 << 7)) + lq * 8;
    sidx = (l15 & 2) ? 0xEEEE : 0x4444;
    mo = C_IMG + lane * 16;
    io = C_IMG + ((tid & ~30) | ((tid & 10) << 1) | ((tid & 20) >> 1)) * 16;
    gvo = tid * 16;
    asm volatile("" : "+v"(sidx), "+v"(mo), "+v"(io), "+v"(eo), "+v"(gvo));
    if constexpr (!QW) { *(u32*)(smem + eo + 4) = 0u; *(u32*)(smem + eo + 516) = 0u; }
  }
  f4 a0, a1;
  u32x4 vm = {0u, 0u, 0u, 0u};
  if constexpr (NV > 0) asm volatile("" : "+v"(vm));
  u32 ts = 0;
  u64 tq = 0;
  int dur = DH0 * 64;
  const int ob = (h >> 4) * 16384 + (h & 15) * 512 + xp;
  int b3 = 0;
  PRIO(QW ? 2 : 1);
#pragma unroll 1
  for (int s = 0;; s++) {
    const int mb = X_M + b3 * 32768 + xp;
    u32x4 g, gx;
    BAR();
    STAMP(13);
    g = __builtin_amdgcn_raw_buffer_load_b128(rsX, gvo, mb, 16);
    if constexpr (R == GX) gx = __builtin_amdgcn_raw_buffer_load_b128(rsX, gvo, mb + 8192 + GXO, 16);
    SB0();
    if constexpr (R == 0) {
#pragma unroll
      for (int j = 0; j < 4; j++) { const char* kx_ = smem + C_KX + fo + j * 2048;
        pre[j >> 1][j & 1] = *(const bf8*)kx_; pre[2 + (j >> 1)][j & 1] = *(const bf8*)(kx_ + 256); }
    } else if constexpr (R > 1) {
      IDX();
#pragma unroll
      for (int d = 0; d < NV; d++) { const char* vb_ = smem + C_VX + (vt + d) * 1024 + (p4 >> 1) * 512 + (((lq * 4 + q4) ^ ((p4 >> 1) * 8)) << 4) + (p4 & 1) * 8;
        vx[d] = cat8(trrd(vb_), trrd(vb_ + 256)); }
    }
    SB0();
    a0 = zero4(); a1 = zero4();
    f4 c0, c1, a2;
#define A2INI() do { u64 z_; float m0_, m1_; asm volatile("v_mov_b32 %0, %3\n\tv_mov_b32 %1, %3\n\tv_mov_b64 %2, 0\n\ts_nop 1" : "=&v"(m0_), "=&v"(m1_), "=v"(z_) : "v"(mm)); \
                     const f2v zz_ = __builtin_bit_cast(f2v, z_); a2 = (f4){m0_, m1_, zz_[0], zz_[1]}; } while (0)
    if constexpr ((ZE >> R) & 1) {
      c1 = zero4();
      if constexpr (NV != 1) c0 = zero4();
      if constexpr (R == 0) { A2INI(); asm volatile("" : "+v"(a2)); }
    }
    int so_ = X_O + b3 * 131072 + ob, an_ = X_ANN + (s & 1) * 1024 + xp + h * ANS;
    int nb = s < 63 ? (s + 1) * 262144 + h * 8192 : PJ_OOB;
    asm volatile("" : "+s"(so_), "+s"(an_), "+s"(nb));
    u32x4 nx[2];
#define NXL1(g, i) __builtin_amdgcn_raw_ptr_buffer_load_lds(rsB, (LDSP void*)(smem + C_KX + (g) * 4096), 16, (g) < 2 ? gvo : po, \
                                                             nb + (1 + ((g) >> 1)) * ACB + ((g) & 1) * 4096 - 4096, (i) * 1024, 16)
#define NXL(g) do { NXL1(g, 0); NXL1(g, 1); NXL1(g, 2); NXL1(g, 3); } while (0)
    SB0();
    if constexpr (!QW) { PRIO(IPR); BARN(); }
    {
      __builtin_amdgcn_s_waitcnt(R == GX ? 0x0F71 : 0x0F70);
      CFENCE();
      *(u32x4*)(smem + io) = g;
      SB0();
      u32 top = umax(umax(umax(g[0], g[1]), g[2]), g[3]);
      if constexpr (R == GX) {
        asm volatile("" : "+v"(top));
        SB0();
        *(u32x4*)(smem + GXO + io) = gx;
        SB0();
        top = umax(umax(umax(umax(top, gx[0]), gx[1]), gx[2]), gx[3]);
      }
#define G_BAD() (umax(umax(umax(g[0], g[1]), umax(g[2], g[3])), R != GX ? 0u : umax(umax(gx[0], gx[1]), umax(gx[2], gx[3]))) == ~0u)
      if (__builtin_expect(__any(top == ~0u), 0)) {
        int it = 0;
        do {
          CFENCE();
          if (G_BAD()) {
            g = __builtin_amdgcn_raw_buffer_load_b128(rsX, gvo, mb, 16);
            if constexpr (R == GX) gx = __builtin_amdgcn_raw_buffer_load_b128(rsX, gvo, mb + 8192 + GXO, 16);
          }
          VMW0C();
        } while (__any(G_BAD()) && ++it < SPIN_CAP);
        *(u32x4*)(smem + io) = g;
        if constexpr (R == GX) *(u32x4*)(smem + GXO + io) = gx;
      }
    }
    SB0();
    STAMP(15);
    if constexpr (!QW) PRIO(1);
    BARC();
    STAMP(0);
#define LDM(j) *(const bf8*)(smem + mo + (j) * 1024)
    constexpr int NB = R == 1 ? 9 : 5;
    bf8 b[NB];
    if constexpr (R == 1) {
      STAMP(6);
      asm volatile("s_memrealtime %0\n\ts_waitcnt lgkmcnt(0)" : "=s"(tq));
      ts = (u32)tq;
      __builtin_amdgcn_raw_buffer_store_b32((((ts << 6) + (u32)dur) & ~63u) | (u32)s, rsX, 0, an_, 16);
      SB0();
#pragma unroll
      for (int j = 0; j < 9; j++) { b[j] = LDM(j); SB0(); }
#pragma unroll
      for (int i = 0; i < NXA; i++) { NXL(i); SB0(); }
    } else {
      if (!QW && KSP) __builtin_amdgcn_s_sleep(KSP);
#pragma unroll
      for (int j = 0; j < 4; j++) { b[j] = LDM(j); SB0(); }
    }
#pragma unroll
    for (int j = 0; j < 9; j++) {
      if constexpr (QW) {
        if (j == BJ) {
          BARN();
#pragma unroll
          for (int i = 4; i < 4 + BJ; i++) { b[i % 5] = LDM(i); SB0(); }
        }
        if (j >= BJ && j + 4 < 9) b[(j + 4) % 5] = LDM(j + 4);
      } else if (R != 1 && j + 4 < 9) b[(j + 4) % 5] = LDM(j + 4);
      SB0();
      a0 = SMF(b[j % NB], w[0][j], a0, sidx);
      if (NV < 2 || j >= 4) a1 = SMF(b[j % NB], w[1][j], a1, sidx);
      else a1 = SMF(b[j % 5], cat16(pre[j & 3][0], pre[j & 3][1]), a1, sidx);
      SB0();
      if (j == 0) STAMP(10);
      if (j == 4) STAMP(11);
      if (j == 8) STAMP(12);
      if (NV == 2 && j >= DJ && j < DJ + 4) LDW(pre[j - DJ], (DSLOT >> ((j - DJ) * 4)) & 15);
    }
    {
      const u32 e0 = pk2(a0[0] + a0[2], a0[1] + a0[3]), e1 = pk2(a1[0] + a1[2], a1[1] + a1[3]);
      asm volatile("ds_write2_b32 %0, %1, %2 offset1:%3" :: "v"(eo), "v"(e0), "v"(e1), "i"(QW ? 96 : 128) : "memory");
      if constexpr (R != 0) asm volatile("ds_write_b32 %0, %1 offset:%2" :: "v"(eo), "v"(sidx), "i"(QW ? F_Q : F_K) : "memory");
    }
    SB0();
    STAMP(1);
    if constexpr (QW) PRIO(R ? 0 : SP1);
    else if (KV0) PRIO(0);
    if constexpr ((ZE >> R) & 1) { a0 = c0; a1 = c1; }
    else if constexpr (R != 1) { a1 = zero4(); if constexpr (NV != 1) a0 = zero4(); if constexpr (R == 0) A2INI(); }
    if constexpr (NV == 0) {
      if constexpr (R == 0) {
        if (QSL) __builtin_amdgcn_s_sleep(QSL);
        bf8 qa[2], km[4];
        u32 fl;
        do {
          CFENCE();
          fl = *(const volatile LDSP u32*)(smem + fq);
          CFENCE(); SB0();
#pragma unroll
          for (int i = 0; i < 2; i++) { qa[i] = cat8(trrd(smem + qo + i * 1536), trrd(smem + qo + i * 1536 + 128)); SB0(); }
        } while (__builtin_expect(__any(fl == 0u), 0));
        STAMP(2);
        auto rdk = [&]() __attribute__((always_inline)) {
          CFENCE(); SB0();
          fl = *(const volatile LDSP u32*)(smem + fk);
          CFENCE(); SB0();
#pragma unroll
          for (int i = 0; i < 4; i++) { km[i] = cat8(trrd(smem + ko + i * 1024), trrd(smem + ko1 + i * 1024)); SB0(); }
        };
        auto mulk = [&]() __attribute__((always_inline)) {
#pragma unroll
          for (int i = 0; i < 2; i++) { a2 = SMF(qa[i], cat16(km[2 * i], km[2 * i + 1]), a2, sidx); asm volatile("" : "+v"(a2)); SB0(); }
        };
#pragma unroll
        for (int i = 0; i < 2; i++) {
          a0 = SMF(qa[i], cat16(pre[i][0], pre[i][1]), a0, sidx); asm volatile("" : "+v"(a0)); SB0();
          a1 = SMF(qa[i], cat16(pre[2 + i][0], pre[2 + i][1]), a1, sidx); asm volatile("" : "+v"(a1)); SB0();
        }
        if (SP2 != SP1) PRIO(SP2);
        if (KP == 0) rdk();
        float e0 = a0[0] + a0[2], e1 = a0[1] + a0[3];
        asm volatile("" : "+v"(e0), "+v"(e1));
        SB0();
        float e2 = a1[0] + a1[2], e3 = a1[1] + a1[3], x0, x1, y0, y1, f0, f1, f2, f3, f4_, f5;
        asm volatile("v_mul_f32_dpp %0, %2, %4 row_newbcast:0 row_mask:0xf bank_mask:0xf\n\tv_mul_f32_dpp %1, %3, %4 row_newbcast:0 row_mask:0xf bank_mask:0xf"
                     : "=&v"(x0), "=&v"(x1) : "v"(e0), "v"(e1), "v"(cv), "v"(e2), "v"(e3));
        if (KP == 1) rdk();
        asm volatile("v_fma_f32 %0, %9, %6, -%7\n\tv_fma_f32 %2, %11, %6, -%8\n\tv_exp_f32 %0, %0\n\tv_exp_f32 %2, %2\n\t"
                     "v_fma_f32 %1, %10, %6, -%7\n\tv_fma_f32 %3, %12, %6, -%8\n\tv_exp_f32 %1, %1\n\tv_exp_f32 %3, %3\n\t"
                     "v_add_f32 %4, %0, %1\n\tv_add_f32 %5, %2, %3"
                     : "=&v"(f0), "=&v"(f2), "=&v"(f1), "=&v"(f3), "=&v"(y0), "=&v"(y1) : "v"(cv), "v"(x0), "v"(x1), "v"(e0), "v"(e2), "v"(e1), "v"(e3));
        if (KP == 2) rdk();
        STAMP(3);
        PRIO(2);
        SB0();
        if (KP == 3) {
          if (KD) asm volatile("s_nop %0" :: "i"(KD));
          do {
            rdk();
          } while (__any(fl == 0u));
        }
        mulk();
        if (KP < 3 && __builtin_expect(__any(fl == 0u), 0)) {
          do rdk(); while (__any(fl == 0u));
          A2INI();
          mulk();
        }
        STAMP(4);
        float e4 = a2[0] + a2[2], e5 = a2[1] + a2[3];
#define P_OUT "v_rcp_f32 %3, %3\n\tv_rcp_f32 %4, %4\n\t" \
              "v_mul_f32 %0, %0, %3\n\tv_mul_f32 %5, %5, %4\n\tv_mul_f32 %1, %1, %3\n\tv_cvt_pk_bf16_f32 %0, %0, %5\n\t" \
              "v_mul_f32 %6, %6, %4\n\tv_mul_f32 %2, %2, %3\n\tv_cvt_pk_bf16_f32 %1, %1, %6\n\tv_mul_f32 %7, %7, %4\n\t" \
              "ds_write2st64_b32 %9, %0, %1 offset1:1\n\tv_cvt_pk_bf16_f32 %2, %2, %7\n\tds_write_b32 %9, %2 offset:512"
        asm volatile("v_fma_f32 %2, %12, %8, -%10\n\tv_fma_f32 %7, %13, %8, -%11\n\tv_exp_f32 %2, %2\n\tv_exp_f32 %7, %7\n\t"
                     "v_add_f32 %3, %3, %2\n\tv_add_f32 %4, %4, %7\n\t" DPROW("v_add_f32_dpp") P_OUT "\n\tv_min_f32 %3, %3, %4"
                     : "+v"(f0), "+v"(f2), "=&v"(f4_), "+v"(y0), "+v"(y1), "+v"(f1), "+v"(f3), "=&v"(f5)
                     : "v"(cv), "v"(po), "v"(x0), "v"(x1), "v"(e4), "v"(e5) : "memory");
        SB0();
        if (__builtin_expect(__any(y0 < 0x1p-64f), 0)) {
          asm volatile("v_max3_f32 %3, %0, %1, %2\n\tv_max3_f32 %4, %5, %6, %7\n\t" DPROW("v_max_f32_dpp")
                       "v_mul_f32 %3, %8, %3\n\tv_mul_f32 %4, %8, %4\n\t"
                       "v_fma_f32 %0, %0, %8, -%3\n\tv_fma_f32 %5, %5, %8, -%4\n\tv_exp_f32 %0, %0\n\tv_exp_f32 %5, %5\n\t"
                       "v_fma_f32 %1, %1, %8, -%3\n\tv_fma_f32 %6, %6, %8, -%4\n\tv_exp_f32 %1, %1\n\tv_exp_f32 %6, %6\n\t"
                       "v_fma_f32 %2, %2, %8, -%3\n\tv_fma_f32 %7, %7, %8, -%4\n\tv_exp_f32 %2, %2\n\tv_exp_f32 %7, %7\n\t"
                       "v_add_f32 %3, %0, %1\n\tv_add_f32 %4, %5, %6\n\tv_add_f32 %3, %3, %2\n\tv_add_f32 %4, %4, %7\n\t" DPROW("v_add_f32_dpp") P_OUT
                       : "+v"(e0), "+v"(e2), "+v"(e4), "=&v"(x0), "=&v"(x1), "+v"(e1), "+v"(e3), "+v"(e5) : "v"(cv), "v"(po) : "memory");
        }
        SB0();
      } else {
#pragma unroll
        for (int i = NXA; i < 4; i++) NXL(i);
        SB0();
        const int rb = X_O + (b3 == 0 ? 2 : b3 - 1) * 131072 + ob;
        const u32x4 e = ones4();
        __builtin_amdgcn_raw_buffer_store_b128(e, rsX, so, rb, 16);
        __builtin_amdgcn_raw_buffer_store_b128(e, rsX, so, rb + 65536, 16);
        PSTAMP();
      }
      STAMP(5);
      BARC();
      if constexpr (R == 0) STAMP(6);
      if constexpr (R == 1) {
        const u32 tp = (u32)wall_clock64(); dur += ((int)umin((tp - ts) << 6, 200u * 64u) - dur) >> DSH;
        SB0();
        VMW0C();
        STAMP(8);
        BARN();
        if (s == 63) break;
      } else {
        *(u32*)(smem + fq) = 0u; *(u32*)(smem + fk) = 0u;
        bf8 km[4];
#pragma unroll
        for (int ks = 0; ks < 4; ks++) km[ks] = cat8(trrd(smem + ko + ks * 1024), trrd(smem + ko1 + ks * 1024));
#pragma unroll
        for (int NT = 1; NT <= 2; NT++) {
          f4 sc[2], sm;
          bf8 qf[4];
#pragma unroll
          for (int ks = 0; ks < 4; ks++) qf[ks] = *(const bf8*)(smem + C_QX + (NT - 1) * 256 + fo + ks * 2048);
#pragma unroll
          for (int ks = 0; ks < 4; ks++) {
#pragma unroll
            for (int j = 0; j < NT; j++) sc[j] = MFMA(pre[2 * j + (ks >> 1)][ks & 1], qf[ks], ks ? sc[j] : (j == NT - 1 ? ((f4){mk[0], mk[1], mk[2], mk[3]}) : ((f4){0, 0, 0, 0})));
            sm = MFMA(km[ks], qf[ks], ks ? sm : ((f4){0, 0, 0, 0}));
          }
          float mx = FMX3(FMX3(sc[0][0], sc[0][1], sc[0][2]), sc[0][3], FMX(sm[0], sm[1]));
          if (NT == 2) mx = FMX3(FMX3(mx, sc[1][0], sc[1][1]), sc[1][2], sc[1][3]);
          { auto t = __builtin_amdgcn_permlane16_swap(__float_as_uint(mx), __float_as_uint(mx), false, false);
            mx = FMX(__uint_as_float(t[0]), __uint_as_float(t[1])); }
          { auto t = __builtin_amdgcn_permlane32_swap(__float_as_uint(mx), __float_as_uint(mx), false, false);
            mx = FMX(__uint_as_float(t[0]), __uint_as_float(t[1])); }
          mx *= -CE;
#pragma unroll
          for (int j = 0; j < NT; j++)
#pragma unroll
            for (int r = 0; r < 4; r++) sc[j][r] = __builtin_amdgcn_exp2f(__builtin_fmaf(sc[j][r], CE, mx));
          sm[0] = __builtin_amdgcn_exp2f(__builtin_fmaf(sm[0], CE, mx)); sm[1] = __builtin_amdgcn_exp2f(__builtin_fmaf(sm[1], CE, mx));
          float sum = ((sc[0][0] + sc[0][1]) + (sc[0][2] + sc[0][3])) + (sm[0] + sm[1]);
          if (NT == 2) sum += (sc[1][0] + sc[1][1]) + (sc[1][2] + sc[1][3]);
          { auto t = __builtin_amdgcn_permlane16_swap(__float_as_uint(sum), __float_as_uint(sum), false, false);
            sum = __uint_as_float(t[0]) + __uint_as_float(t[1]); }
          sum = swadd(sum, sum);
          const float inv = __builtin_amdgcn_rcpf(sum);
          *(u32x4*)(smem + C_PX + (NT - 1) * 1024 + gvo) = (u32x4){pk2(sc[0][0] * inv, sc[0][1] * inv), pk2(sc[0][2] * inv, sc[0][3] * inv),
                                                                  NT == 2 ? pk2(sc[1][0] * inv, sc[1][1] * inv) : 0u, NT == 2 ? pk2(sc[1][2] * inv, sc[1][3] * inv) : 0u};
          *(u32*)(smem + C_PX + 2048 + (NT - 1) * 256 + (gvo >> 2)) = pk2(sm[0] * inv, sm[1] * inv);
        }
        STAMP(8);
        BAR();
        if (s == 63) break;
        if (known < (s < 61 ? s + 3 : 64)) {
          int n = 0;
          while (true) {
            u32 bf[3];
#pragma unroll
            for (int i = 0; i < 3; i++) bf[i] = ld4_sc1(WSP(u32, 0) + SY_BLK(i, cb, gvo >> 4));
            const u64 miss = __ballot(!(bf[0] >= 8u && bf[1] >= 8u && bf[2] >= 8u));
            known = miss ? __builtin_ctzll(miss) : 64;
            if (known >= (s < 61 ? s + 3 : 64) || ++n >= SPIN_CAP) break;
            __builtin_amdgcn_s_sleep(KSL);
          }
        }
      }
      STAMP(9);
      YEND();
      if constexpr (R == 1) { u32 dl; const int c = 1; AD_WAIT(true, K1 + K3, 4096); }
    } else {
      if (NV == 2 && DJ > 5) {
#pragma unroll
        for (int j = 0; j < 4; j++) LDW(pre[j], (DSLOT >> (j * 4)) & 15);
        SB0();
      }
      if (QW && (NV == 2 ? VSL : VSL1)) __builtin_amdgcn_s_sleep(NV == 2 ? VSL : VSL1);
      if constexpr (NV == 2) {
        int u = 0;
#pragma unroll
        for (int i = 0; i < 9; i++) {
          const int bs = (0x31042 >> ((i % 5) * 4)) & 7;
#define RING(a) do { a = SMF(b[bs], cat16(pre[u & 3][0], pre[u & 3][1]), a, sidx); SB0(); \
                     if (u < 7) LDW(pre[u & 3], (DSLOT >> (u * 4 + 16)) & 15); u++; } while (0)
          SB0();
          if (i == 0 || i == 4) RING(a0);
          else a0 = SMF(b[bs], i < 4 ? wx[i - 1] : w[1][i - 5], a0, sidx);
          SB0();
          RING(a1);
          if (i < 4) b[bs] = LDM(i);
        }
      } else {
#pragma unroll
        for (int i = 0; i < 9; i++) {
          SB0();
          if (i < 3) a1 = SMF(b[(i + 4) % 5], wx[i], a1, sidx);
          else a1 = SMF(b[(i + 4) % 5], cat16(pre[(i + 1) & 3][0], pre[(i + 1) & 3][1]), a1, sidx);
          SB0();
          if (i < 4) b[(i + 4) % 5] = LDM(i);
          if (i == 3 || i == 4) LDW(pre[(i + 1) & 3], 1 + i);
        }
      }
      vm[NV - 1] = pk2(a1[0] + a1[2], a1[1] + a1[3]);
      if constexpr (NV == 2) vm[0] = pk2(a0[0] + a0[2], a0[1] + a0[3]);
      asm volatile("" : "+v"(vm));
      const bool hi = so & 128;
      if (QW && PVP) PRIO(2);
      if (!QW && KV0) PRIO(1);
      SB0();
      STAMP(5);
      BARN();
      {
        const bf4 p0 = trrd(smem + po), p1 = trrd(smem + po + 256);
        SB0();
        const bf4 m1 = trrd(smem + (NV == 2 ? qo : po) + 512);
        SB0();
        bf4 m0;
        if constexpr (NV == 2) m0 = trrd(smem + po + 512);
        SB0();
        const u32x8 pp = ilv(m0, m1, p0, p1);
        f4 o[NV];
#pragma unroll
        for (int d = NV - 1; d >= 0; d--) {
          o[d] = MFMA(vx[d], __builtin_bit_cast(bf8, __builtin_shufflevector(pp, pp, 4, 5, 6, 7)), ((f4){0, 0, 0, 0})); asm volatile("" : "+v"(o[d])); SB0();
        }
        o[NV - 1] = MFMA(__builtin_bit_cast(bf8, vm), __builtin_bit_cast(bf8, __builtin_shufflevector(pp, pp, 2, 3, 4, 5)), o[NV - 1]);
        asm volatile("" : "+v"(o[NV - 1]));
        SB0();
        if constexpr (NV == 2) {
          o[0] = MFMA(__builtin_bit_cast(bf8, vm), __builtin_bit_cast(bf8, __builtin_shufflevector(pp, pp, 0, 1, 2, 3)), o[0]);
          asm volatile("" : "+v"(o[0]));
        }
        asm volatile("" :: "v"(pp));
        SB0();
        u32x2 ov = {pk2(o[0][0], o[0][1]), pk2(o[0][2], o[0][3])};
        if constexpr (NV == 2) {
          const u32x2 ow = {pk2(o[1][0], o[1][1]), pk2(o[1][2], o[1][3])};
          if (hi) ov = ow;
        }
        ov[0] = umin(ov[0], 0xFFFFFFFEu);
        __builtin_amdgcn_raw_buffer_store_b64(ov, rsX, so, so_, 16);
        SB0();
      }
      STAMP(7);
      if (QW && !PVP) PRIO(2);
      int xo, lo, pa = 0;
      {
        IDX();
        if constexpr (NV == 1) {
          __builtin_amdgcn_s_sleep(LSL);
#pragma unroll
          for (int i = 0; i < 2; i++) nx[i] = __builtin_amdgcn_raw_buffer_load_b128(rsB, lane * 16 + i * 1024, nb + vt * 2048, 16);
          pa = C_QX + vt * 2048 + lane * 16;
          asm volatile("" : "+v"(pa));
        }
        xo = OB_OFF((lq & 1) * 16 + l15, (lq >> 1) * 8);
        lo = lane * 4;
        asm volatile("" : "+v"(xo), "+v"(lo));
#pragma unroll
        for (int j = 0; j < 4; j++) LDW(pre[j], NV == 2 ? 2 + j : j);
      }
      SB0();
      BARN();
      STAMP(8);
      {
        f4 o[2][NV];
#pragma unroll
        for (int it = 0; it < 2; it++) {
          const bf8 pf = *(const bf8*)(smem + C_PX + it * 1024 + lo * 4);
          const u32 pm = *(const u32*)(smem + C_PX + 2048 + it * 256 + lo);
#pragma unroll
          for (int d = 0; d < NV; d++) {
            o[it][d] = MFMA(vx[d], pf, ((f4){0, 0, 0, 0}));
            o[it][d] = MFMA(__builtin_bit_cast(bf8, vm), __builtin_bit_cast(bf8, d ? (u32x4){0u, pm, 0u, 0u} : (u32x4){pm, 0u, 0u, 0u}), o[it][d]);
          }
        }
#pragma unroll
        for (int d = 0; d < NV; d++)
          __builtin_amdgcn_raw_buffer_store_b128(pair16(umin(pk2(o[0][d][0], o[0][d][1]), 0xFFFFFFFEu), pk2(o[0][d][2], o[0][d][3]),
                                                        umin(pk2(o[1][d][0], o[1][d][1]), 0xFFFFFFFEu), pk2(o[1][d][2], o[1][d][3])), rsB,
              xo, 3 * ACB + OB_OFF(s * 32, h * 128 + (vt + d) * 16), 16);
        SB0();
        if (s == 63) break;
        if constexpr (NV == 1) { *(u32x4*)(smem + pa) = nx[0]; *(u32x4*)(smem + pa + 1024) = nx[1]; }
      }
      STAMP(9);
      YEND();
    }
    b3 = b3 == 2 ? 0 : b3 + 1;
    STAMP(14);
  }
  };
  if (wid == 0) run(std::integral_constant<int, 0>{});
  else if (wid == 4) run(std::integral_constant<int, 1>{});
  else if (wid > 4) run(std::integral_constant<int, 3>{});
  else if (wid == 3) run(std::integral_constant<int, 2>{});
  else run(std::integral_constant<int, 4>{});
  PRIO(0);
  TLS(0);
}

__device__ __forceinline__ void chain_out(const Params& p, char* smem, const int c, const int wv) {
  STS(32 + c, 1);
  constexpr int R = 5; (void)R;
  const int wid = wv, tq = c & 1, cg = c >> 1;
  u32x8 w[4][5];
  const __amdgpu_buffer_rsrc_t rsX = RSRC(XWIN(p), X_END);
  int xp, mo, wo, ro;
  {
    IDX();
    mo = wid < 4 ? cg * 512 + tq * 256 + wid * 64 : 16384 + (cg >> 2) * 512 + tq * 256 + (cg & 3) * 64;
    if (wid > 4 || (wid == 4 && cg >= 8)) mo = PJ_OOB;
    mo = __builtin_amdgcn_readfirstlane(mo);
    __builtin_amdgcn_raw_buffer_store_b128(ones4(), rsX, lane < 16 ? X_M + 32768 + (lane >> 3) * 32768 + ((lane >> 2) & 1) * 8192 + (lane & 3) * 16 : PJ_OOB, mo, 16);
    {
      const u32 m = *(const u32*)(p.Bm + (tq * 4 + q4) * 576 + (wid < 4 ? cg * 32 + wid * 8 : 512 + (cg & 7) * 8) + p4 * 2);
      __builtin_amdgcn_raw_buffer_store_b32(umin(m, 0xFFFFFFFEu), rsX, lq < 2 ? X_M + lq * 8192 + l15 * 4 : PJ_OOB, mo, 16);
      __builtin_amdgcn_raw_buffer_store_b32(DO0 * 64u, rsX, tid < 4 ? X_ANN + (tid & 1) * 1024 + (tid >> 1) * 8192 + (32 + c) * ANS : PJ_OOB, 0, 16);
    }
#pragma unroll
    for (int i = 0; i < 2; i++) __builtin_amdgcn_raw_buffer_store_b128(ones4(), RSRC(WSP(char, O_OB), ACB), tid * 16, OB_OFF((62 + i) * 32, c * 128), 16);
    VMW0();
    BARN();
    if (tid == 0) st4_sc1(WSP(u32, 0) + SY_INIT(32 + c), 1u);
    SB0();
    const __amdgpu_buffer_rsrc_t rw = RSRC(p.BWo, 576 * 4096 * 2);
    u32x4 st[4][8];
    const int va = ((lane >> 2) * 576 + cg * 32 + p4 * 8) * 2;
#pragma unroll
    for (int i = 0; i < 4; i++) {
#pragma unroll
      for (int k = 0; k < 8; k++) st[i][k] = __builtin_amdgcn_raw_buffer_load_b128(rw, va, (((i & 1) * 16 + wid * 2 + (k >> 2)) * 128 + (i >> 1) * 64 + (k & 3) * 16) * 1152, OSTG_AUX);
      SB0();
    }
    char* sc = smem + wid * 10240;
    auto frag = [&](const char* b, int rs) {
      bf4 tt[4];
#pragma unroll
      for (int kk = 0; kk < 4; kk++) {
        const int r0 = ((kk >> 1) * 2 + (lq >> 1)) * 32 + (p4 >> 1) * 16 + (q4 >> 1) * 8 + (lq & 1) * 4 + (kk & 1) * 2 + (q4 & 1);
        tt[kk] = trrd(b + r0 * rs + (p4 & 1) * 8);
      }
      return ilv(tt[0], tt[1], tt[2], tt[3]);
    };
#pragma unroll
    for (int i = 0; i < 4; i++) {
#pragma unroll
      for (int k = 0; k < 8; k++) *(u32x4*)(sc + (k * 16 + (lane >> 2)) * 80 + p4 * 16) = st[i][k];
      __builtin_amdgcn_fence(__ATOMIC_RELEASE, "wavefront");
      __builtin_amdgcn_wave_barrier();
#pragma unroll
      for (int t = 0; t < 4; t++) w[i][t] = frag(sc + t * 16, 80);
      asm volatile("s_waitcnt lgkmcnt(0)" ::: "memory");
      __builtin_amdgcn_wave_barrier();
      SB0();
    }
    TLS(8);
    BARN();
    if (wid == 0) {
      u32 best[2] = {~0u, ~0u};
#pragma unroll 1
      for (int r = 0; r < 3; r++) {
#pragma unroll
        for (int k = 0; k < 2; k++) {
          const u32 t0 = (u32)__builtin_readcyclecounter();
          __builtin_amdgcn_raw_buffer_store_b32((u32)r, rsX, X_CAL + k * 8192 + c * 128, 0, 16);
          VMW0();
          best[k] = umin(best[k], (u32)__builtin_readcyclecounter() - t0);
        }
      }
      if (tid == 0) st4_sc1(WSP(u32, 0) + SY_VOTE(c), best[1] < best[0] ? 2u : 1u);
      TLS(11);
      int it = 0;
      while (c < 16 && ld4_sc1(WSP(u32, 0) + SY_WX) < 32u && ++it < SPIN_CAP) __builtin_amdgcn_s_sleep(GPS);
    }
    BARN();
    {
      const __amdgpu_buffer_rsrc_t rx = RSRC(WSP(char, O_WX), 524288);
#pragma unroll
      for (int k = 0; k < 8; k++) st[0][k] = __builtin_amdgcn_raw_buffer_load_b128(rx, lane * 16, cg * 65536 + (((k & 2) * 8 + wid * 2 + (k & 1)) * 128 + (k >> 2) * 64) * 16, 16);
#pragma unroll
      for (int k = 0; k < 8; k++) *(u32x4*)(sc + k * 1024 + lane * 16) = st[0][k];
      __builtin_amdgcn_fence(__ATOMIC_RELEASE, "wavefront");
      __builtin_amdgcn_wave_barrier();
#pragma unroll
      for (int i = 0; i < 4; i++) w[i][4] = frag(sc + i * 2048, 16);
    }
    if (wid == 0) {
      u32 fl, fv;
      int it = 0;
      do { fl = ld4_sc1(WSP(u32, 0) + SY_INIT(lane)); fv = ld4_sc1(WSP(u32, 0) + SY_VOTE(lane & 31));
           if (__all(fl != 0u && fv != 0u)) break;
           __builtin_amdgcn_s_sleep(GPS); } while (++it < SPIN_CAP);
      *(int*)(smem + C_XP) = __popcll(__ballot(fv == 2u)) > 32 ? 8192 : 0;
    }
    BAR();
    xp = __builtin_amdgcn_readfirstlane(*(const int*)(smem + C_XP));
    const int row = (lq & 1) * 4 + ((l15 >> 1) & 3);
    wo = ((lq >> 1) == hi8 ? row * 128 : 1024) + (((wid * 2 + hi8) ^ (row * 2)) << 3) + (l15 & 1) * 4;
    ro = wid * 2560 + (lane >> 5) * 1280 + ((lane >> 2) & 7) * 128 + (lane & 8) * 8 + p4 * 16;
  }
  __syncthreads();
  STS(32 + c, 14);
  TLS(9);
  int go, sidx, po;
  { IDX(); go = lane * 16; sidx = (l15 & 2) ? 0xEEEE : 0x4444;
    po = p4 == 0 ? (lq & 1) * 32 + (lq >> 1) * 16 + q4 * 4 : PJ_OOB; }
  int ro1 = ro ^ 64;
  asm volatile("" : "+v"(ro), "+v"(ro1), "+v"(wo));
#define PUTP(t) asm volatile("ds_write2st64_b32 %0, %1, %2 offset0:%3 offset1:%4" :: "v"(wo), "v"(d[t][0] + d[t][2]), "v"(d[t][1] + d[t][3]), "i"((t) * 10), "i"((t) * 10 + 5) : "memory")
#define ONOP 7
  int b3 = 0, dur = DO0 * 64;
#pragma unroll 1
  for (int s = 0; s < 63; s++) {
    f4 d[5];
    int gb[4], pb = mo + X_M + (b3 == 2 ? 0 : b3 + 1) * 32768 + xp;
#pragma unroll
    for (int i = 0; i < 4; i++) { gb[i] = X_O + b3 * 131072 + tq * 65536 + wid * 1024 + xp + i * 16384; asm volatile("" : "+s"(gb[i])); }
    asm volatile("" : "+s"(pb));
    u32x4 g[4];
    u32 bad;
    auto mult = [&]() __attribute__((always_inline)) {
      CFENCE();
#pragma unroll
      for (int i = 0; i < 4; i++) {
        g[i] = __builtin_amdgcn_raw_buffer_load_b128(rsX, go, gb[i], 16);
        SB0();
        if (ONOP >= 0 && i < 3) asm volatile("s_nop %0" :: "i"(ONOP >= 0 ? ONOP : 0));
        SB0();
      }
      bad = 0u;
#pragma unroll
      for (int i = 0; i < 4; i++) {
        SB0();
#pragma unroll
        for (int t = 0; t < 5; t++) {
          d[t] = SMF(g[i], w[i][t], d[t], sidx);
          if (t == 0) bad = umax(umax(bad, g[i][0]), g[i][2]);
          if (i == 3 && t >= 2) { SB0(); PUTP(t - 2); SB0(); }
        }
      }
      SB0();
#pragma unroll
      for (int t = 3; t < 5; t++) PUTP(t);
      SB0();
    };
#pragma unroll
    for (int t = 0; t < 5; t++) d[t] = zero4();
    SB0();
    u32 dl = 0;
    if (wid == 7) AD_WAIT(false, K1, SPIN_CAP);
    STAMP(15);
    BARN();
    STAMP(0);
    mult();
    if (__builtin_expect(__any(bad == ~0u), 0)) {
      int it = 0;
      do {
#pragma unroll
        for (int t = 0; t < 5; t++) d[t] = zero4();
        mult();
      } while (__any(bad == ~0u) && ++it < SPIN_CAP);
    }
    STAMP(1);
    BARC();
    STAMP(2);
    if (__builtin_expect(wid < 5, 1)) {
      const f4 t0 = *(const f4*)(smem + ro), t1 = *(const f4*)(smem + ro1);
      float a = (t0[0] + t0[2]) + (t1[0] + t1[2]), b = (t0[1] + t0[3]) + (t1[1] + t1[3]);
      a += __uint_as_float(__builtin_amdgcn_mov_dpp(__float_as_uint(a), 0xB1, 0xf, 0xf, true));
      b += __uint_as_float(__builtin_amdgcn_mov_dpp(__float_as_uint(b), 0xB1, 0xf, 0xf, true));
      a += __uint_as_float(__builtin_amdgcn_mov_dpp(__float_as_uint(a), 0x4E, 0xf, 0xf, true));
      b += __uint_as_float(__builtin_amdgcn_mov_dpp(__float_as_uint(b), 0x4E, 0xf, 0xf, true));
      __builtin_amdgcn_raw_buffer_store_b32(umin(pk2(a, b), 0xFFFFFFFEu), rsX, po, pb, 16);
      SB0();
      STAMP(3);
      __builtin_amdgcn_s_sleep(4);
      __builtin_amdgcn_raw_buffer_store_b32(~0u, rsX, po, mo + X_M + b3 * 32768 + xp, 16);
    } else if (wid == 7) {
      dur += (min((int)(((u32)wall_clock64() << 6) - dl), dur + DOCAP * 64) - dur) >> DSH;
      __builtin_amdgcn_raw_buffer_store_b32((u32)dur, rsX, 0, X_ANN + (~s & 1) * 1024 + xp + (32 + c) * ANS, 16);
    }
    VMW0C();
    YEND();
    b3 = b3 == 2 ? 0 : b3 + 1;
  }
  __syncthreads();
}

__device__ __forceinline__ void wait_ge(const u32* w, u32 val, const int wv) {
  if (TIDX == 0) {
    int it = 0;
    while (ld4_sc1(w) < val && ++it < SPIN_CAP) __builtin_amdgcn_s_sleep(WPS);
  }
}
__device__ __forceinline__ void acquire_all(const int wv) {
  if (TIDX == 0) {
    __builtin_amdgcn_fence(__ATOMIC_ACQUIRE, "agent");
    VMW0();
  }
  __syncthreads();
}

#define T_KV 0
#define T_Q 0
#define T_A 192
#define T_W (T_A + 64)
#define T_S (T_W + 256)
#define T_L (T_S + 72)
#define T_O T_L
#define T_CHAIN (1 << 20)
#define CODE_KB 84
#define WWARM 0
#define HWARM 112
#define AORD 0x01234567u
#define OORD 0x67014523u

__device__ __forceinline__ int next_task(const Params& p, int* s_task, int xq, int& xk, const int wv, const int pt) {
  __syncthreads();
  if (TIDX == 0) {
    int t = -1;
    while (xk < 6) {
      int x = xq + xk; if (x >= 6) x -= 6;
      const int i = (int)__hip_atomic_fetch_add(WSP(u32, 0) + SY_XQ(x), 1u, __ATOMIC_RELAXED, __HIP_MEMORY_SCOPE_AGENT);
      if (i < 32) { t = x * 32 + i; break; }
      xk++;
    }
    if (t < 0 && xk == 6) {
      xk = 7;
      int it = 0;
      while (ld4_sc1(WSP(u32, 0) + SY_NCL) < 64u && ld4_sc1(WSP(u32, 0) + SY_ARR) < 256u && ++it < SPIN_CAP) __builtin_amdgcn_s_sleep(8);
      if (ld4_sc1(WSP(u32, 0) + SY_NCL) < 64u) {
        for (int k = 0; k < 64; k++)
          if (__hip_atomic_exchange(WSP(u32, 0) + SY_CLAIM(k), 1u, __ATOMIC_RELAXED, __HIP_MEMORY_SCOPE_AGENT) == 0u) {
            __hip_atomic_fetch_add(WSP(u32, 0) + SY_NCL, 1u, __ATOMIC_RELAXED, __HIP_MEMORY_SCOPE_AGENT);
            t = T_CHAIN + k; break;
          }
      }
    }
    if (t < 0) t = T_A + (int)__hip_atomic_fetch_add(WSP(u32, 0) + SY_QHEAD, 1u, __ATOMIC_RELAXED, __HIP_MEMORY_SCOPE_AGENT);
    *s_task = t;
  }
  __syncthreads();
  return __builtin_amdgcn_readfirstlane(*s_task);
}

__device__ __forceinline__ int take_task(int* s_task, const int pre, const int wv, const int pb, const int pl, const int pt) {
  if (TIDX == 0) {
    *s_task = pre;
  }
  __syncthreads();
  const int t = __builtin_amdgcn_readfirstlane(*s_task);
  __syncthreads();
  return t;
}
#if defined(__HIP_DEVICE_COMPILE__) && !defined(KEEP_PK)
#define NOPK __attribute__((target("no-packed-fp32-ops")))
#else
#define NOPK
#endif
#define P0V 2
__device__ __forceinline__ void mega_body(const Params& p, char* smem) {
  u64 pc0; asm volatile("s_getpc_b64 %0" : "=s"(pc0));
  int* const s_task = (int*)(smem + LDS_TOTAL);
  const int wv = __builtin_amdgcn_readfirstlane(threadIdx.x >> 6);
  if (blockIdx.x == 0 && TIDX == 0) asm volatile("s_nop 0" ::: "memory");
#define tid TIDX
  const int xcc = (int)(__builtin_amdgcn_s_getreg(20 | (3 << 11)) & 7u);
  const int xq = xcc >= 2 ? xcc - 2 : xcc;
  int xk = 0, pre = 0;
  u32* const qh = WSP(u32, 0) + SY_QHEAD;
  if (wv) {
    const int g = ((blockIdx.x >> 3) & 31) * 7 + wv - 1, vo = (tid & 63) * 64;
#define WARM(ptr, bytes, so) __builtin_amdgcn_raw_ptr_buffer_load_lds(RSRC(ptr, bytes), (LDSP void*)(smem + LDS_ALL - 2048 + wv * 256), 4, vo, so, 0, 0)
    WARM(pc0, CODE_KB * 1024, g * 4096);
    if (xcc > 1) {
      const u16* w = xq < 2 ? p.BWk : xq < 4 ? p.BWv : p.BWq;
      WARM(p.Bx, 256 * 1152, (223 - g) * 4096);
#pragma unroll
      for (int i = 0; i < WWARM; i++) WARM(w, WOB, (g + i * 224) * 8192 + (xq & 1) * 4096);
    } else if (xcc == 0) {
      WARM(p.BWq, HWARM * 8192, g * 4096); WARM(p.BWk, HWARM * 8192, g * 4096); WARM(p.BWv, HWARM * 8192, g * 4096);
    }
  }
  if (tid == 0) {
    int t0 = -1;
    if (xcc < 2) {
      const int k = ((blockIdx.x >> 3) & 31) + xcc * 32;
      if (__hip_atomic_exchange(WSP(u32, 0) + SY_CLAIM(k), 1u, __ATOMIC_RELAXED, __HIP_MEMORY_SCOPE_AGENT) == 0u) {
        __hip_atomic_fetch_add(WSP(u32, 0) + SY_NCL, 1u, __ATOMIC_RELAXED, __HIP_MEMORY_SCOPE_AGENT);
        t0 = T_CHAIN + k;
      }
    } else {
      const int i = (int)__hip_atomic_fetch_add(WSP(u32, 0) + SY_XQ(xq), 1u, __ATOMIC_RELAXED, __HIP_MEMORY_SCOPE_AGENT);
      if (i < 32) t0 = xq * 32 + i;
    }
    __hip_atomic_fetch_add(WSP(u32, 0) + SY_ARR, 1u, __ATOMIC_RELAXED, __HIP_MEMORY_SCOPE_AGENT);
    *s_task = t0;
  }
  BAR();
  int t = __builtin_amdgcn_readfirstlane(*s_task);
  if (t < 0) t = next_task(p, s_task, xq, xk, wv, t);
  {
    while (t < T_A) {
      const int x = t >> 5, j = (t & 7) * 4 + ((0x9C >> ((t >> 2) & 6)) & 3);
      TLS(10);
      proj_slot(p, x, j, smem, wv);
      t = next_task(p, s_task, xq, xk, wv, t);
    }
  }
  if (t >= T_CHAIN) { if (t < T_CHAIN + 32) chain_head(p, smem, t - T_CHAIN, wv); else chain_out(p, smem, t - T_CHAIN - 32, wv);
    const int l = tid & 7;
    const u32 v = ld4_sc1(WSP(u32, 0) + (l < 7 ? SY_OQ(l) : SY_QHEAD));
    if (tid == 0) *s_task = 0;
    __syncthreads();
    if (v < (l == 7 ? T_S - T_A : l == 6 ? 72 : (l & 1) ? XS0 / 2 : 32)) *s_task = 1;
    __syncthreads();
    if (__builtin_amdgcn_readfirstlane(*s_task) == 0) {
      if (xcc == 0) TLS(3); else TLS(4);
      return;
    }
    xk = 7; t = next_task(p, s_task, xq, xk, wv, -1);
  }
  while (t < T_W) {
    wof_task(p, t - T_A, smem, wv, qh, pre);
    if (tid == 0) __hip_atomic_fetch_add(WSP(u32, 0) + SY_CW, 1u, __ATOMIC_RELAXED, __HIP_MEMORY_SCOPE_AGENT);
    t = T_A + take_task(s_task, pre, wv, T_A, T_S - T_A, t);
  }
  while (t < T_S) {
    const int qb = (AORD >> (((t - T_W) >> 5) * 4)) & 7, h = (t - T_W) & 31;
    if (tid < 64) {
      int it = 0;
      while (true) {
        const u32 f0 = ld4_sc1(WSP(u32, 0) + SY_BLK(3, h >> 1, tid)), f1 = ld4_sc1(WSP(u32, 0) + SY_BLK(4, h >> 1, tid)), f2 = ld4_sc1(WSP(u32, 0) + SY_BLK(5, h >> 1, tid));
        if (__all(tid >= qb * 8 + 8 || ((tid < qb * 8 || f0 >= 8u) && f1 >= 8u && f2 >= 8u)) || ++it >= SPIN_CAP) break;
        __builtin_amdgcn_s_sleep(8);
      }
    }
    acquire_all(wv);
    fa::attn_block(WSP(char, O_QA) + FR_OFF(qb * 256, h * 128), WSP(char, O_KA) + h * 8192, WSP(char, O_VA) + h * 8192,
                   WSP(char, O_CA) + OB_OFF(qb * 256, h * 128), qb * 256, smem, wv, qh, pre);
    if (tid == 0) __hip_atomic_fetch_add(WSP(u32, 0) + SY_CC(qb), 1u, __ATOMIC_RELAXED, __HIP_MEMORY_SCOPE_AGENT);
    t = T_A + take_task(s_task, pre, wv, T_A, T_S - T_A, t);
  }
  int rest = 64 | (1 << xq);
#pragma unroll 1
  while (rest) {
    const int x = __builtin_ctz(rest);
    rest &= rest - 1;
    if (x == 6) {
      CLAIMS();
      t = take_task(s_task, pre, wv, T_S, 72, -1);
      while ((t & 0xFFFF) < 72) {
        t &= 0xFFFF;
        small_out_task(p, t & 3, t >> 2, smem, wv, pre);
        t = take_task(s_task, pre, wv, T_S, 72, T_S + t);
      }
      rest = t >> 16;
      continue;
    }
    const int part = x & 1, cg = x >> 1, n = part ? XS0 / 2 : 32;
    u32* const qh = WSP(u32, 0) + SY_OQ(x);
    CLAIM();
    t = take_task(s_task, pre, wv, T_O + x * 32, n, -1);
    while (t < n) {
      const int rb = part ? t : ((OORD >> ((t >> 2) * 4)) & 7) * 4 + (t & 3);
      if (tid == 0) {
        const u32* w = WSP(u32, 0) + (part ? SY_CS(rb * 2 + 1) : SY_CC(rb >> 2));
        int it = 0;
        while ((ld4_sc1(w) < (part ? 1u : 32u) || ld4_sc1(WSP(u32, 0) + SY_CW) < 64u) && ++it < SPIN_CAP) __builtin_amdgcn_s_sleep(WPS);
      }
      acquire_all(wv);
      out_tile(WSP(char, part ? O_OB : O_CA) + rb * 2 * 262144, WSP(char, O_WOF) + part * WOB + cg * 12 * 131072,
               p.out + (size_t)rb * 64 * 1152 + part * 576 + cg * 192, smem, wv, qh, pre);
      t = take_task(s_task, pre, wv, T_O + x * 32, n, T_O + x * 32 + t);
    }
  }
  if (xcc == 0) TLS(3); else if (xcc == 1) TLS(4); else TLS(5);
}

#undef tid
__global__ NOPK void __launch_bounds__(512, 2) mega(Params p) {
  __shared__ __attribute__((aligned(16))) char smem[LDS_ALL];
  mega_body(p, smem);
}
extern "C" void kernel_launch(void* const* d_in, const int* in_sizes, int n_in, void* d_out, int out_size, void* d_ws,
                              size_t ws_size, hipStream_t stream) {
  Params p;
  p.Ax = (const u16*)d_in[0]; p.Ac = (const u16*)d_in[1]; p.Awq = (const u16*)d_in[2]; p.Awk = (const u16*)d_in[3];
  p.Awv = (const u16*)d_in[4]; p.Awo = (const u16*)d_in[5]; p.Bx = (const u16*)d_in[6]; p.Bm = (const u16*)d_in[7];
  p.BWq = (const u16*)d_in[8]; p.BWk = (const u16*)d_in[9]; p.BWv = (const u16*)d_in[10]; p.BWo = (const u16*)d_in[11];
  p.out = (u16*)d_out;
  p.ws = (char*)d_ws;
  const size_t syncBytes = SY_WORDS * 4;
  (void)hipMemsetAsync(d_ws, 0, syncBytes, stream);
  mega<<<256, 512, 0, stream>>>(p);
}
```

```cpp
#include <hip/hip_runtime.h>
#include <hip/hip_bf16.h>
#include <stdint.h>
#include <type_traits>

typedef unsigned short u16;
typedef unsigned int u32;
typedef unsigned long long u64;
typedef __attribute__((ext_vector_type(8))) short bf8;
typedef __attribute__((ext_vector_type(4))) short bf4;
typedef __attribute__((ext_vector_type(4))) float f4;
typedef __attribute__((ext_vector_type(2))) float f2v;
typedef __attribute__((ext_vector_type(2))) __bf16 bf2v;
typedef __attribute__((ext_vector_type(4))) unsigned u32x4;
typedef __attribute__((ext_vector_type(2))) unsigned u32x2;
typedef float f32x16 __attribute__((ext_vector_type(16)));
typedef __attribute__((ext_vector_type(8))) unsigned u32x8;
typedef __attribute__((ext_vector_type(8))) __bf16 bh8;
typedef __attribute__((ext_vector_type(16))) __bf16 bh16;
#define LDSP __attribute__((address_space(3)))

__device__ __forceinline__ u32 pk2(float a, float b) {
  f2v x = {a, b};
  bf2v y = __builtin_convertvector(x, bf2v);
  return __builtin_bit_cast(u32, y);
}
__device__ __forceinline__ u32 umin(u32 a, u32 b) { return a < b ? a : b; }
__device__ __forceinline__ u32 umax(u32 a, u32 b) { return a > b ? a : b; }
__device__ __forceinline__ float rbf(float f) { return __uint_as_float(pk2(f, f) << 16); }
#define MFMA(a, b, c) __builtin_amdgcn_mfma_f32_16x16x32_bf16(a, b, c, 0, 0, 0)
#define MFMA16(a, b, c) __builtin_amdgcn_mfma_f32_16x16x16bf16_1k(a, b, c, 0, 0, 0)
#define SMF(a, b, c, idx) __builtin_amdgcn_smfmac_f32_16x16x64_bf16(__builtin_bit_cast(bh8, a), __builtin_bit_cast(bh16, b), c, idx, 0, 0)
__device__ __forceinline__ u32x8 ilv(bf4 a, bf4 b, bf4 c, bf4 d) {
  const u32x2 x = __builtin_bit_cast(u32x2, a), y = __builtin_bit_cast(u32x2, b), z = __builtin_bit_cast(u32x2, c), w = __builtin_bit_cast(u32x2, d);
  return (u32x8){x[0], x[1], y[0], y[1], z[0], z[1], w[0], w[1]};
}
__device__ __forceinline__ u32x8 cat16(bf8 a, bf8 b) {
  const u32x4 x = __builtin_bit_cast(u32x4, a), y = __builtin_bit_cast(u32x4, b);
  return (u32x8){x[0], x[1], x[2], x[3], y[0], y[1], y[2], y[3]};
}
__device__ __forceinline__ float swadd(float a, float b) {
  auto x = __builtin_amdgcn_permlane32_swap(__float_as_uint(a), __float_as_uint(b), false, false);
  return __uint_as_float(x[0]) + __uint_as_float(x[1]);
}
__device__ __forceinline__ bf4 trrd(const char* p) {
  return __builtin_amdgcn_ds_read_tr16_b64_v4i16((LDSP bf4*)(p));
}
__device__ __forceinline__ bf8 cat8(bf4 a, bf4 b) { return __builtin_shufflevector(a, b, 0, 1, 2, 3, 4, 5, 6, 7); }
__device__ __forceinline__ void st8_sc1(void* p, u64 v) { __hip_atomic_store((u64*)p, v, __ATOMIC_RELAXED, __HIP_MEMORY_SCOPE_AGENT); }
__device__ __forceinline__ u32 ld4_sc1(const u32* p) { return __hip_atomic_load(p, __ATOMIC_RELAXED, __HIP_MEMORY_SCOPE_AGENT); }
__device__ __forceinline__ void st4_sc1(u32* p, u32 v) { __hip_atomic_store(p, v, __ATOMIC_RELAXED, __HIP_MEMORY_SCOPE_AGENT); }
#define VMW0() asm volatile("s_waitcnt vmcnt(0)" ::: "memory")
#define CFENCE() asm volatile("" ::: "memory")
#define VMW0C() do { __builtin_amdgcn_s_waitcnt(0x0F70); asm volatile("" ::: "memory"); } while (0)
#define SPIN_CAP (1 << 22)
#define XPS 4
#define SPS 4
#define WPS 8
__device__ __forceinline__ int tid_now(int wv) {
  int l; asm volatile("v_mbcnt_lo_u32_b32 %0, -1, 0\n\tv_mbcnt_hi_u32_b32 %0, -1, %0" : "=v"(l));
  return wv * 64 + l;
}
#define TIDX tid_now(wv)
#define CLAIM() do { if (TIDX == 0) pre = (int)__hip_atomic_fetch_add(qh, 1u, __ATOMIC_RELAXED, __HIP_MEMORY_SCOPE_AGENT); } while (0)

#define TLS(i)
struct Params {
  const u16 *Ax, *Ac, *Awq, *Awk, *Awv, *Awo, *Bx, *Bm, *BWq, *BWk, *BWv, *BWo;
  u16* out;
  char* ws;
};
#define SY_QHEAD 0
#define SY_CC(rb) (64 + (rb) * 16)
#define SY_CW (64 + 8 * 16)
#define SY_OQ(x) (64 + 9 * 16 + (x) * 16)
#define SY_CS(s) (64 + 16 * 16 + (s) * 16)
#define SY_B0 (64 + 80 * 16)
#define SY_INIT(c) (SY_B0 + 64 + (c))
#define SY_VOTE(i) (SY_B0 + 128 + (i))
#define SY_CLAIM(c) (SY_B0 + 192 + (c))
#define SY_XQ(x) (SY_B0 + 256 + (x) * 16)
#define SY_ARR (SY_B0 + 384)
#define SY_NCL (SY_ARR + 16)
#define SY_WX (SY_ARR + 32)
#define BLKS 1
#define SY_BLK(mi, cb, g) (SY_NCL + 48 + (((mi) * 16 + (cb)) * 64 + (g)) * BLKS)
#define SY_WORDS (SY_NCL + 48 + 6 * 16 * 64 * BLKS)

#define AL256(x) (((x) + 255) & ~255)
#define O_SYNC_END (SY_WORDS * 4)
#define ACB (2048 * 4096 * 2)
#define O_QA AL256(O_SYNC_END)
#define O_KA (O_QA + ACB)
#define O_VA (O_KA + ACB)
#define O_CA (O_VA + ACB)
#define O_QB (O_CA + ACB)
#define O_KB (O_QB + ACB)
#define O_VB (O_KB + ACB)
#define O_OB (O_VB + ACB)
#define O_WOF (O_OB + ACB)
#define WOB (576 * 4096 * 2)
#define O_WX (O_WOF + 2 * WOB)
#define O_XW (O_WX + 524288)
#define X_HS 0x4000
#define X_O(b) ((b) * 0x80000 + ((0x140 >> ((b) * 4)) & 15) * 4096)
#define X_MS X_HS
#define X_M (0x185000 ^ X_MS)
#define X_ANN (0x185000 + 7 * 32768)
#define X_CAL 0x40000
#define X_END 0x200000
#define OB_OFF(r, c) (((r) >> 5) * 262144 + ((c) >> 4) * 1024 + ((r) & 31) * 32 + ((c) & 15) * 2)
#define FR_OFF(r, c) (((r) >> 5) * 262144 + ((c) >> 3) * 512 + ((r) & 31) * 16 + ((c) & 7) * 2)
#define WSP(T, off) ((T*)(p.ws + (off)))
#define XWIN(p) (((uintptr_t)(p).ws + O_XW + 0x1FFFFFu) & ~(uintptr_t)0x1FFFFFu)

#define XS0 48
#define NAB 8
#define NBB 10
#define NOA (32 - NAB)
#define NOB (XS0 / 2 - NBB)
#define LDS_TOTAL (131072 + 8192 + 16384)
#define LDS_ALL 163840

#define SB0() __builtin_amdgcn_sched_barrier(0)
#define PJ_OOB 0x7F000000
#define RSRC(ptr, bytes) __builtin_amdgcn_make_buffer_rsrc((void*)(ptr), 0, (bytes), 0x00020000)
__device__ __forceinline__ bf8 ldsc1(__amdgpu_buffer_rsrc_t r, int off) {
  return __builtin_bit_cast(bf8, __builtin_amdgcn_raw_buffer_load_b128(r, off, 0, 16));
}
__device__ __forceinline__ u32x4 pair16(u32 a0, u32 a1, u32 b0, u32 b1) {
  auto r0 = __builtin_amdgcn_permlane16_swap(a0, b0, false, false);
  auto r1 = __builtin_amdgcn_permlane16_swap(a1, b1, false, false);
  return (u32x4){r0[0], r1[0], r0[1], r1[1]};
}
__device__ __forceinline__ int fsw(int k) { return (k & 3) | (((k >> 3) & 1) << 2); }

#define C_PARK 131072
#define PJ_PAD 0
__device__ __forceinline__ void proj_slot(const Params& p, int x, int j, char* smem, const int wv, int& pre) {
  const int tid = TIDX, lane = tid & 63, wid = wv;
  const int l15 = lane & 15, lq = lane >> 4, q4 = l15 >> 2, p4 = lane & 3, wr = wid >> 2, wc = wid & 3;
  const __amdgpu_buffer_rsrc_t rc = RSRC(p.ws, O_XW);
  const int vB = ((wid * 4 + lq) * 4096 + (((l15 >> 1) ^ (lq | (((wid >> 1) & 1) << 2))) * 16 + (lane & 1) * 8)) * 2, vB1 = vB + 32 * 8192;
  int ao = (wr * 64 + l15) * 128 + ((lq ^ (l15 & 7)) << 4);
  int bo = (int)(uintptr_t)smem + 65536 + (lq * 8 + q4) * 256 + (((wc * 2) ^ (q4 | ((lq & 1) << 2))) << 5) + p4 * 8;
  const int vD = lane * 16;
#define PJ_ORD 1
#define PJ_BXU 2
#define PJ_NRT 0
#define PJ_NEXT (((PJ_ORD ? 0x1312 : 0x3321) >> (u * 4)) & 3)
  const int cb = (x & 1) * 8 + (j >> 2);
  __amdgpu_buffer_rsrc_t rsA, rsB;
  const u16 *pA0 = p.Bx, *pB0 = (x < 2 ? p.BWk : x < 4 ? p.BWv : p.BWq) + cb * 256;
  const u16 *pA1 = x < 2 ? p.Ax : p.Ac, *pB1 = (x < 2 ? p.Awq : x < 4 ? p.Awk : p.Awv) + cb * 256;
  asm volatile("" : "+s"(pA0), "+s"(pB0), "+s"(pA1), "+s"(pB1));
  int k2, vA, vA1, sa, sb, nvA;
#define PJ_K2(u) ((u) < 2 || x < 2 ? 1152 : 1024)
#define PJ_PREP(u) do { int ln_ = vD; asm volatile("" : "+v"(ln_)); \
    nvA = (wid * 8 + (ln_ >> 7)) * PJ_K2(u) + ((ln_ ^ (ln_ >> 3)) & 0x70); \
    asm volatile("" : "+v"(nvA)); } while (0)
#define PJ_UNIT(u) do { k2 = PJ_K2(u); rsA = RSRC((u) < 2 ? pA0 : pA1, 2048 * k2); rsB = RSRC((u) < 2 ? pB0 : pB1, k2 * 4096 - cb * 512); vA = nvA; vA1 = nvA + 64 * k2; \
    sa = ((j & 3) + ((u) & 1) * 4) * 256 * k2 - 128; sb = 0; } while (0)
#define PJ_DEST(u, rt) do { const int mi_ = ((u) >> 1) * 3 + (x >> 1), g_ = ((rt) + ((u) & 1) * 4) * 8; \
    dco = O_QA + (mi_ == 2 ? 4 : (mi_ + 5) & 7) * ACB + cb * 16384 + wc * 2048 + (g_ + wr * 2) * 262144; \
    dfl = SY_BLK(mi_, cb, g_) * 4; } while (0)
#define PJ_STAGE(dst, rs, v0, v1, so) do { const int so_ = (so); \
    __builtin_amdgcn_raw_ptr_buffer_load_lds(rs, (LDSP void*)(smem + (dst) + wid * 1024), 16, v0, so_, 0, 0); \
    __builtin_amdgcn_raw_ptr_buffer_load_lds(rs, (LDSP void*)(smem + (dst) + wid * 1024 + 8192), 16, v1, so_, 0, 0); } while (0)
#define PJ_SA(dst, so) PJ_STAGE(dst, rsA, vA, vA1, so)
#define PJ_SB(dst, so) PJ_STAGE(dst, rsB, vB, vB1, so)
#define PJ_LDA(off) do { _Pragma("unroll") for (int m = 0; m < 4; m++) _Pragma("unroll") for (int k = 0; k < 2; k++) \
    At[m][k] = *(const bf8*)(smem + (ao ^ (k * 64)) + (off) + m * 2048); } while (0)
#define PJ_TR(dst, a, off) asm volatile("ds_read_b64_tr_b16 %0, %1 offset:%2" : "=&v"(dst) : "v"(a), "i"(off) : "memory")
#define PJ_LDB(D, h) do { const int b1_ = bo ^ 32; _Pragma("unroll") for (int n = 0; n < 2; n++) _Pragma("unroll") for (int k = 0; k < 2; k++) { \
    bf4 lo_, hi_; PJ_TR(lo_, n ? b1_ : bo, (h) * 16384 + k * 8192); PJ_TR(hi_, n ? b1_ : bo, (h) * 16384 + k * 8192 + 1024); D[n][k] = cat8(lo_, hi_); } } while (0)
#define PJ_CV(ai, bj, q) ((u32x4){pk2(acc[ai][bj][(q) & 2][(q) & 1][0], acc[ai][bj][(q) & 2][(q) & 1][1]), pk2(acc[ai][bj][(q) & 2][(q) & 1][2], acc[ai][bj][(q) & 2][(q) & 1][3]), \
                                  pk2(acc[ai][bj][((q) & 2) + 1][(q) & 1][0], acc[ai][bj][((q) & 2) + 1][(q) & 1][1]), pk2(acc[ai][bj][((q) & 2) + 1][(q) & 1][2], acc[ai][bj][((q) & 2) + 1][(q) & 1][3])})
#define PJ_SW(v) pair16(v[0], v[1], v[2], v[3])
#define PJ_MMA(Z, ai, bj, D, F) do { PJ_SCHED; __builtin_amdgcn_s_setprio(1); \
    _Pragma("unroll") for (int k = 0; k < 2; k++) _Pragma("unroll") for (int m = 0; m < 4; m++) _Pragma("unroll") for (int n = 0; n < 2; n++) { \
      if (k == 0 && (Z)) asm volatile("v_mfma_f32_16x16x32_bf16 %0, %1, %2, 0" : "+v"(acc[ai][bj][m][n]) : "v"(D[n][k]), "v"(At[m][k])); \
      else asm volatile("v_mfma_f32_16x16x32_bf16 %0, %1, %2, %0" : "+v"(acc[ai][bj][m][n]) : "v"(D[n][k]), "v"(At[m][k])); \
      F(k * 8 + m * 2 + n); } \
    __builtin_amdgcn_s_setprio(0); if (PJ_PAD) __builtin_amdgcn_s_sleep(PJ_PAD); PJ_SCHED; } while (0)
#define PJ_SWI(a, b) asm volatile("v_permlane16_swap_b32 %0, %1" : "+v"(a), "+v"(b))
#define PJ_CVI(d, s, e) asm volatile("v_cvt_pk_bf16_f32 %0, %1, %2" : "=v"(d) : "v"(s[e]), "v"(s[(e) + 1]))
#define PJ_PACK(P, ai, bj, i) do { const int q_ = (i) >> 2; \
    if (((i) & 3) < 2) { PJ_CVI(P[q_][(i) & 1], acc[ai][bj][q_ & 2][q_ & 1], ((i) & 1) * 2); PJ_CVI(P[q_][2 + ((i) & 1)], acc[ai][bj][(q_ & 2) + 1][q_ & 1], ((i) & 1) * 2); } \
    else PJ_SWI(P[q_][(i) & 1], P[q_][2 + ((i) & 1)]); } while (0)
#define PJ_WV(n) asm volatile("s_waitcnt vmcnt(" #n ")" ::: "memory")
#define PJ_WL(n) asm volatile("s_waitcnt lgkmcnt(" #n ")" ::: "memory")
#define PJ_WL0 do { __builtin_amdgcn_s_waitcnt(0xC07F); asm volatile("" ::: "memory"); } while (0)
#define PJ_BAR __builtin_amdgcn_s_barrier()
#define PJ_SCHED __builtin_amdgcn_sched_barrier(0)
#define PJ_NOP do { PJ_SCHED; asm volatile("s_nop 7"); PJ_SCHED; } while (0)
#define PJ_DS(v, ai, bj, q) do { PJ_SCHED; __builtin_amdgcn_raw_buffer_store_b128(v, rc, vD + ((q) & 1) * 1024, dco + ((ai) * 4 + ((q) >> 1)) * 262144 + (bj) * 8192, 16); \
    asm volatile("s_nop 1"); PJ_SCHED; } while (0)
#define PJ_PARK(q) (*(u32x4*)(smem + C_PARK + wid * 4096 + vD + (q) * 1024))
#define PJ_PUB(ai, n) do { if (wid == 4) __builtin_amdgcn_raw_buffer_store_b32(8u, rc, vD < (n) * 16 ? vD >> 2 : PJ_OOB, dfl + (ai) * 16, 16); } while (0)
#define PJ_ST()
#define PJ_TOUCH(ptr, bytes, vo, so) __builtin_amdgcn_raw_ptr_buffer_load_lds(RSRC(ptr, bytes), (LDSP void*)(smem + C_PARK + wid * 4096), 4, vo, so, 0, 0)
#define PJ_WARM(S) do { int vt_ = vD; asm volatile("" : "+v"(vt_)); vt_ <<= 2; \
    const int g_ = u == 1 || (PJ_NRT && u == 3) || !(j & 3) ? PJ_OOB : ((((x & 1) * 8 + (j >> 2)) * 3 + (j & 3) - 1) * 8 + wid) * 4096, h_ = g_ + (x >> 1) * 384 * 4096; \
    const u16* w_ = (u ? pB0 : pB1) - cb * 256; \
    const int wb_ = 4096 * PJ_K2(PJ_NEXT); \
    if (S == 2) PJ_TOUCH(u == 2 ? pA1 : w_, u == 2 ? 2048 * PJ_K2(2) : wb_, vt_, u == 2 ? g_ + 384 * 4096 : g_); \
    if (S == 3) PJ_TOUCH(u ? pA0 + 1024 * 576 : pA1, u ? 1024 * 1152 : 2048 * PJ_K2(2), vt_, u ? (u == PJ_BXU ? h_ : PJ_OOB) : g_); \
    if (S == 4) PJ_TOUCH(u == 2 ? p.Awo : w_, u == 2 ? WOB : wb_, vt_, u == 2 ? h_ : g_ + 384 * 4096); \
    if (S == 5) PJ_TOUCH(w_, wb_, vt_, u == 2 ? PJ_OOB : g_ + 768 * 4096); } while (0)
#define PJ_F0(i)
#define PJ_F1(i) do { if (S == 8) PJ_PACK(pa, 0, 0, i); } while (0)
#define PJ_F2(i) do { if (S == 8) PJ_PACK(pb, 0, 1, i); if (S == 0) PJ_PACK(pb, 1, 1, i); } while (0)
#define PJ_F3(i) do { if (S == 8) PJ_PACK(pa, 1, 0, i); if (S == 5 && (i) == 0) { PJ_SCHED; PJ_PREP(PJ_NEXT); PJ_SCHED; } } while (0)
#define PJ_STEP(SS) do { constexpr int S = SS; PJ_ST(); \
    PJ_LDB(B0, 0); PJ_SCHED; PJ_LDA(0); PJ_SA((bsel ^ 32768) + 16384, sa + 128 * k2); \
    PJ_WL(8); PJ_BAR; PJ_WL0; PJ_MMA(S == 0, 0, 0, B0, PJ_F0); PJ_BAR; PJ_SCHED; \
    if (S == 7) PJ_UNIT(PJ_NEXT); \
    PJ_LDB(B1, 1); PJ_SB(65536 + bsel, sb); \
    if (S == 0) { PJ_DS(pb[2], 0, 1, 2); PJ_DS(pb[3], 0, 1, 3); } \
    if (S >= 2 && S < 6) PJ_WARM(S); \
    PJ_BAR; PJ_WL0; PJ_MMA(S == 0, 0, 1, B1, PJ_F1); PJ_BAR; \
    PJ_LDA(16384); sa += 128; PJ_SA(bsel, sa); \
    if (S == 8) { PJ_SCHED; PJ_DEST(u, j & 3); _Pragma("unroll") for (int q = 0; q < 4; q++) PJ_PARK(q) = pa[q]; PJ_SCHED; } \
    PJ_BAR; PJ_WL0; PJ_MMA(S == 0, 1, 0, B0, PJ_F2); PJ_BAR; PJ_SCHED; \
    { u32x4 c0_, c1_; \
      if (S < 2) { c0_ = PJ_PARK(2 * S); c1_ = PJ_PARK(2 * S + 1); PJ_SCHED; } \
      PJ_SB(65536 + bsel + 16384, sb + 256); sb += 64 * 8192; \
      if (S == 8) { PJ_DS(pb[0], 0, 1, 0); PJ_DS(pb[1], 0, 1, 1); } \
      else if (S < 2) { PJ_DS(c0_, 0, 0, (2 * S) & 3); PJ_DS(c1_, 0, 0, (2 * S + 1) & 3); } \
      else if (S < 4) { PJ_DS(pa[(2 * S) & 3], 1, 0, (2 * S) & 3); PJ_DS(pa[(2 * S + 1) & 3], 1, 0, (2 * S + 1) & 3); } \
      else if (S < 6) { PJ_DS(pb[(2 * S) & 3], 1, 1, (2 * S) & 3); PJ_DS(pb[(2 * S + 1) & 3], 1, 1, (2 * S + 1) & 3); } } \
      \
    if (S == 0) PJ_WV(10); else if (S >= 2 && S < 6) PJ_WV(9); else if (S == 1 || S == 8) PJ_WV(8); else PJ_WV(6); \
    PJ_BAR; \
    if (S == 2) PJ_PUB(0, 4); \
    if (S == 7) PJ_PUB(1, 4); \
    PJ_MMA(S == 0, 1, 1, B1, PJ_F3); PJ_BAR; \
    bsel ^= 32768; ao ^= 32768; bo ^= 32768; } while (0)
  f4 acc[2][2][4][2];
  bf8 At[4][2], B0[2][2], B1[2][2];
  u32x4 pa[4], pb[4];
  int bsel = 0, dco = PJ_OOB, dfl = PJ_OOB, u = 0;
  auto quick = [&](auto NBC, const int hf, const int ch) __attribute__((always_inline)) {
    constexpr int NB = decltype(NBC)::value, NS = 5 - NB;
#define PJ_FS(t) do { PJ_SA(((t) % NS) * 16384, hf * 128 * 1152 + (t) * 128); \
      _Pragma("unroll") for (int h_ = 0; h_ < NB; h_++) PJ_SB(NS * 16384 + (((t) % NS) * NB + h_) * 16384, (t) * 64 * 8192 + (ch + h_) * 256); PJ_SCHED; } while (0)
#pragma unroll
    for (int t = 0; t < NS - 1; t++) PJ_FS(t);
    bo += NS * 16384 - 65536;
#pragma unroll
    for (int t = 0; t < 9; t++) {
      PJ_ST();
      if (t == 8) PJ_WV(0); else if (NB == 2) PJ_WV(6); else if (t == 7) PJ_WV(4); else PJ_WV(8);
      PJ_BAR; PJ_SCHED;
      if (wr && t + NS - 1 < 9) PJ_FS(t + NS - 1);
      PJ_LDB(B0, 0); PJ_LDA((t % NS) * 16384);
      if (NB == 2) PJ_LDB(B1, 1);
      PJ_SCHED;
      bo += t == 8 ? 65536 - NS * 16384 - (8 % NS) * NB * 16384 : (t % NS == NS - 1 ? -(NS - 1) * NB * 16384 : NB * 16384);
      PJ_WL0;
      PJ_MMA(t == 0, 0, 0, B0, PJ_F0);
      if (NB == 2) PJ_MMA(t == 0, 0, 1, B1, PJ_F0);
      if (!wr && t + NS - 1 < 9) PJ_FS(t + NS - 1);
    }
    PJ_NOP;
#pragma unroll
    for (int q = 0; q < 4 * NB; q++) { const u32x4 v = PJ_CV(0, q >> 2, q & 3); PJ_DS(PJ_SW(v), hf, ch + (q >> 2), q & 3); }
    PJ_WV(0); PJ_BAR;
    if (tid < 4) __hip_atomic_fetch_add(WSP(u32, dfl) + hf * 4 + tid, 4u * NB, __ATOMIC_RELAXED, __HIP_MEMORY_SCOPE_AGENT);
  };
  if (((j + 1) & 3) < 2) {
    PJ_PREP(0); PJ_UNIT(0); PJ_DEST(0, 0);
    quick(std::integral_constant<int, 1>{}, 0, j & 1);
    if ((j & 3) == 0) { quick(std::integral_constant<int, 2>{}, 1, 0); u = PJ_NEXT; }
    dco = PJ_OOB; dfl = PJ_OOB;
  }
#pragma unroll
  for (int a = 0; a < 16; a++) asm volatile("" : "=v"(acc[a >> 3][(a >> 2) & 1][a & 3][0]), "=v"(acc[a >> 3][(a >> 2) & 1][a & 3][1]));
#pragma unroll
  for (int a = 0; a < 4; a++) asm volatile("" : "=v"(pa[a]), "=v"(pb[a]));
  PJ_PREP(u); PJ_UNIT(u);
  const int cs = x < 4 && (j & 3) == 2 ? (x * 8 + (j >> 2)) * 128 : -1;
#pragma unroll
  for (int i = 0; i < 2; i++) pa[i] = __builtin_amdgcn_raw_buffer_load_b128(RSRC(p.BWo, WOB), (tid >> 3) * 1152 + (tid & 7) * 16, cs >= 0 ? (cs + i * 64) * 1152 + 1024 : PJ_OOB, 0);
  PJ_SCHED;
  PJ_SB(65536, 0); PJ_SA(0, sa + 128);
  PJ_SB(65536 + 16384, 256); PJ_SA(16384, sa + 128 + 128 * k2);
  PJ_SCHED;
#pragma unroll
  for (int i = 0; i < 2; i++) __builtin_amdgcn_raw_buffer_store_b128(pa[i], rc, (tid & 7) * 65536 + (tid >> 3) * 16, cs >= 0 ? O_WX + (cs + i * 64) * 16 : PJ_OOB, 16);
  PJ_SCHED;
  if (wr == 1) PJ_BAR;
  PJ_WV(6); PJ_BAR;
  PJ_SB(65536 + 32768, 64 * 8192); PJ_SA(32768, sa + 256);
  PJ_SB(65536 + 32768 + 16384, 64 * 8192 + 256);
  PJ_WV(6); PJ_BAR;
  if (tid == 256 && cs >= 0) __hip_atomic_fetch_add(WSP(u32, 0) + SY_WX, 1u, __ATOMIC_RELAXED, __HIP_MEMORY_SCOPE_AGENT);
  sa += 256; sb = 128 * 8192;
#pragma unroll 1
  for (;; u = PJ_NEXT) {
    PJ_STEP(0); PJ_STEP(1); PJ_STEP(2); PJ_STEP(3); PJ_STEP(4); PJ_STEP(5);
    int more = k2 - 1024; asm volatile("" : "+s"(more));
    if (more) PJ_STEP(6);
    PJ_STEP(7); PJ_STEP(8);
    if (u == (PJ_ORD ? 1 : 3)) break;
  }
  PJ_ST();
  PJ_NOP;
  const u32 left = __builtin_amdgcn_raw_buffer_load_b32(rc, wid ? PJ_OOB : (vD < 96 ? SY_XQ(vD >> 4) : SY_NCL) * 4, 0, 16);
#define FSL 0
#define FWN 15
#define PJ_FD(v, ai, bj, q) do { PJ_DS(v, ai, bj, q); if (FSL) __builtin_amdgcn_s_sleep(FSL); if (FWN < 15) asm volatile("s_waitcnt vmcnt(%0)" :: "i"(FWN) : "memory"); } while (0)
  PJ_FD(pb[2], 0, 1, 2); PJ_FD(pb[3], 0, 1, 3);
#pragma unroll
  for (int q = 0; q < 4; q++) pb[q] = PJ_PARK(q);
#pragma unroll
  for (int q = 0; q < 4; q++) PJ_FD(pa[q], 1, 0, q);
#pragma unroll
  for (int q = 0; q < 4; q++) PJ_FD(pb[q], 0, 0, q);
#pragma unroll
  for (int q = 0; q < 4; q++) { const u32x4 v = PJ_CV(1, 1, q); PJ_FD(PJ_SW(v), 1, 1, q); }
  pre = -1;
  if (__all(left >= (vD < 96 ? 32u : 64u)) && vD == 0) pre = (int)__hip_atomic_fetch_add(WSP(u32, 0) + SY_QHEAD, 1u, __ATOMIC_RELAXED, __HIP_MEMORY_SCOPE_AGENT);
  PJ_WV(0);
  if (wr == 0) PJ_BAR;
  PJ_BAR;
  PJ_PUB(0, 8);
  PJ_ST();
}

namespace fa {
constexpr float SCALE = 0.08838834764831845f;
constexpr int NW = 8, QBLK = 32, KVBLK = 64, QB = NW * QBLK, D = 128;
constexpr int SHM_V = KVBLK * D * 2, SHM_K = KVBLK * D * 2;
#define SBAR() __builtin_amdgcn_sched_barrier(0)
#define MF32(a, b, c) __builtin_amdgcn_mfma_f32_32x32x16_bf16(a, b, c, 0, 0, 0)
#define FMX3(a, b, c) __builtin_elementwise_maximum(__builtin_elementwise_maximum(a, b), c)
#define AT_PAD 0
#define EBAR 14
#define MX0 3
#define ATHR 8.f
#define fadd1(a, b) ((a) + (b))
#define fmul1(a, b) ((a) * (b))
__device__ __forceinline__ void attn_block(const char* __restrict__ Qp, const char* __restrict__ Kh, const char* __restrict__ Vh,
                                           char* __restrict__ Op, int P0, char* lds, const int wv, u32* qh, int& pre) {
  const int tid = TIDX, wid = wv, lane = tid & 63, r32 = lane & 31, hi = lane >> 5, grp = wid >> 2;
  const int NT = (P0 + QB) / KVBLK;
  const int rb = wid ^ 4;
  const int TL = NT - 4 + (rb >> 1);
  const int rem = 2 * (NT - TL) - 2 - grp;
  float l_reg = 0.f;
  f32x16 o[4] = {}, s0, s1, nm = {};
  int ko = lane * 16;
  int vo = 4 * SHM_K + ((lane >> 4) & 1) * (1024 + 128) + ((lane >> 1) & 1) * 512 + ((hi ^ ((lane >> 1) & 1)) << 6) + ((lane >> 2) & 3) * 16 + (lane & 1) * 8;
  const __amdgpu_buffer_rsrc_t rk = RSRC(Kh, (2 * NT - 1) * 262144 + 8192), rv = RSRC(Vh, (2 * NT - 1) * 262144 + 8192);
  const int gk = tid * 16, gv = gk ^ (((tid >> 5) & 3) << 6);
#define DMA(rs, go, dst, so) __builtin_amdgcn_raw_ptr_buffer_load_lds(rs, (LDSP void*)(lds + (dst) + wid * 1024), 16, go, so, 0, 0)
#define ISSUE_K(t) do { const int s_ = (t) * 524288, d_ = ((t) & 3) * SHM_K; DMA(rk, gk, d_, s_); DMA(rk, gk, d_ + 8192, s_ + 262144); } while (0)
#define ISSUE_V(t) do { const int s_ = (t) * 524288, d_ = 4 * SHM_K + ((t) & 3) * SHM_V; DMA(rv, gv, d_, s_); DMA(rv, gv, d_ + 8192, s_ + 262144); } while (0)
#define PHASE(n) do { SBAR(); asm volatile("s_waitcnt vmcnt(" #n ")" ::: "memory"); __builtin_amdgcn_s_barrier(); asm volatile("" ::: "memory"); SBAR(); } while (0)
#define LDK(d0) do { kf[(d0) % 3][0] = *(const bf8*)(lds + ko + (d0) * 1024); kf[(d0) % 3][1] = *(const bf8*)(lds + ko + (d0) * 1024 + 8192); } while (0)
#define LDV(i) do { const int o_ = ((i) & 3) * 2048 + (((i) >> 2) & 1) * 256 + ((i) >> 3) * 8192; vf[(i) & 3] = cat8(trrd(lds + vo + o_), trrd(lds + vo1 + o_)); } while (0)
#define AST(i)
#define AP(i)
  AP(0);
#define QK(NEXT) do { _Pragma("unroll") for (int d0 = 0; d0 < 8; ++d0) { \
      if (d0 + 2 < 8) LDK(d0 + 2); else { NEXT(2 * (d0 - 6)); NEXT(2 * (d0 - 6) + 1); } \
      SBAR(); \
      if (d0) { s0 = MF32(kf[d0 % 3][0], qr[d0], s0); s1 = MF32(kf[d0 % 3][1], qr[d0], s1); } \
      else { asm volatile("v_mfma_f32_32x32x16_bf16 %0, %1, %2, %3" : "+v"(s0) : "v"(kf[0][0]), "v"(qr[0]), "v"(nm)); \
             asm volatile("v_mfma_f32_32x32x16_bf16 %0, %1, %2, %3" : "+v"(s1) : "v"(kf[0][1]), "v"(qr[0]), "v"(nm)); } \
      SBAR(); } } while (0)
#define NOV(i)
#define MAXS(i) do { \
      if ((i) == 0) { mx = FMX3(s0[0], s0[1], s0[2]); my = FMX3(s1[0], s1[1], s1[2]); } \
      else if ((i) < 7) { mx = FMX3(mx, s0[2 * (i) + 1], s0[2 * (i) + 2]); my = FMX3(my, s1[2 * (i) + 1], s1[2 * (i) + 2]); } \
      else { mx = FMX3(mx, s0[15], s1[15]); mx = __builtin_elementwise_maximum(mx, my); asm volatile("" : "+v"(mx)); } } while (0)
  float mx, my;
  constexpr float C2 = 1.4426950408889634f * SCALE;
  bf8 qr[8], kf[3][2], vf[4], pa[4];
#pragma unroll
  for (int d0 = 0; d0 < 8; ++d0) qr[d0] = *(const bf8*)(Qp + rb * 262144 + d0 * 1024 + lane * 16);
  SBAR();
  ISSUE_K(0); ISSUE_K(1); ISSUE_V(0); ISSUE_K(2); ISSUE_V(1);
  SBAR();
  asm volatile("" : "=v"(s0), "=v"(s1), "+v"(nm), "+v"(o[0]), "+v"(o[1]), "+v"(o[2]), "+v"(o[3]));
  SBAR();
#pragma unroll
  for (int d0 = 0; d0 < 8; ++d0) {
    u32x4 q = __builtin_bit_cast(u32x4, qr[d0]);
#pragma unroll
    for (int i = 0; i < 4; i++) q[i] = pk2(__uint_as_float(q[i] << 16) * C2, __uint_as_float(q[i] & 0xFFFF0000u) * C2);
    qr[d0] = __builtin_bit_cast(bf8, q);
    asm volatile("" : "+v"(qr[d0]));
  }
  AP(8);
  PHASE(8);
  AP(1);
  if (grp) PHASE(6);
  LDK(0); LDK(1);
  QK(NOV);
#pragma unroll
  for (int i = 0; i < 8; ++i) MAXS(i);
  AP(2);
  PHASE(4);
  AP(3);
  auto tile = [&](const int t, auto LASTC) __attribute__((always_inline)) {
    constexpr bool LAST = decltype(LASTC)::value;
    AST(0);
    ISSUE_K(t + 3); ISSUE_V(t + 2);
    SBAR();
    if constexpr (LAST) {
      const int dq = r32 - 4 * hi;
      if (rb & 1) {
#pragma unroll
        for (int r = 0; r < 16; ++r) { if (dq < (r & 3) + 8 * (r >> 2)) s1[r] = -__builtin_inff(); SBAR(); }
      } else {
#pragma unroll
        for (int r = 0; r < 16; ++r) { if (dq < (r & 3) + 8 * (r >> 2)) s0[r] = -__builtin_inff(); s1[r] = -__builtin_inff(); SBAR(); }
      }
#pragma unroll
      for (int i = 0; i < 8; ++i) MAXS(i);
    }
    {
      if (__builtin_expect(t == 0 || __any(mx > ATHR), 0)) {
        auto rr = __builtin_amdgcn_permlane32_swap(__float_as_uint(mx), __float_as_uint(mx), false, false);
        mx = __builtin_elementwise_maximum(__uint_as_float(rr[0]), __uint_as_float(rr[1]));
        if (t) mx = __builtin_elementwise_maximum(mx, 0.f);
        const float alpha = t ? __builtin_amdgcn_exp2f(-mx) : 1.f, nn = nm[0] - mx;
        mx = -mx;
#pragma unroll
        for (int r = 0; r < 16; ++r) { s0[r] = fadd1(s0[r], mx); s1[r] = fadd1(s1[r], mx); nm[r] = nn; }
#pragma unroll
        for (int d = 0; d < 4; ++d)
#pragma unroll
          for (int r = 0; r < 16; ++r) o[d][r] = fmul1(o[d][r], alpha);
        l_reg *= alpha;
        asm volatile("" : "+v"(nm));
      }
    }
    AST(1);
    float x[32], sb = 0.f;
    u32 pw[16];
#pragma unroll
    for (int r = 0; r < 18; ++r) {
      SBAR();
      if (r < 16) { x[2 * r] = __builtin_amdgcn_exp2f(r < 8 ? s0[2 * r] : s1[2 * r - 16]); x[2 * r + 1] = __builtin_amdgcn_exp2f(r < 8 ? s0[2 * r + 1] : s1[2 * r - 15]); }
      if (r >= 2) {
        l_reg = fadd1(l_reg, x[2 * r - 4]);
        sb = r == 2 ? x[1] : fadd1(sb, x[2 * r - 3]);
        pw[r - 2] = pk2(x[2 * r - 4], x[2 * r - 3]); }
    }
    SBAR();
    l_reg = fadd1(l_reg, sb);
#pragma unroll
    for (int i = 0; i < 4; ++i) pa[i] = __builtin_bit_cast(bf8, (u32x4){pw[4 * i], pw[4 * i + 1], pw[4 * i + 2], pw[4 * i + 3]});
    asm volatile("" : "+v"(pa[0]), "+v"(pa[1]), "+v"(pa[2]), "+v"(pa[3]), "+v"(l_reg));
    ko ^= (t & 1) ? 3 * SHM_K : SHM_K;
    const int vo1 = vo ^ 128;
    SBAR();
    AST(3);
    if constexpr (!LAST) { LDK(0); LDK(1); } else { LDV(0); LDV(1); LDV(2); LDV(3); }
    AST(4);
    PHASE(4);
    AST(5);
    __builtin_amdgcn_s_setprio(1);
    if constexpr (!LAST) QK(LDV);
    AST(6);
#pragma unroll
    for (int i = 0; i < 16; ++i) {
      SBAR();
      o[i & 3] = MF32(vf[i & 3], pa[i >> 2], o[i & 3]);
      SBAR();
      if (i + 4 < 16) LDV(i + 4);
      if constexpr (!LAST) if (i >= MX0 && i < MX0 + 8) MAXS(i - MX0);
      if (AT_PAD && i == EBAR) __builtin_amdgcn_s_sleep(AT_PAD);
      if (i == EBAR && (!LAST || rem)) PHASE(4);
    }
    __builtin_amdgcn_s_setprio(0);
    vo ^= (t & 1) ? 3 * SHM_V : SHM_V;
    AST(7);
  };
#pragma unroll 1
  for (int t = 0; t < TL; t++) {
    tile(t, std::false_type{});
  }
  AP(4);
  tile(TL, std::true_type{});
  AP(5);
  ISSUE_V(TL + 3);
  SBAR();
  {
    const float inv = __builtin_amdgcn_rcpf(swadd(l_reg, l_reg));
    const __amdgpu_buffer_rsrc_t ro = RSRC(Op + rb * 262144, 8192);
#pragma unroll
    for (int i = 0; i < 8; ++i) {
      const int g = (i & 1) * 8;
#define ON(k) fmul1(o[i >> 1][g + (k)], inv)
      auto r0 = __builtin_amdgcn_permlane32_swap(pk2(ON(0), ON(1)), pk2(ON(4), ON(5)), false, false);
      auto r1 = __builtin_amdgcn_permlane32_swap(pk2(ON(2), ON(3)), pk2(ON(6), ON(7)), false, false);
#undef ON
      __builtin_amdgcn_raw_buffer_store_b128((u32x4){r0[0], r1[0], r0[1], r1[1]}, ro, r32 * 32 + hi * 16 + (i & 3) * 1024, (i >> 2) * 4096, 16);
      if (i == 3 && rem > 1) PHASE(6);
    }
  }
  AP(6);
  if (rem > 2) PHASE(10);
  if (rem > 3) { PHASE(8); PHASE(8); }
  CLAIM();
  VMW0();
  AP(7);
  __syncthreads();
}
#undef DMA
#undef ISSUE_K
#undef ISSUE_V
#undef PHASE
#undef LDK
#undef LDV
#undef QK
#undef NOV
#undef MAXS
}

__device__ __forceinline__ void wof_task(const Params& p, int kc, char* smem, const int wv, u32* qh, int& pre) {
  const int tid = TIDX, lane = tid & 63, wid = wv;
  const int l15 = lane & 15, lq = lane >> 4, q4 = l15 >> 2, p4 = lane & 3;
  const __amdgpu_buffer_rsrc_t rw = RSRC(WSP(u16, O_WOF), 2 * WOB);
  bf8 st[2][9];
#pragma unroll
  for (int m = 0; m < 2; m++)
#pragma unroll
    for (int i = 0; i < 9; i++) st[m][i] = *(const bf8*)((m ? p.BWo : p.Awo) + (size_t)kc * 64 * 576 + (tid + 512 * i) * 8);
  CLAIM();
#pragma unroll
  for (int m = 0; m < 2; m++) {
#pragma unroll
    for (int i = 0; i < 9; i++) {
      const int id = tid + 512 * i, row = id / 72, ch = id % 72;
      *(bf8*)(smem + row * 1280 + (((ch >> 1) ^ fsw(row)) << 5) + ((ch & 1) << 4)) = st[m][i];
    }
    asm volatile("s_waitcnt lgkmcnt(0)" ::: "memory"); __builtin_amdgcn_s_barrier(); asm volatile("" ::: "memory");
#pragma unroll
    for (int i = 0; i < 9; i++) {
      const int f = wid * 9 + i, nt = f >> 1, k2 = f & 1;
      const int r0 = k2 * 32 + lq * 8 + q4, r1 = r0 + 4;
      bf4 lo = trrd(smem + r0 * 1280 + ((nt ^ fsw(r0)) << 5) + p4 * 8);
      bf4 hi = trrd(smem + r1 * 1280 + ((nt ^ fsw(r1)) << 5) + p4 * 8);
      __builtin_amdgcn_raw_buffer_store_b128(__builtin_bit_cast(u32x4, cat8(lo, hi)), rw, ((nt * 128 + kc * 2 + k2) * 64 + lane) * 16, m * WOB, 16);
    }
    if (m == 0) { asm volatile("s_waitcnt lgkmcnt(0)" ::: "memory"); __builtin_amdgcn_s_barrier(); asm volatile("" ::: "memory"); }
  }
  VMW0();
  __syncthreads();
}

template <int MT>
__device__ __forceinline__ void out_tile(const char* A, const char* W, u16* C, char* smem, const int wv, u32* qh, int& pre) {
  constexpr int U = 32 / MT, SC = U / 2, DP = MT / 4, NCH = 4 * MT, CW = 32768 / MT;
  const int tid = TIDX, lane = tid & 63, wid = wv;
  const int l15 = lane & 15, lq = lane >> 4, kh = wid >> 2, wc = wid & 3;
  const __amdgpu_buffer_rsrc_t ra = RSRC(A, MT / 2 * 262144), rw = RSRC(W + wc * 3 * 131072 + kh * 1024, 3 * 131072 - kh * 1024);
  const int va = tid * 16, vw = lane * 16;
  int ao = kh * 2048 + (lq >> 1) * 1024 + l15 * 32 + (lq & 1) * 16;
  f4 acc[MT][3];
  bf8 wf[U][3], af[2][MT];
#define OT_DMA(i, buf, so) __builtin_amdgcn_raw_ptr_buffer_load_lds(ra, (LDSP void*)(smem + (buf) + (i) * 8192 + wid * 1024), 16, va, \
                                                                    (so) + (MT == 4 ? ((i) >> 1) * 262144 + ((i) & 1) * 8192 : (i) * 262144), 0, 0)
#define OT_LDA(d, base, off) do { _Pragma("unroll") for (int m = 0; m < MT; m++) af[d][m] = *(const bf8*)(smem + (base) + (off) + (m >> 1) * 2 * CW + (m & 1) * 512); } while (0)
#pragma unroll
  for (int i = 0; i < 4; i++) OT_DMA(i, 0, 0);
  SB0();
#pragma unroll
  for (int u = 0; u < U; u++) {
#pragma unroll
    for (int d = 0; d < DP; d++) OT_DMA((u % SC) * DP + d, 32768 + (u / SC) * 32768, (1 + u / SC) * 2 * CW);
#pragma unroll
    for (int j = 0; j < 3; j++) wf[u][j] = __builtin_bit_cast(bf8, __builtin_amdgcn_raw_buffer_load_b128(rw, vw, u * 2048 + j * 131072, 0));
    SB0();
  }
#pragma unroll
  for (int m = 0; m < MT; m++)
#pragma unroll
    for (int j = 0; j < 3; j++) acc[m][j] = (f4){0, 0, 0, 0};
  asm volatile("s_waitcnt vmcnt(%0)" :: "i"(U * (DP + 3)) : "memory"); __builtin_amdgcn_s_barrier(); asm volatile("" ::: "memory");
  OT_LDA(0, ao, 0);
  int bs = 0;
#pragma unroll 1
  for (int c = 0; c < NCH; c += 2) {
    const int sw = c < NCH - 2 ? (c + 2) * CW : PJ_OOB;
#pragma unroll
    for (int u = 0; u < U; u++) {
      SB0();
      __builtin_amdgcn_s_waitcnt(MT == 4 ? 0x4F7C : 0x0F7F);
      if (u % SC == SC - 1) { asm volatile("s_waitcnt lgkmcnt(0)" ::: "memory"); __builtin_amdgcn_s_barrier(); asm volatile("" ::: "memory"); }
      if (u == U - 1) ao ^= 65536;
      OT_LDA((u + 1) & 1, ao, u == U - 1 ? 0 : ((u + 1) / SC) * 32768 + ((u + 1) % SC) * 4096);
      SB0();
      const int cn = c + 3 + u / SC;
#pragma unroll
      for (int j = 0; j < 3; j++) {
#pragma unroll
        for (int m = 0; m < MT; m++) {
          acc[m][j] = MFMA(wf[u][j], af[u & 1][m], acc[m][j]);
          if (j == 0 && (m & 3) == 1) { SB0(); OT_DMA((u % SC) * DP + (m >> 2), u < SC ? (bs ^ 65536) + 32768 : bs, cn < NCH ? cn * 2 * CW : PJ_OOB); SB0(); }
        }
        SB0();
        wf[u][j] = __builtin_bit_cast(bf8, __builtin_amdgcn_raw_buffer_load_b128(rw, vw, sw + u * 2048 + j * 131072, 0));
        SB0();
      }
    }
    bs ^= 65536;
  }
  CLAIM();
  VMW0C();
  asm volatile("s_waitcnt lgkmcnt(0)" ::: "memory"); __builtin_amdgcn_s_barrier(); asm volatile("" ::: "memory");
#pragma unroll
  for (int m = 0; m < MT / 2; m++)
#pragma unroll
    for (int j = 0; j < 3; j++) *(f4*)(smem + (m * 3 + j) * 8192 + va) = kh ? acc[m][j] : acc[MT / 2 + m][j];
  __syncthreads();
#pragma unroll
  for (int m = 0; m < MT / 2; m++) {
    f4 t[3];
#pragma unroll
    for (int j = 0; j < 3; j++) { const f4 o = *(const f4*)(smem + (m * 3 + j) * 8192 + (va ^ 4096)); t[j] = kh ? o + acc[MT / 2 + m][j] : acc[m][j] + o; }
    u16* c = C + (size_t)(kh * MT * 8 + m * 16 + l15) * 1152 + wc * 48;
    *(u32x4*)(c + (lq & 1) * 16 + (lq >> 1) * 8) = pair16(pk2(t[0][0], t[0][1]), pk2(t[0][2], t[0][3]), pk2(t[1][0], t[1][1]), pk2(t[1][2], t[1][3]));
    *(u32x2*)(c + 32 + lq * 4) = (u32x2){pk2(t[2][0], t[2][1]), pk2(t[2][2], t[2][3])};
  }
  __syncthreads();
#undef OT_DMA
#undef OT_LDA
}

#define CLAIMS() do { const int l_ = TIDX; if (l_ < 64) { \
    const u32 v_ = ld4_sc1(WSP(u32, 0) + SY_OQ(l_ < 6 ? l_ : 0)); \
    if (l_ == 0) pre = (int)__hip_atomic_fetch_add(WSP(u32, 0) + SY_OQ(6), 1u, __ATOMIC_RELAXED, __HIP_MEMORY_SCOPE_AGENT); \
    pre |= (int)((u32)__ballot(l_ < 6 && v_ < ((l_ & 1) ? NOB : NOA)) << 16); } } while (0)
#define CLS 57
#define TPS 2
__device__ __forceinline__ void small_out_task(const Params& p, int rq, int cp, char* smem, const int wv, int& pre) {
  const int tid = TIDX, lane = tid & 63, wid = wv;
  const int l15 = lane & 15, lq = lane >> 4;
  const __amdgpu_buffer_rsrc_t rw = RSRC(WSP(char, O_WOF) + WOB, WOB), ro = RSRC(WSP(u16, O_OB), 2048 * 4096 * 2);
  if (tid == 0) {
    int it = 0;
    while (ld4_sc1(WSP(u32, 0) + SY_CW) < 64u && ++it < SPIN_CAP) __builtin_amdgcn_s_sleep(8);
  }
  __syncthreads();
  bf8 wf[8][4];
#pragma unroll
  for (int k = 0; k < 8; k++)
#pragma unroll
    for (int j = 0; j < 4; j++)
      wf[k][j] = __builtin_bit_cast(bf8, __builtin_amdgcn_raw_buffer_load_b128(rw, (l15 >> 3) * 1024 + (lq * 16 + (l15 & 7)) * 16 + (j & 1) * 128,
                                                                              ((cp * 2 + (j >> 1)) * 128 + wid * 16 + k * 2) * 1024, 16));
  const int va = OB_OFF(rq * 8 + (l15 & 7), wid * 512 + (l15 >> 3) * 32 + lq * 8);
  const int wo = (lq >> 1) == (l15 >> 3) ? (wid * 2 + (lq >> 1)) * 272 + (l15 & 7) * 32 + (lq & 1) * 16 : 4 * 4352 + lane * 16;
  const int rd = (lane >> 4) * 4352 + (lane & 7) * 544 + wid * 32 + ((lane >> 3) & 1) * 16;
  int rdy = 0;
  auto step = [&](u32x4 (&af)[8], u32x4 (&nf)[8], const int s) __attribute__((always_inline)) {
    if (s >= rdy) {
      if (s < 62) {
        if (tid < 64) {
          int it = 0;
          u64 m;
          while (m = __ballot(ld4_sc1(WSP(u32, 0) + SY_CS(lane < s || it ? s : lane)) == 0u || lane > (it ? s : 61)), (m >> s & 1) && ++it < SPIN_CAP)
            __builtin_amdgcn_s_sleep(SPS);
          *(int*)(smem + 40960) = __builtin_ctzll(m);
        }
        asm volatile("s_waitcnt lgkmcnt(0)" ::: "memory"); __builtin_amdgcn_s_barrier(); asm volatile("" ::: "memory");
        rdy = __builtin_amdgcn_readfirstlane(*(const int*)(smem + 40960));
      }
      if (s == 63) TLS(1);
      int it = 0;
      while (true) {
        CFENCE();
#pragma unroll
        for (int k = 0; k < 8; k++) af[k] = __builtin_amdgcn_raw_buffer_load_b128(ro, va, s * 262144 + k * 4096, 16);
        if (s < 62) break;
        u32 bad = 0u;
#pragma unroll
        for (int k = 0; k < 8; k++) bad = umax(umax(bad, af[k][0]), af[k][2]);
        if (!__any(bad == ~0u) || ++it >= SPIN_CAP) break;
        __builtin_amdgcn_s_sleep(TPS);
      }
    }
    SB0();
    const int nb = s + 1 < rdy ? (s + 1) * 262144 : PJ_OOB;
#pragma unroll
    for (int k = 0; k < 8; k++) nf[k] = __builtin_amdgcn_raw_buffer_load_b128(ro, va, nb + k * 4096, 16);
    SB0();
    f4 acc[4];
#pragma unroll
    for (int k = 0; k < 8; k++)
#pragma unroll
      for (int j = 0; j < 4; j++) acc[j] = MFMA(wf[k][j], __builtin_bit_cast(bf8, af[k]), k ? acc[j] : ((f4){0, 0, 0, 0}));
    char* const sm = smem + (s & 1) * 20480;
#pragma unroll
    for (int j = 0; j < 4; j++) *(f4*)(sm + wo + ((lq >> 1) == (l15 >> 3) ? j * 4352 : 0)) = acc[j];
    asm volatile("s_waitcnt lgkmcnt(0)" ::: "memory"); __builtin_amdgcn_s_barrier(); asm volatile("" ::: "memory");
    if (s == 63) TLS(2);
    f4 t = *(const f4*)(sm + rd) + *(const f4*)(sm + rd + 272);
#pragma unroll
    for (int r = 0; r < 4; r++) {
      t[r] += __uint_as_float(__builtin_amdgcn_mov_dpp(__float_as_uint(t[r]), 0xB1, 0xf, 0xf, true));
      t[r] += __uint_as_float(__builtin_amdgcn_mov_dpp(__float_as_uint(t[r]), 0x4E, 0xf, 0xf, true));
      t[r] += __uint_as_float(__builtin_amdgcn_mov_dpp(__float_as_uint(t[r]), 0x141, 0xf, 0xf, true));
    }
    __builtin_amdgcn_raw_buffer_store_b64((u32x2){pk2(t[0], t[1]), pk2(t[2], t[3])}, RSRC(p.out, 2048 * 2304), (lane & 7) == 0 ? lane : PJ_OOB,
                                          (s * 32 + rq * 8 + wid) * 2304 + 1152 + cp * 64, 0);
    if (s == CLS) CLAIMS();
  };
  u32x4 fa[8], fb[8];
#pragma unroll
  for (int k = 0; k < 8; k++) asm volatile("" : "=v"(fa[k]));
#pragma unroll 1
  for (int s = XS0; s < 64; s += 2) { step(fa, fb, s); step(fb, fa, s + 1); }
}
#define PSTAMP()
#define STAMP(i)
#define YEND()
#define ANS 16
#define ANX(slot) (X_ANN ^ xp ^ ((slot) * X_HS))
#define KSL 1
#define DSH 2
#define K1 14
#define K3 11
#define DOMAX 90
#define DO0 80
#define DH0 54
#define DOCAP 6
#define C_IMG 0
#define C_QT 9216
#define C_KT (C_QT + 3072)
#define C_P (C_KT + 4096)
#define C_PX (C_P + 768)
#define C_KX (C_PX + 2560)
#define C_VX (C_KX + 8192)
#define C_QX (C_VX + 8192)
#define C_XP 161024
#define CALR 2
#define GPS 6
#define C_WL 49920
__device__ __forceinline__ float ror8(float v) {
  return __uint_as_float(__builtin_amdgcn_mov_dpp(__float_as_uint(v), 0x128, 0xf, 0xf, true));
}
__device__ __forceinline__ float rb1(float f) { return __uint_as_float(pk2(0.f, f)); }
#define FMX(a, b) __builtin_elementwise_maximum(a, b)
__device__ __forceinline__ float tmax(float mx) {
  mx = FMX(mx, ror8(mx));
  { auto t = __builtin_amdgcn_permlane16_swap(__float_as_uint(mx), __float_as_uint(mx), false, false);
    mx = FMX(__uint_as_float(t[0]), __uint_as_float(t[1])); }
  { auto t = __builtin_amdgcn_permlane32_swap(__float_as_uint(mx), __float_as_uint(mx), false, false);
    mx = FMX(__uint_as_float(t[0]), __uint_as_float(t[1])); }
  return mx;
}
__device__ __forceinline__ int hmax(int x) {
  x = max(x, __builtin_amdgcn_mov_dpp(x, 0x128, 0xf, 0xf, true));
  x = max(x, __builtin_amdgcn_mov_dpp(x, 0x124, 0xf, 0xf, true));
  x = max(x, __builtin_amdgcn_mov_dpp(x, 0x4E, 0xf, 0xf, true));
  x = max(x, __builtin_amdgcn_mov_dpp(x, 0xB1, 0xf, 0xf, true));
  { auto t = __builtin_amdgcn_permlane16_swap((u32)x, (u32)x, false, false); x = max((int)t[0], (int)t[1]); }
  return x;
}
typedef __attribute__((ext_vector_type(2))) unsigned long long u64x2;
__device__ __forceinline__ f4 zero4() {
  u64 a, b; asm volatile("v_mov_b64 %0, 0\n\tv_mov_b64 %1, 0\n\ts_nop 1" : "=v"(a), "=v"(b));
  return __builtin_bit_cast(f4, (u64x2){a, b});
}
__device__ __forceinline__ u32x4 ones4() {
  u64 a, b; asm volatile("v_mov_b64 %0, -1\n\tv_mov_b64 %1, -1" : "=v"(a), "=v"(b));
  return __builtin_bit_cast(u32x4, (u64x2){a, b});
}
#define BAR() do { asm volatile("s_waitcnt lgkmcnt(0)" ::: "memory"); __builtin_amdgcn_s_barrier(); asm volatile("" ::: "memory"); } while (0)
#define BARC() do { asm volatile("" ::: "memory"); __builtin_amdgcn_s_waitcnt(0xC07F); __builtin_amdgcn_s_barrier(); asm volatile("" ::: "memory"); } while (0)
#define BARN() do { asm volatile("" ::: "memory"); __builtin_amdgcn_s_barrier(); asm volatile("" ::: "memory"); } while (0)
#define IDX() const int tid = TIDX, lane = tid & 63, l15 = lane & 15, lq = lane >> 4, q4 = l15 >> 2, p4 = lane & 3, hi8 = l15 >> 3; \
    (void)q4; (void)p4; (void)hi8; (void)lq
#define AD_LD(HEAD) __builtin_amdgcn_raw_buffer_load_b32(rsX, (TIDX & (HEAD ? 63 : 31)) * ANS, ANX(s & 1), 16)
#define AD_GET(HEAD, K, CAP) do { int n_ = 0; u32 an = AD_LD(HEAD); \
    while ((u32)__ballot((an & 63u) != (u32)s) != 0u && ++n_ < (CAP)) { __builtin_amdgcn_s_sleep(1); CFENCE(); an = AD_LD(HEAD); } \
    if (!HEAD && c == 0 && s > 0 && n_ < (CAP)) st4_sc1(WSP(u32, 0) + SY_CS(s - 1), 1u); \
    const u32 r_ = (u32)__builtin_amdgcn_readfirstlane((int)an); \
    const int m_ = hmax((TIDX & 32) ? (int)umin(an, DOMAX * 64u) : (int)(an - r_)); \
    dl = ((r_ + (u32)__builtin_amdgcn_readlane(m_, 0)) & ~63u) + (u32)(K) * 64u + (HEAD ? (u32)__builtin_amdgcn_readlane(m_, 32) : 0u); } while (0)
#define AD_SPIN() do { int n_ = 160; while ((int)(((u32)wall_clock64() << 6) - dl) < 0 && --n_ > 0) {} } while (0)
#define STG_AUX 0
#define OSTG_AUX 0
#define STG_D 3
#define STG_POLL 7
#define STS(c, i)

#define DPR(OP, N) OP " %3, %3, %3 row_ror:" #N " row_mask:0xf bank_mask:0xf\n\t" OP " %4, %4, %4 row_ror:" #N " row_mask:0xf bank_mask:0xf\n\t"
#define DPROW(OP) "s_nop 0\n\t" DPR(OP, 8) "s_nop 0\n\t" DPR(OP, 4) "s_nop 0\n\t" DPR(OP, 2) "s_nop 0\n\t" DPR(OP, 1)
#define NXA 0
#define NXE 0
#define ADE 1
#define NXH 0
#define NXV 1
#define NXB 4
#define NXS 3
#define BJ 2
#define KSP 0
#define SP1 2
#define SP2 0
#define KP 3
#define GX 1
#define GXO (GX == 1 ? 4096 : 1024)
#define ZE 0x0D
#define KD 0
#define QSL 0
#define DJ 9
#define VSL 0
#define VSL1 4
#define KV0 1
#define PVP 0
#define IPR 3
#define DSLOT 0x9876E1CBAD0ull
#define F_K (44288 - C_KT)
#define F_Q (47872 - 768 - C_QT)
#define PRIO(n) __builtin_amdgcn_s_setprio(n)
#define LSL 6
__device__ __forceinline__ void chain_head(const Params& p, char* smem, const int h, const int wv) {
  STS(h, 1);
  TLS(12);
  const int wid = wv;
  u32x8 w[2][9], wx[3];
  const __amdgpu_buffer_rsrc_t rsX = RSRC(XWIN(p), X_END);
  const __amdgpu_buffer_rsrc_t rsB = RSRC(WSP(u16, O_QB), 4 * ACB);
  const int cb = h >> 1;
  const int nvt = (wid & 3) == 0 ? 0 : (wid < 3 ? 2 : 1);
  const int vt = wid < 3 ? wid * 2 + 2 : (wid == 3 ? 0 : wid - 4);
  const int wl = C_WL + (nvt == 1 ? vt * 12288 : 49152 + (nvt == 2 ? wid - 1 : wid >> 2) * 30720);
  int xp;
  {
    IDX();
    u32x4 st[54], nk, nv, nq;
    u32 fl, fk;
    const __amdgpu_buffer_rsrc_t rq = RSRC(p.BWq, 576 * 4096 * 2), rk = RSRC(p.BWk, 576 * 4096 * 2), rv = RSRC(p.BWv, 576 * 4096 * 2);
    const int vw = ((tid >> 4) * 4096 + h * 128 + (tid & 15) * 8) * 2;
    auto req = [&](int kc) {
#pragma unroll
      for (int i = 0; i < 6; i++) st[kc * 6 + i] = __builtin_amdgcn_raw_buffer_load_b128(i < 2 ? rq : (i < 4 ? rk : rv), vw, (kc * 64 + (i & 1) * 32) * 8192, STG_AUX);
      SB0();
    };
#pragma unroll
    for (int kc = 0; kc < STG_D; kc++) { if (kc == 0) TLS(13); req(kc); __builtin_amdgcn_s_barrier(); SB0(); }
    TLS(14);
    auto cell = [](int row, int slot) { return (((row * 2 + slot) ^ ((((row >> 2) ^ (row >> 4)) & 1) << 2) ^ ((row >> 5) & 1)) << 5); };
    auto put = [&](int unit, int row, const u32x4& v) { *(u32x4*)(smem + unit * 4160 + cell(row, (tid >> 1) & 1) + (tid & 1) * 16) = v; };
    auto frag = [&](int tile) {
      bf4 tt[4];
#pragma unroll
      for (int kk = 0; kk < 4; kk++) {
        const int r0 = (kk >> 1) * 32 + (lq >> 1) * 16 + (q4 >> 1) * 8 + (lq & 1) * 4 + (kk & 1) * 2 + (q4 & 1);
        tt[kk] = trrd(smem + (tile >> 1) * 4160 + cell(r0, tile & 1) + p4 * 8);
      }
      return ilv(tt[0], tt[1], tt[2], tt[3]);
    };
    auto park = [&](int slot, const u32x8& f) {
      char* d = smem + wl + slot * 2048 + lane * 16;
      *(u32x4*)d = (u32x4){f[0], f[1], f[2], f[3]};
      *(u32x4*)(d + 1024) = (u32x4){f[4], f[5], f[6], f[7]};
    };
    auto poll = [&]() {
      fl = ld4_sc1(WSP(u32, 0) + SY_INIT(l15));
      fk = ld4_sc1(WSP(u32, 0) + SY_BLK(lane < 12 ? lane >> 2 : 0, cb, lane & 3));
    };
    auto rows = [&]() {
      nq = __builtin_amdgcn_raw_buffer_load_b128(rsB, tid * 16, h * 8192, 16);
      nk = __builtin_amdgcn_raw_buffer_load_b128(rsB, tid * 16, h * 8192 + ACB, 16);
      nv = __builtin_amdgcn_raw_buffer_load_b128(rsB, tid * 16, h * 8192 + 2 * ACB, 16);
    };
    bool early = false;
#pragma unroll
    for (int kc = 0; kc < 9; kc++) {
      if (kc == 10 - STG_D) { early = __all(fk >= 8u); CFENCE(); if (early) rows(); if (wid == 0) poll(); SB0(); }
#pragma unroll
      for (int i = 0; i < 6; i++) put((i >> 1) * 4 + ((tid & 15) >> 2), (i & 1) * 32 + (tid >> 4), st[kc * 6 + i]);
      BAR();
      if (kc == 0) TLS(15);
      if (kc + STG_D == 8) { fk = ld4_sc1(WSP(u32, 0) + SY_BLK(lane < 12 ? lane >> 2 : 0, cb, lane & 3)); SB0(); }
      if (kc + STG_D < 9) req(kc + STG_D);
      w[0][kc] = frag(wid * 2);
      w[1][kc] = frag(nvt == 2 && kc < 4 ? 16 + vt : wid * 2 + 1);
      const u32x8 tp = frag(nvt == 0 ? 21 + (wid >> 1) : (nvt == 2 && kc < 4 ? wid * 2 + 1 : 16 + vt));
      if (kc >= 4 && kc < 7) { wx[kc - 4] = tp; if (nvt == 0) park(6 + kc, tp); }
      else park(nvt == 0 ? 6 + kc : (kc >= 7 ? kc - 7 : 2 + kc), tp);
      BAR();
      if (kc == 0 || kc == 8) STS(h, kc + 3);
    }
    TLS(6);
    if (wid == 0) {
      int it = 0;
      while (!__all(fl != 0u && fk >= 8u) && ++it < SPIN_CAP) { __builtin_amdgcn_s_sleep(GPS); poll(); }
      *(int*)(smem + C_XP) = fl & 0x7800;
    }
    BAR();
    if (!early) rows();
    xp = __builtin_amdgcn_readfirstlane(*(const int*)(smem + C_XP));
    TLS(7);
    SB0();
    *(u32x4*)(smem + C_QX + tid * 16) = nq;
    *(u32x4*)(smem + C_KX + tid * 16) = nk;
    *(u32x4*)(smem + C_VX + ((tid * 16) ^ ((tid & 32) << 2))) = nv;
  }
  STS(h, 14);
  constexpr float CE = 0.08838834764831845f * 1.4426950408889634f;
  auto run = [&](auto RC) __attribute__((always_inline)) {
  constexpr int R = decltype(RC)::value;
  constexpr int NV = R < 2 ? 0 : (R < 4 ? 1 : 2);
  constexpr bool QW = !(R & 1);
  int known = 0;
  bf8 pre[4][2], vx[NV ? NV : 1];
  int sidx, mo, io, eo, gvo, wvo = 0, so = 0, po = 0, qo = 0, ko = 0, ko1 = 0, fo = 0, fq = 0, fk = 0;
  float mk[4], mm = 0.f, cv = CE;
#define LDW(dst, slot) do { dst[0] = *(const bf8*)(smem + wvo + (slot) * 2048); dst[1] = *(const bf8*)(smem + wvo + (slot) * 2048 + 1024); } while (0)
  {
    IDX();
    if constexpr (R == 1) {
      po = (tid * 16) ^ ((tid & 32) << 2);
      so = (lane >> 5) * 131072 + (lane & 31) * 16;
      asm volatile("" : "+v"(po), "+v"(so));
    } else if constexpr (R == 0) {
      qo = C_QT + lq * 384 + q4 * 32 + p4 * 8; ko = C_KT + ((((lq * 8 + q4) * 32) ^ ((lq & 1) << 7)) + p4 * 8); ko1 = C_KT + ((((lq * 8 + 4 + q4) * 32) ^ ((lq & 1) << 7)) + p4 * 8);
      po = C_P + l15 * 16 + lq * 4;
      fo = lq * 512 + l15 * 16;
#pragma unroll
      for (int r = 0; r < 4; r++) { mk[r] = lq * 4 + r <= l15 ? 0.f : -__builtin_inff(); asm volatile("" : "+v"(mk[r])); }
      mm = (l15 & 2) ? -__builtin_inff() : 0.f;
      asm volatile("" : "+v"(mm), "+v"(cv));
      fq = C_QT + F_Q + (lq ? lq : 1) * 768 + lq * 8; fk = C_KT + F_K + lq * 1032;
      asm volatile("" : "+v"(qo), "+v"(ko), "+v"(ko1), "+v"(po), "+v"(fo), "+v"(fq), "+v"(fk));
      *(u32*)(smem + fq) = 0u; *(u32*)(smem + fk) = 0u;
    } else {
      wvo = wl + lane * 16;
      po = C_P + (lq * 4 + q4) * 16 + (p4 & 1) * 8;
      so = ((l15 >> 2) & 1) * 131072 + (((vt & 3) + (NV == 2 ? hi8 : 0)) * 8 + ((l15 >> 1) & 1) * 4 + (lq >> 1) * 2 + (l15 & 1)) * 16 + (lq & 1) * 8;
      if (NV == 1 && hi8) so = PJ_OOB;
      asm volatile("" : "+v"(wvo), "+v"(po), "+v"(so));
      if constexpr (NV == 2) { qo = po ^ 32; asm volatile("" : "+v"(qo)); }
#pragma unroll
      for (int j = 0; j < 4; j++) LDW(pre[j], NV == 2 ? 2 + j : j);
    }
    eo = QW ? C_QT + wid * 768 + ((l15 >> 2) * 2 + (l15 & 1)) * 32 + lq * 8 + ((l15 >> 1) & 1) * 4
            : C_KT + (wid & 3) * 1024 + ((l15 * 32) ^ (hi8 << 7)) + lq * 8;
    sidx = (l15 & 2) ? 0xEEEE : 0x4444;
    mo = C_IMG + lane * 16;
    io = C_IMG + ((tid & ~30) | ((tid & 10) << 1) | ((tid & 20) >> 1)) * 16;
    gvo = tid * 16;
    asm volatile("" : "+v"(sidx), "+v"(mo), "+v"(io), "+v"(eo));
    if constexpr (R < 2) asm volatile("" : "+v"(gvo));
    if constexpr (!QW) { *(u32*)(smem + eo + 4) = 0u; *(u32*)(smem + eo + 516) = 0u; }
  }
  f4 a0, a1;
  u32x4 vm = {0u, 0u, 0u, 0u};
  if constexpr (NV > 0) asm volatile("" : "+v"(vm));
  u32 ts = 0;
  u64 tq = 0;
  int dur = DH0 * 64;
  const int ob = ((h >> 3) * 32768 + (h & 7) * 512) ^ xp ^ (NV == 1 ? X_HS : 0);
  const int ib = X_M ^ xp ^ ((wid >> 2) * 32768 + (wid & 3) * 1024);
  int b3 = 0;
  PRIO(QW ? 2 : 1);
#pragma unroll 1
  for (int s = 0;; s++) {
    int mb = ib + b3 * 65536, mbx = X_M ^ xp ^ (196608 + b3 * 1024);
    if constexpr (R == GX) asm volatile("" : "+s"(mb), "+s"(mbx));
    u32x4 g, gx;
    BAR();
    STAMP(13);
    if constexpr (R == 1 && NXE) {
      asm volatile("buffer_load_dwordx4 %0, %2, %3, %4 offen sc1\n\tbuffer_load_dwordx4 %1, %2, %3, %5 offen sc1"
                   : "=&v"(g), "=&v"(gx) : "v"(mo), "s"(rsX), "s"(mb), "s"(mbx) : "memory");
    } else {
      g = __builtin_amdgcn_raw_buffer_load_b128(rsX, mo, mb, 16);
      if constexpr (R == GX) gx = __builtin_amdgcn_raw_buffer_load_b128(rsX, mo, mbx, 16);
    }
    SB0();
    if constexpr (R == 0) {
#pragma unroll
      for (int j = 0; j < 4; j++) { const char* kx_ = smem + C_KX + fo + j * 2048;
        pre[j >> 1][j & 1] = *(const bf8*)kx_; pre[2 + (j >> 1)][j & 1] = *(const bf8*)(kx_ + 256); }
    } else if constexpr (R > 1) {
      IDX();
#pragma unroll
      for (int d = 0; d < NV; d++) { const char* vb_ = smem + C_VX + (vt + d) * 1024 + (p4 >> 1) * 512 + (((lq * 4 + q4) ^ ((p4 >> 1) * 8)) << 4) + (p4 & 1) * 8;
        vx[d] = cat8(trrd(vb_), trrd(vb_ + 256)); }
    }
    SB0();
    a0 = zero4(); a1 = zero4();
    f4 c0, c1, a2;
#define A2INI() do { u64 z_; float m0_, m1_; asm volatile("v_mov_b32 %0, %3\n\tv_mov_b32 %1, %3\n\tv_mov_b64 %2, 0\n\ts_nop 1" : "=&v"(m0_), "=&v"(m1_), "=v"(z_) : "v"(mm)); \
                     const f2v zz_ = __builtin_bit_cast(f2v, z_); a2 = (f4){m0_, m1_, zz_[0], zz_[1]}; } while (0)
    if constexpr ((ZE >> R) & 1) {
      c1 = zero4();
      if constexpr (NV != 1) c0 = zero4();
      if constexpr (R == 0) { A2INI(); asm volatile("" : "+v"(a2)); }
    }
    int so_ = X_O(b3) ^ ob, an_ = ANX(s & 1) + h * ANS;
    int nb = s < 63 ? (s + 1) * 262144 + h * 8192 : PJ_OOB;
    asm volatile("" : "+s"(so_), "+s"(an_), "+s"(nb));
    u32x4 nx[2];
#define NXL1(g, i) __builtin_amdgcn_raw_ptr_buffer_load_lds(rsB, (LDSP void*)(smem + C_KX + (g) * 4096), 16, (g) < 2 ? gvo : po, \
                                                             nb + (1 + ((g) >> 1)) * ACB + ((g) & 1) * 4096 - 4096, (i) * 1024, 16)
#define NXL(g) do { NXL1(g, 0); NXL1(g, 1); NXL1(g, 2); NXL1(g, 3); } while (0)
    SB0();
    if (NXE) {
      BARC();
      if constexpr (R == 1) {
        if (NXE > 1) __builtin_amdgcn_s_sleep(NXE - 1);
#pragma unroll
        for (int i = 0; i < 4; i++)
          asm volatile("s_mov_b32 m0, %3\n\ts_nop 0\n\tbuffer_load_dwordx4 %0, %1, %2 offen sc1 lds\n\tbuffer_load_dwordx4 %0, %1, %2 offen offset:1024 sc1 lds\n\t"
                       "buffer_load_dwordx4 %0, %1, %2 offen offset:2048 sc1 lds\n\tbuffer_load_dwordx4 %0, %1, %2 offen offset:3072 sc1 lds"
                       :: "v"(i < 2 ? gvo : po), "s"(rsB), "s"(nb + (1 + (i >> 1)) * ACB + (i & 1) * 4096 - 4096), "i"(C_KX + i * 4096) : "memory", "m0");
      }
      SB0();
    }
    if constexpr (!QW) { PRIO(IPR); BARN(); }
    {
      if constexpr (R == 1 && NXE) asm volatile("s_waitcnt vmcnt(17)" : "+v"(g));
      else __builtin_amdgcn_s_waitcnt(R == GX ? 0x0F71 : 0x0F70);
      CFENCE();
      *(u32x4*)(smem + io) = g;
      SB0();
      u32 top = umax(umax(umax(g[0], g[1]), g[2]), g[3]);
      if constexpr (R == GX) {
        asm volatile("" : "+v"(top));
        SB0();
        if constexpr (R == 1 && NXE) asm volatile("s_waitcnt vmcnt(16)" : "+v"(gx));
        *(u32x4*)(smem + GXO + io) = gx;
        SB0();
        top = umax(umax(umax(umax(top, gx[0]), gx[1]), gx[2]), gx[3]);
      }
#define G_BAD() (umax(umax(umax(g[0], g[1]), umax(g[2], g[3])), R != GX ? 0u : umax(umax(gx[0], gx[1]), umax(gx[2], gx[3]))) == ~0u)
      if (__builtin_expect(__any(top == ~0u), 0)) {
        int it = 0;
        do {
          CFENCE();
          if (G_BAD()) {
            g = __builtin_amdgcn_raw_buffer_load_b128(rsX, mo, mb, 16);
            if constexpr (R == GX) gx = __builtin_amdgcn_raw_buffer_load_b128(rsX, mo, mbx, 16);
          }
          VMW0C();
        } while (__any(G_BAD()) && ++it < SPIN_CAP);
        *(u32x4*)(smem + io) = g;
        if constexpr (R == GX) *(u32x4*)(smem + GXO + io) = gx;
      }
    }
    SB0();
    STAMP(15);
    if constexpr (!QW) PRIO(1);
    BARC();
    STAMP(0);
#define LDM(j) *(const bf8*)(smem + mo + (j) * 1024)
    constexpr int NB = R == 1 ? 9 : 5;
    bf8 b[NB];
    if constexpr (R == 1) {
      STAMP(6);
      asm volatile("s_memrealtime %0\n\ts_waitcnt lgkmcnt(0)" : "=s"(tq));
      ts = (u32)tq;
      __builtin_amdgcn_raw_buffer_store_b32((((ts << 6) + (u32)dur) & ~63u) | (u32)s, rsX, 0, an_, 16);
      SB0();
#pragma unroll
      for (int j = 0; j < 9; j++) { b[j] = LDM(j); SB0(); }
#pragma unroll
      for (int i = 0; i < NXA; i++) { NXL(i); SB0(); }
    } else {
      if (!QW && KSP) __builtin_amdgcn_s_sleep(KSP);
#pragma unroll
      for (int j = 0; j < 4; j++) { b[j] = LDM(j); SB0(); }
    }
#pragma unroll
    for (int j = 0; j < 9; j++) {
      if constexpr (QW) {
        if (j == BJ) {
          BARN();
#pragma unroll
          for (int i = 4; i < 4 + BJ; i++) { b[i % 5] = LDM(i); SB0(); }
        }
        if (j >= BJ && j + 4 < 9) b[(j + 4) % 5] = LDM(j + 4);
      } else if (R != 1 && j + 4 < 9) b[(j + 4) % 5] = LDM(j + 4);
      SB0();
      a0 = SMF(b[j % NB], w[0][j], a0, sidx);
      if (NV < 2 || j >= 4) a1 = SMF(b[j % NB], w[1][j], a1, sidx);
      else a1 = SMF(b[j % 5], cat16(pre[j & 3][0], pre[j & 3][1]), a1, sidx);
      SB0();
      if (j == 0) STAMP(10);
      if (j == 4) STAMP(11);
      if (j == 8) STAMP(12);
      if (NV == 2 && j >= DJ && j < DJ + 4) LDW(pre[j - DJ], (DSLOT >> ((j - DJ) * 4)) & 15);
    }
    {
      const u32 e0 = pk2(a0[0] + a0[2], a0[1] + a0[3]), e1 = pk2(a1[0] + a1[2], a1[1] + a1[3]);
      asm volatile("ds_write2_b32 %0, %1, %2 offset1:%3" :: "v"(eo), "v"(e0), "v"(e1), "i"(QW ? 96 : 128) : "memory");
      if constexpr (R != 0) asm volatile("ds_write_b32 %0, %1 offset:%2" :: "v"(eo), "v"(sidx), "i"(QW ? F_Q : F_K) : "memory");
    }
    SB0();
    STAMP(1);
    if constexpr (QW) PRIO(R ? 0 : SP1);
    else if (KV0) PRIO(0);
    if constexpr ((ZE >> R) & 1) { a0 = c0; a1 = c1; }
    else if constexpr (R != 1) { a1 = zero4(); if constexpr (NV != 1) a0 = zero4(); if constexpr (R == 0) A2INI(); }
    if constexpr (NV == 0) {
      u32 dl = 0;
      const int c = 1;
      if constexpr (R == 0) {
        if (QSL) __builtin_amdgcn_s_sleep(QSL);
        bf8 qa[2], km[4];
        u32 fl;
        do {
          CFENCE();
          fl = *(const volatile LDSP u32*)(smem + fq);
          CFENCE(); SB0();
#pragma unroll
          for (int i = 0; i < 2; i++) { qa[i] = cat8(trrd(smem + qo + i * 1536), trrd(smem + qo + i * 1536 + 128)); SB0(); }
        } while (__builtin_expect(__any(fl == 0u), 0));
        STAMP(2);
        auto rdk = [&]() __attribute__((always_inline)) {
          CFENCE(); SB0();
          fl = *(const volatile LDSP u32*)(smem + fk);
          CFENCE(); SB0();
#pragma unroll
          for (int i = 0; i < 4; i++) { km[i] = cat8(trrd(smem + ko + i * 1024), trrd(smem + ko1 + i * 1024)); SB0(); }
        };
        auto mulk = [&]() __attribute__((always_inline)) {
#pragma unroll
          for (int i = 0; i < 2; i++) { a2 = SMF(qa[i], cat16(km[2 * i], km[2 * i + 1]), a2, sidx); asm volatile("" : "+v"(a2)); SB0(); }
        };
#pragma unroll
        for (int i = 0; i < 2; i++) {
          a0 = SMF(qa[i], cat16(pre[i][0], pre[i][1]), a0, sidx); asm volatile("" : "+v"(a0)); SB0();
          a1 = SMF(qa[i], cat16(pre[2 + i][0], pre[2 + i][1]), a1, sidx); asm volatile("" : "+v"(a1)); SB0();
        }
        if (SP2 != SP1) PRIO(SP2);
        if (KP == 0) rdk();
        float e0 = a0[0] + a0[2], e1 = a0[1] + a0[3];
        asm volatile("" : "+v"(e0), "+v"(e1));
        SB0();
        float e2 = a1[0] + a1[2], e3 = a1[1] + a1[3], x0, x1, y0, y1, f0, f1, f2, f3, f4_, f5;
        asm volatile("v_mul_f32_dpp %0, %2, %4 row_newbcast:0 row_mask:0xf bank_mask:0xf\n\tv_mul_f32_dpp %1, %3, %4 row_newbcast:0 row_mask:0xf bank_mask:0xf"
                     : "=&v"(x0), "=&v"(x1) : "v"(e0), "v"(e1), "v"(cv), "v"(e2), "v"(e3));
        if (KP == 1) rdk();
        asm volatile("v_fma_f32 %0, %9, %6, -%7\n\tv_fma_f32 %2, %11, %6, -%8\n\tv_exp_f32 %0, %0\n\tv_exp_f32 %2, %2\n\t"
                     "v_fma_f32 %1, %10, %6, -%7\n\tv_fma_f32 %3, %12, %6, -%8\n\tv_exp_f32 %1, %1\n\tv_exp_f32 %3, %3\n\t"
                     "v_add_f32 %4, %0, %1\n\tv_add_f32 %5, %2, %3"
                     : "=&v"(f0), "=&v"(f2), "=&v"(f1), "=&v"(f3), "=&v"(y0), "=&v"(y1) : "v"(cv), "v"(x0), "v"(x1), "v"(e0), "v"(e2), "v"(e1), "v"(e3));
        if (KP == 2) rdk();
        STAMP(3);
        PRIO(2);
        SB0();
        if (KP == 3) {
          if (KD) asm volatile("s_nop %0" :: "i"(KD));
          do {
            rdk();
          } while (__any(fl == 0u));
        }
        mulk();
        if (KP < 3 && __builtin_expect(__any(fl == 0u), 0)) {
          do rdk(); while (__any(fl == 0u));
          A2INI();
          mulk();
        }
        STAMP(4);
        float e4 = a2[0] + a2[2], e5 = a2[1] + a2[3];
#define P_OUT "v_rcp_f32 %3, %3\n\tv_rcp_f32 %4, %4\n\t" \
              "v_mul_f32 %0, %0, %3\n\tv_mul_f32 %5, %5, %4\n\tv_mul_f32 %1, %1, %3\n\tv_cvt_pk_bf16_f32 %0, %0, %5\n\t" \
              "v_mul_f32 %6, %6, %4\n\tv_mul_f32 %2, %2, %3\n\tv_cvt_pk_bf16_f32 %1, %1, %6\n\tv_mul_f32 %7, %7, %4\n\t" \
              "ds_write2st64_b32 %9, %0, %1 offset1:1\n\tv_cvt_pk_bf16_f32 %2, %2, %7\n\tds_write_b32 %9, %2 offset:512"
        asm volatile("v_fma_f32 %2, %12, %8, -%10\n\tv_fma_f32 %7, %13, %8, -%11\n\tv_exp_f32 %2, %2\n\tv_exp_f32 %7, %7\n\t"
                     "v_add_f32 %3, %3, %2\n\tv_add_f32 %4, %4, %7\n\t" DPROW("v_add_f32_dpp") P_OUT "\n\tv_min_f32 %3, %3, %4"
                     : "+v"(f0), "+v"(f2), "=&v"(f4_), "+v"(y0), "+v"(y1), "+v"(f1), "+v"(f3), "=&v"(f5)
                     : "v"(cv), "v"(po), "v"(x0), "v"(x1), "v"(e4), "v"(e5) : "memory");
        SB0();
        if (__builtin_expect(__any(y0 < 0x1p-64f), 0)) {
          asm volatile("v_max3_f32 %3, %0, %1, %2\n\tv_max3_f32 %4, %5, %6, %7\n\t" DPROW("v_max_f32_dpp")
                       "v_mul_f32 %3, %8, %3\n\tv_mul_f32 %4, %8, %4\n\t"
                       "v_fma_f32 %0, %0, %8, -%3\n\tv_fma_f32 %5, %5, %8, -%4\n\tv_exp_f32 %0, %0\n\tv_exp_f32 %5, %5\n\t"
                       "v_fma_f32 %1, %1, %8, -%3\n\tv_fma_f32 %6, %6, %8, -%4\n\tv_exp_f32 %1, %1\n\tv_exp_f32 %6, %6\n\t"
                       "v_fma_f32 %2, %2, %8, -%3\n\tv_fma_f32 %7, %7, %8, -%4\n\tv_exp_f32 %2, %2\n\tv_exp_f32 %7, %7\n\t"
                       "v_add_f32 %3, %0, %1\n\tv_add_f32 %4, %5, %6\n\tv_add_f32 %3, %3, %2\n\tv_add_f32 %4, %4, %7\n\t" DPROW("v_add_f32_dpp") P_OUT
                       : "+v"(e0), "+v"(e2), "+v"(e4), "=&v"(x0), "=&v"(x1), "+v"(e1), "+v"(e3), "+v"(e5) : "v"(cv), "v"(po) : "memory");
        }
        SB0();
      } else {
#pragma unroll
        for (int i = NXA; i < (NXV ? NXH : NXB) && !NXE; i++) NXL(i);
        SB0();
        const int rb = X_O(b3 == 0 ? 2 : b3 - 1) ^ ob;
        const u32x4 e = ones4();
        __builtin_amdgcn_raw_buffer_store_b128(e, rsX, so, rb, 16);
        __builtin_amdgcn_raw_buffer_store_b128(e, rsX, so, rb ^ X_HS, 16);
        PSTAMP();
      }
      STAMP(5);
      BARC();
      if constexpr (R == 0) STAMP(6);
      if constexpr (R == 1) {
        const u32 tp = (u32)wall_clock64(); dur += ((int)umin((tp - ts) << 6, 200u * 64u) - dur) >> DSH;
        SB0();
        if (ADE && s < 63) AD_GET(true, K1 + K3, 4096);
        if (NXB < 4) {
          __builtin_amdgcn_s_sleep(NXS);
#pragma unroll
          for (int i = NXB; i < 4; i++) NXL(i);
          BARN();
        }
        VMW0C();
        STAMP(8);
        if (NXB == 4) BARN();
        if (s == 63) break;
      } else {
        *(u32*)(smem + fq) = 0u; *(u32*)(smem + fk) = 0u;
        bf8 km[4];
#pragma unroll
        for (int ks = 0; ks < 4; ks++) km[ks] = cat8(trrd(smem + ko + ks * 1024), trrd(smem + ko1 + ks * 1024));
#pragma unroll
        for (int NT = 1; NT <= 2; NT++) {
          f4 sc[2], sm;
          bf8 qf[4];
#pragma unroll
          for (int ks = 0; ks < 4; ks++) qf[ks] = *(const bf8*)(smem + C_QX + (NT - 1) * 256 + fo + ks * 2048);
#pragma unroll
          for (int ks = 0; ks < 4; ks++) {
#pragma unroll
            for (int j = 0; j < NT; j++) sc[j] = MFMA(pre[2 * j + (ks >> 1)][ks & 1], qf[ks], ks ? sc[j] : (j == NT - 1 ? ((f4){mk[0], mk[1], mk[2], mk[3]}) : ((f4){0, 0, 0, 0})));
            sm = MFMA(km[ks], qf[ks], ks ? sm : ((f4){0, 0, 0, 0}));
          }
          float mx = FMX3(FMX3(sc[0][0], sc[0][1], sc[0][2]), sc[0][3], FMX(sm[0], sm[1]));
          if (NT == 2) mx = FMX3(FMX3(mx, sc[1][0], sc[1][1]), sc[1][2], sc[1][3]);
          { auto t = __builtin_amdgcn_permlane16_swap(__float_as_uint(mx), __float_as_uint(mx), false, false);
            mx = FMX(__uint_as_float(t[0]), __uint_as_float(t[1])); }
          { auto t = __builtin_amdgcn_permlane32_swap(__float_as_uint(mx), __float_as_uint(mx), false, false);
            mx = FMX(__uint_as_float(t[0]), __uint_as_float(t[1])); }
          mx *= -CE;
#pragma unroll
          for (int j = 0; j < NT; j++)
#pragma unroll
            for (int r = 0; r < 4; r++) sc[j][r] = __builtin_amdgcn_exp2f(__builtin_fmaf(sc[j][r], CE, mx));
          sm[0] = __builtin_amdgcn_exp2f(__builtin_fmaf(sm[0], CE, mx)); sm[1] = __builtin_amdgcn_exp2f(__builtin_fmaf(sm[1], CE, mx));
          float sum = ((sc[0][0] + sc[0][1]) + (sc[0][2] + sc[0][3])) + (sm[0] + sm[1]);
          if (NT == 2) sum += (sc[1][0] + sc[1][1]) + (sc[1][2] + sc[1][3]);
          { auto t = __builtin_amdgcn_permlane16_swap(__float_as_uint(sum), __float_as_uint(sum), false, false);
            sum = __uint_as_float(t[0]) + __uint_as_float(t[1]); }
          sum = swadd(sum, sum);
          const float inv = __builtin_amdgcn_rcpf(sum);
          *(u32x4*)(smem + C_PX + (NT - 1) * 1024 + gvo) = (u32x4){pk2(sc[0][0] * inv, sc[0][1] * inv), pk2(sc[0][2] * inv, sc[0][3] * inv),
                                                                  NT == 2 ? pk2(sc[1][0] * inv, sc[1][1] * inv) : 0u, NT == 2 ? pk2(sc[1][2] * inv, sc[1][3] * inv) : 0u};
          *(u32*)(smem + C_PX + 2048 + (NT - 1) * 256 + (gvo >> 2)) = pk2(sm[0] * inv, sm[1] * inv);
        }
        STAMP(8);
        BAR();
        if (s == 63) break;
        if (known < (s < 61 ? s + 3 : 64)) {
          int n = 0;
          while (true) {
            u32 bf[3];
#pragma unroll
            for (int i = 0; i < 3; i++) bf[i] = ld4_sc1(WSP(u32, 0) + SY_BLK(i, cb, gvo >> 4));
            const u64 miss = __ballot(!(bf[0] >= 8u && bf[1] >= 8u && bf[2] >= 8u));
            known = miss ? __builtin_ctzll(miss) : 64;
            if (known >= (s < 61 ? s + 3 : 64) || ++n >= SPIN_CAP) break;
            __builtin_amdgcn_s_sleep(KSL);
          }
        }
      }
      STAMP(9);
      YEND();
      if constexpr (R == 1) { if (!ADE) AD_GET(true, K1 + K3, 4096); AD_SPIN(); }
    } else {
      if (NV == 2 && DJ > 5) {
#pragma unroll
        for (int j = 0; j < 4; j++) LDW(pre[j], (DSLOT >> (j * 4)) & 15);
        SB0();
      }
      if (QW && (NV == 2 ? VSL : VSL1)) __builtin_amdgcn_s_sleep(NV == 2 ? VSL : VSL1);
      if constexpr (NV == 2) {
        int u = 0;
#pragma unroll
        for (int i = 0; i < 9; i++) {
          const int bs = (0x31042 >> ((i % 5) * 4)) & 7;
#define RING(a) do { a = SMF(b[bs], cat16(pre[u & 3][0], pre[u & 3][1]), a, sidx); SB0(); \
                     if (u < 7) LDW(pre[u & 3], (DSLOT >> (u * 4 + 16)) & 15); u++; } while (0)
          SB0();
          if (i == 0 || i == 4) RING(a0);
          else a0 = SMF(b[bs], i < 4 ? wx[i - 1] : w[1][i - 5], a0, sidx);
          SB0();
          RING(a1);
          if (i < 4) b[bs] = LDM(i);
        }
      } else {
#pragma unroll
        for (int i = 0; i < 9; i++) {
          SB0();
          if (i < 3) a1 = SMF(b[(i + 4) % 5], wx[i], a1, sidx);
          else a1 = SMF(b[(i + 4) % 5], cat16(pre[(i + 1) & 3][0], pre[(i + 1) & 3][1]), a1, sidx);
          SB0();
          if (i < 4) b[(i + 4) % 5] = LDM(i);
          if (i == 3 || i == 4) LDW(pre[(i + 1) & 3], 1 + i);
        }
      }
      vm[NV - 1] = pk2(a1[0] + a1[2], a1[1] + a1[3]);
      if constexpr (NV == 2) vm[0] = pk2(a0[0] + a0[2], a0[1] + a0[3]);
      asm volatile("" : "+v"(vm));
      const bool hi = so & 128;
      if (QW && PVP) PRIO(2);
      if (!QW && KV0) PRIO(1);
      SB0();
      STAMP(5);
      BARN();
      {
        const bf4 p0 = trrd(smem + po), p1 = trrd(smem + po + 256);
        SB0();
        const bf4 m1 = trrd(smem + (NV == 2 ? qo : po) + 512);
        SB0();
        bf4 m0;
        if constexpr (NV == 2) m0 = trrd(smem + po + 512);
        SB0();
        const u32x8 pp = ilv(m0, m1, p0, p1);
        f4 o[NV];
#pragma unroll
        for (int d = NV - 1; d >= 0; d--) {
          o[d] = MFMA(vx[d], __builtin_bit_cast(bf8, __builtin_shufflevector(pp, pp, 4, 5, 6, 7)), ((f4){0, 0, 0, 0})); asm volatile("" : "+v"(o[d])); SB0();
        }
        o[NV - 1] = MFMA(__builtin_bit_cast(bf8, vm), __builtin_bit_cast(bf8, __builtin_shufflevector(pp, pp, 2, 3, 4, 5)), o[NV - 1]);
        asm volatile("" : "+v"(o[NV - 1]));
        SB0();
        if constexpr (NV == 2) {
          o[0] = MFMA(__builtin_bit_cast(bf8, vm), __builtin_bit_cast(bf8, __builtin_shufflevector(pp, pp, 0, 1, 2, 3)), o[0]);
          asm volatile("" : "+v"(o[0]));
        }
        asm volatile("" :: "v"(pp));
        SB0();
        u32x2 ov = {pk2(o[0][0], o[0][1]), pk2(o[0][2], o[0][3])};
        if constexpr (NV == 2) {
          const u32x2 ow = {pk2(o[1][0], o[1][1]), pk2(o[1][2], o[1][3])};
          if (hi) ov = ow;
        }
        ov[0] = umin(ov[0], 0xFFFFFFFEu);
        __builtin_amdgcn_raw_buffer_store_b64(ov, rsX, so, so_, 16);
        SB0();
      }
      STAMP(7);
      if (QW && !PVP) PRIO(2);
      int xo, lo, pa = 0;
      {
        IDX();
        if constexpr (NV == 1) {
          __builtin_amdgcn_s_sleep(LSL);
          if (NXV && vt >= NXH) {
            asm volatile("s_mov_b32 m0, %3\n\ts_nop 0\n\tbuffer_load_dwordx4 %0, %1, %2 offen sc1 lds\n\tbuffer_load_dwordx4 %0, %1, %2 offen offset:1024 sc1 lds\n\t"
                         "buffer_load_dwordx4 %0, %1, %2 offen offset:2048 sc1 lds\n\tbuffer_load_dwordx4 %0, %1, %2 offen offset:3072 sc1 lds"
                         :: "v"((lane * 16) ^ ((lane & (vt * 16) & 32) << 2)), "s"(rsB), "s"(nb + (1 + (vt >> 1)) * ACB + (vt & 1) * 4096), "s"(C_KX + vt * 4096) : "memory", "m0");
            SB0();
          }
#pragma unroll
          for (int i = 0; i < 2; i++) nx[i] = __builtin_amdgcn_raw_buffer_load_b128(rsB, lane * 16 + i * 1024, nb + vt * 2048, 16);
          pa = C_QX + vt * 2048 + lane * 16;
          asm volatile("" : "+v"(pa));
        }
        xo = OB_OFF((lq & 1) * 16 + l15, (lq >> 1) * 8);
        lo = lane * 4;
        asm volatile("" : "+v"(xo), "+v"(lo));
#pragma unroll
        for (int j = 0; j < 4; j++) LDW(pre[j], NV == 2 ? 2 + j : j);
      }
      SB0();
      BARN();
      STAMP(8);
      {
        f4 o[2][NV];
#pragma unroll
        for (int it = 0; it < 2; it++) {
          const bf8 pf = *(const bf8*)(smem + C_PX + it * 1024 + lo * 4);
          const u32 pm = *(const u32*)(smem + C_PX + 2048 + it * 256 + lo);
#pragma unroll
          for (int d = 0; d < NV; d++) {
            o[it][d] = MFMA(vx[d], pf, ((f4){0, 0, 0, 0}));
            o[it][d] = MFMA(__builtin_bit_cast(bf8, vm), __builtin_bit_cast(bf8, d ? (u32x4){0u, pm, 0u, 0u} : (u32x4){pm, 0u, 0u, 0u}), o[it][d]);
          }
        }
#pragma unroll
        for (int d = 0; d < NV; d++)
          __builtin_amdgcn_raw_buffer_store_b128(pair16(umin(pk2(o[0][d][0], o[0][d][1]), 0xFFFFFFFEu), pk2(o[0][d][2], o[0][d][3]),
                                                        umin(pk2(o[1][d][0], o[1][d][1]), 0xFFFFFFFEu), pk2(o[1][d][2], o[1][d][3])), rsB,
              xo, 3 * ACB + OB_OFF(s * 32, h * 128 + (vt + d) * 16), 16);
        SB0();
        if (s == 63) break;
        if constexpr (NV == 1) { *(u32x4*)(smem + pa) = nx[0]; *(u32x4*)(smem + pa + 1024) = nx[1]; }
      }
      STAMP(9);
      YEND();
    }
    b3 = b3 == 2 ? 0 : b3 + 1;
    STAMP(14);
  }
  };
  if (wid == 0) run(std::integral_constant<int, 0>{});
  else if (wid == 4) run(std::integral_constant<int, 1>{});
  else if (wid > 4) run(std::integral_constant<int, 3>{});
  else if (wid == 3) run(std::integral_constant<int, 2>{});
  else run(std::integral_constant<int, 4>{});
  PRIO(0);
  TLS(0);
}

__device__ __forceinline__ void chain_out(const Params& p, char* smem, const int c, const int wv) {
  STS(32 + c, 1);
  constexpr int R = 5; (void)R;
  const int wid = wv, tq = c & 1, cg = c >> 1;
  u32x8 w[4][5];
  const __amdgpu_buffer_rsrc_t rsX = RSRC(XWIN(p), X_END);
  int xp = 0, mo, wo, ro;
  const int ms = wid < 4 ? 65536 : 1024;
  {
    IDX();
    mo = wid < 4 ? (cg >> 3) * 32768 + (cg & 7) * 512 + tq * 256 + wid * 64 : 196608 + (cg >> 2) * 512 + tq * 256 + (cg & 3) * 64;
    if (wid > 4 || (wid == 4 && cg >= 8)) mo = PJ_OOB;
    mo ^= X_M;
    if (tq) {
      u32 m0[2];
#pragma unroll
      for (int i = 0; i < 2; i++) m0[i] = *(const u32*)(p.Bm + (i * 4 + q4) * 576 + (wid < 4 ? cg * 32 + wid * 8 : 512 + (cg & 7) * 8) + p4 * 2);
      const int cal = X_CAL + (cg & 7) * 32768 + (cg >> 3) * 1024;
      __builtin_amdgcn_raw_buffer_store_b32(0u, rsX, tid < 4 * CALR ? ((2048 << p4) & ~2048) + q4 * 2304 : PJ_OOB, cal, 16);
      asm volatile("" :: "v"(m0[0]), "v"(m0[1]));
      VMW0();
      BARN();
      TLS(8);
      if (wid == 0) {
        u32 d[4] = {0u, 0u, 0u, 0u};
#pragma unroll 1
        for (int r = 0; r < CALR; r++) {
#pragma unroll
          for (int k = 0; k < 4; k++) {
            const u32 t0 = (u32)__builtin_readcyclecounter();
            __builtin_amdgcn_raw_buffer_store_b32(1u, rsX, lane ? PJ_OOB : 0, cal + ((2048 << k) & ~2048) + r * 2304, 16);
            VMW0();
            d[k] += (u32)__builtin_readcyclecounter() - t0;
          }
        }
        if (lane == 0) st4_sc1(WSP(u32, 0) + SY_VOTE(cg), 8u + (((d[1] < d[0] ? 1u : 0u) + (d[3] < d[0] ? 4u : 0u)) ^ (d[2] < d[0] ? 3u : 0u)));
        TLS(11);
        u32 fv;
        int it = 0;
        while (fv = ld4_sc1(WSP(u32, 0) + SY_VOTE(l15)), !__all(fv != 0u) && ++it < SPIN_CAP) __builtin_amdgcn_s_sleep(2);
        int x = 0;
#pragma unroll
        for (int j = 0; j < 3; j++) x |= __popcll(__ballot((fv >> j) & 1u)) > 32 ? 4096 << j : 0;
        *(int*)(smem + C_XP) = x;
      }
      BAR();
      xp = __builtin_amdgcn_readfirstlane(*(const int*)(smem + C_XP));
#pragma unroll
      for (int i = 0; i < 2; i++) {
        const int hc = cg * 2 + i;
        __builtin_amdgcn_raw_buffer_store_b32(lq ? ~0u : umin(m0[i], 0xFFFFFFFEu), rsX, lq < 3 ? (mo ^ xp ^ (lq * ms) ^ (256 - i * 256)) + l15 * 4 : PJ_OOB, 0, 16);
        __builtin_amdgcn_raw_buffer_store_b128(ones4(), rsX, (lane >> 5) * 131072 + (lane & 31) * 16,
                                               wid < 6 ? X_O(wid >> 1) ^ ((hc >> 3) * 32768 + (hc & 7) * 512) ^ xp ^ ((wid & 1) * X_HS) : PJ_OOB, 16);
#pragma unroll
        for (int k = 0; k < 2; k++) __builtin_amdgcn_raw_buffer_store_b128(ones4(), RSRC(WSP(char, O_OB), ACB), tid * 16, OB_OFF((62 + k) * 32, hc * 128), 16);
      }
      __builtin_amdgcn_raw_buffer_store_b32(tid & 2 ? DO0 * 64u : ~0u, rsX, tid < 8 ? ANX(tid & 1) + ((tid & 2) * 16 + cg * 2 + (tid >> 2)) * ANS : PJ_OOB, 0, 16);
    }
    SB0();
    const __amdgpu_buffer_rsrc_t rw = RSRC(p.BWo, 576 * 4096 * 2);
    u32x4 st[4][8];
    const int va = ((lane >> 2) * 576 + cg * 32 + p4 * 8) * 2;
#pragma unroll
    for (int i = 0; i < 4; i++) {
#pragma unroll
      for (int k = 0; k < 8; k++) st[i][k] = __builtin_amdgcn_raw_buffer_load_b128(rw, va, (((i & 1) * 16 + wid * 2 + (k >> 2)) * 128 + (i >> 1) * 64 + (k & 3) * 16) * 1152, OSTG_AUX);
      SB0();
    }
    char* sc = smem + wid * 10240;
    auto frag = [&](const char* b, int rs) {
      bf4 tt[4];
#pragma unroll
      for (int kk = 0; kk < 4; kk++) {
        const int r0 = ((kk >> 1) * 2 + (lq >> 1)) * 32 + (p4 >> 1) * 16 + (q4 >> 1) * 8 + (lq & 1) * 4 + (kk & 1) * 2 + (q4 & 1);
        tt[kk] = trrd(b + r0 * rs + (p4 & 1) * 8);
      }
      return ilv(tt[0], tt[1], tt[2], tt[3]);
    };
#pragma unroll
    for (int i = 0; i < 4; i++) {
#pragma unroll
      for (int k = 0; k < 8; k++) *(u32x4*)(sc + (k * 16 + (lane >> 2)) * 80 + p4 * 16) = st[i][k];
      __builtin_amdgcn_fence(__ATOMIC_RELEASE, "wavefront");
      __builtin_amdgcn_wave_barrier();
#pragma unroll
      for (int t = 0; t < 4; t++) w[i][t] = frag(sc + t * 16, 80);
      asm volatile("s_waitcnt lgkmcnt(0)" ::: "memory");
      __builtin_amdgcn_wave_barrier();
      if (i == 0 && tq) {
        BARN();
        if (tid == 0) st4_sc1(WSP(u32, 0) + SY_INIT(cg), (u32)xp + 1u);
        TLS(10);
      }
      SB0();
    }
    if (wid == 0) {
      u32 fl;
      int it = 0;
      while (fl = ld4_sc1(WSP(u32, 0) + (lane < 16 ? SY_INIT(lane) : SY_WX)), !__all(fl >= (lane < 16 ? 1u : 32u)) && ++it < SPIN_CAP) __builtin_amdgcn_s_sleep(GPS);
      if (lane == 0) *(int*)(smem + C_XP) = fl & 0x7800;
    }
    asm volatile("s_waitcnt lgkmcnt(0)" ::: "memory");
    BARN();
    {
      const __amdgpu_buffer_rsrc_t rx = RSRC(WSP(char, O_WX), 524288);
#pragma unroll
      for (int k = 0; k < 8; k++) st[0][k] = __builtin_amdgcn_raw_buffer_load_b128(rx, lane * 16, cg * 65536 + (((k & 2) * 8 + wid * 2 + (k & 1)) * 128 + (k >> 2) * 64) * 16, 16);
#pragma unroll
      for (int k = 0; k < 8; k++) *(u32x4*)(sc + k * 1024 + lane * 16) = st[0][k];
      __builtin_amdgcn_fence(__ATOMIC_RELEASE, "wavefront");
      __builtin_amdgcn_wave_barrier();
#pragma unroll
      for (int i = 0; i < 4; i++) w[i][4] = frag(sc + i * 2048, 16);
    }
    xp = __builtin_amdgcn_readfirstlane(*(const int*)(smem + C_XP));
    mo = __builtin_amdgcn_readfirstlane(mo ^ xp);
    const int row = (lq & 1) * 4 + ((l15 >> 1) & 3);
    wo = ((lq >> 1) == hi8 ? row * 128 : 1024) + (((wid * 2 + hi8) ^ (row * 2)) << 3) + (l15 & 1) * 4;
    ro = wid * 2560 + (lane >> 5) * 1280 + ((lane >> 2) & 7) * 128 + (lane & 8) * 8 + p4 * 16;
  }
  __syncthreads();
  STS(32 + c, 14);
  TLS(9);
  int go, sidx, po;
  { IDX(); go = lane * 16; sidx = (l15 & 2) ? 0xEEEE : 0x4444;
    po = p4 == 0 ? (lq & 1) * 32 + (lq >> 1) * 16 + q4 * 4 : PJ_OOB; }
  int ro1 = ro ^ 64;
  asm volatile("" : "+v"(ro), "+v"(ro1), "+v"(wo));
#define PUTP(t) asm volatile("ds_write2st64_b32 %0, %1, %2 offset0:%3 offset1:%4" :: "v"(wo), "v"(d[t][0] + d[t][2]), "v"(d[t][1] + d[t][3]), "i"((t) * 10), "i"((t) * 10 + 5) : "memory")
#define APS 18
#define ONOP 7
  int b3 = 0, dur = DO0 * 64;
#pragma unroll 1
  for (int s = 0; s < 63; s++) {
    f4 d[5];
    int gb[4], pb = mo ^ ((b3 == 2 ? 0 : b3 + 1) * ms);
#pragma unroll
    for (int i = 0; i < 4; i++) { gb[i] = X_O(b3) ^ xp ^ (tq * 131072 + ((i & 1) * 2 + (wid >> 2)) * 32768 + (wid & 3) * 1024 + (i < 2 ? X_HS : 0)); asm volatile("" : "+s"(gb[i])); }
    asm volatile("" : "+s"(pb));
    u32x4 g[4];
    u32 bad;
    auto mult = [&]() __attribute__((always_inline)) {
      CFENCE();
#pragma unroll
      for (int i = 0; i < 4; i++) {
        g[i] = __builtin_amdgcn_raw_buffer_load_b128(rsX, go, gb[i], 16);
        SB0();
        if (ONOP >= 0 && i < 3) asm volatile("s_nop %0" :: "i"(ONOP >= 0 ? ONOP : 0));
        SB0();
      }
      bad = 0u;
#pragma unroll
      for (int i = 0; i < 4; i++) {
        SB0();
#pragma unroll
        for (int t = 0; t < 5; t++) {
          d[t] = SMF(g[i], w[i][t], d[t], sidx);
          if (t == 0) bad = umax(umax(bad, g[i][0]), g[i][2]);
          if (i == 3 && t >= 2) { SB0(); PUTP(t - 2); SB0(); }
        }
      }
      SB0();
#pragma unroll
      for (int t = 3; t < 5; t++) PUTP(t);
      SB0();
    };
#pragma unroll
    for (int t = 0; t < 5; t++) d[t] = zero4();
    SB0();
    u32 dl = 0;
    if (wid == 7) { __builtin_amdgcn_s_sleep(APS); STAMP(4); AD_GET(false, K1, SPIN_CAP); STAMP(5); AD_SPIN(); }
    STAMP(15);
    BARN();
    STAMP(0);
    mult();
    if (__builtin_expect(__any(bad == ~0u), 0)) {
      int it = 0;
      do {
#pragma unroll
        for (int t = 0; t < 5; t++) d[t] = zero4();
        mult();
      } while (__any(bad == ~0u) && ++it < SPIN_CAP);
    }
    STAMP(1);
    BARC();
    STAMP(2);
    if (__builtin_expect(wid < 5, 1)) {
      const f4 t0 = *(const f4*)(smem + ro), t1 = *(const f4*)(smem + ro1);
      float a = (t0[0] + t0[2]) + (t1[0] + t1[2]), b = (t0[1] + t0[3]) + (t1[1] + t1[3]);
      a += __uint_as_float(__builtin_amdgcn_mov_dpp(__float_as_uint(a), 0xB1, 0xf, 0xf, true));
      b += __uint_as_float(__builtin_amdgcn_mov_dpp(__float_as_uint(b), 0xB1, 0xf, 0xf, true));
      a += __uint_as_float(__builtin_amdgcn_mov_dpp(__float_as_uint(a), 0x4E, 0xf, 0xf, true));
      b += __uint_as_float(__builtin_amdgcn_mov_dpp(__float_as_uint(b), 0x4E, 0xf, 0xf, true));
      __builtin_amdgcn_raw_buffer_store_b32(umin(pk2(a, b), 0xFFFFFFFEu), rsX, po, pb, 16);
      SB0();
      STAMP(3);
      __builtin_amdgcn_s_sleep(4);
      __builtin_amdgcn_raw_buffer_store_b32(~0u, rsX, po, mo ^ (b3 * ms), 16);
    } else if (wid == 7) {
      dur += (min((int)(((u32)wall_clock64() << 6) - dl), dur + DOCAP * 64) - dur) >> DSH;
      __builtin_amdgcn_raw_buffer_store_b32((u32)dur, rsX, 0, ANX(~s & 1) + (32 + c) * ANS, 16);
    }
    VMW0C();
    YEND();
    b3 = b3 == 2 ? 0 : b3 + 1;
  }
  __syncthreads();
}

__device__ __forceinline__ void wait_ge(const u32* w, u32 val, const int wv) {
  if (TIDX == 0) {
    int it = 0;
    while (ld4_sc1(w) < val && ++it < SPIN_CAP) __builtin_amdgcn_s_sleep(WPS);
  }
}
__device__ __forceinline__ void acquire_all(const int wv) {
  if (TIDX == 0) {
    __builtin_amdgcn_fence(__ATOMIC_ACQUIRE, "agent");
    VMW0();
  }
  __syncthreads();
}

#define T_KV 0
#define T_Q 0
#define T_A 192
#define T_W (T_A + 64)
#define T_S (T_W + 256)
#define T_L (T_S + 72)
#define T_O T_L
#define T_CHAIN (1 << 20)
#define CODE_KB 84
#define WWARM 0
#define HWARM 112
#define AORD 0x01234567u
#define OORD 0x67014523u

__device__ __forceinline__ int next_task(const Params& p, int* s_task, int xq, int& xk, const int wv, const int pt, const int pre = -1) {
  __syncthreads();
  if (TIDX == 0) {
    int t = -1;
    if (pre >= 0) { t = T_A + pre; xk = 7; }
    while (xk < 6) {
      int x = xq + xk; if (x >= 6) x -= 6;
      const int i = (int)__hip_atomic_fetch_add(WSP(u32, 0) + SY_XQ(x), 1u, __ATOMIC_RELAXED, __HIP_MEMORY_SCOPE_AGENT);
      if (i < 32) { t = x * 32 + i; break; }
      xk++;
    }
    if (t < 0 && xk == 6) {
      xk = 7;
      int it = 0;
      while (ld4_sc1(WSP(u32, 0) + SY_NCL) < 64u && ld4_sc1(WSP(u32, 0) + SY_ARR) < 256u && ++it < SPIN_CAP) __builtin_amdgcn_s_sleep(8);
      if (ld4_sc1(WSP(u32, 0) + SY_NCL) < 64u) {
        for (int k = 0; k < 64; k++)
          if (__hip_atomic_exchange(WSP(u32, 0) + SY_CLAIM(k), 1u, __ATOMIC_RELAXED, __HIP_MEMORY_SCOPE_AGENT) == 0u) {
            __hip_atomic_fetch_add(WSP(u32, 0) + SY_NCL, 1u, __ATOMIC_RELAXED, __HIP_MEMORY_SCOPE_AGENT);
            t = T_CHAIN + k; break;
          }
      }
    }
    if (t < 0) t = T_A + (int)__hip_atomic_fetch_add(WSP(u32, 0) + SY_QHEAD, 1u, __ATOMIC_RELAXED, __HIP_MEMORY_SCOPE_AGENT);
    *s_task = t;
  }
  __syncthreads();
  return __builtin_amdgcn_readfirstlane(*s_task);
}

__device__ __forceinline__ int take_task(int* s_task, const int pre, const int wv, const int pb, const int pl, const int pt) {
  if (TIDX == 0) {
    *s_task = pre;
  }
  __syncthreads();
  const int t = __builtin_amdgcn_readfirstlane(*s_task);
  __syncthreads();
  return t;
}
#if defined(__HIP_DEVICE_COMPILE__) && !defined(KEEP_PK)
#define NOPK __attribute__((target("no-packed-fp32-ops")))
#else
#define NOPK
#endif
#define P0V 2
__device__ __forceinline__ void mega_body(const Params& p, char* smem) {
  u64 pc0; asm volatile("s_getpc_b64 %0" : "=s"(pc0));
  int* const s_task = (int*)(smem + LDS_TOTAL);
  const int wv = __builtin_amdgcn_readfirstlane(threadIdx.x >> 6);
  if (blockIdx.x == 0 && TIDX == 0) asm volatile("s_nop 0" ::: "memory");
#define tid TIDX
  const int xcc = (int)(__builtin_amdgcn_s_getreg(20 | (3 << 11)) & 7u);
  const int xq = xcc >= 2 ? xcc - 2 : xcc;
  int xk = 0, pre = 0;
  u32* const qh = WSP(u32, 0) + SY_QHEAD;
  if (wv) {
    const int g = ((blockIdx.x >> 3) & 31) * 7 + wv - 1, vo = (tid & 63) * 64;
#define WARM(ptr, bytes, so) __builtin_amdgcn_raw_ptr_buffer_load_lds(RSRC(ptr, bytes), (LDSP void*)(smem + LDS_ALL - 2048 + wv * 256), 4, vo, so, 0, 0)
    WARM(pc0, CODE_KB * 1024, g * 4096);
    if (xcc > 1) {
      const u16* w = xq < 2 ? p.BWk : xq < 4 ? p.BWv : p.BWq;
      WARM(p.Bx, 256 * 1152, (223 - g) * 4096);
#pragma unroll
      for (int i = 0; i < WWARM; i++) WARM(w, WOB, (g + i * 224) * 8192 + (xq & 1) * 4096);
    } else if (xcc == 0) {
      WARM(p.BWq, HWARM * 8192, g * 4096); WARM(p.BWk, HWARM * 8192, g * 4096); WARM(p.BWv, HWARM * 8192, g * 4096);
    }
  }
  if (tid == 0) {
    int t0 = -1;
    if (xcc < 2) {
      const int k = ((blockIdx.x >> 3) & 31) + xcc * 32;
      if (__hip_atomic_exchange(WSP(u32, 0) + SY_CLAIM(k), 1u, __ATOMIC_RELAXED, __HIP_MEMORY_SCOPE_AGENT) == 0u) {
        __hip_atomic_fetch_add(WSP(u32, 0) + SY_NCL, 1u, __ATOMIC_RELAXED, __HIP_MEMORY_SCOPE_AGENT);
        t0 = T_CHAIN + k;
      }
    } else {
      const int i = (int)__hip_atomic_fetch_add(WSP(u32, 0) + SY_XQ(xq), 1u, __ATOMIC_RELAXED, __HIP_MEMORY_SCOPE_AGENT);
      if (i < 32) t0 = xq * 32 + i;
    }
    __hip_atomic_fetch_add(WSP(u32, 0) + SY_ARR, 1u, __ATOMIC_RELAXED, __HIP_MEMORY_SCOPE_AGENT);
    *s_task = t0;
  }
  BAR();
  int t = __builtin_amdgcn_readfirstlane(*s_task);
  if (t < 0) t = next_task(p, s_task, xq, xk, wv, t);
  {
    while (t < T_A) {
      const int x = t >> 5, j = (t & 7) * 4 + ((0x9C >> ((t >> 2) & 6)) & 3);
      proj_slot(p, x, j, smem, wv, pre);
      t = next_task(p, s_task, xq, xk, wv, t, pre);
    }
  }
  if (t >= T_CHAIN) { if (t < T_CHAIN + 32) chain_head(p, smem, t - T_CHAIN, wv); else chain_out(p, smem, t - T_CHAIN - 32, wv);
    const int l = tid & 7;
    const u32 v = ld4_sc1(WSP(u32, 0) + (l < 7 ? SY_OQ(l) : SY_QHEAD));
    if (tid == 0) *s_task = 0;
    __syncthreads();
    if (v < (l == 7 ? T_S - T_A : l == 6 ? 72 : (l & 1) ? NOB : NOA)) *s_task = 1;
    __syncthreads();
    if (__builtin_amdgcn_readfirstlane(*s_task) == 0) {
      if (xcc == 0) TLS(3); else TLS(4);
      return;
    }
    xk = 7; t = next_task(p, s_task, xq, xk, wv, -1);
  }
  while (t < T_W) {
    wof_task(p, t - T_A, smem, wv, qh, pre);
    if (tid == 0) __hip_atomic_fetch_add(WSP(u32, 0) + SY_CW, 1u, __ATOMIC_RELAXED, __HIP_MEMORY_SCOPE_AGENT);
    t = T_A + take_task(s_task, pre, wv, T_A, T_S - T_A, t);
  }
  while (t < T_S) {
    const int qb = (AORD >> (((t - T_W) >> 5) * 4)) & 7, h = (t - T_W) & 31;
    if (tid < 64) {
      int it = 0;
      while (true) {
        const u32 f0 = ld4_sc1(WSP(u32, 0) + SY_BLK(3, h >> 1, tid)), f1 = ld4_sc1(WSP(u32, 0) + SY_BLK(4, h >> 1, tid)), f2 = ld4_sc1(WSP(u32, 0) + SY_BLK(5, h >> 1, tid));
        if (__all(tid >= qb * 8 + 8 || ((tid < qb * 8 || f0 >= 8u) && f1 >= 8u && f2 >= 8u)) || ++it >= SPIN_CAP) break;
        __builtin_amdgcn_s_sleep(8);
      }
    }
    acquire_all(wv);
    fa::attn_block(WSP(char, O_QA) + FR_OFF(qb * 256, h * 128), WSP(char, O_KA) + h * 8192, WSP(char, O_VA) + h * 8192,
                   WSP(char, O_CA) + OB_OFF(qb * 256, h * 128), qb * 256, smem, wv, qh, pre);
    if (tid == 0) __hip_atomic_fetch_add(WSP(u32, 0) + SY_CC(qb), 1u, __ATOMIC_RELAXED, __HIP_MEMORY_SCOPE_AGENT);
    t = T_A + take_task(s_task, pre, wv, T_A, T_S - T_A, t);
  }
  int rest = 64 | (1 << xq);
#pragma unroll 1
  while (rest) {
    const int x = __builtin_ctz(rest);
    rest &= rest - 1;
    if (x == 6) {
      CLAIMS();
      t = take_task(s_task, pre, wv, T_S, 72, -1);
      while ((t & 0xFFFF) < 72) {
        t &= 0xFFFF;
        small_out_task(p, t & 3, t >> 2, smem, wv, pre);
        t = take_task(s_task, pre, wv, T_S, 72, T_S + t);
      }
      rest = t >> 16;
      continue;
    }
    const int part = x & 1, cg = x >> 1, n = part ? NOB : NOA;
    u32* const qh = WSP(u32, 0) + SY_OQ(x);
    CLAIM();
    t = take_task(s_task, pre, wv, T_O + x * 32, n, -1);
    while (t < n) {
      const int nb = part ? NBB : NAB, big = t < nb, e = big ? t * 2 : t + nb;
      const int rb = part ? e : ((OORD >> ((e >> 2) * 4)) & 7) * 4 + (e & 3);
      if (tid == 0) {
        const u32* w = WSP(u32, 0) + (part ? SY_CS((rb + big) * 2 + 1) : SY_CC(rb >> 2));
        int it = 0;
        while ((ld4_sc1(w) < (part ? 1u : 32u) || ld4_sc1(WSP(u32, 0) + SY_CW) < 64u) && ++it < SPIN_CAP) __builtin_amdgcn_s_sleep(WPS);
      }
      acquire_all(wv);
      const char *pa = WSP(char, part ? O_OB : O_CA) + rb * 2 * 262144, *pw = WSP(char, O_WOF) + part * WOB + cg * 12 * 131072;
      u16* const pc = p.out + (size_t)rb * 64 * 1152 + part * 576 + cg * 192;
      if (big) out_tile<8>(pa, pw, pc, smem, wv, qh, pre);
      else out_tile<4>(pa, pw, pc, smem, wv, qh, pre);
      t = take_task(s_task, pre, wv, T_O + x * 32, n, T_O + x * 32 + t);
    }
  }
  if (xcc == 0) TLS(3); else if (xcc == 1) TLS(4); else TLS(5);
}

#undef tid
__global__ NOPK void __launch_bounds__(512, 2) mega(Params p) {
  __shared__ __attribute__((aligned(16))) char smem[LDS_ALL];
  mega_body(p, smem);
}
extern "C" void kernel_launch(void* const* d_in, const int* in_sizes, int n_in, void* d_out, int out_size, void* d_ws,
                              size_t ws_size, hipStream_t stream) {
  Params p;
  p.Ax = (const u16*)d_in[0]; p.Ac = (const u16*)d_in[1]; p.Awq = (const u16*)d_in[2]; p.Awk = (const u16*)d_in[3];
  p.Awv = (const u16*)d_in[4]; p.Awo = (const u16*)d_in[5]; p.Bx = (const u16*)d_in[6]; p.Bm = (const u16*)d_in[7];
  p.BWq = (const u16*)d_in[8]; p.BWk = (const u16*)d_in[9]; p.BWv = (const u16*)d_in[10]; p.BWo = (const u16*)d_in[11];
  p.out = (u16*)d_out;
  p.ws = (char*)d_ws;
  const size_t syncBytes = SY_WORDS * 4;
  (void)hipMemsetAsync(d_ws, 0, syncBytes, stream);
  mega<<<256, 512, 0, stream>>>(p);
}
```

```cpp
#include <hip/hip_runtime.h>
#include <hip/hip_bf16.h>
#include <stdint.h>
#include <type_traits>

typedef unsigned short u16;
typedef unsigned int u32;
typedef unsigned long long u64;
typedef __attribute__((ext_vector_type(8))) short bf8;
typedef __attribute__((ext_vector_type(4))) short bf4;
typedef __attribute__((ext_vector_type(4))) float f4;
typedef __attribute__((ext_vector_type(2))) float f2v;
typedef __attribute__((ext_vector_type(2))) __bf16 bf2v;
typedef __attribute__((ext_vector_type(4))) unsigned u32x4;
typedef __attribute__((ext_vector_type(2))) unsigned u32x2;
typedef float f32x16 __attribute__((ext_vector_type(16)));
typedef __attribute__((ext_vector_type(8))) unsigned u32x8;
typedef __attribute__((ext_vector_type(8))) __bf16 bh8;
typedef __attribute__((ext_vector_type(16))) __bf16 bh16;
#define LDSP __attribute__((address_space(3)))

__device__ __forceinline__ u32 pk2(float a, float b) {
  f2v x = {a, b};
  bf2v y = __builtin_convertvector(x, bf2v);
  return __builtin_bit_cast(u32, y);
}
__device__ __forceinline__ u32 umin(u32 a, u32 b) { return a < b ? a : b; }
__device__ __forceinline__ u32 umax(u32 a, u32 b) { return a > b ? a : b; }
__device__ __forceinline__ float rbf(float f) { return __uint_as_float(pk2(f, f) << 16); }
#define MFMA(a, b, c) __builtin_amdgcn_mfma_f32_16x16x32_bf16(a, b, c, 0, 0, 0)
#define MFMA16(a, b, c) __builtin_amdgcn_mfma_f32_16x16x16bf16_1k(a, b, c, 0, 0, 0)
#define SMF(a, b, c, idx) __builtin_amdgcn_smfmac_f32_16x16x64_bf16(__builtin_bit_cast(bh8, a), __builtin_bit_cast(bh16, b), c, idx, 0, 0)
__device__ __forceinline__ u32x8 ilv(bf4 a, bf4 b, bf4 c, bf4 d) {
  const u32x2 x = __builtin_bit_cast(u32x2, a), y = __builtin_bit_cast(u32x2, b), z = __builtin_bit_cast(u32x2, c), w = __builtin_bit_cast(u32x2, d);
  return (u32x8){x[0], x[1], y[0], y[1], z[0], z[1], w[0], w[1]};
}
__device__ __forceinline__ u32x8 cat16(bf8 a, bf8 b) {
  const u32x4 x = __builtin_bit_cast(u32x4, a), y = __builtin_bit_cast(u32x4, b);
  return (u32x8){x[0], x[1], x[2], x[3], y[0], y[1], y[2], y[3]};
}
__device__ __forceinline__ float swadd(float a, float b) {
  auto x = __builtin_amdgcn_permlane32_swap(__float_as_uint(a), __float_as_uint(b), false, false);
  return __uint_as_float(x[0]) + __uint_as_float(x[1]);
}
__device__ __forceinline__ bf4 trrd(const char* p) {
  return __builtin_amdgcn_ds_read_tr16_b64_v4i16((LDSP bf4*)(p));
}
__device__ __forceinline__ bf8 cat8(bf4 a, bf4 b) { return __builtin_shufflevector(a, b, 0, 1, 2, 3, 4, 5, 6, 7); }
__device__ __forceinline__ void st8_sc1(void* p, u64 v) { __hip_atomic_store((u64*)p, v, __ATOMIC_RELAXED, __HIP_MEMORY_SCOPE_AGENT); }
__device__ __forceinline__ u32 ld4_sc1(const u32* p) { return __hip_atomic_load(p, __ATOMIC_RELAXED, __HIP_MEMORY_SCOPE_AGENT); }
__device__ __forceinline__ void st4_sc1(u32* p, u32 v) { __hip_atomic_store(p, v, __ATOMIC_RELAXED, __HIP_MEMORY_SCOPE_AGENT); }
#define VMW0() asm volatile("s_waitcnt vmcnt(0)" ::: "memory")
#define CFENCE() asm volatile("" ::: "memory")
#define VMW0C() do { __builtin_amdgcn_s_waitcnt(0x0F70); asm volatile("" ::: "memory"); } while (0)
#define SPIN_CAP (1 << 22)
#define XPS 4
#define SPS 4
#define WPS 8
__device__ __forceinline__ int tid_now(int wv) {
  int l; asm volatile("v_mbcnt_lo_u32_b32 %0, -1, 0\n\tv_mbcnt_hi_u32_b32 %0, -1, %0" : "=v"(l));
  return wv * 64 + l;
}
#define TIDX tid_now(wv)
#define CLAIM() do { if (TIDX == 0) pre = (int)__hip_atomic_fetch_add(qh, 1u, __ATOMIC_RELAXED, __HIP_MEMORY_SCOPE_AGENT); } while (0)

#define TLS(i)
struct Params {
  const u16 *Ax, *Ac, *Awq, *Awk, *Awv, *Awo, *Bx, *Bm, *BWq, *BWk, *BWv, *BWo;
  u16* out;
  char* ws;
};
#define SY_QHEAD 0
#define SY_CC(rb) (64 + (rb) * 16)
#define SY_CW (64 + 8 * 16)
#define SY_OQ(x) (64 + 9 * 16 + (x) * 16)
#define SY_CS(s) (64 + 16 * 16 + (s) * 16)
#define SY_B0 (64 + 80 * 16)
#define SY_INIT(c) (SY_B0 + 64 + (c))
#define SY_VOTE(i) (SY_B0 + 128 + (i))
#define SY_JOB(x) (SY_B0 + 256 + (x) * 16)
#define SY_ARR (SY_B0 + 384)
#define SY_WX (SY_ARR + 32)
#define SY_BLK(mi, cb, g) (SY_ARR + 64 + ((mi) * 16 + (cb)) * 64 + (g))
#define FULL 12u
#define SY_WORDS (SY_ARR + 64 + 6 * 16 * 64)
#define SY_BEAT(r) (SY_WORDS + (r) * 32)

#define ACB (2048 * 4096 * 2)
#define O_PAD 0
#define O_QA (SY_BEAT(8) * 4 + O_PAD)
#define O_KA (O_QA + ACB)
#define O_VA (O_KA + ACB)
#define O_CA (O_VA + ACB)
#define O_QB (O_CA + ACB)
#define O_KB (O_QB + ACB)
#define O_VB (O_KB + ACB)
#define O_OB (O_VB + ACB)
#define O_WOF (O_OB + ACB)
#define WOB (576 * 4096 * 2)
#define O_WX (O_WOF + 2 * WOB)
#define O_XW (O_WX + 524288)
#define X_HS 0x4000
#define X_O(b) ((b) * 0x80000 + ((0x140 >> ((b) * 4)) & 15) * 4096)
#define X_MS X_HS
#define X_M (0x185000 ^ X_MS)
#define X_ANN (0x185000 + 7 * 32768)
#define X_CAL 0x40000
#define X_HOT(xp) (0xC0000 + (~(xp) & 0x2000))
#define X_END 0x200000
#define OB_OFF(r, c) (((r) >> 5) * 262144 + ((c) >> 4) * 1024 + ((r) & 31) * 32 + ((c) & 15) * 2)
#define FR_OFF(r, c) (((r) >> 5) * 262144 + ((c) >> 3) * 512 + ((r) & 31) * 16 + ((c) & 7) * 2)
#define GLB __attribute__((address_space(1)))
#define WSP(T, off) ((T*)((GLB char*)p.ws + (off)))
#define XWIN(p) (((uintptr_t)(p).ws + O_XW + 0x1FFFFFu) & ~(uintptr_t)0x1FFFFFu)

#define HOTP(xp) ((u32*)(GLB u32*)(XWIN(p) + X_HOT(xp)))

#define XS0 48
#define TG0 58
#define NAB 8
#define NBB 10
#define NOA (32 - NAB)
#define NOB (XS0 / 2 - NBB)
#define LDS_TOTAL (131072 + 8192 + 16384)
#define LDS_ALL 163840

#define SB0() __builtin_amdgcn_sched_barrier(0)
#define PJ_OOB 0x7F000000
#define RSRC(ptr, bytes) __builtin_amdgcn_make_buffer_rsrc((void*)(ptr), 0, (bytes), 0x00020000)
__device__ __forceinline__ bf8 ldsc1(__amdgpu_buffer_rsrc_t r, int off) {
  return __builtin_bit_cast(bf8, __builtin_amdgcn_raw_buffer_load_b128(r, off, 0, 16));
}
__device__ __forceinline__ u32x4 pair16(u32 a0, u32 a1, u32 b0, u32 b1) {
  auto r0 = __builtin_amdgcn_permlane16_swap(a0, b0, false, false);
  auto r1 = __builtin_amdgcn_permlane16_swap(a1, b1, false, false);
  return (u32x4){r0[0], r1[0], r0[1], r1[1]};
}
__device__ __forceinline__ int fsw(int k) { return (k & 3) | (((k >> 3) & 1) << 2); }

#define AADD(w, v) __hip_atomic_fetch_add(WSP(u32, 0) + (w), v, __ATOMIC_RELAXED, __HIP_MEMORY_SCOPE_AGENT)
#define AOR(w, v) __hip_atomic_fetch_or(WSP(u32, 0) + (w), v, __ATOMIC_RELAXED, __HIP_MEMORY_SCOPE_AGENT)
#define C_B0 (LDS_ALL - 2048)
#define BEAT_LD(job) ld4_sc1(WSP(u32, 0) + SY_BEAT((job) & 7))
#define BEAT_ON(it) do { if (((it) & 31) == 31 && (TIDX & 63) < 8) AADD(SY_BEAT(TIDX & 63), 1u); } while (0)
__device__ __forceinline__ u32 confirm(const Params& p, const char* smem, const int job, u32 b1, const int wv) {
  asm volatile("" :: "v"(b1) : "memory");
  const u32 b0 = *(const u32*)(smem + C_B0);
  if (__builtin_expect(__any(b1 == b0), 0)) {
    int it = 0;
    while (__any(BEAT_LD(job) == b0) && ++it < SPIN_CAP) __builtin_amdgcn_s_sleep(2);
  }
  u32 old = 0u;
  if (TIDX == 0) old = AOR(SY_JOB(job >> 5) + TIDX, 1u << (job & 31));
  if (TIDX < 2) AADD(TIDX ? SY_BEAT(job & 7) : SY_ARR, 1u);
  return old;
}

#define C_PARK 131072
#define PJ_PAD 0
__device__ __forceinline__ void proj_slot(const Params& p, int x, int j, char* smem, const int wv, int& pre, int job) {
  const int tid = TIDX, lane = tid & 63, wid = wv;
  const int l15 = lane & 15, lq = lane >> 4, q4 = l15 >> 2, p4 = lane & 3, wr = wid >> 2, wc = wid & 3;
  const __amdgpu_buffer_rsrc_t rc = RSRC(p.ws, O_XW);
  const int vB = ((wid * 4 + lq) * 4096 + (((l15 >> 1) ^ (lq | (((wid >> 1) & 1) << 2))) * 16 + (lane & 1) * 8)) * 2, vB1 = vB + 32 * 8192;
  int ao = (wr * 64 + l15) * 128 + ((lq ^ (l15 & 7)) << 4);
  int bo = (int)(uintptr_t)smem + 65536 + (lq * 8 + q4) * 256 + (((wc * 2) ^ (q4 | ((lq & 1) << 2))) << 5) + p4 * 8;
  const int vD = lane * 16;
#define PJ_ORD 0x1312
  const int ord = (j & 3) == 3 ? PJ_ORD : 0x1312;
#define PJ_NEXT ((ord >> (u * 4)) & 3)
  const int cb = (x & 1) * 8 + (j >> 2);
  __amdgpu_buffer_rsrc_t rsA, rsB;
  const u16 *pA0 = p.Bx, *pB0 = (x < 2 ? p.BWk : x < 4 ? p.BWv : p.BWq) + cb * 256;
  const u16 *pA1 = x < 2 ? p.Ax : p.Ac, *pB1 = (x < 2 ? p.Awq : x < 4 ? p.Awk : p.Awv) + cb * 256;
  asm volatile("" : "+s"(pA0), "+s"(pB0), "+s"(pA1), "+s"(pB1));
  int k2, vA, vA1, sa, sb, nvA;
#define PJ_K2(u) ((u) < 2 || x < 2 ? 1152 : 1024)
#define PJ_PREP(u) do { int ln_ = vD; asm volatile("" : "+v"(ln_)); \
    nvA = (wid * 8 + (ln_ >> 7)) * PJ_K2(u) + ((ln_ ^ (ln_ >> 3)) & 0x70); \
    asm volatile("" : "+v"(nvA)); } while (0)
#define PJ_UNIT(u) do { k2 = PJ_K2(u); rsA = RSRC((u) < 2 ? pA0 : pA1, 2048 * k2); rsB = RSRC((u) < 2 ? pB0 : pB1, k2 * 4096 - cb * 512); vA = nvA; vA1 = nvA + 64 * k2; \
    sa = ((j & 3) + ((u) & 1) * 4) * 256 * k2 - 128; sb = 0; } while (0)
#define PJ_DEST(u, rt) do { const int mi_ = ((u) >> 1) * 3 + (x >> 1), g_ = ((rt) + ((u) & 1) * 4) * 8; \
    dco = O_QA + (mi_ == 2 ? 4 : (mi_ + 5) & 7) * ACB + cb * 16384 + wc * 2048 + (g_ + wr * 2) * 262144; \
    dfl = SY_BLK(mi_, cb, g_) * 4; } while (0)
#define PJ_STAGE(dst, rs, v0, v1, so) do { const int so_ = (so); \
    __builtin_amdgcn_raw_ptr_buffer_load_lds(rs, (LDSP void*)(smem + (dst) + wid * 1024), 16, v0, so_, 0, 0); \
    __builtin_amdgcn_raw_ptr_buffer_load_lds(rs, (LDSP void*)(smem + (dst) + wid * 1024 + 8192), 16, v1, so_, 0, 0); } while (0)
#define PJ_SA(dst, so) PJ_STAGE(dst, rsA, vA, vA1, so)
#define PJ_SB(dst, so) PJ_STAGE(dst, rsB, vB, vB1, so)
#define PJ_LDA(off) do { _Pragma("unroll") for (int m = 0; m < 4; m++) _Pragma("unroll") for (int k = 0; k < 2; k++) \
    At[m][k] = *(const bf8*)(smem + (ao ^ (k * 64)) + (off) + m * 2048); } while (0)
#define PJ_TR(dst, a, off) asm volatile("ds_read_b64_tr_b16 %0, %1 offset:%2" : "=&v"(dst) : "v"(a), "i"(off) : "memory")
#define PJ_LDB(D, h) do { const int b1_ = bo ^ 32; _Pragma("unroll") for (int n = 0; n < 2; n++) _Pragma("unroll") for (int k = 0; k < 2; k++) { \
    bf4 lo_, hi_; PJ_TR(lo_, n ? b1_ : bo, (h) * 16384 + k * 8192); PJ_TR(hi_, n ? b1_ : bo, (h) * 16384 + k * 8192 + 1024); D[n][k] = cat8(lo_, hi_); } } while (0)
#define PJ_CV(ai, bj, q) ((u32x4){pk2(acc[ai][bj][(q) & 2][(q) & 1][0], acc[ai][bj][(q) & 2][(q) & 1][1]), pk2(acc[ai][bj][(q) & 2][(q) & 1][2], acc[ai][bj][(q) & 2][(q) & 1][3]), \
                                  pk2(acc[ai][bj][((q) & 2) + 1][(q) & 1][0], acc[ai][bj][((q) & 2) + 1][(q) & 1][1]), pk2(acc[ai][bj][((q) & 2) + 1][(q) & 1][2], acc[ai][bj][((q) & 2) + 1][(q) & 1][3])})
#define PJ_SW(v) pair16(v[0], v[1], v[2], v[3])
#define PJ_MMA(Z, ai, bj, D, F) do { PJ_SCHED; __builtin_amdgcn_s_setprio(1); \
    _Pragma("unroll") for (int k = 0; k < 2; k++) _Pragma("unroll") for (int m = 0; m < 4; m++) _Pragma("unroll") for (int n = 0; n < 2; n++) { \
      if (k == 0 && (Z)) asm volatile("v_mfma_f32_16x16x32_bf16 %0, %1, %2, 0" : "+v"(acc[ai][bj][m][n]) : "v"(D[n][k]), "v"(At[m][k])); \
      else asm volatile("v_mfma_f32_16x16x32_bf16 %0, %1, %2, %0" : "+v"(acc[ai][bj][m][n]) : "v"(D[n][k]), "v"(At[m][k])); \
      F(k * 8 + m * 2 + n); } \
    __builtin_amdgcn_s_setprio(0); if (PJ_PAD) __builtin_amdgcn_s_sleep(PJ_PAD); PJ_SCHED; } while (0)
#define PJ_SWI(a, b) asm volatile("v_permlane16_swap_b32 %0, %1" : "+v"(a), "+v"(b))
#define PJ_CVI(d, s, e) asm volatile("v_cvt_pk_bf16_f32 %0, %1, %2" : "=v"(d) : "v"(s[e]), "v"(s[(e) + 1]))
#define PJ_PACK(P, ai, bj, i) do { const int q_ = (i) >> 2; \
    if (((i) & 3) < 2) { PJ_CVI(P[q_][(i) & 1], acc[ai][bj][q_ & 2][q_ & 1], ((i) & 1) * 2); PJ_CVI(P[q_][2 + ((i) & 1)], acc[ai][bj][(q_ & 2) + 1][q_ & 1], ((i) & 1) * 2); } \
    else PJ_SWI(P[q_][(i) & 1], P[q_][2 + ((i) & 1)]); } while (0)
#define PJ_WV(n) asm volatile("s_waitcnt vmcnt(" #n ")" ::: "memory")
#define PJ_WL(n) asm volatile("s_waitcnt lgkmcnt(" #n ")" ::: "memory")
#define PJ_WL0 do { __builtin_amdgcn_s_waitcnt(0xC07F); asm volatile("" ::: "memory"); } while (0)
#define PJ_BAR __builtin_amdgcn_s_barrier()
#define PJ_SCHED __builtin_amdgcn_sched_barrier(0)
#define PJ_NOP do { PJ_SCHED; asm volatile("s_nop 7"); PJ_SCHED; } while (0)
#define PJ_DS(v, ai, bj, q) do { PJ_SCHED; __builtin_amdgcn_raw_buffer_store_b128(v, rc, vD + ((q) & 1) * 1024, dco + ((ai) * 4 + ((q) >> 1)) * 262144 + (bj) * 8192, 16); \
    asm volatile("s_nop 1"); PJ_SCHED; } while (0)
#define PJ_PARK(q) (*(u32x4*)(smem + C_PARK + wid * 4096 + vD + (q) * 1024))
#define PJ_PUB(ai, n) do { if (wid == 4) __builtin_amdgcn_raw_buffer_store_b32(FULL, rc, vD < (n) * 16 ? vD >> 2 : PJ_OOB, dfl + (ai) * 16, 16); } while (0)
#define PJ_ST()
#define PJ_TOUCH(ptr, bytes, vo, so) __builtin_amdgcn_raw_ptr_buffer_load_lds(RSRC(ptr, bytes), (LDSP void*)(smem + C_PARK + wid * 4096), 4, vo, so, 0, 0)
#define PJ_SL 0x1E
#define PJ_PT 0
#define PJ_WARM(k) do { int vt_ = vD; asm volatile("" : "+v"(vt_)); vt_ <<= 3; \
    if (u == 0 && (j & 3) != 3) { \
      const int i_ = (((x & 1) * 8 + (j >> 2)) * 2 + (j & 1)) * 8 + wid + (k) * 256; \
      const int nw_ = x < 2 ? 576 : 512, na_ = x < 2 ? 288 : x < 4 ? 256 : 144, no_ = (x >> 1) * 96 + 96; \
      const u64 w_ = (u64)(pB1 - cb * 256), a_ = x < 4 ? (u64)pA1 : (u64)(pA0 + 1024 * 576), o_ = (u64)p.Awo + (x < 2 ? 0 : x < 4 ? 96 : 288) * 8192; \
      PJ_TOUCH(i_ < nw_ ? w_ : i_ < nw_ + na_ ? a_ : o_, i_ < nw_ + na_ ? WOB : no_ * 8192, vt_, (i_ < nw_ ? i_ : i_ < nw_ + na_ ? i_ - nw_ : i_ - nw_ - na_) * 8192); \
    } else __builtin_amdgcn_raw_ptr_buffer_load_lds(rc, (LDSP void*)(smem + C_PARK + wid * 4096), 4, vt_, PJ_OOB, 0, 0); } while (0)
#define PJ_F0(i)
#define PJ_F1(i) do { if (S == 8) PJ_PACK(pa, 0, 0, i); } while (0)
#define PJ_F2(i) do { if (S == 8) PJ_PACK(pb, 0, 1, i); if (S == 0) PJ_PACK(pb, 1, 1, i); } while (0)
#define PJ_F3(i) do { if (S == 8) PJ_PACK(pa, 1, 0, i); if (S == 5 && (i) == 0) { PJ_SCHED; PJ_PREP(PJ_NEXT); PJ_SCHED; } } while (0)
#define PJ_STEP(SS) do { constexpr int S = SS; PJ_ST(); \
    PJ_LDB(B0, 0); PJ_SCHED; PJ_LDA(0); PJ_SA((bsel ^ 32768) + 16384, sa + 128 * k2); \
    PJ_WL(8); PJ_BAR; PJ_WL0; PJ_MMA(S == 0, 0, 0, B0, PJ_F0); PJ_BAR; PJ_SCHED; \
    if (S == 7) PJ_UNIT(PJ_NEXT); \
    PJ_LDB(B1, 1); PJ_SB(65536 + bsel, sb); \
    if (S == 0) { PJ_DS(pb[2], 0, 1, 2); PJ_DS(pb[3], 0, 1, 3); } \
    if ((PJ_SL >> S) & 1) PJ_WARM(PJ_PT + __builtin_popcount(PJ_SL & ((1 << S) - 1))); \
    PJ_BAR; PJ_WL0; PJ_MMA(S == 0, 0, 1, B1, PJ_F1); PJ_BAR; \
    PJ_LDA(16384); sa += 128; PJ_SA(bsel, sa); \
    if (S == 8) { PJ_SCHED; PJ_DEST(u, j & 3); _Pragma("unroll") for (int q = 0; q < 4; q++) PJ_PARK(q) = pa[q]; PJ_SCHED; } \
    PJ_BAR; PJ_WL0; PJ_MMA(S == 0, 1, 0, B0, PJ_F2); PJ_BAR; PJ_SCHED; \
    { u32x4 c0_, c1_; \
      if (S < 2) { c0_ = PJ_PARK(2 * S); c1_ = PJ_PARK(2 * S + 1); PJ_SCHED; } \
      PJ_SB(65536 + bsel + 16384, sb + 256); sb += 64 * 8192; \
      if (S == 8) { PJ_DS(pb[0], 0, 1, 0); PJ_DS(pb[1], 0, 1, 1); } \
      else if (S < 2) { PJ_DS(c0_, 0, 0, (2 * S) & 3); PJ_DS(c1_, 0, 0, (2 * S + 1) & 3); } \
      else if (S < 4) { PJ_DS(pa[(2 * S) & 3], 1, 0, (2 * S) & 3); PJ_DS(pa[(2 * S + 1) & 3], 1, 0, (2 * S + 1) & 3); } \
      else if (S < 6) { PJ_DS(pb[(2 * S) & 3], 1, 1, (2 * S) & 3); PJ_DS(pb[(2 * S + 1) & 3], 1, 1, (2 * S + 1) & 3); } } \
      \
    asm volatile("s_waitcnt vmcnt(%0)" :: "i"((S == 0 ? 10 : S < 6 || S == 8 ? 8 : 6) + ((PJ_SL >> S) & 1)) : "memory"); \
    PJ_BAR; \
    if (S == 2) PJ_PUB(0, 4); \
    if (S == 7) PJ_PUB(1, 4); \
    PJ_MMA(S == 0, 1, 1, B1, PJ_F3); PJ_BAR; \
    bsel ^= 32768; ao ^= 32768; bo ^= 32768; } while (0)
  f4 acc[2][2][4][2];
  bf8 At[4][2], B0[2][2], B1[2][2];
  u32x4 pa[4], pb[4];
  int bsel = 0, dco = PJ_OOB, dfl = PJ_OOB, u = 0;
  auto quick = [&](auto NBC, const int hf, const int ch) __attribute__((always_inline)) {
    constexpr int NB = decltype(NBC)::value, NS = 5 - NB;
#define PJ_FS(t) do { PJ_SA(((t) % NS) * 16384, hf * 128 * 1152 + (t) * 128); \
      _Pragma("unroll") for (int h_ = 0; h_ < NB; h_++) PJ_SB(NS * 16384 + (((t) % NS) * NB + h_) * 16384, (t) * 64 * 8192 + (ch + h_) * 256); PJ_SCHED; } while (0)
#pragma unroll
    for (int t = 0; t < NS - 1; t++) PJ_FS(t);
    bo += NS * 16384 - 65536;
#pragma unroll
    for (int t = 0; t < 9; t++) {
      PJ_ST();
      if (t == 8) PJ_WV(0); else if (NB == 2) PJ_WV(6); else if (t == 7) PJ_WV(4); else PJ_WV(8);
      PJ_BAR; PJ_SCHED;
      if (wr && t + NS - 1 < 9) PJ_FS(t + NS - 1);
      PJ_LDB(B0, 0); PJ_LDA((t % NS) * 16384);
      if (NB == 2) PJ_LDB(B1, 1);
      PJ_SCHED;
      bo += t == 8 ? 65536 - NS * 16384 - (8 % NS) * NB * 16384 : (t % NS == NS - 1 ? -(NS - 1) * NB * 16384 : NB * 16384);
      PJ_WL0;
      PJ_MMA(t == 0, 0, 0, B0, PJ_F0);
      if (NB == 2) PJ_MMA(t == 0, 0, 1, B1, PJ_F0);
      if (!wr && t + NS - 1 < 9) PJ_FS(t + NS - 1);
    }
    PJ_NOP;
    u32 b1;
    if (NB == 1 && job >= 0 && wid == 0) b1 = BEAT_LD(job);
#pragma unroll
    for (int q = 0; q < 4 * NB; q++) { const u32x4 v = PJ_CV(0, q >> 2, q & 3); PJ_DS(PJ_SW(v), hf, ch + (q >> 2), q & 3); }
    PJ_WV(0); PJ_BAR;
    if (NB == 1 && job >= 0 && wid == 0) confirm(p, smem, job, b1, wv);
    if (tid < 4) __hip_atomic_fetch_or(WSP(u32, dfl) + hf * 4 + tid, NB == 2 ? FULL : 4u << ch, __ATOMIC_RELAXED, __HIP_MEMORY_SCOPE_AGENT);
  };
  if (((j + 1) & 3) < 2) {
    PJ_PREP(0); PJ_UNIT(0); PJ_DEST(0, 0);
    quick(std::integral_constant<int, 1>{}, 0, j & 1);
    if ((j & 3) == 0) quick(std::integral_constant<int, 2>{}, 1, 0);
    if ((j & 3) == 0 || PJ_ORD != 0x1312) u = 2;
    dco = PJ_OOB; dfl = PJ_OOB; job = -1;
  }
#pragma unroll
  for (int a = 0; a < 16; a++) asm volatile("" : "=v"(acc[a >> 3][(a >> 2) & 1][a & 3][0]), "=v"(acc[a >> 3][(a >> 2) & 1][a & 3][1]));
#pragma unroll
  for (int a = 0; a < 4; a++) asm volatile("" : "=v"(pa[a]), "=v"(pb[a]));
  PJ_PREP(u); PJ_UNIT(u);
  const int cs = x < 4 && (j & 3) == 2 ? (x * 8 + (j >> 2)) * 128 : -1;
#pragma unroll
  for (int i = 0; i < 2; i++) pa[i] = __builtin_amdgcn_raw_buffer_load_b128(RSRC(p.BWo, WOB), (tid >> 3) * 1152 + (tid & 7) * 16, cs >= 0 ? (cs + i * 64) * 1152 + 1024 : PJ_OOB, 0);
  PJ_SCHED;
  PJ_SB(65536, 0); PJ_SA(0, sa + 128);
  PJ_SB(65536 + 16384, 256); PJ_SA(16384, sa + 128 + 128 * k2);
  PJ_SCHED;
#pragma unroll
  for (int i = 0; i < 2; i++) __builtin_amdgcn_raw_buffer_store_b128(pa[i], rc, (tid & 7) * 65536 + (tid >> 3) * 16, cs >= 0 ? O_WX + (cs + i * 64) * 16 : PJ_OOB, 16);
  PJ_SCHED;
  if (wr == 1) PJ_BAR;
  PJ_WV(6); PJ_BAR;
  u32 b1;
  if (job >= 0 && wid == 0) b1 = BEAT_LD(job);
  PJ_SCHED;
  PJ_SB(65536 + 32768, 64 * 8192); PJ_SA(32768, sa + 256);
  PJ_SB(65536 + 32768 + 16384, 64 * 8192 + 256);
  PJ_WV(6);
  if (job >= 0 && wid == 0) confirm(p, smem, job, b1, wv);
  PJ_BAR;
  if (tid == 256 && cs >= 0) AOR(SY_WX, 1u << (cs >> 7));
#pragma unroll
  for (int i = 0; i < PJ_PT; i++) PJ_WARM(i);
  sa += 256; sb = 128 * 8192;
#pragma unroll 1
  for (;; u = PJ_NEXT) {
    PJ_STEP(0); PJ_STEP(1); PJ_STEP(2); PJ_STEP(3); PJ_STEP(4); PJ_STEP(5);
    int more = k2 - 1024; asm volatile("" : "+s"(more));
    if (more) PJ_STEP(6);
    PJ_STEP(7); PJ_STEP(8);
    if (u == 1) break;
  }
  PJ_ST();
  PJ_NOP;
  const u32 left = __builtin_amdgcn_raw_buffer_load_b32(rc, wid ? PJ_OOB : vD < 128 ? SY_JOB(0) * 4 + vD * 4 : SY_INIT(0) * 4, 0, 16);
#define FSL 0
#define FWN 15
#define PJ_FD(v, ai, bj, q) do { PJ_DS(v, ai, bj, q); if (FSL) __builtin_amdgcn_s_sleep(FSL); if (FWN < 15) asm volatile("s_waitcnt vmcnt(%0)" :: "i"(FWN) : "memory"); } while (0)
  PJ_FD(pb[2], 0, 1, 2); PJ_FD(pb[3], 0, 1, 3);
#pragma unroll
  for (int q = 0; q < 4; q++) pb[q] = PJ_PARK(q);
#pragma unroll
  for (int q = 0; q < 4; q++) PJ_FD(pa[q], 1, 0, q);
#pragma unroll
  for (int q = 0; q < 4; q++) PJ_FD(pb[q], 0, 0, q);
#pragma unroll
  for (int q = 0; q < 4; q++) { const u32x4 v = PJ_CV(1, 1, q); PJ_FD(PJ_SW(v), 1, 1, q); }
  pre = -1;
  const int xp = __builtin_amdgcn_readlane(left, 8);
  if (__all(left >= (vD < 128 ? ~0u : 1u)) && vD == 0) pre = (int)__hip_atomic_fetch_add(HOTP(xp) + SY_QHEAD, 1u, __ATOMIC_RELAXED, __HIP_MEMORY_SCOPE_AGENT) + (xp & 0x2000);
  PJ_WV(0);
  if (wr == 0) PJ_BAR;
  PJ_BAR;
  PJ_PUB(0, 8);
  PJ_ST();
}

namespace fa {
constexpr float SCALE = 0.08838834764831845f;
constexpr int NW = 8, QBLK = 32, KVBLK = 64, QB = NW * QBLK, D = 128;
constexpr int SHM_V = KVBLK * D * 2, SHM_K = KVBLK * D * 2;
#define SBAR() __builtin_amdgcn_sched_barrier(0)
#define MF32(a, b, c) __builtin_amdgcn_mfma_f32_32x32x16_bf16(a, b, c, 0, 0, 0)
#define FMX3(a, b, c) __builtin_elementwise_maximum(__builtin_elementwise_maximum(a, b), c)
#define AT_PAD 0
#define EBAR 14
#define MX0 3
#define ATHR 8.f
#define fadd1(a, b) ((a) + (b))
#define fmul1(a, b) ((a) * (b))
__device__ __forceinline__ void attn_block(const char* __restrict__ Qp, const char* __restrict__ Kh, const char* __restrict__ Vh,
                                           char* __restrict__ Op, int P0, char* lds, const int wv, u32* qh, int& pre) {
  const int tid = TIDX, wid = wv, lane = tid & 63, r32 = lane & 31, hi = lane >> 5, grp = wid >> 2;
  const int NT = (P0 + QB) / KVBLK;
  const int rb = wid ^ 4;
  const int TL = NT - 4 + (rb >> 1);
  const int rem = 2 * (NT - TL) - 2 - grp;
  float l_reg = 0.f;
  f32x16 o[4] = {}, s0, s1, nm = {};
  int ko = lane * 16;
  int vo = 4 * SHM_K + ((lane >> 4) & 1) * (1024 + 128) + ((lane >> 1) & 1) * 512 + ((hi ^ ((lane >> 1) & 1)) << 6) + ((lane >> 2) & 3) * 16 + (lane & 1) * 8;
  const __amdgpu_buffer_rsrc_t rk = RSRC(Kh, (2 * NT - 1) * 262144 + 8192), rv = RSRC(Vh, (2 * NT - 1) * 262144 + 8192);
  const int gk = tid * 16, gv = gk ^ (((tid >> 5) & 3) << 6);
#define DMA(rs, go, dst, so) __builtin_amdgcn_raw_ptr_buffer_load_lds(rs, (LDSP void*)(lds + (dst) + wid * 1024), 16, go, so, 0, 0)
#define ISSUE_K(t) do { const int s_ = (t) * 524288, d_ = ((t) & 3) * SHM_K; DMA(rk, gk, d_, s_); DMA(rk, gk, d_ + 8192, s_ + 262144); } while (0)
#define ISSUE_V(t) do { const int s_ = (t) * 524288, d_ = 4 * SHM_K + ((t) & 3) * SHM_V; DMA(rv, gv, d_, s_); DMA(rv, gv, d_ + 8192, s_ + 262144); } while (0)
#define PHASE(n) do { SBAR(); asm volatile("s_waitcnt vmcnt(" #n ")" ::: "memory"); __builtin_amdgcn_s_barrier(); asm volatile("" ::: "memory"); SBAR(); } while (0)
#define LDK(d0) do { kf[(d0) % 3][0] = *(const bf8*)(lds + ko + (d0) * 1024); kf[(d0) % 3][1] = *(const bf8*)(lds + ko + (d0) * 1024 + 8192); } while (0)
#define LDV(i) do { const int o_ = ((i) & 3) * 2048 + (((i) >> 2) & 1) * 256 + ((i) >> 3) * 8192; vf[(i) & 3] = cat8(trrd(lds + vo + o_), trrd(lds + vo1 + o_)); } while (0)
#define AST(i)
#define AP(i)
  AP(0);
#define QK(NEXT) do { _Pragma("unroll") for (int d0 = 0; d0 < 8; ++d0) { \
      if (d0 + 2 < 8) LDK(d0 + 2); else { NEXT(2 * (d0 - 6)); NEXT(2 * (d0 - 6) + 1); } \
      SBAR(); \
      if (d0) { s0 = MF32(kf[d0 % 3][0], qr[d0], s0); s1 = MF32(kf[d0 % 3][1], qr[d0], s1); } \
      else { asm volatile("v_mfma_f32_32x32x16_bf16 %0, %1, %2, %3" : "+v"(s0) : "v"(kf[0][0]), "v"(qr[0]), "v"(nm)); \
             asm volatile("v_mfma_f32_32x32x16_bf16 %0, %1, %2, %3" : "+v"(s1) : "v"(kf[0][1]), "v"(qr[0]), "v"(nm)); } \
      SBAR(); } } while (0)
#define NOV(i)
#define MAXS(i) do { \
      if ((i) == 0) { mx = FMX3(s0[0], s0[1], s0[2]); my = FMX3(s1[0], s1[1], s1[2]); } \
      else if ((i) < 7) { mx = FMX3(mx, s0[2 * (i) + 1], s0[2 * (i) + 2]); my = FMX3(my, s1[2 * (i) + 1], s1[2 * (i) + 2]); } \
      else { mx = FMX3(mx, s0[15], s1[15]); mx = __builtin_elementwise_maximum(mx, my); asm volatile("" : "+v"(mx)); } } while (0)
  float mx, my;
  constexpr float C2 = 1.4426950408889634f * SCALE;
  bf8 qr[8], kf[3][2], vf[4], pa[4];
#pragma unroll
  for (int d0 = 0; d0 < 8; ++d0) qr[d0] = *(const bf8*)(Qp + rb * 262144 + d0 * 1024 + lane * 16);
  SBAR();
  ISSUE_K(0); ISSUE_K(1); ISSUE_V(0); ISSUE_K(2); ISSUE_V(1);
  SBAR();
  asm volatile("" : "=v"(s0), "=v"(s1), "+v"(nm), "+v"(o[0]), "+v"(o[1]), "+v"(o[2]), "+v"(o[3]));
  SBAR();
#pragma unroll
  for (int d0 = 0; d0 < 8; ++d0) {
    u32x4 q = __builtin_bit_cast(u32x4, qr[d0]);
#pragma unroll
    for (int i = 0; i < 4; i++) q[i] = pk2(__uint_as_float(q[i] << 16) * C2, __uint_as_float(q[i] & 0xFFFF0000u) * C2);
    qr[d0] = __builtin_bit_cast(bf8, q);
    asm volatile("" : "+v"(qr[d0]));
  }
  AP(8);
  PHASE(8);
  AP(1);
  if (grp) PHASE(6);
  LDK(0); LDK(1);
  QK(NOV);
#pragma unroll
  for (int i = 0; i < 8; ++i) MAXS(i);
  AP(2);
  PHASE(4);
  AP(3);
  auto tile = [&](const int t, auto LASTC) __attribute__((always_inline)) {
    constexpr bool LAST = decltype(LASTC)::value;
    AST(0);
    ISSUE_K(t + 3); ISSUE_V(t + 2);
    SBAR();
    if constexpr (LAST) {
      const int dq = r32 - 4 * hi;
      if (rb & 1) {
#pragma unroll
        for (int r = 0; r < 16; ++r) { if (dq < (r & 3) + 8 * (r >> 2)) s1[r] = -__builtin_inff(); SBAR(); }
      } else {
#pragma unroll
        for (int r = 0; r < 16; ++r) { if (dq < (r & 3) + 8 * (r >> 2)) s0[r] = -__builtin_inff(); s1[r] = -__builtin_inff(); SBAR(); }
      }
#pragma unroll
      for (int i = 0; i < 8; ++i) MAXS(i);
    }
    {
      if (__builtin_expect(t == 0 || __any(mx > ATHR), 0)) {
        auto rr = __builtin_amdgcn_permlane32_swap(__float_as_uint(mx), __float_as_uint(mx), false, false);
        mx = __builtin_elementwise_maximum(__uint_as_float(rr[0]), __uint_as_float(rr[1]));
        if (t) mx = __builtin_elementwise_maximum(mx, 0.f);
        const float alpha = t ? __builtin_amdgcn_exp2f(-mx) : 1.f, nn = nm[0] - mx;
        mx = -mx;
#pragma unroll
        for (int r = 0; r < 16; ++r) { s0[r] = fadd1(s0[r], mx); s1[r] = fadd1(s1[r], mx); nm[r] = nn; }
#pragma unroll
        for (int d = 0; d < 4; ++d)
#pragma unroll
          for (int r = 0; r < 16; ++r) o[d][r] = fmul1(o[d][r], alpha);
        l_reg *= alpha;
        asm volatile("" : "+v"(nm));
      }
    }
    AST(1);
    float x[32], sb = 0.f;
    u32 pw[16];
#pragma unroll
    for (int r = 0; r < 18; ++r) {
      SBAR();
      if (r < 16) { x[2 * r] = __builtin_amdgcn_exp2f(r < 8 ? s0[2 * r] : s1[2 * r - 16]); x[2 * r + 1] = __builtin_amdgcn_exp2f(r < 8 ? s0[2 * r + 1] : s1[2 * r - 15]); }
      if (r >= 2) {
        l_reg = fadd1(l_reg, x[2 * r - 4]);
        sb = r == 2 ? x[1] : fadd1(sb, x[2 * r - 3]);
        pw[r - 2] = pk2(x[2 * r - 4], x[2 * r - 3]); }
    }
    SBAR();
    l_reg = fadd1(l_reg, sb);
#pragma unroll
    for (int i = 0; i < 4; ++i) pa[i] = __builtin_bit_cast(bf8, (u32x4){pw[4 * i], pw[4 * i + 1], pw[4 * i + 2], pw[4 * i + 3]});
    asm volatile("" : "+v"(pa[0]), "+v"(pa[1]), "+v"(pa[2]), "+v"(pa[3]), "+v"(l_reg));
    ko ^= (t & 1) ? 3 * SHM_K : SHM_K;
    const int vo1 = vo ^ 128;
    SBAR();
    AST(3);
    if constexpr (!LAST) { LDK(0); LDK(1); } else { LDV(0); LDV(1); LDV(2); LDV(3); }
    AST(4);
    PHASE(4);
    AST(5);
    __builtin_amdgcn_s_setprio(1);
    if constexpr (!LAST) QK(LDV);
    AST(6);
#pragma unroll
    for (int i = 0; i < 16; ++i) {
      SBAR();
      o[i & 3] = MF32(vf[i & 3], pa[i >> 2], o[i & 3]);
      SBAR();
      if (i + 4 < 16) LDV(i + 4);
      if constexpr (!LAST) if (i >= MX0 && i < MX0 + 8) MAXS(i - MX0);
      if (AT_PAD && i == EBAR) __builtin_amdgcn_s_sleep(AT_PAD);
      if (i == EBAR && (!LAST || rem)) PHASE(4);
    }
    __builtin_amdgcn_s_setprio(0);
    vo ^= (t & 1) ? 3 * SHM_V : SHM_V;
    AST(7);
  };
#pragma unroll 1
  for (int t = 0; t < TL; t++) {
    tile(t, std::false_type{});
  }
  AP(4);
  tile(TL, std::true_type{});
  AP(5);
  ISSUE_V(TL + 3);
  SBAR();
  {
    const float inv = __builtin_amdgcn_rcpf(swadd(l_reg, l_reg));
    const __amdgpu_buffer_rsrc_t ro = RSRC(Op + rb * 262144, 8192);
#pragma unroll
    for (int i = 0; i < 8; ++i) {
      const int g = (i & 1) * 8;
#define ON(k) fmul1(o[i >> 1][g + (k)], inv)
      auto r0 = __builtin_amdgcn_permlane32_swap(pk2(ON(0), ON(1)), pk2(ON(4), ON(5)), false, false);
      auto r1 = __builtin_amdgcn_permlane32_swap(pk2(ON(2), ON(3)), pk2(ON(6), ON(7)), false, false);
#undef ON
      __builtin_amdgcn_raw_buffer_store_b128((u32x4){r0[0], r1[0], r0[1], r1[1]}, ro, r32 * 32 + hi * 16 + (i & 3) * 1024, (i >> 2) * 4096, 16);
      if (i == 3 && rem > 1) PHASE(6);
    }
  }
  AP(6);
  if (rem > 2) PHASE(10);
  if (rem > 3) { PHASE(8); PHASE(8); }
  CLAIM();
  VMW0();
  AP(7);
  __syncthreads();
}
#undef DMA
#undef ISSUE_K
#undef ISSUE_V
#undef PHASE
#undef LDK
#undef LDV
#undef QK
#undef NOV
#undef MAXS
}

__device__ __forceinline__ void wof_task(const Params& p, int kc, char* smem, const int wv, u32* qh, int& pre) {
  const int tid = TIDX, lane = tid & 63, wid = wv;
  const int l15 = lane & 15, lq = lane >> 4, q4 = l15 >> 2, p4 = lane & 3;
  const __amdgpu_buffer_rsrc_t rw = RSRC(WSP(u16, O_WOF), 2 * WOB);
  bf8 st[2][9];
#pragma unroll
  for (int m = 0; m < 2; m++)
#pragma unroll
    for (int i = 0; i < 9; i++) st[m][i] = *(const bf8*)((m ? p.BWo : p.Awo) + (size_t)kc * 64 * 576 + (tid + 512 * i) * 8);
  CLAIM();
#pragma unroll
  for (int m = 0; m < 2; m++) {
#pragma unroll
    for (int i = 0; i < 9; i++) {
      const int id = tid + 512 * i, row = id / 72, ch = id % 72;
      *(bf8*)(smem + row * 1280 + (((ch >> 1) ^ fsw(row)) << 5) + ((ch & 1) << 4)) = st[m][i];
    }
    asm volatile("s_waitcnt lgkmcnt(0)" ::: "memory"); __builtin_amdgcn_s_barrier(); asm volatile("" ::: "memory");
#pragma unroll
    for (int i = 0; i < 9; i++) {
      const int f = wid * 9 + i, nt = f >> 1, k2 = f & 1;
      const int r0 = k2 * 32 + lq * 8 + q4, r1 = r0 + 4;
      bf4 lo = trrd(smem + r0 * 1280 + ((nt ^ fsw(r0)) << 5) + p4 * 8);
      bf4 hi = trrd(smem + r1 * 1280 + ((nt ^ fsw(r1)) << 5) + p4 * 8);
      __builtin_amdgcn_raw_buffer_store_b128(__builtin_bit_cast(u32x4, cat8(lo, hi)), rw, ((nt * 128 + kc * 2 + k2) * 64 + lane) * 16, m * WOB, 16);
    }
    if (m == 0) { asm volatile("s_waitcnt lgkmcnt(0)" ::: "memory"); __builtin_amdgcn_s_barrier(); asm volatile("" ::: "memory"); }
  }
  VMW0();
  __syncthreads();
}

template <int MT>
__device__ __forceinline__ void out_tile(const char* A, const char* W, u16* C, char* smem, const int wv, u32* qh, int& pre) {
  constexpr int U = 32 / MT, SC = U / 2, DP = MT / 4, NCH = 4 * MT, CW = 32768 / MT;
  const int tid = TIDX, lane = tid & 63, wid = wv;
  const int l15 = lane & 15, lq = lane >> 4, kh = wid >> 2, wc = wid & 3;
  const __amdgpu_buffer_rsrc_t ra = RSRC(A, MT / 2 * 262144), rw = RSRC(W + wc * 3 * 131072 + kh * 1024, 3 * 131072 - kh * 1024);
  const int va = tid * 16, vw = lane * 16;
  int ao = kh * 2048 + (lq >> 1) * 1024 + l15 * 32 + (lq & 1) * 16;
  f4 acc[MT][3];
  bf8 wf[U][3], af[2][MT];
#define OT_DMA(i, buf, so) __builtin_amdgcn_raw_ptr_buffer_load_lds(ra, (LDSP void*)(smem + (buf) + (i) * 8192 + wid * 1024), 16, va, \
                                                                    (so) + (MT == 4 ? ((i) >> 1) * 262144 + ((i) & 1) * 8192 : (i) * 262144), 0, 0)
#define OT_LDA(d, base, off) do { _Pragma("unroll") for (int m = 0; m < MT; m++) af[d][m] = *(const bf8*)(smem + (base) + (off) + (m >> 1) * 2 * CW + (m & 1) * 512); } while (0)
#define OT_VM(n) __builtin_amdgcn_s_waitcnt(0x0F70 | ((n) & 15) | (((n) >> 4) << 14))
#define OT_PAIR(buf, so) do { _Pragma("unroll") for (int i = 0; i < 8; i++) OT_DMA(i & 3, (buf) + (i >> 2) * 32768, (so) + (i >> 2) * 2 * CW); SB0(); } while (0)
  if constexpr (MT == 4) {
  OT_PAIR(0, 0);
#pragma unroll
  for (int u = 0; u < U; u++) {
    if (u == U - 1) OT_PAIR(65536, 4 * CW);
#pragma unroll
    for (int j = 0; j < 3; j++) wf[u][j] = __builtin_bit_cast(bf8, __builtin_amdgcn_raw_buffer_load_b128(rw, vw, u * 2048 + j * 131072, 0));
    SB0();
  }
#pragma unroll
  for (int m = 0; m < MT; m++)
#pragma unroll
    for (int j = 0; j < 3; j++) acc[m][j] = (f4){0, 0, 0, 0};
  asm volatile("s_waitcnt vmcnt(%0)" :: "i"(U * 3 + 8) : "memory"); __builtin_amdgcn_s_barrier(); asm volatile("" ::: "memory");
  OT_LDA(0, ao, 0);
  int bs = 0;
#pragma unroll 1
  for (int c = 0; c < NCH; c += 2) {
    const int sw = c < NCH - 2 ? (c + 2) * CW : PJ_OOB, sd = c < NCH - 4 ? (c + 4) * 2 * CW : PJ_OOB;
#pragma unroll
    for (int u = 0; u < U; u++) {
      SB0();
      if (u < U - 1) OT_VM(3 * (U - 1) + 8); else OT_VM(3 * (U - 1));
      if (u == U - 1) {
        asm volatile("s_waitcnt lgkmcnt(0)" ::: "memory"); __builtin_amdgcn_s_barrier(); asm volatile("" ::: "memory");
        ao ^= 65536;
      }
      OT_LDA((u + 1) & 1, ao, u == U - 1 ? 0 : ((u + 1) / SC) * 32768 + ((u + 1) % SC) * 4096);
      SB0();
      if (u == U - 1) OT_PAIR(bs, sd);
#pragma unroll
      for (int j = 0; j < 3; j++) {
#pragma unroll
        for (int m = 0; m < MT; m++) acc[m][j] = MFMA(wf[u][j], af[u & 1][m], acc[m][j]);
        SB0();
        wf[u][j] = __builtin_bit_cast(bf8, __builtin_amdgcn_raw_buffer_load_b128(rw, vw, sw + u * 2048 + j * 131072, 0));
        SB0();
      }
    }
    bs ^= 65536;
  }
  } else {
#pragma unroll
  for (int i = 0; i < 4; i++) OT_DMA(i, 0, 0);
  SB0();
#pragma unroll
  for (int u = 0; u < U; u++) {
#pragma unroll
    for (int d = 0; d < DP; d++) OT_DMA((u % SC) * DP + d, 32768 + (u / SC) * 32768, (1 + u / SC) * 2 * CW);
#pragma unroll
    for (int j = 0; j < 3; j++) wf[u][j] = __builtin_bit_cast(bf8, __builtin_amdgcn_raw_buffer_load_b128(rw, vw, u * 2048 + j * 131072, 0));
    SB0();
  }
#pragma unroll
  for (int m = 0; m < MT; m++)
#pragma unroll
    for (int j = 0; j < 3; j++) acc[m][j] = (f4){0, 0, 0, 0};
  asm volatile("s_waitcnt vmcnt(%0)" :: "i"(U * (DP + 3)) : "memory"); __builtin_amdgcn_s_barrier(); asm volatile("" ::: "memory");
  OT_LDA(0, ao, 0);
  int bs = 0;
#pragma unroll 1
  for (int c = 0; c < NCH; c += 2) {
    const int sw = c < NCH - 2 ? (c + 2) * CW : PJ_OOB;
#pragma unroll
    for (int u = 0; u < U; u++) {
      SB0();
      __builtin_amdgcn_s_waitcnt(MT == 4 ? 0x4F7C : 0x0F7F);
      if (u % SC == SC - 1) { asm volatile("s_waitcnt lgkmcnt(0)" ::: "memory"); __builtin_amdgcn_s_barrier(); asm volatile("" ::: "memory"); }
      if (u == U - 1) ao ^= 65536;
      OT_LDA((u + 1) & 1, ao, u == U - 1 ? 0 : ((u + 1) / SC) * 32768 + ((u + 1) % SC) * 4096);
      SB0();
      const int cn = c + 3 + u / SC;
#pragma unroll
      for (int j = 0; j < 3; j++) {
#pragma unroll
        for (int m = 0; m < MT; m++) {
          acc[m][j] = MFMA(wf[u][j], af[u & 1][m], acc[m][j]);
          if (j == 0 && (m & 3) == 1) { SB0(); OT_DMA((u % SC) * DP + (m >> 2), u < SC ? (bs ^ 65536) + 32768 : bs, cn < NCH ? cn * 2 * CW : PJ_OOB); SB0(); }
        }
        SB0();
        wf[u][j] = __builtin_bit_cast(bf8, __builtin_amdgcn_raw_buffer_load_b128(rw, vw, sw + u * 2048 + j * 131072, 0));
        SB0();
      }
    }
    bs ^= 65536;
  }
  }
  CLAIM();
  VMW0C();
  asm volatile("s_waitcnt lgkmcnt(0)" ::: "memory"); __builtin_amdgcn_s_barrier(); asm volatile("" ::: "memory");
#pragma unroll
  for (int m = 0; m < MT / 2; m++)
#pragma unroll
    for (int j = 0; j < 3; j++) *(f4*)(smem + (m * 3 + j) * 8192 + va) = kh ? acc[m][j] : acc[MT / 2 + m][j];
  __syncthreads();
#pragma unroll
  for (int m = 0; m < MT / 2; m++) {
    f4 t[3];
#pragma unroll
    for (int j = 0; j < 3; j++) { const f4 o = *(const f4*)(smem + (m * 3 + j) * 8192 + (va ^ 4096)); t[j] = kh ? o + acc[MT / 2 + m][j] : acc[m][j] + o; }
    u16* c = C + (size_t)(kh * MT * 8 + m * 16 + l15) * 1152 + wc * 48;
    *(u32x4*)(c + (lq & 1) * 16 + (lq >> 1) * 8) = pair16(pk2(t[0][0], t[0][1]), pk2(t[0][2], t[0][3]), pk2(t[1][0], t[1][1]), pk2(t[1][2], t[1][3]));
    *(u32x2*)(c + 32 + lq * 4) = (u32x2){pk2(t[2][0], t[2][1]), pk2(t[2][2], t[2][3])};
  }
  __syncthreads();
#undef OT_DMA
#undef OT_LDA
}

#define CLAIMS() do { const int l_ = TIDX; if (l_ < 64) { \
    const u32 v_ = ld4_sc1(hot + SY_OQ(l_ < 6 ? l_ : 0)); \
    if (l_ == 0) pre = (int)__hip_atomic_fetch_add(hot + SY_OQ(6), 1u, __ATOMIC_RELAXED, __HIP_MEMORY_SCOPE_AGENT); \
    pre |= (int)((u32)__ballot(l_ < 6 && v_ < ((l_ & 1) ? NOB : NOA)) << 16); } } while (0)
#define CLS 51
#define PRB 0
#define PRC 112
#define TPS 2
__device__ __forceinline__ void small_out_task(const Params& p, int rq, int cp, char* smem, const int wv, u32* hot, int& pre) {
  const int tid = TIDX, lane = tid & 63, wid = wv;
  const int l15 = lane & 15, lq = lane >> 4;
  const __amdgpu_buffer_rsrc_t rw = RSRC(WSP(char, O_WOF) + WOB, WOB), ro = RSRC(WSP(u16, O_OB), 2048 * 4096 * 2);
  if (tid == 0) {
    int it = 0;
    while (ld4_sc1(hot + SY_CW) < 64u && ++it < SPIN_CAP) __builtin_amdgcn_s_sleep(8);
  }
  __syncthreads();
  bf8 wf[8][4];
#pragma unroll
  for (int k = 0; k < 8; k++)
#pragma unroll
    for (int j = 0; j < 4; j++)
      wf[k][j] = __builtin_bit_cast(bf8, __builtin_amdgcn_raw_buffer_load_b128(rw, (l15 >> 3) * 1024 + (lq * 16 + (l15 & 7)) * 16 + (j & 1) * 128,
                                                                              ((cp * 2 + (j >> 1)) * 128 + wid * 16 + k * 2) * 1024, 16));
  const int va = OB_OFF(rq * 8 + (l15 & 7), wid * 512 + (l15 >> 3) * 32 + lq * 8);
  const int wo = (lq >> 1) == (l15 >> 3) ? (wid * 2 + (lq >> 1)) * 272 + (l15 & 7) * 32 + (lq & 1) * 16 : 4 * 4352 + lane * 16;
  const int rd = (lane >> 4) * 4352 + (lane & 7) * 544 + wid * 32 + ((lane >> 3) & 1) * 16;
  int rdy = 0;
  bool look = false;
  u32 left = 0u;
  auto step = [&](u32x4 (&af)[8], u32x4 (&nf)[8], const int s) __attribute__((always_inline)) {
    if (s >= rdy) {
      if (s < TG0) {
        if (tid < 64) {
          int it = 0;
          u64 m;
          while (m = __ballot(ld4_sc1(hot + SY_CS(lane < s || it ? s : lane)) == 0u || lane > (it ? s : TG0 - 1)), (m >> s & 1) && ++it < SPIN_CAP)
            __builtin_amdgcn_s_sleep(SPS);
          *(int*)(smem + 40960) = __builtin_ctzll(m);
        }
        asm volatile("s_waitcnt lgkmcnt(0)" ::: "memory"); __builtin_amdgcn_s_barrier(); asm volatile("" ::: "memory");
        rdy = __builtin_amdgcn_readfirstlane(*(const int*)(smem + 40960));
      }
      if (s == 63) TLS(1);
      int it = 0;
      while (true) {
        while (PRB && look) {
          CFENCE();
          const u32 v = __builtin_amdgcn_raw_buffer_load_b32(ro, lane < 4 ? OB_OFF(rq * 8, wid * 512 + lane * 128 + PRC) : PJ_OOB, s * 262144, 16);
          if (!__any(v == ~0u) || ++it >= SPIN_CAP) break;
          __builtin_amdgcn_s_sleep(PRB - 1);
        }
        CFENCE();
#pragma unroll
        for (int k = 0; k < 8; k++) af[k] = __builtin_amdgcn_raw_buffer_load_b128(ro, va, s * 262144 + k * 4096, 16);
        if (s < TG0) break;
        u32 bad = 0u;
#pragma unroll
        for (int k = 0; k < 8; k++) bad = umax(umax(bad, af[k][0]), af[k][2]);
        if (!__any(bad == ~0u) || ++it >= SPIN_CAP) break;
        look = true;
        __builtin_amdgcn_s_sleep(TPS);
      }
    }
    SB0();
    if (s == CLS) left = __builtin_amdgcn_raw_buffer_load_b32(RSRC(hot, wid ? 0 : 8192), SY_OQ(lane < 6 ? lane : 6) * 4, 0, 16);
    const int nb = s + 1 < rdy ? (s + 1) * 262144 : PJ_OOB;
#pragma unroll
    for (int k = 0; k < 8; k++) nf[k] = __builtin_amdgcn_raw_buffer_load_b128(ro, va, nb + k * 4096, 16);
    SB0();
    f4 acc[4];
#pragma unroll
    for (int k = 0; k < 8; k++)
#pragma unroll
      for (int j = 0; j < 4; j++) acc[j] = MFMA(wf[k][j], __builtin_bit_cast(bf8, af[k]), k ? acc[j] : ((f4){0, 0, 0, 0}));
    char* const sm = smem + (s & 1) * 20480;
#pragma unroll
    for (int j = 0; j < 4; j++) *(f4*)(sm + wo + ((lq >> 1) == (l15 >> 3) ? j * 4352 : 0)) = acc[j];
    asm volatile("s_waitcnt lgkmcnt(0)" ::: "memory"); __builtin_amdgcn_s_barrier(); asm volatile("" ::: "memory");
    if (s == 63) TLS(2);
    f4 t = *(const f4*)(sm + rd) + *(const f4*)(sm + rd + 272);
#pragma unroll
    for (int r = 0; r < 4; r++) {
      t[r] += __uint_as_float(__builtin_amdgcn_mov_dpp(__float_as_uint(t[r]), 0xB1, 0xf, 0xf, true));
      t[r] += __uint_as_float(__builtin_amdgcn_mov_dpp(__float_as_uint(t[r]), 0x4E, 0xf, 0xf, true));
      t[r] += __uint_as_float(__builtin_amdgcn_mov_dpp(__float_as_uint(t[r]), 0x141, 0xf, 0xf, true));
    }
    __builtin_amdgcn_raw_buffer_store_b64((u32x2){pk2(t[0], t[1]), pk2(t[2], t[3])}, RSRC(p.out, 2048 * 2304), (lane & 7) == 0 ? lane : PJ_OOB,
                                          (s * 32 + rq * 8 + wid) * 2304 + 1152 + cp * 64, 0);
  };
  u32x4 fa[8], fb[8];
#pragma unroll
  for (int k = 0; k < 8; k++) asm volatile("" : "=v"(fa[k]));
#pragma unroll 1
  for (int s = XS0; s < 64; s += 2) { step(fa, fb, s); step(fb, fa, s + 1); }
  pre = 72;
  if (__any(left < (lane > 5 ? 72u : (lane & 1) ? NOB : NOA))) CLAIMS();
}
#define PSTAMP()
#define STAMP(i)
#define YEND()
#define ANS 16
#define ANX(slot) (X_ANN ^ xp ^ ((slot) * X_HS))
#define KSL 1
#define DSH 2
#define K1 14
#define K3 11
#define DOMAX 90
#define DO0 80
#define DH0 54
#define DOCAP 6
#define C_IMG 0
#define C_QT 9216
#define C_KT (C_QT + 3072)
#define C_P (C_KT + 4096)
#define C_PX (C_P + 768)
#define C_KX (C_PX + 2560)
#define C_VX (C_KX + 8192)
#define C_QX (C_VX + 8192)
#define C_XP 161024
#define OBE 0
#define CALR 2
#define GPS 6
#define C_WL 49920
__device__ __forceinline__ float ror8(float v) {
  return __uint_as_float(__builtin_amdgcn_mov_dpp(__float_as_uint(v), 0x128, 0xf, 0xf, true));
}
__device__ __forceinline__ float rb1(float f) { return __uint_as_float(pk2(0.f, f)); }
#define FMX(a, b) __builtin_elementwise_maximum(a, b)
__device__ __forceinline__ float tmax(float mx) {
  mx = FMX(mx, ror8(mx));
  { auto t = __builtin_amdgcn_permlane16_swap(__float_as_uint(mx), __float_as_uint(mx), false, false);
    mx = FMX(__uint_as_float(t[0]), __uint_as_float(t[1])); }
  { auto t = __builtin_amdgcn_permlane32_swap(__float_as_uint(mx), __float_as_uint(mx), false, false);
    mx = FMX(__uint_as_float(t[0]), __uint_as_float(t[1])); }
  return mx;
}
__device__ __forceinline__ int hmax(int x) {
  x = max(x, __builtin_amdgcn_mov_dpp(x, 0x128, 0xf, 0xf, true));
  x = max(x, __builtin_amdgcn_mov_dpp(x, 0x124, 0xf, 0xf, true));
  x = max(x, __builtin_amdgcn_mov_dpp(x, 0x4E, 0xf, 0xf, true));
  x = max(x, __builtin_amdgcn_mov_dpp(x, 0xB1, 0xf, 0xf, true));
  { auto t = __builtin_amdgcn_permlane16_swap((u32)x, (u32)x, false, false); x = max((int)t[0], (int)t[1]); }
  return x;
}
typedef __attribute__((ext_vector_type(2))) unsigned long long u64x2;
__device__ __forceinline__ f4 zero4() {
  u64 a, b; asm volatile("v_mov_b64 %0, 0\n\tv_mov_b64 %1, 0\n\ts_nop 1" : "=v"(a), "=v"(b));
  return __builtin_bit_cast(f4, (u64x2){a, b});
}
__device__ __forceinline__ u32x4 ones4() {
  u64 a, b; asm volatile("v_mov_b64 %0, -1\n\tv_mov_b64 %1, -1" : "=v"(a), "=v"(b));
  return __builtin_bit_cast(u32x4, (u64x2){a, b});
}
#define BAR() do { asm volatile("s_waitcnt lgkmcnt(0)" ::: "memory"); __builtin_amdgcn_s_barrier(); asm volatile("" ::: "memory"); } while (0)
#define BARC() do { asm volatile("" ::: "memory"); __builtin_amdgcn_s_waitcnt(0xC07F); __builtin_amdgcn_s_barrier(); asm volatile("" ::: "memory"); } while (0)
#define BARN() do { asm volatile("" ::: "memory"); __builtin_amdgcn_s_barrier(); asm volatile("" ::: "memory"); } while (0)
#define IDX() const int tid = TIDX, lane = tid & 63, l15 = lane & 15, lq = lane >> 4, q4 = l15 >> 2, p4 = lane & 3, hi8 = l15 >> 3; \
    (void)q4; (void)p4; (void)hi8; (void)lq
#define AD_LD(HEAD) __builtin_amdgcn_raw_buffer_load_b32(rsX, (TIDX & (HEAD ? 63 : 31)) * ANS, ANX(s & 1), 16)
#define AD_GET(HEAD, K, CAP) do { int n_ = 0; u32 an = AD_LD(HEAD); \
    while ((u32)__ballot((an & 63u) != (u32)s) != 0u && ++n_ < (CAP)) { __builtin_amdgcn_s_sleep(1); CFENCE(); an = AD_LD(HEAD); } \
    if (!HEAD && c == 0 && s > 0 && n_ < (CAP)) __builtin_amdgcn_raw_buffer_store_b32(1u, rsX, 0, X_HOT(xp) + SY_CS(s - 1) * 4, 16); \
    const u32 r_ = (u32)__builtin_amdgcn_readfirstlane((int)an); \
    const int m_ = hmax((TIDX & 32) ? (int)umin(an, DOMAX * 64u) : (int)(an - r_)); \
    dl = ((r_ + (u32)__builtin_amdgcn_readlane(m_, 0)) & ~63u) + (u32)(K) * 64u + (HEAD ? (u32)__builtin_amdgcn_readlane(m_, 32) : 0u); } while (0)
#define AD_SPIN() do { int n_ = 160; while ((int)(((u32)wall_clock64() << 6) - dl) < 0 && --n_ > 0) {} } while (0)
#define STG_AUX 0
#define OSTG_AUX 0
#define STG_D 2
#define STG_POLL 7
#define STS(c, i)

#define DPR(OP, N) OP " %3, %3, %3 row_ror:" #N " row_mask:0xf bank_mask:0xf\n\t" OP " %4, %4, %4 row_ror:" #N " row_mask:0xf bank_mask:0xf\n\t"
#define DPROW(OP) "s_nop 0\n\t" DPR(OP, 8) "s_nop 0\n\t" DPR(OP, 4) "s_nop 0\n\t" DPR(OP, 2) "s_nop 0\n\t" DPR(OP, 1)
#define NXA 0
#define NXE 0
#define ADE 1
#define NXH 0
#define NXV 1
#define NXB 4
#define NXS 3
#define BJ 2
#define KSP 0
#define SP1 2
#define SP2 0
#define KP 3
#define GX 1
#define GXO (GX == 1 ? 4096 : 1024)
#define ZE 0x0D
#define KD 0
#define QSL 0
#define DJ 9
#define VSL 0
#define VSL1 4
#define KV0 1
#define PVP 0
#define IPR 3
#define DSLOT 0x9876E1CBAD0ull
#define F_K (44288 - C_KT)
#define F_Q (47872 - 768 - C_QT)
#define HAL 2
#define OAL 2
#define ALIGN(n) asm volatile(".p2align %0" :: "i"(n))
#define QXP 4
#define RORD 0
#define PRIO(n) __builtin_amdgcn_s_setprio(n)
#define LSL 6
__device__ __forceinline__ int chain_head(const Params& p, char* smem, const int h, const int wv, const bool spec) {
  STS(h, 1);
  TLS(12);
  const int wid = wv;
  u32x8 w[2][9], wx[3];
  const __amdgpu_buffer_rsrc_t rsX = RSRC(XWIN(p), X_END);
  const __amdgpu_buffer_rsrc_t rsB = RSRC(WSP(u16, O_QB), 4 * ACB);
  const int cb = h >> 1;
  const int nvt = (wid & 3) == 0 ? 0 : (wid < 3 ? 2 : 1);
  const int vt = wid < 3 ? wid * 2 + 2 : (wid == 3 ? 0 : wid - 4);
  const int wl = C_WL + (nvt == 1 ? vt * 12288 : 49152 + (nvt == 2 ? wid - 1 : wid >> 2) * 30720);
  int xp;
  {
    IDX();
    u32x4 st[54], nk, nv, nq;
    u32 fl, fk;
    const int vw = ((tid >> 4) * 4096 + h * 128 + (tid & 15) * 8) * 2;
#define NTL 64
#define AH 1
    u32 tc[9], b1, old = 0u;
#define NTE 8
#define TEC 1
    const u16 *pq = p.BWq, *pk = p.BWk, *pv = p.BWv;
    asm volatile("" : "+s"(pq), "+s"(pk), "+s"(pv));
    const bool te = wid < 3 && (h & 3) == TEC;
    const int tm = te ? wid : (h & 3) - 1;
    const u64 pt = tm == 1 ? (u64)pk : tm == 2 ? (u64)pv : (u64)pq;
    const __amdgpu_buffer_rsrc_t rt = RSRC(pt, te || (tm && h != 31 && wid == 7) ? 576 * 4096 * 2 : 0);
    const int tv = lane < (te ? NTE : NTL) ? lane * 8192 : PJ_OOB, ts_ = te ? (h >> 2) * 8 * 8192 + 8064 : (h >> 2) * 1024 + 384;
    const __amdgpu_buffer_rsrc_t rq = RSRC(pq, 576 * 4096 * 2), rk = RSRC(pk, 576 * 4096 * 2), rv = RSRC(pv, 576 * 4096 * 2);
    auto req = [&](int kc) {
#pragma unroll
      for (int i = 0; i < 6; i++) {
        st[kc * 6 + i] = __builtin_amdgcn_raw_buffer_load_b128(i < 2 ? rq : (i < 4 ? rk : rv), vw, (kc * 64 + (i & 1) * 32) * 8192, STG_AUX);
        if (NTL && kc + AH < 9 && i == (kc ? 0 : 4)) { SB0(); tc[kc] = __builtin_amdgcn_raw_buffer_load_b32(rt, tv, (kc + AH) * 64 * 8192 + ts_, 0); }
      }
      SB0();
    };
#pragma unroll
    for (int kc = 0; kc < STG_D; kc++) { if (kc == 0) TLS(13); req(kc); __builtin_amdgcn_s_barrier(); SB0(); }
    TLS(14);
    auto cell = [](int row, int slot) { return (((row * 2 + slot) ^ ((((row >> 2) ^ (row >> 4)) & 1) << 2) ^ ((row >> 5) & 1)) << 5); };
    auto put = [&](int unit, int row, const u32x4& v) { *(u32x4*)(smem + unit * 4160 + cell(row, (tid >> 1) & 1) + (tid & 1) * 16) = v; };
    auto frag = [&](int tile) {
      bf4 tt[4];
#pragma unroll
      for (int kk = 0; kk < 4; kk++) {
        const int r0 = (kk >> 1) * 32 + (lq >> 1) * 16 + (q4 >> 1) * 8 + (lq & 1) * 4 + (kk & 1) * 2 + (q4 & 1);
        tt[kk] = trrd(smem + (tile >> 1) * 4160 + cell(r0, tile & 1) + p4 * 8);
      }
      return ilv(tt[0], tt[1], tt[2], tt[3]);
    };
    auto park = [&](int slot, const u32x8& f) {
      char* d = smem + wl + slot * 2048 + lane * 16;
      *(u32x4*)d = (u32x4){f[0], f[1], f[2], f[3]};
      *(u32x4*)(d + 1024) = (u32x4){f[4], f[5], f[6], f[7]};
    };
    const __amdgpu_buffer_rsrc_t rs0 = RSRC(p.ws, wid ? 0 : O_QA);
    auto poll = [&]() {
      fl = __builtin_amdgcn_raw_buffer_load_b32(rs0, (lane < 32 ? SY_INIT(lane) : SY_ARR) * 4, 0, 16);
      fk = __builtin_amdgcn_raw_buffer_load_b32(rs0, SY_BLK(lane < 12 ? lane >> 2 : 0, cb, lane & 3) * 4, 0, 16);
    };
    auto rows = [&](int vo) {
      nq = __builtin_amdgcn_raw_buffer_load_b128(rsB, vo, h * 8192, 16);
      nk = __builtin_amdgcn_raw_buffer_load_b128(rsB, vo, h * 8192 + ACB, 16);
      nv = __builtin_amdgcn_raw_buffer_load_b128(rsB, vo, h * 8192 + 2 * ACB, 16);
    };
    bool early = false;
    int lost = 0;
#pragma unroll
    for (int kc = 0; kc < 9; kc++) {
      if (kc == STG_D - 1) { if (spec && wid == 0) old = confirm(p, smem, h, b1, wv); SB0(); }
      if (kc == 10 - STG_D) { early = __all(fk >= FULL); CFENCE(); rows(early ? tid * 16 : PJ_OOB); poll(); SB0(); }
#pragma unroll
      for (int i = 0; i < 6; i++) put((i >> 1) * 4 + ((tid & 15) >> 2), (i & 1) * 32 + (tid >> 4), st[kc * 6 + i]);
      if (NTL && kc + AH < 9) asm volatile("" :: "v"(tc[kc]));
      if (kc == 2 * STG_D - 1) lost = (__builtin_amdgcn_readfirstlane(old) >> h) & 1;
      BAR();
      if (kc == 0) TLS(15);
      if (kc == 0) { b1 = __builtin_amdgcn_raw_buffer_load_b32(rs0, 0, SY_BEAT(h & 7) * 4, 16); SB0(); }
      if (kc + STG_D == 8) { fk = ld4_sc1(WSP(u32, 0) + SY_BLK(lane < 12 ? lane >> 2 : 0, cb, lane & 3)); SB0(); }
      if (kc + STG_D < 9) req(kc + STG_D);
      w[0][kc] = frag(wid * 2);
      w[1][kc] = frag(nvt == 2 && kc < 4 ? 16 + vt : wid * 2 + 1);
      const u32x8 tp = frag(nvt == 0 ? 21 + (wid >> 1) : (nvt == 2 && kc < 4 ? wid * 2 + 1 : 16 + vt));
      if (kc >= 4 && kc < 7) { wx[kc - 4] = tp; if (nvt == 0) park(6 + kc, tp); }
      else park(nvt == 0 ? 6 + kc : (kc >= 7 ? kc - 7 : 2 + kc), tp);
      BAR();
      if (kc == 0 || kc == 8) STS(h, kc + 3);
    }
    TLS(6);
    if (wid == 0) {
      int it = 0;
      while (!lost && !__all(fl >= (lane < 32 ? 1u : 256u) && fk >= FULL) && ++it < SPIN_CAP) { BEAT_ON(it); __builtin_amdgcn_s_sleep(GPS); CFENCE(); poll(); }
      if (lane == 0) *(int*)(smem + C_XP) = lost ? -1 : fl & 0x7800;
    }
    BAR();
    if (!early) rows(tid * 16);
    xp = __builtin_amdgcn_readfirstlane(*(const int*)(smem + C_XP));
    if (xp < 0) return xp;
    TLS(7);
    SB0();
    *(u32x4*)(smem + C_QX + tid * 16) = nq;
    *(u32x4*)(smem + C_KX + tid * 16) = nk;
    *(u32x4*)(smem + C_VX + ((tid * 16) ^ ((tid & 32) << 2))) = nv;
  }
  STS(h, 14);
  constexpr float CE = 0.08838834764831845f * 1.4426950408889634f;
  auto run = [&](auto RC) __attribute__((always_inline)) {
  constexpr int R = decltype(RC)::value;
  constexpr int NV = R < 2 ? 0 : (R < 4 ? 1 : 2);
  constexpr bool QW = !(R & 1);
  int known = 0;
  bf8 pre[4][2], vx[NV ? NV : 1];
  int sidx, mo, io, eo, gvo, wvo = 0, so = 0, po = 0, qo = 0, ko = 0, ko1 = 0, fo = 0, fq = 0, fk = 0;
  float mk[4], mm = 0.f, cv = CE;
#define LDW(dst, slot) do { dst[0] = *(const bf8*)(smem + wvo + (slot) * 2048); dst[1] = *(const bf8*)(smem + wvo + (slot) * 2048 + 1024); } while (0)
  {
    IDX();
    if constexpr (R == 1) {
      po = (tid * 16) ^ ((tid & 32) << 2);
      so = (lane >> 5) * 131072 + (lane & 31) * 16;
      asm volatile("" : "+v"(po), "+v"(so));
    } else if constexpr (R == 0) {
      qo = C_QT + lq * 384 + q4 * 32 + p4 * 8; ko = C_KT + ((((lq * 8 + q4) * 32) ^ ((lq & 1) << 7)) + p4 * 8); ko1 = C_KT + ((((lq * 8 + 4 + q4) * 32) ^ ((lq & 1) << 7)) + p4 * 8);
      po = C_P + l15 * 16 + lq * 4;
      fo = lq * 512 + l15 * 16;
#pragma unroll
      for (int r = 0; r < 4; r++) { mk[r] = lq * 4 + r <= l15 ? 0.f : -__builtin_inff(); asm volatile("" : "+v"(mk[r])); }
      mm = (l15 & 2) ? -__builtin_inff() : 0.f;
      asm volatile("" : "+v"(mm), "+v"(cv));
      fq = C_QT + F_Q + (lq ? lq : 1) * 768 + lq * 8; fk = C_KT + F_K + lq * 1032;
      asm volatile("" : "+v"(qo), "+v"(ko), "+v"(ko1), "+v"(po), "+v"(fo), "+v"(fq), "+v"(fk));
      *(u32*)(smem + fq) = 0u; *(u32*)(smem + fk) = 0u;
    } else {
      wvo = wl + lane * 16;
      po = C_P + (lq * 4 + q4) * 16 + (p4 & 1) * 8;
      so = ((l15 >> 2) & 1) * 131072 + (((vt & 3) + (NV == 2 ? hi8 : 0)) * 8 + ((l15 >> 1) & 1) * 4 + (lq >> 1) * 2 + (l15 & 1)) * 16 + (lq & 1) * 8;
      if (NV == 1 && hi8) so = PJ_OOB;
      asm volatile("" : "+v"(wvo), "+v"(po), "+v"(so));
      if constexpr (NV == 2) { qo = po ^ 32; asm volatile("" : "+v"(qo)); }
#pragma unroll
      for (int j = 0; j < 4; j++) LDW(pre[j], NV == 2 ? 2 + j : j);
    }
    eo = QW ? C_QT + wid * 768 + ((l15 >> 2) * 2 + (l15 & 1)) * 32 + lq * 8 + ((l15 >> 1) & 1) * 4
            : C_KT + (wid & 3) * 1024 + ((l15 * 32) ^ (hi8 << 7)) + lq * 8;
    sidx = (l15 & 2) ? 0xEEEE : 0x4444;
    mo = C_IMG + lane * 16;
    io = C_IMG + ((tid & ~30) | ((tid & 10) << 1) | ((tid & 20) >> 1)) * 16;
    gvo = tid * 16;
    asm volatile("" : "+v"(sidx), "+v"(mo), "+v"(io), "+v"(eo));
    if constexpr (R < 2) asm volatile("" : "+v"(gvo));
    if constexpr (!QW) { *(u32*)(smem + eo + 4) = 0u; *(u32*)(smem + eo + 516) = 0u; }
  }
  f4 a0, a1;
  u32x4 vm = {0u, 0u, 0u, 0u};
  if constexpr (NV > 0) asm volatile("" : "+v"(vm));
  u32 ts = 0;
  u64 tq = 0;
  int dur = DH0 * 64;
  const int ob = ((h >> 3) * 32768 + (h & 7) * 512) ^ xp ^ (NV == 1 ? X_HS : 0);
  const int ib = X_M ^ xp ^ ((wid >> 2) * 32768 + (wid & 3) * 1024);
  int b3 = 0;
  PRIO(QW ? 2 : 1);
#pragma unroll 1
  for (int s = 0;; s++) {
    int mb = ib + b3 * 65536, mbx = X_M ^ xp ^ (196608 + b3 * 1024);
    if constexpr (R == GX) asm volatile("" : "+s"(mb), "+s"(mbx));
    u32x4 g, gx;
    if constexpr (R != 1) ALIGN(HAL);
    BAR();
    STAMP(13);
    if constexpr (R == 1 && NXE) {
      asm volatile("buffer_load_dwordx4 %0, %2, %3, %4 offen sc1\n\tbuffer_load_dwordx4 %1, %2, %3, %5 offen sc1"
                   : "=&v"(g), "=&v"(gx) : "v"(mo), "s"(rsX), "s"(mb), "s"(mbx) : "memory");
    } else {
      g = __builtin_amdgcn_raw_buffer_load_b128(rsX, mo, mb, 16);
      if constexpr (R == GX) gx = __builtin_amdgcn_raw_buffer_load_b128(rsX, mo, mbx, 16);
    }
    SB0();
    if constexpr (R == 0) {
#pragma unroll
      for (int j = 0; j < 4; j++) { const char* kx_ = smem + C_KX + fo + j * 2048;
        pre[j >> 1][j & 1] = *(const bf8*)kx_; pre[2 + (j >> 1)][j & 1] = *(const bf8*)(kx_ + 256); }
    } else if constexpr (R > 1) {
      IDX();
#pragma unroll
      for (int d = 0; d < NV; d++) { const char* vb_ = smem + C_VX + (vt + d) * 1024 + (p4 >> 1) * 512 + (((lq * 4 + q4) ^ ((p4 >> 1) * 8)) << 4) + (p4 & 1) * 8;
        vx[d] = cat8(trrd(vb_), trrd(vb_ + 256)); }
    }
    SB0();
    a0 = zero4(); a1 = zero4();
    f4 c0, c1, a2;
#define A2INI() do { u64 z_; float m0_, m1_; asm volatile("v_mov_b32 %0, %3\n\tv_mov_b32 %1, %3\n\tv_mov_b64 %2, 0\n\ts_nop 1" : "=&v"(m0_), "=&v"(m1_), "=v"(z_) : "v"(mm)); \
                     const f2v zz_ = __builtin_bit_cast(f2v, z_); a2 = (f4){m0_, m1_, zz_[0], zz_[1]}; } while (0)
    if constexpr ((ZE >> R) & 1) {
      c1 = zero4();
      if constexpr (NV != 1) c0 = zero4();
      if constexpr (R == 0) { A2INI(); asm volatile("" : "+v"(a2)); }
    }
    int so_ = X_O(b3) ^ ob, an_ = ANX(s & 1) + h * ANS;
    int nb = s < 63 ? (s + 1) * 262144 + h * 8192 : PJ_OOB;
    asm volatile("" : "+s"(so_), "+s"(an_), "+s"(nb));
    u32x4 nx[2];
#define NXL1(g, i) __builtin_amdgcn_raw_ptr_buffer_load_lds(rsB, (LDSP void*)(smem + C_KX + (g) * 4096), 16, (g) < 2 ? gvo : po, \
                                                             nb + (1 + ((g) >> 1)) * ACB + ((g) & 1) * 4096 - 4096, (i) * 1024, 16)
#define NXL(g) do { NXL1(g, 0); NXL1(g, 1); NXL1(g, 2); NXL1(g, 3); } while (0)
    SB0();
    if (NXE) {
      BARC();
      if constexpr (R == 1) {
        if (NXE > 1) __builtin_amdgcn_s_sleep(NXE - 1);
#pragma unroll
        for (int i = 0; i < 4; i++)
          asm volatile("s_mov_b32 m0, %3\n\ts_nop 0\n\tbuffer_load_dwordx4 %0, %1, %2 offen sc1 lds\n\tbuffer_load_dwordx4 %0, %1, %2 offen offset:1024 sc1 lds\n\t"
                       "buffer_load_dwordx4 %0, %1, %2 offen offset:2048 sc1 lds\n\tbuffer_load_dwordx4 %0, %1, %2 offen offset:3072 sc1 lds"
                       :: "v"(i < 2 ? gvo : po), "s"(rsB), "s"(nb + (1 + (i >> 1)) * ACB + (i & 1) * 4096 - 4096), "i"(C_KX + i * 4096) : "memory", "m0");
      }
      SB0();
    }
    if constexpr (!QW) { PRIO(IPR); BARN(); }
    {
      if constexpr (R == 1 && NXE) asm volatile("s_waitcnt vmcnt(17)" : "+v"(g));
      else __builtin_amdgcn_s_waitcnt(R == GX ? 0x0F71 : 0x0F70);
      CFENCE();
      *(u32x4*)(smem + io) = g;
      SB0();
      u32 top = umax(umax(umax(g[0], g[1]), g[2]), g[3]);
      if constexpr (R == GX) {
        asm volatile("" : "+v"(top));
        SB0();
        if constexpr (R == 1 && NXE) asm volatile("s_waitcnt vmcnt(16)" : "+v"(gx));
        *(u32x4*)(smem + GXO + io) = gx;
        SB0();
        top = umax(umax(umax(umax(top, gx[0]), gx[1]), gx[2]), gx[3]);
      }
#define G_BAD() (umax(umax(umax(g[0], g[1]), umax(g[2], g[3])), R != GX ? 0u : umax(umax(gx[0], gx[1]), umax(gx[2], gx[3]))) == ~0u)
      if (__builtin_expect(__any(top == ~0u), 0)) {
        int it = 0;
        do {
          CFENCE();
          if (G_BAD()) {
            g = __builtin_amdgcn_raw_buffer_load_b128(rsX, mo, mb, 16);
            if constexpr (R == GX) gx = __builtin_amdgcn_raw_buffer_load_b128(rsX, mo, mbx, 16);
          }
          VMW0C();
        } while (__any(G_BAD()) && ++it < SPIN_CAP);
        *(u32x4*)(smem + io) = g;
        if constexpr (R == GX) *(u32x4*)(smem + GXO + io) = gx;
      }
    }
    SB0();
    STAMP(15);
    if constexpr (!QW) PRIO(1);
    BARC();
    STAMP(0);
#define LDM(j) *(const bf8*)(smem + mo + (j) * 1024)
    constexpr int NB = R == 1 ? 9 : 5;
    bf8 b[NB];
    if constexpr (R == 1) {
      STAMP(6);
      asm volatile("s_memrealtime %0\n\ts_waitcnt lgkmcnt(0)" : "=s"(tq));
      ts = (u32)tq;
      __builtin_amdgcn_raw_buffer_store_b32((((ts << 6) + (u32)dur) & ~63u) | (u32)s, rsX, 0, an_, 16);
      SB0();
#pragma unroll
      for (int j = 0; j < 9; j++) { b[j] = LDM(j); SB0(); }
#pragma unroll
      for (int i = 0; i < NXA; i++) { NXL(i); SB0(); }
    } else {
      if (!QW && KSP) __builtin_amdgcn_s_sleep(KSP);
#pragma unroll
      for (int j = 0; j < 4; j++) { b[j] = LDM(j); SB0(); }
    }
#pragma unroll
    for (int j = 0; j < 9; j++) {
      if constexpr (QW) {
        if (j == BJ) {
          BARN();
#pragma unroll
          for (int i = 4; i < 4 + BJ; i++) { b[i % 5] = LDM(i); SB0(); }
        }
        if (j >= BJ && j + 4 < 9) b[(j + 4) % 5] = LDM(j + 4);
      } else if (R != 1 && j + 4 < 9) b[(j + 4) % 5] = LDM(j + 4);
      SB0();
      a0 = SMF(b[j % NB], w[0][j], a0, sidx);
      if (NV < 2 || j >= 4) a1 = SMF(b[j % NB], w[1][j], a1, sidx);
      else a1 = SMF(b[j % 5], cat16(pre[j & 3][0], pre[j & 3][1]), a1, sidx);
      SB0();
      if (j == 0) STAMP(10);
      if (j == 4) STAMP(11);
      if (j == 8) STAMP(12);
      if (NV == 2 && j >= DJ && j < DJ + 4) LDW(pre[j - DJ], (DSLOT >> ((j - DJ) * 4)) & 15);
    }
    {
      const u32 e0 = pk2(a0[0] + a0[2], a0[1] + a0[3]), e1 = pk2(a1[0] + a1[2], a1[1] + a1[3]);
      asm volatile("ds_write2_b32 %0, %1, %2 offset1:%3" :: "v"(eo), "v"(e0), "v"(e1), "i"(QW ? 96 : 128) : "memory");
      if constexpr (R != 0) asm volatile("ds_write_b32 %0, %1 offset:%2" :: "v"(eo), "v"(sidx), "i"(QW ? F_Q : F_K) : "memory");
    }
    SB0();
    STAMP(1);
    if constexpr (QW) PRIO(R ? 0 : SP1);
    else if (KV0) PRIO(0);
    if constexpr ((ZE >> R) & 1) { a0 = c0; a1 = c1; }
    else if constexpr (R != 1) { a1 = zero4(); if constexpr (NV != 1) a0 = zero4(); if constexpr (R == 0) A2INI(); }
    if constexpr (NV == 0) {
      u32 dl = 0;
      const int c = 1;
      bf8 km[4];
      if constexpr (R == 0) {
        if (QSL) __builtin_amdgcn_s_sleep(QSL);
        bf8 qa[2];
        u32 fl;
        do {
          CFENCE();
          fl = *(const volatile LDSP u32*)(smem + fq);
          CFENCE(); SB0();
#pragma unroll
          for (int i = 0; i < 2; i++) { qa[i] = cat8(trrd(smem + qo + i * 1536), trrd(smem + qo + i * 1536 + 128)); SB0(); }
        } while (__builtin_expect(__any(fl == 0u), 0));
        STAMP(2);
        auto rdk = [&]() __attribute__((always_inline)) {
          CFENCE(); SB0();
          fl = *(const volatile LDSP u32*)(smem + fk);
          CFENCE(); SB0();
#pragma unroll
          for (int i = 0; i < 4; i++) { km[i] = cat8(trrd(smem + ko + i * 1024), trrd(smem + ko1 + i * 1024)); SB0(); }
        };
        auto mulk = [&]() __attribute__((always_inline)) {
#pragma unroll
          for (int i = 0; i < 2; i++) { a2 = SMF(qa[i], cat16(km[2 * i], km[2 * i + 1]), a2, sidx); asm volatile("" : "+v"(a2)); SB0(); }
        };
#pragma unroll
        for (int i = 0; i < 2; i++) {
          a0 = SMF(qa[i], cat16(pre[i][0], pre[i][1]), a0, sidx); asm volatile("" : "+v"(a0)); SB0();
          a1 = SMF(qa[i], cat16(pre[2 + i][0], pre[2 + i][1]), a1, sidx); asm volatile("" : "+v"(a1)); SB0();
        }
        if (SP2 != SP1) PRIO(SP2);
        if (KP == 0) rdk();
        float e0 = a0[0] + a0[2], e1 = a0[1] + a0[3];
        asm volatile("" : "+v"(e0), "+v"(e1));
        SB0();
        float e2 = a1[0] + a1[2], e3 = a1[1] + a1[3], x0, x1, y0, y1, f0, f1, f2, f3, f4_, f5;
        asm volatile("v_mul_f32_dpp %0, %2, %4 row_newbcast:0 row_mask:0xf bank_mask:0xf\n\tv_mul_f32_dpp %1, %3, %4 row_newbcast:0 row_mask:0xf bank_mask:0xf"
                     : "=&v"(x0), "=&v"(x1) : "v"(e0), "v"(e1), "v"(cv), "v"(e2), "v"(e3));
        if (KP == 1) rdk();
        asm volatile("v_fma_f32 %0, %9, %6, -%7\n\tv_fma_f32 %2, %11, %6, -%8\n\tv_exp_f32 %0, %0\n\tv_exp_f32 %2, %2\n\t"
                     "v_fma_f32 %1, %10, %6, -%7\n\tv_fma_f32 %3, %12, %6, -%8\n\tv_exp_f32 %1, %1\n\tv_exp_f32 %3, %3\n\t"
                     "v_add_f32 %4, %0, %1\n\tv_add_f32 %5, %2, %3"
                     : "=&v"(f0), "=&v"(f2), "=&v"(f1), "=&v"(f3), "=&v"(y0), "=&v"(y1) : "v"(cv), "v"(x0), "v"(x1), "v"(e0), "v"(e2), "v"(e1), "v"(e3));
        if (KP == 2) rdk();
        STAMP(3);
        PRIO(2);
        SB0();
        if (KP == 3) {
          if (KD) asm volatile("s_nop %0" :: "i"(KD));
          do {
            rdk();
          } while (__any(fl == 0u));
        }
        mulk();
        if (KP < 3 && __builtin_expect(__any(fl == 0u), 0)) {
          do rdk(); while (__any(fl == 0u));
          A2INI();
          mulk();
        }
        STAMP(4);
        float e4 = a2[0] + a2[2], e5 = a2[1] + a2[3];
#define P_OUT "v_rcp_f32 %3, %3\n\tv_rcp_f32 %4, %4\n\t" \
              "v_mul_f32 %0, %0, %3\n\tv_mul_f32 %5, %5, %4\n\tv_mul_f32 %1, %1, %3\n\tv_cvt_pk_bf16_f32 %0, %0, %5\n\t" \
              "v_mul_f32 %6, %6, %4\n\tv_mul_f32 %2, %2, %3\n\tv_cvt_pk_bf16_f32 %1, %1, %6\n\tv_mul_f32 %7, %7, %4\n\t" \
              "ds_write2st64_b32 %9, %0, %1 offset1:1\n\tv_cvt_pk_bf16_f32 %2, %2, %7\n\tds_write_b32 %9, %2 offset:512"
        asm volatile("v_fma_f32 %2, %12, %8, -%10\n\tv_fma_f32 %7, %13, %8, -%11\n\tv_exp_f32 %2, %2\n\tv_exp_f32 %7, %7\n\t"
                     "v_add_f32 %3, %3, %2\n\tv_add_f32 %4, %4, %7\n\t" DPROW("v_add_f32_dpp") P_OUT "\n\tv_min_f32 %3, %3, %4"
                     : "+v"(f0), "+v"(f2), "=&v"(f4_), "+v"(y0), "+v"(y1), "+v"(f1), "+v"(f3), "=&v"(f5)
                     : "v"(cv), "v"(po), "v"(x0), "v"(x1), "v"(e4), "v"(e5) : "memory");
        SB0();
        if (__builtin_expect(__any(y0 < 0x1p-64f), 0)) {
          asm volatile("v_max3_f32 %3, %0, %1, %2\n\tv_max3_f32 %4, %5, %6, %7\n\t" DPROW("v_max_f32_dpp")
                       "v_mul_f32 %3, %8, %3\n\tv_mul_f32 %4, %8, %4\n\t"
                       "v_fma_f32 %0, %0, %8, -%3\n\tv_fma_f32 %5, %5, %8, -%4\n\tv_exp_f32 %0, %0\n\tv_exp_f32 %5, %5\n\t"
                       "v_fma_f32 %1, %1, %8, -%3\n\tv_fma_f32 %6, %6, %8, -%4\n\tv_exp_f32 %1, %1\n\tv_exp_f32 %6, %6\n\t"
                       "v_fma_f32 %2, %2, %8, -%3\n\tv_fma_f32 %7, %7, %8, -%4\n\tv_exp_f32 %2, %2\n\tv_exp_f32 %7, %7\n\t"
                       "v_add_f32 %3, %0, %1\n\tv_add_f32 %4, %5, %6\n\tv_add_f32 %3, %3, %2\n\tv_add_f32 %4, %4, %7\n\t" DPROW("v_add_f32_dpp") P_OUT
                       : "+v"(e0), "+v"(e2), "+v"(e4), "=&v"(x0), "=&v"(x1), "+v"(e1), "+v"(e3), "+v"(e5) : "v"(cv), "v"(po) : "memory");
        }
        SB0();
      } else {
#pragma unroll
        for (int i = NXA; i < (NXV ? NXH : NXB) && !NXE; i++) NXL(i);
        SB0();
        const int rb = X_O(b3 == 0 ? 2 : b3 - 1) ^ ob;
        const u32x4 e = ones4();
        __builtin_amdgcn_raw_buffer_store_b128(e, rsX, so, rb, 16);
        __builtin_amdgcn_raw_buffer_store_b128(e, rsX, so, rb ^ X_HS, 16);
        PSTAMP();
      }
      STAMP(5);
      BARC();
      if constexpr (R == 0) STAMP(6);
      if constexpr (R == 1) {
        const u32 tp = (u32)wall_clock64(); dur += ((int)umin((tp - ts) << 6, 200u * 64u) - dur) >> DSH;
        SB0();
        if (ADE && s < 63) AD_GET(true, K1 + K3, 4096);
        if (NXB < 4) {
          __builtin_amdgcn_s_sleep(NXS);
#pragma unroll
          for (int i = NXB; i < 4; i++) NXL(i);
          BARN();
        }
        ALIGN(HAL);
        VMW0C();
        STAMP(8);
        if (NXB == 4) BARN();
        if (s == 63) break;
      } else {
        *(u32*)(smem + fq) = 0u; *(u32*)(smem + fk) = 0u;
        bf8 qx[2][4];
#pragma unroll
        for (int ks = 0; ks < 8; ks++) { qx[ks >> 2][ks & 3] = *(const bf8*)(smem + C_QX + (ks >> 2) * 256 + fo + (ks & 3) * 2048); if (ks < QXP) SB0(); }
#pragma unroll
        for (int NT = 1; NT <= 2; NT++) {
          f4 sc[2], sm;
          const bf8* qf = qx[NT - 1];
#pragma unroll
          for (int ks = 0; ks < 4; ks++) {
#pragma unroll
            for (int j = 0; j < NT; j++) sc[j] = MFMA(pre[2 * j + (ks >> 1)][ks & 1], qf[ks], ks ? sc[j] : (j == NT - 1 ? ((f4){mk[0], mk[1], mk[2], mk[3]}) : ((f4){0, 0, 0, 0})));
            sm = MFMA(km[ks], qf[ks], ks ? sm : ((f4){0, 0, 0, 0}));
          }
          float mx = FMX3(FMX3(sc[0][0], sc[0][1], sc[0][2]), sc[0][3], FMX(sm[0], sm[1]));
          if (NT == 2) mx = FMX3(FMX3(mx, sc[1][0], sc[1][1]), sc[1][2], sc[1][3]);
          { auto t = __builtin_amdgcn_permlane16_swap(__float_as_uint(mx), __float_as_uint(mx), false, false);
            mx = FMX(__uint_as_float(t[0]), __uint_as_float(t[1])); }
          { auto t = __builtin_amdgcn_permlane32_swap(__float_as_uint(mx), __float_as_uint(mx), false, false);
            mx = FMX(__uint_as_float(t[0]), __uint_as_float(t[1])); }
          mx *= -CE;
#pragma unroll
          for (int j = 0; j < NT; j++)
#pragma unroll
            for (int r = 0; r < 4; r++) sc[j][r] = __builtin_amdgcn_exp2f(__builtin_fmaf(sc[j][r], CE, mx));
          sm[0] = __builtin_amdgcn_exp2f(__builtin_fmaf(sm[0], CE, mx)); sm[1] = __builtin_amdgcn_exp2f(__builtin_fmaf(sm[1], CE, mx));
          float sum = ((sc[0][0] + sc[0][1]) + (sc[0][2] + sc[0][3])) + (sm[0] + sm[1]);
          if (NT == 2) sum += (sc[1][0] + sc[1][1]) + (sc[1][2] + sc[1][3]);
          { auto t = __builtin_amdgcn_permlane16_swap(__float_as_uint(sum), __float_as_uint(sum), false, false);
            sum = __uint_as_float(t[0]) + __uint_as_float(t[1]); }
          sum = swadd(sum, sum);
          const float inv = __builtin_amdgcn_rcpf(sum);
          *(u32x4*)(smem + C_PX + (NT - 1) * 1024 + gvo) = (u32x4){pk2(sc[0][0] * inv, sc[0][1] * inv), pk2(sc[0][2] * inv, sc[0][3] * inv),
                                                                  NT == 2 ? pk2(sc[1][0] * inv, sc[1][1] * inv) : 0u, NT == 2 ? pk2(sc[1][2] * inv, sc[1][3] * inv) : 0u};
          *(u32*)(smem + C_PX + 2048 + (NT - 1) * 256 + (gvo >> 2)) = pk2(sm[0] * inv, sm[1] * inv);
        }
        STAMP(8);
        BAR();
        if (s == 63) break;
        if (known < (s < 61 ? s + 3 : 64)) {
          int n = 0;
          while (true) {
            u32 bf[3];
#pragma unroll
            for (int i = 0; i < 3; i++) bf[i] = ld4_sc1(WSP(u32, 0) + SY_BLK(i, cb, gvo >> 4));
            const u64 miss = __ballot(!(bf[0] >= FULL && bf[1] >= FULL && bf[2] >= FULL));
            known = miss ? __builtin_ctzll(miss) : 64;
            if (known >= (s < 61 ? s + 3 : 64) || ++n >= SPIN_CAP) break;
            __builtin_amdgcn_s_sleep(KSL);
          }
        }
      }
      STAMP(9);
      YEND();
      if constexpr (R == 1) { if (!ADE) AD_GET(true, K1 + K3, 4096); AD_SPIN(); }
    } else {
      if (NV == 2 && DJ > 5) {
#pragma unroll
        for (int j = 0; j < 4; j++) LDW(pre[j], (DSLOT >> (j * 4)) & 15);
        SB0();
      }
      if (QW && (NV == 2 ? VSL : VSL1)) __builtin_amdgcn_s_sleep(NV == 2 ? VSL : VSL1);
      if constexpr (NV == 2) {
        int u = 0;
#pragma unroll
        for (int i = 0; i < 9; i++) {
          const int bs = (0x31042 >> ((i % 5) * 4)) & 7;
#define RING(a) do { a = SMF(b[bs], cat16(pre[u & 3][0], pre[u & 3][1]), a, sidx); SB0(); \
                     if (u < 7) LDW(pre[u & 3], (DSLOT >> (u * 4 + 16)) & 15); u++; } while (0)
          SB0();
          if (i == 0 || i == 4) RING(a0);
          else a0 = SMF(b[bs], i < 4 ? wx[i - 1] : w[1][i - 5], a0, sidx);
          SB0();
          RING(a1);
          if (i < 4) b[bs] = LDM(i);
        }
      } else {
#pragma unroll
        for (int i = 0; i < 9; i++) {
          SB0();
          if (i < 3) a1 = SMF(b[(i + 4) % 5], wx[i], a1, sidx);
          else a1 = SMF(b[(i + 4) % 5], cat16(pre[(i + 1) & 3][0], pre[(i + 1) & 3][1]), a1, sidx);
          SB0();
          if (i < 4) b[(i + 4) % 5] = LDM(i);
          if (i == 3 || i == 4) LDW(pre[(i + 1) & 3], 1 + i);
        }
      }
      vm[NV - 1] = pk2(a1[0] + a1[2], a1[1] + a1[3]);
      if constexpr (NV == 2) vm[0] = pk2(a0[0] + a0[2], a0[1] + a0[3]);
      asm volatile("" : "+v"(vm));
      const bool hi = so & 128;
      if (QW && PVP) PRIO(2);
      if (!QW && KV0) PRIO(1);
      SB0();
      STAMP(5);
      BARN();
      {
        const bf4 p0 = trrd(smem + po), p1 = trrd(smem + po + 256);
        SB0();
        const bf4 m1 = trrd(smem + (NV == 2 ? qo : po) + 512);
        SB0();
        bf4 m0;
        if constexpr (NV == 2) m0 = trrd(smem + po + 512);
        SB0();
        const u32x8 pp = ilv(m0, m1, p0, p1);
        f4 o[NV];
#pragma unroll
        for (int d = NV - 1; d >= 0; d--) {
          o[d] = MFMA(vx[d], __builtin_bit_cast(bf8, __builtin_shufflevector(pp, pp, 4, 5, 6, 7)), ((f4){0, 0, 0, 0})); asm volatile("" : "+v"(o[d])); SB0();
        }
        o[NV - 1] = MFMA(__builtin_bit_cast(bf8, vm), __builtin_bit_cast(bf8, __builtin_shufflevector(pp, pp, 2, 3, 4, 5)), o[NV - 1]);
        asm volatile("" : "+v"(o[NV - 1]));
        SB0();
        if constexpr (NV == 2) {
          o[0] = MFMA(__builtin_bit_cast(bf8, vm), __builtin_bit_cast(bf8, __builtin_shufflevector(pp, pp, 0, 1, 2, 3)), o[0]);
          asm volatile("" : "+v"(o[0]));
        }
        asm volatile("" :: "v"(pp));
        SB0();
        u32x2 ov = {pk2(o[0][0], o[0][1]), pk2(o[0][2], o[0][3])};
        if constexpr (NV == 2) {
          const u32x2 ow = {pk2(o[1][0], o[1][1]), pk2(o[1][2], o[1][3])};
          if (hi) ov = ow;
        }
        ov[0] = umin(ov[0], 0xFFFFFFFEu);
        __builtin_amdgcn_raw_buffer_store_b64(ov, rsX, so, so_, 16);
        SB0();
      }
      STAMP(7);
      if (QW && !PVP) PRIO(2);
      int xo, lo, pa = 0;
      {
        IDX();
        if constexpr (NV == 1) {
          __builtin_amdgcn_s_sleep(LSL);
          if (NXV && vt >= NXH) {
            asm volatile("s_mov_b32 m0, %3\n\ts_nop 0\n\tbuffer_load_dwordx4 %0, %1, %2 offen sc1 lds\n\tbuffer_load_dwordx4 %0, %1, %2 offen offset:1024 sc1 lds\n\t"
                         "buffer_load_dwordx4 %0, %1, %2 offen offset:2048 sc1 lds\n\tbuffer_load_dwordx4 %0, %1, %2 offen offset:3072 sc1 lds"
                         :: "v"((lane * 16) ^ ((lane & (vt * 16) & 32) << 2)), "s"(rsB), "s"(nb + (1 + (vt >> 1)) * ACB + (vt & 1) * 4096), "s"(C_KX + vt * 4096) : "memory", "m0");
            SB0();
          }
#pragma unroll
          for (int i = 0; i < 2; i++) nx[i] = __builtin_amdgcn_raw_buffer_load_b128(rsB, lane * 16 + i * 1024, nb + vt * 2048, 16);
          pa = C_QX + vt * 2048 + lane * 16;
          asm volatile("" : "+v"(pa));
        }
        xo = OB_OFF((lq & 1) * 16 + l15, (lq >> 1) * 8);
        lo = lane * 4;
        asm volatile("" : "+v"(xo), "+v"(lo));
#pragma unroll
        for (int j = 0; j < 4; j++) LDW(pre[j], NV == 2 ? 2 + j : j);
      }
      SB0();
      BARN();
      STAMP(8);
      {
        f4 o[2][NV];
#pragma unroll
        for (int it = 0; it < 2; it++) {
          const bf8 pf = *(const bf8*)(smem + C_PX + it * 1024 + lo * 4);
          const u32 pm = *(const u32*)(smem + C_PX + 2048 + it * 256 + lo);
#pragma unroll
          for (int d = 0; d < NV; d++) {
            o[it][d] = MFMA(vx[d], pf, ((f4){0, 0, 0, 0}));
            o[it][d] = MFMA(__builtin_bit_cast(bf8, vm), __builtin_bit_cast(bf8, d ? (u32x4){0u, pm, 0u, 0u} : (u32x4){pm, 0u, 0u, 0u}), o[it][d]);
          }
        }
#pragma unroll
        for (int d = 0; d < NV; d++)
          __builtin_amdgcn_raw_buffer_store_b128(pair16(umin(pk2(o[0][d][0], o[0][d][1]), 0xFFFFFFFEu), pk2(o[0][d][2], o[0][d][3]),
                                                        umin(pk2(o[1][d][0], o[1][d][1]), 0xFFFFFFFEu), pk2(o[1][d][2], o[1][d][3])), rsB,
              xo, 3 * ACB + OB_OFF(s * 32, h * 128 + (vt + d) * 16), 16);
        SB0();
        if (s == 63) break;
        if constexpr (NV == 1) { *(u32x4*)(smem + pa) = nx[0]; *(u32x4*)(smem + pa + 1024) = nx[1]; }
      }
      STAMP(9);
      YEND();
    }
    b3 = b3 == 2 ? 0 : b3 + 1;
    STAMP(14);
  }
  };
  if (wid == 0) run(std::integral_constant<int, 0>{});
  else if (wid == 4) run(std::integral_constant<int, 1>{});
  else if (wid > 4) run(std::integral_constant<int, 3>{});
  else if (wid == 3) run(std::integral_constant<int, 2>{});
  else run(std::integral_constant<int, 4>{});
  PRIO(0);
  TLS(0);
  return xp;
}

__device__ __forceinline__ int chain_out(const Params& p, char* smem, const int c, const int wv, const bool spec) {
  STS(32 + c, 1);
  constexpr int R = 5; (void)R;
  const int wid = wv, tq = c & 1, cg = c >> 1;
  u32x8 w[4][5];
  const __amdgpu_buffer_rsrc_t rsX = RSRC(XWIN(p), X_END);
  int xp = 0, mo, wo, ro;
  const int ms = wid < 4 ? 65536 : 1024;
#define OST 0
  const int r1 = tq & OST, r2 = cg & (OST >> 1);
  {
    IDX();
    mo = wid < 4 ? (cg >> 3) * 32768 + (cg & 7) * 512 + tq * 256 + wid * 64 : 196608 + (cg >> 2) * 512 + tq * 256 + (cg & 3) * 64;
    if (wid > 4 || (wid == 4 && cg >= 8)) mo = PJ_OOB;
    mo ^= X_M;
    u32 m0[2], old = 0u;
    if (wid == 0) {
#pragma unroll
      for (int i = 0; i < 2; i++) m0[i] = *(const GLB u32*)(p.Bm + (tq * 4 + q4) * 576 + (i ? 512 + (cg & 7) * 8 : cg * 32 + lq * 8) + p4 * 2);
      const int cal = X_CAL + (cg & 7) * 32768 + (cg >> 3) * 1024 + tq * 512;
      __builtin_amdgcn_raw_buffer_store_b32(0u, rsX, lane < 8 ? ((2048 << p4) & ~2048) + q4 * 2304 : PJ_OOB, cal, 16);
      asm volatile("" :: "v"(m0[0]), "v"(m0[1]));
      VMW0();
      TLS(8);
      u32 d[4] = {0u, 0u, 0u, 0u};
#pragma unroll 1
      for (int r = 0; r < CALR; r++) {
#pragma unroll
        for (int k = 0; k < 4; k++) {
          const u32 t0 = (u32)__builtin_readcyclecounter();
          __builtin_amdgcn_raw_buffer_store_b32(1u, rsX, lane ? PJ_OOB : 0, cal + ((2048 << k) & ~2048) + (r ^ tq) * 2304, 16);
          VMW0();
          d[k] += (u32)__builtin_readcyclecounter() - t0;
        }
      }
      if (OBE) BARN();
      if (spec) old = confirm(p, smem, 32 + c, BEAT_LD(c), wv);
      if (lane == 0) st4_sc1(WSP(u32, 0) + SY_VOTE(c), 8u + (((d[1] < d[0] ? 1u : 0u) + (d[3] < d[0] ? 4u : 0u)) ^ (d[2] < d[0] ? 3u : 0u)));
      TLS(11);
    }
    if (!OBE || wid) BARN();
    if (wid == 0) {
      u32 fv;
      int it = 0;
      while (fv = ld4_sc1(WSP(u32, 0) + SY_VOTE(lane & 31)), xp = -((__builtin_amdgcn_readfirstlane(old) >> c) & 1), !(xp || __all(fv != 0u)) && ++it < SPIN_CAP) {
        BEAT_ON(it); __builtin_amdgcn_s_sleep(2);
      }
#pragma unroll
      for (int j = 0; j < 3; j++) xp |= __popcll(__ballot((fv >> j) & 1u)) > 32 ? 4096 << j : 0;
      if (xp >= 0) {
#pragma unroll
        for (int i = 0; i < 3; i++) __builtin_amdgcn_raw_buffer_store_b32(i ? ~0u : umin(m0[0], 0xFFFFFFFEu), rsX, lane * 4, mo ^ xp ^ (i * 65536), 16);
        __builtin_amdgcn_raw_buffer_store_b32(lq ? ~0u : umin(m0[1], 0xFFFFFFFEu), rsX, lq < 3 && cg < 8 ? lq * 1024 + l15 * 4 : PJ_OOB,
                                              (196608 + (cg >> 2) * 512 + tq * 256 + (cg & 3) * 64) ^ X_M ^ xp, 16);
#pragma unroll
        for (int i = 0; i < 6; i++)
          __builtin_amdgcn_raw_buffer_store_b128(ones4(), rsX, (lane >> 5) * 131072 + (lane & 31) * 16, X_O(i >> 1) ^ ((c >> 3) * 32768 + (c & 7) * 512) ^ xp ^ ((i & 1) * X_HS), 16);
        __builtin_amdgcn_raw_buffer_store_b32(lane & 2 ? DO0 * 64u : ~0u, rsX, lane < 4 ? ANX(lane & 1) + ((lane & 2) * 16 + c) * ANS : PJ_OOB, 0, 16);
      }
    }
    SB0();
    const __amdgpu_buffer_rsrc_t rw = RSRC(p.BWo, 576 * 4096 * 2);
    u32x4 st[4][8];
    u32 fl;
    bool early = false;
    const int va = ((lane >> 2) * 576 + cg * 32 + p4 * 8) * 2;
#pragma unroll
    for (int i = 0; i < 4; i++) {
#pragma unroll
      for (int k = 0; k < 8; k++) st[i][k] = __builtin_amdgcn_raw_buffer_load_b128(rw, va, ((((i & 1) ^ r1) * 16 + wid * 2 + ((k >> 2) ^ r2)) * 128 + (i >> 1) * 64 + (k & 3) * 16) * 1152, OSTG_AUX);
      SB0();
      if (i == 1 && wid == 0 && xp >= 0) {
        asm volatile("s_waitcnt vmcnt(16)" ::: "memory");
        if (lane == 0) st4_sc1(WSP(u32, 0) + SY_INIT(c), (u32)xp + 1u);
        TLS(10);
      }
      if (i == 2) fl = ld4_sc1(WSP(u32, 0) + SY_WX);
      SB0();
    }
    auto wx = [&](int vo) {
#pragma unroll
      for (int k = 0; k < 8; k++)
        st[0][k] = __builtin_amdgcn_raw_buffer_load_b128(RSRC(WSP(char, O_WX), 524288), vo, cg * 65536 + ((((k & 2) ^ (r1 * 2)) * 8 + wid * 2 + (k & 1)) * 128 + (k >> 2) * 64) * 16, 16);
    };
    auto poll = [&]() { fl = __builtin_amdgcn_raw_buffer_load_b32(RSRC(p.ws, wid ? 0 : O_QA), (lane < 32 ? SY_INIT(lane) : lane < 48 ? SY_ARR : SY_WX) * 4, 0, 16); };
    char* sc = smem + wid * 10240;
    auto frag = [&](const char* b, int rs) {
      bf4 tt[4];
#pragma unroll
      for (int kk = 0; kk < 4; kk++) {
        const int r0 = ((kk >> 1) * 2 + (lq >> 1)) * 32 + (p4 >> 1) * 16 + (q4 >> 1) * 8 + (lq & 1) * 4 + (kk & 1) * 2 + (q4 & 1);
        tt[kk] = trrd(b + r0 * rs + (p4 & 1) * 8);
      }
      return ilv(tt[0], tt[1], tt[2], tt[3]);
    };
#pragma unroll
    for (int i = 0; i < 4; i++) {
#pragma unroll
      for (int k = 0; k < 8; k++) *(u32x4*)(sc + ((k ^ (r2 * 4)) * 16 + (lane >> 2)) * 80 + p4 * 16) = st[i][k];
      __builtin_amdgcn_fence(__ATOMIC_RELEASE, "wavefront");
      __builtin_amdgcn_wave_barrier();
#pragma unroll
      for (int t = 0; t < 4; t++) w[i][t] = frag(sc + t * 16, 80);
      asm volatile("s_waitcnt lgkmcnt(0)" ::: "memory");
      __builtin_amdgcn_wave_barrier();
      if (i == 2) { early = __all(fl == ~0u); CFENCE(); poll(); wx(early ? lane * 16 : PJ_OOB); }
      SB0();
    }
    if (wid == 0) {
      int it = 0;
      while (xp >= 0 && !__all(fl >= (lane < 32 ? 1u : lane < 48 ? 256u : ~0u)) && ++it < SPIN_CAP) { BEAT_ON(it); __builtin_amdgcn_s_sleep(GPS); CFENCE(); poll(); }
      *(int*)(smem + C_XP) = xp;
    }
    asm volatile("s_waitcnt lgkmcnt(0)" ::: "memory");
    BARN();
    xp = __builtin_amdgcn_readfirstlane(*(const int*)(smem + C_XP));
    if (xp < 0) return xp;
    {
      if (!early) wx(lane * 16);
#pragma unroll
      for (int k = TG0; k < 64; k++) __builtin_amdgcn_raw_buffer_store_b128(ones4(), RSRC(WSP(char, O_OB), ACB), tid * 16, OB_OFF(k * 32, c * 128), 16);
#pragma unroll
      for (int k = 0; k < 8; k++) *(u32x4*)(sc + k * 1024 + lane * 16) = st[0][k];
      __builtin_amdgcn_fence(__ATOMIC_RELEASE, "wavefront");
      __builtin_amdgcn_wave_barrier();
#pragma unroll
      for (int i = 0; i < 4; i++) w[i][4] = frag(sc + i * 2048, 16);
    }
    mo = __builtin_amdgcn_readfirstlane(mo ^ xp);
    const int row = (lq & 1) * 4 + ((l15 >> 1) & 3);
    wo = ((lq >> 1) == hi8 ? row * 128 : 1024) + (((wid * 2 + hi8) ^ (row * 2)) << 3) + (l15 & 1) * 4;
    ro = wid * 2560 + (lane >> 5) * 1280 + ((lane >> 2) & 7) * 128 + (lane & 8) * 8 + p4 * 16;
  }
  __syncthreads();
  STS(32 + c, 14);
  TLS(9);
  int go, sidx, po;
  { IDX(); go = lane * 16; sidx = (l15 & 2) ? 0xEEEE : 0x4444;
    po = p4 == 0 ? (lq & 1) * 32 + (lq >> 1) * 16 + q4 * 4 : PJ_OOB; }
  int ro1 = ro ^ 64;
  asm volatile("" : "+v"(ro), "+v"(ro1), "+v"(wo));
#define PUTP(t) asm volatile("ds_write2st64_b32 %0, %1, %2 offset0:%3 offset1:%4" :: "v"(wo), "v"(d[t][0] + d[t][2]), "v"(d[t][1] + d[t][3]), "i"((t) * 10), "i"((t) * 10 + 5) : "memory")
#define APS 18
#define ONOP 7
  int b3 = 0, dur = DO0 * 64;
#pragma unroll 1
  for (int s = 0; s < 63; s++) {
    f4 d[5];
    int gb[4], pb = mo ^ ((b3 == 2 ? 0 : b3 + 1) * ms);
#pragma unroll
    for (int i = 0; i < 4; i++) { gb[i] = X_O(b3) ^ xp ^ (tq * 131072 + (((i & 1) ^ r1) * 2 + (wid >> 2)) * 32768 + (wid & 3) * 1024 + (i < 2 ? X_HS : 0)); asm volatile("" : "+s"(gb[i])); }
    asm volatile("" : "+s"(pb));
    u32x4 g[4];
    u32 bad;
    auto mult = [&]() __attribute__((always_inline)) {
      CFENCE();
#pragma unroll
      for (int i = 0; i < 4; i++) {
        g[i] = __builtin_amdgcn_raw_buffer_load_b128(rsX, go, gb[i], 16);
        SB0();
        if (ONOP >= 0 && i < 3) asm volatile("s_nop %0" :: "i"(ONOP >= 0 ? ONOP : 0));
        SB0();
      }
      bad = 0u;
#pragma unroll
      for (int i = 0; i < 4; i++) {
        SB0();
#pragma unroll
        for (int t = 0; t < 5; t++) {
          d[t] = SMF(g[i], w[i][t], d[t], sidx);
          if (t == 0) bad = umax(umax(bad, g[i][0]), g[i][2]);
          if (i == 3 && t >= 2) { SB0(); PUTP(t - 2); SB0(); }
        }
      }
      SB0();
#pragma unroll
      for (int t = 3; t < 5; t++) PUTP(t);
      SB0();
    };
#pragma unroll
    for (int t = 0; t < 5; t++) d[t] = zero4();
    SB0();
    u32 dl = 0;
    ALIGN(OAL);
    if (wid == 7) { __builtin_amdgcn_s_sleep(APS); STAMP(4); AD_GET(false, K1, SPIN_CAP); STAMP(5); AD_SPIN(); }
    STAMP(15);
    BARN();
    STAMP(0);
    mult();
    if (__builtin_expect(__any(bad == ~0u), 0)) {
      int it = 0;
      do {
#pragma unroll
        for (int t = 0; t < 5; t++) d[t] = zero4();
        mult();
      } while (__any(bad == ~0u) && ++it < SPIN_CAP);
    }
    STAMP(1);
    BARC();
    STAMP(2);
    if (__builtin_expect(wid < 5, 1)) {
      const f4 t0 = *(const f4*)(smem + ro), t1 = *(const f4*)(smem + ro1);
      float a = (t0[0] + t0[2]) + (t1[0] + t1[2]), b = (t0[1] + t0[3]) + (t1[1] + t1[3]);
      a += __uint_as_float(__builtin_amdgcn_mov_dpp(__float_as_uint(a), 0xB1, 0xf, 0xf, true));
      b += __uint_as_float(__builtin_amdgcn_mov_dpp(__float_as_uint(b), 0xB1, 0xf, 0xf, true));
      a += __uint_as_float(__builtin_amdgcn_mov_dpp(__float_as_uint(a), 0x4E, 0xf, 0xf, true));
      b += __uint_as_float(__builtin_amdgcn_mov_dpp(__float_as_uint(b), 0x4E, 0xf, 0xf, true));
      __builtin_amdgcn_raw_buffer_store_b32(umin(pk2(a, b), 0xFFFFFFFEu), rsX, po, pb, 16);
      SB0();
      STAMP(3);
      __builtin_amdgcn_s_sleep(4);
      __builtin_amdgcn_raw_buffer_store_b32(~0u, rsX, po, mo ^ (b3 * ms), 16);
    } else if (wid == 7) {
      dur += (min((int)(((u32)wall_clock64() << 6) - dl), dur + DOCAP * 64) - dur) >> DSH;
      __builtin_amdgcn_raw_buffer_store_b32((u32)dur, rsX, 0, ANX(~s & 1) + (32 + c) * ANS, 16);
    }
    VMW0C();
    YEND();
    b3 = b3 == 2 ? 0 : b3 + 1;
  }
  __syncthreads();
  return xp;
}

__device__ __forceinline__ void wait_ge(const u32* w, u32 val, const int wv) {
  if (TIDX == 0) {
    int it = 0;
    while (ld4_sc1(w) < val && ++it < SPIN_CAP) __builtin_amdgcn_s_sleep(WPS);
  }
}
__device__ __forceinline__ void acquire_all(const int wv) {
  if (TIDX == 0) {
    __builtin_amdgcn_fence(__ATOMIC_ACQUIRE, "agent");
    VMW0();
  }
  __syncthreads();
}

#define T_KV 0
#define T_Q 0
#define T_A 192
#define T_W (T_A + 64)
#define T_S (T_W + 256)
#define T_L (T_S + 72)
#define T_O T_L
#define T_CHAIN (1 << 20)
#define CODE_KB 84
#define OWARM 2
#define BXW 1
#define AORD 0x01234567u
#define OORD 0x67014523u

__device__ __forceinline__ int next_task(const Params& p, int* s_task, const int wv, const int pt, const int pre) {
  __syncthreads();
  if (wv == 0) {
    int t = T_A + pre;
    if (__builtin_amdgcn_readfirstlane(pre) < 0) {
      t = -1;
      int xp = 0;
      for (int it = 0; it < SPIN_CAP; it++) {
        const int l = TIDX & 15;
        const u32 v = ld4_sc1(WSP(u32, 0) + (l < 8 ? SY_JOB(l) : l == 8 ? SY_ARR : SY_INIT(0)));
        const u32 open = (u32)__ballot(v != ~0u) & 255u;
        xp = __builtin_amdgcn_readlane(v, 9);
        if (!open && xp) break;
        if (!open || __builtin_amdgcn_readlane(v, 8) < 256u) { BEAT_ON(it); __builtin_amdgcn_s_sleep(8); continue; }
        const int x = __builtin_ctz(open), k = __builtin_ctz(~(u32)__builtin_amdgcn_readlane(v, x));
        u32 old = 0u;
        if (TIDX == 0) old = AOR(SY_JOB(x), 1u << k);
        if (!((__builtin_amdgcn_readfirstlane(old) >> k) & 1)) { t = x * 32 + k + (x < 2 ? T_CHAIN : -64); break; }
      }
      if (t < 0 && TIDX == 0) t = T_A + (int)__hip_atomic_fetch_add(HOTP(xp) + SY_QHEAD, 1u, __ATOMIC_RELAXED, __HIP_MEMORY_SCOPE_AGENT) + (xp & 0x2000);
    }
    if (TIDX == 0) *s_task = t;
  }
  __syncthreads();
  return __builtin_amdgcn_readfirstlane(*s_task);
}

__device__ __forceinline__ int take_task(int* s_task, const int pre, const int wv, const int pb, const int pl, const int pt) {
  if (TIDX == 0) {
    *s_task = pre;
  }
  __syncthreads();
  const int t = __builtin_amdgcn_readfirstlane(*s_task);
  __syncthreads();
  return t;
}
#if defined(__HIP_DEVICE_COMPILE__) && !defined(KEEP_PK)
#define NOPK __attribute__((target("no-packed-fp32-ops")))
#else
#define NOPK
#endif
__device__ __forceinline__ void mega_body(const Params& p0, char* smem) {
  int* const s_task = (int*)(smem + LDS_TOTAL);
  int wv = __builtin_amdgcn_readfirstlane(threadIdx.x >> 6), t, xs;
#define tid TIDX
  {
    const Params& p = p0;
    u64 pc0; asm volatile("s_getpc_b64 %0" : "=s"(pc0));
#define ZB 1
    const int xcc = (int)(__builtin_amdgcn_s_getreg(20 | (3 << 11)) & 7u), xq = xcc >= 2 ? xcc - 2 : xcc, idx = (blockIdx.x >> 3) & 31;
    t = xq * 32 + (xcc < 2 ? T_CHAIN + idx : (idx + 31) & 31);
    xs = xcc + 8;
#define WARM(ptr, bytes, vo, so, sc) asm volatile("s_mov_b32 m0, %3\n\ts_nop 0\n\tbuffer_load_dword %0, %1, %2 offen" sc " lds" \
                                                   :: "v"(vo), "s"(RSRC(ptr, bytes)), "s"(so), "s"(C_B0 + wv * 256) : "memory", "m0")
    if (wv == 0) WARM(p.ws, O_QA, 0, SY_BEAT(t & 7) * 4, " sc1");
    if (blockIdx.x == ZB) {
#pragma unroll
      for (int i = 0; i < 4; i++) __builtin_amdgcn_raw_buffer_store_b128((u32x4){0u, 0u, 0u, 0u}, RSRC(p.ws, SY_WORDS * 4), tid * 16 + i * 8192, 0, 16);
#pragma unroll
      for (int i = 0; i < 2; i++) __builtin_amdgcn_raw_buffer_store_b128((u32x4){0u, 0u, 0u, 0u}, RSRC(HOTP(i * 0x2000), SY_B0 * 4), tid * 16, 0, 16);
      VMW0();
      BARN();
      if (tid < 8) AADD(SY_BEAT(tid), 1u);
    }
    if (wv) {
      const int g = idx * 7 + wv - 1, vo = (tid & 63) * 64;
      if (g < CODE_KB / 4) WARM(pc0, CODE_KB * 1024, vo, g * 4096, "");
      if (xcc > 1) {
        if (OWARM == 1 && g >= 32) WARM(p.BWo, WOB, vo, (xq * 192 + g - 32) * 4096, "");
        if (OWARM == 2 && g >= 128) WARM(p.BWo, WOB, vo * 2, (xq * 96 + g - 128) * 8192, "");
        if (BXW) WARM(p.Bx, 256 * 1152, vo, (223 - g) * 4096, "");
      }
    }
  }
  u64 pp = (u64)__builtin_amdgcn_kernarg_segment_ptr();
  SB0();
  asm volatile(".Lmega_again:" : "+{s[40:41]}"(pp), "+{s42}"(wv), "+{s43}"(t), "+{s44}"(xs) :: "memory");
  SB0();
  const Params& p = *(const Params*)(const __attribute__((address_space(4))) Params*)pp;
  const int xcc = xs & 7, xq = xcc >= 2 ? xcc - 2 : xcc;
  const bool spec = xs > 7;
  int pre = -1;
  if (t < T_A) {
    const int x = t >> 5, j = (t & 7) * 4 + ((0x9C >> ((t >> 2) & 6)) & 3);
    proj_slot(p, x, j, smem, wv, pre, spec ? t + 64 : -1);
    VMW0C();
  } else {
    int own;
    if (t < T_CHAIN + 32) { own = chain_head(p, smem, t - T_CHAIN, wv, spec); VMW0C(); }
    else { own = chain_out(p, smem, t - T_CHAIN - 32, wv, spec); VMW0C(); asm volatile("s_nop 0"); }
    if (own >= 0) {
      const int l = tid & 7;
      const u32 v = ld4_sc1(HOTP(own) + (l < 7 ? SY_OQ(l) : SY_QHEAD));
      if (tid == 0) *s_task = 0;
      __syncthreads();
      if (v < (l == 7 ? T_S - T_A : l == 6 ? 72 : (l & 1) ? NOB : NOA)) *s_task = 1;
      __syncthreads();
      if (__builtin_amdgcn_readfirstlane(*s_task) == 0) {
        if (xcc == 0) TLS(3); else TLS(4);
        return;
      }
    }
  }
  t = next_task(p, s_task, wv, t, pre);
  if (t < T_A || t >= T_CHAIN) {
    asm volatile("s_waitcnt vmcnt(0) lgkmcnt(0)\n\ts_barrier\n\ts_branch .Lmega_again" :: "{s[40:41]}"(pp), "{s42}"(wv), "{s43}"(t), "{s44}"(xcc) : "memory");
    return;
  }
  u32* const hot = HOTP(t);
  u32* const qh = hot + SY_QHEAD;
  t &= ~0x2000;
#define WPOS 0
  while (t < T_S) {
    if ((u32)(t - T_A - WPOS) < 64u) {
      wof_task(p, t - T_A - WPOS, smem, wv, qh, pre);
      if (tid == 0) __hip_atomic_fetch_add(hot + SY_CW, 1u, __ATOMIC_RELAXED, __HIP_MEMORY_SCOPE_AGENT);
      t = T_A + take_task(s_task, pre, wv, T_A, T_S - T_A, t);
      continue;
    }
    const int qb = (AORD >> (((t - (t < T_A + WPOS ? T_A : T_W)) >> 5) * 4)) & 7, h = t & 31;
    if (tid < 64) {
      int it = 0;
      while (true) {
        const u32 f0 = ld4_sc1(WSP(u32, 0) + SY_BLK(3, h >> 1, tid)), f1 = ld4_sc1(WSP(u32, 0) + SY_BLK(4, h >> 1, tid)), f2 = ld4_sc1(WSP(u32, 0) + SY_BLK(5, h >> 1, tid));
        if (__all(tid >= qb * 8 + 8 || ((tid < qb * 8 || f0 >= FULL) && f1 >= FULL && f2 >= FULL)) || ++it >= SPIN_CAP) break;
        __builtin_amdgcn_s_sleep(8);
      }
    }
    acquire_all(wv);
    fa::attn_block(WSP(char, O_QA) + FR_OFF(qb * 256, h * 128), WSP(char, O_KA) + h * 8192, WSP(char, O_VA) + h * 8192,
                   WSP(char, O_CA) + OB_OFF(qb * 256, h * 128), qb * 256, smem, wv, qh, pre);
    if (tid == 0) __hip_atomic_fetch_add(hot + SY_CC(qb), 1u, __ATOMIC_RELAXED, __HIP_MEMORY_SCOPE_AGENT);
    t = T_A + take_task(s_task, pre, wv, T_A, T_S - T_A, t);
  }
  int rest = 64 | (1 << xq);
#pragma unroll 1
  while (rest) {
    const int x = __builtin_ctz(rest);
    rest &= rest - 1;
    if (x == 6) {
      CLAIMS();
      t = take_task(s_task, pre, wv, T_S, 72, -1);
      while ((t & 0xFFFF) < 72) {
        t &= 0xFFFF;
        small_out_task(p, t & 3, t >> 2, smem, wv, hot, pre);
        t = take_task(s_task, pre, wv, T_S, 72, T_S + t);
      }
      rest = t >> 16;
      continue;
    }
    const int part = x & 1, cg = x >> 1, n = part ? NOB : NOA;
    u32* const qh = hot + SY_OQ(x);
    CLAIM();
    t = take_task(s_task, pre, wv, T_O + x * 32, n, -1);
    while (t < n) {
      const int nb = part ? NBB : NAB, big = t < nb, e = big ? t * 2 : t + nb;
      const int rb = part ? e : ((OORD >> ((e >> 2) * 4)) & 7) * 4 + (e & 3);
      if (tid == 0) {
        const u32* w = hot + (part ? SY_CS((rb + big) * 2 + 1) : SY_CC(rb >> 2));
        int it = 0;
        while ((ld4_sc1(w) < (part ? 1u : 32u) || ld4_sc1(hot + SY_CW) < 64u) && ++it < SPIN_CAP) __builtin_amdgcn_s_sleep(WPS);
      }
      acquire_all(wv);
      const char *pa = WSP(char, part ? O_OB : O_CA) + rb * 2 * 262144, *pw = WSP(char, O_WOF) + part * WOB + cg * 12 * 131072;
      u16* const pc = p.out + (size_t)rb * 64 * 1152 + part * 576 + cg * 192;
      if (big) out_tile<8>(pa, pw, pc, smem, wv, qh, pre);
      else out_tile<4>(pa, pw, pc, smem, wv, qh, pre);
      t = take_task(s_task, pre, wv, T_O + x * 32, n, T_O + x * 32 + t);
    }
  }
  if (xcc == 0) TLS(3); else if (xcc == 1) TLS(4); else TLS(5);
}

#undef tid
__global__ NOPK void __launch_bounds__(512, 2) mega(Params p) {
  __shared__ __attribute__((aligned(16))) char smem[LDS_ALL];
  mega_body(p, smem);
}
extern "C" void kernel_launch(void* const* d_in, const int* in_sizes, int n_in, void* d_out, int out_size, void* d_ws,
                              size_t ws_size, hipStream_t stream) {
  Params p;
  p.Ax = (const u16*)d_in[0]; p.Ac = (const u16*)d_in[1]; p.Awq = (const u16*)d_in[2]; p.Awk = (const u16*)d_in[3];
  p.Awv = (const u16*)d_in[4]; p.Awo = (const u16*)d_in[5]; p.Bx = (const u16*)d_in[6]; p.Bm = (const u16*)d_in[7];
  p.BWq = (const u16*)d_in[8]; p.BWk = (const u16*)d_in[9]; p.BWv = (const u16*)d_in[10]; p.BWo = (const u16*)d_in[11];
  p.out = (u16*)d_out;
  p.ws = (char*)d_ws;
  mega<<<256, 512, 0, stream>>>(p);
}
```

```cpp
#include <hip/hip_runtime.h>
#include <hip/hip_bf16.h>
#include <stdint.h>
#include <type_traits>

typedef unsigned short u16;
typedef unsigned int u32;
typedef unsigned long long u64;
typedef __attribute__((ext_vector_type(8))) short bf8;
typedef __attribute__((ext_vector_type(4))) short bf4;
typedef __attribute__((ext_vector_type(4))) float f4;
typedef __attribute__((ext_vector_type(2))) float f2v;
typedef __attribute__((ext_vector_type(2))) __bf16 bf2v;
typedef __attribute__((ext_vector_type(4))) unsigned u32x4;
typedef __attribute__((ext_vector_type(2))) unsigned u32x2;
typedef float f32x16 __attribute__((ext_vector_type(16)));
typedef __attribute__((ext_vector_type(8))) unsigned u32x8;
typedef __attribute__((ext_vector_type(8))) __bf16 bh8;
typedef __attribute__((ext_vector_type(16))) __bf16 bh16;
#define LDSP __attribute__((address_space(3)))

__device__ __forceinline__ u32 pk2(float a, float b) {
  f2v x = {a, b};
  bf2v y = __builtin_convertvector(x, bf2v);
  return __builtin_bit_cast(u32, y);
}
__device__ __forceinline__ u32 umin(u32 a, u32 b) { return a < b ? a : b; }
__device__ __forceinline__ u32 umax(u32 a, u32 b) { return a > b ? a : b; }
__device__ __forceinline__ float rbf(float f) { return __uint_as_float(pk2(f, f) << 16); }
#define MFMA(a, b, c) __builtin_amdgcn_mfma_f32_16x16x32_bf16(a, b, c, 0, 0, 0)
#define MFMA16(a, b, c) __builtin_amdgcn_mfma_f32_16x16x16bf16_1k(a, b, c, 0, 0, 0)
#define SMF(a, b, c, idx) __builtin_amdgcn_smfmac_f32_16x16x64_bf16(__builtin_bit_cast(bh8, a), __builtin_bit_cast(bh16, b), c, idx, 0, 0)
__device__ __forceinline__ u32x8 ilv(bf4 a, bf4 b, bf4 c, bf4 d) {
  const u32x2 x = __builtin_bit_cast(u32x2, a), y = __builtin_bit_cast(u32x2, b), z = __builtin_bit_cast(u32x2, c), w = __builtin_bit_cast(u32x2, d);
  return (u32x8){x[0], x[1], y[0], y[1], z[0], z[1], w[0], w[1]};
}
__device__ __forceinline__ u32x8 cat16(bf8 a, bf8 b) {
  const u32x4 x = __builtin_bit_cast(u32x4, a), y = __builtin_bit_cast(u32x4, b);
  return (u32x8){x[0], x[1], x[2], x[3], y[0], y[1], y[2], y[3]};
}
__device__ __forceinline__ float swadd(float a, float b) {
  auto x = __builtin_amdgcn_permlane32_swap(__float_as_uint(a), __float_as_uint(b), false, false);
  return __uint_as_float(x[0]) + __uint_as_float(x[1]);
}
__device__ __forceinline__ bf4 trrd(const char* p) {
  return __builtin_amdgcn_ds_read_tr16_b64_v4i16((LDSP bf4*)(p));
}
__device__ __forceinline__ bf8 cat8(bf4 a, bf4 b) { return __builtin_shufflevector(a, b, 0, 1, 2, 3, 4, 5, 6, 7); }
__device__ __forceinline__ void st8_sc1(void* p, u64 v) { __hip_atomic_store((u64*)p, v, __ATOMIC_RELAXED, __HIP_MEMORY_SCOPE_AGENT); }
__device__ __forceinline__ u32 ld4_sc1(const u32* p) { return __hip_atomic_load(p, __ATOMIC_RELAXED, __HIP_MEMORY_SCOPE_AGENT); }
__device__ __forceinline__ void st4_sc1(u32* p, u32 v) { __hip_atomic_store(p, v, __ATOMIC_RELAXED, __HIP_MEMORY_SCOPE_AGENT); }
#define VMW0() asm volatile("s_waitcnt vmcnt(0)" ::: "memory")
#define CFENCE() asm volatile("" ::: "memory")
#define VMW0C() do { __builtin_amdgcn_s_waitcnt(0x0F70); asm volatile("" ::: "memory"); } while (0)
#define SPIN_CAP (1 << 22)
#define XPS 4
#define SPS 4
#define WPS 8
__device__ __forceinline__ int tid_now(int wv) {
  int l; asm volatile("v_mbcnt_lo_u32_b32 %0, -1, 0\n\tv_mbcnt_hi_u32_b32 %0, -1, %0" : "=v"(l));
  return wv * 64 + l;
}
#define TIDX tid_now(wv)
#define CLAIM() do { if (TIDX == 0) pre = (int)__hip_atomic_fetch_add(qh, 1u, __ATOMIC_RELAXED, __HIP_MEMORY_SCOPE_AGENT); } while (0)

#define TLS(i)
struct Params {
  const u16 *Ax, *Ac, *Awq, *Awk, *Awv, *Awo, *Bx, *Bm, *BWq, *BWk, *BWv, *BWo;
  u16* out;
  char* ws;
};
#define SY_QHEAD 0
#define SY_CC(rb) (64 + (rb) * 16)
#define SY_CW (64 + 8 * 16)
#define SY_OQ(x) (64 + 9 * 16 + (x) * 16)
#define SY_B0 (64 + 80 * 16)
#define SY_INIT(c) (SY_B0 + 64 + (c))
#define SY_VOTE(i) (SY_B0 + 128 + (i))
#define SY_JOB(x) (SY_B0 + 256 + (x) * 16)
#define SY_ARR (SY_B0 + 384)
#define SY_WX (SY_ARR + 32)
#define SY_BLK(mi, cb, g) (SY_ARR + 64 + ((mi) * 16 + (cb)) * 64 + (g))
#define FULL 12u
#define SY_WORDS (SY_ARR + 64 + 6 * 16 * 64)
#define SY_BEAT(r) (SY_WORDS + (r) * 32)

#define ACB (2048 * 4096 * 2)
#define O_PAD 0
#define O_QA (SY_BEAT(8) * 4 + O_PAD)
#define O_KA (O_QA + ACB)
#define O_VA (O_KA + ACB)
#define O_CA (O_VA + ACB)
#define O_QB (O_CA + ACB)
#define O_KB (O_QB + ACB)
#define O_VB (O_KB + ACB)
#define O_OB (O_VB + ACB)
#define O_WOF (O_OB + ACB)
#define WOB (576 * 4096 * 2)
#define O_WX (O_WOF + 2 * WOB)
#define O_XW (O_WX + 524288)
#define X_HS 0x4000
#define X_O(b) ((b) * 0x80000 + ((0x140 >> ((b) * 4)) & 15) * 4096)
#define X_MS X_HS
#define X_M (0x185000 ^ X_MS)
#define X_ANN (0x185000 + 7 * 32768)
#define X_CAL 0x40000
#define HFL 0x2000
#define CFL 0x1000
#define X_HOT(xp) ((0xC4000 ^ HFL) ^ ((xp) & 0x7000))
#define X_CS (HFL ^ CFL ^ 0x8000)
#define CSP(s) ((const u32*)(const GLB u32*)((uintptr_t)hot ^ X_CS) + (s) * 16)
#define X_END 0x200000
#define OB_OFF(r, c) (((r) >> 5) * 262144 + ((c) >> 4) * 1024 + ((r) & 31) * 32 + ((c) & 15) * 2)
#define FR_OFF(r, c) (((r) >> 5) * 262144 + ((c) >> 3) * 512 + ((r) & 31) * 16 + ((c) & 7) * 2)
#define GLB __attribute__((address_space(1)))
#define WSP(T, off) ((T*)((GLB char*)p.ws + (off)))
#define XWIN(p) (((uintptr_t)(p).ws + O_XW + 0x1FFFFFu) & ~(uintptr_t)0x1FFFFFu)

#define HOTP(xp) ((u32*)(GLB u32*)(XWIN(p) + X_HOT(xp)))

#define XS0 48
#define NAB 8
#define NBB 10
#define NOA (32 - NAB)
#define NOB (XS0 / 2 - NBB)
#define LDS_TOTAL (131072 + 8192 + 16384)
#define LDS_ALL 163840

#define SB0() __builtin_amdgcn_sched_barrier(0)
#define PJ_OOB 0x7F000000
#define RSRC(ptr, bytes) __builtin_amdgcn_make_buffer_rsrc((void*)(ptr), 0, (bytes), 0x00020000)
__device__ __forceinline__ bf8 ldsc1(__amdgpu_buffer_rsrc_t r, int off) {
  return __builtin_bit_cast(bf8, __builtin_amdgcn_raw_buffer_load_b128(r, off, 0, 16));
}
__device__ __forceinline__ u32x4 pair16(u32 a0, u32 a1, u32 b0, u32 b1) {
  auto r0 = __builtin_amdgcn_permlane16_swap(a0, b0, false, false);
  auto r1 = __builtin_amdgcn_permlane16_swap(a1, b1, false, false);
  return (u32x4){r0[0], r1[0], r0[1], r1[1]};
}
__device__ __forceinline__ int fsw(int k) { return (k & 3) | (((k >> 3) & 1) << 2); }

#define AADD(w, v) __hip_atomic_fetch_add(WSP(u32, 0) + (w), v, __ATOMIC_RELAXED, __HIP_MEMORY_SCOPE_AGENT)
#define AOR(w, v) __hip_atomic_fetch_or(WSP(u32, 0) + (w), v, __ATOMIC_RELAXED, __HIP_MEMORY_SCOPE_AGENT)
#define C_B0 (LDS_ALL - 2048)
#define BEAT_LD(job) ld4_sc1(WSP(u32, 0) + SY_BEAT((job) & 7))
#define BEAT_ON(it) do { if (((it) & 31) == 31 && (TIDX & 63) < 8) AADD(SY_BEAT(TIDX & 63), 1u); } while (0)
__device__ __forceinline__ u32 confirm(const Params& p, const char* smem, const int job, u32 b1, const int wv) {
  asm volatile("" :: "v"(b1) : "memory");
  const u32 b0 = *(const u32*)(smem + C_B0);
  if (__builtin_expect(__any(b1 == b0), 0)) {
    int it = 0;
    while (__any(BEAT_LD(job) == b0) && ++it < SPIN_CAP) __builtin_amdgcn_s_sleep(2);
  }
  u32 old = 0u;
  if (TIDX == 0) old = AOR(SY_JOB(job >> 5) + TIDX, 1u << (job & 31));
  if (TIDX < 2) AADD(TIDX ? SY_BEAT(job & 7) : SY_ARR, 1u);
  return old;
}

typedef __attribute__((ext_vector_type(2))) unsigned long long u64x2;
__device__ __forceinline__ f4 zero4() {
  u64 a, b; asm volatile("v_mov_b64 %0, 0\n\tv_mov_b64 %1, 0\n\ts_nop 1" : "=v"(a), "=v"(b));
  return __builtin_bit_cast(f4, (u64x2){a, b});
}
__device__ __forceinline__ u32x4 ones4() {
  u64 a, b; asm volatile("v_mov_b64 %0, -1\n\tv_mov_b64 %1, -1" : "=v"(a), "=v"(b));
  return __builtin_bit_cast(u32x4, (u64x2){a, b});
}
#define C_PARK 131072
#define PJ_PAD 0
__device__ __forceinline__ void proj_slot(const Params& p, int x, int j, char* smem, const int wv, int& pre, int job) {
  const int tid = TIDX, lane = tid & 63, wid = wv;
  const int l15 = lane & 15, lq = lane >> 4, q4 = l15 >> 2, p4 = lane & 3, wr = wid >> 2, wc = wid & 3;
  const __amdgpu_buffer_rsrc_t rc = RSRC(p.ws, O_XW);
  const int vB = ((wid * 4 + lq) * 4096 + (((l15 >> 1) ^ (lq | (((wid >> 1) & 1) << 2))) * 16 + (lane & 1) * 8)) * 2, vB1 = vB + 32 * 8192;
  int ao = (wr * 64 + l15) * 128 + ((lq ^ (l15 & 7)) << 4);
  int bo = (int)(uintptr_t)smem + 65536 + (lq * 8 + q4) * 256 + (((wc * 2) ^ (q4 | ((lq & 1) << 2))) << 5) + p4 * 8;
  const int vD = lane * 16;
#define PJ_ORD 0x1312
  const int ord = (j & 3) == 3 ? PJ_ORD : 0x1312;
#define PJ_NEXT ((ord >> (u * 4)) & 3)
  const int cb = (x & 1) * 8 + (j >> 2);
  __amdgpu_buffer_rsrc_t rsA, rsB;
  const u16 *pA0 = p.Bx, *pB0 = (x < 2 ? p.BWk : x < 4 ? p.BWv : p.BWq) + cb * 256;
  const u16 *pA1 = x < 2 ? p.Ax : p.Ac, *pB1 = (x < 2 ? p.Awq : x < 4 ? p.Awk : p.Awv) + cb * 256;
  asm volatile("" : "+s"(pA0), "+s"(pB0), "+s"(pA1), "+s"(pB1));
  int k2, vA, vA1, sa, sb, nvA;
#define PJ_K2(u) ((u) < 2 || x < 2 ? 1152 : 1024)
#define PJ_PREP(u) do { int ln_ = vD; asm volatile("" : "+v"(ln_)); \
    nvA = (wid * 8 + (ln_ >> 7)) * PJ_K2(u) + ((ln_ ^ (ln_ >> 3)) & 0x70); \
    asm volatile("" : "+v"(nvA)); } while (0)
#define PJ_UNIT(u) do { k2 = PJ_K2(u); rsA = RSRC((u) < 2 ? pA0 : pA1, 2048 * k2); rsB = RSRC((u) < 2 ? pB0 : pB1, k2 * 4096 - cb * 512); vA = nvA; vA1 = nvA + 64 * k2; \
    sa = ((j & 3) + ((u) & 1) * 4) * 256 * k2 - 128; sb = 0; } while (0)
#define PJ_DEST(u, rt) do { const int mi_ = ((u) >> 1) * 3 + (x >> 1), g_ = ((rt) + ((u) & 1) * 4) * 8; \
    dco = O_QA + (mi_ == 2 ? 4 : (mi_ + 5) & 7) * ACB + cb * 16384 + wc * 2048 + (g_ + wr * 2) * 262144; \
    dfl = SY_BLK(mi_, cb, g_) * 4; } while (0)
#define PJ_STAGE(dst, rs, v0, v1, so) do { const int so_ = (so); \
    __builtin_amdgcn_raw_ptr_buffer_load_lds(rs, (LDSP void*)(smem + (dst) + wid * 1024), 16, v0, so_, 0, 0); \
    __builtin_amdgcn_raw_ptr_buffer_load_lds(rs, (LDSP void*)(smem + (dst) + wid * 1024 + 8192), 16, v1, so_, 0, 0); } while (0)
#define PJ_SA(dst, so) PJ_STAGE(dst, rsA, vA, vA1, so)
#define PJ_SB(dst, so) PJ_STAGE(dst, rsB, vB, vB1, so)
#define PJ_LDA(off) do { _Pragma("unroll") for (int m = 0; m < 4; m++) _Pragma("unroll") for (int k = 0; k < 2; k++) \
    At[m][k] = *(const bf8*)(smem + (ao ^ (k * 64)) + (off) + m * 2048); } while (0)
#define PJ_TR(dst, a, off) asm volatile("ds_read_b64_tr_b16 %0, %1 offset:%2" : "=&v"(dst) : "v"(a), "i"(off) : "memory")
#define PJ_LDB(D, h) do { const int b1_ = bo ^ 32; _Pragma("unroll") for (int n = 0; n < 2; n++) _Pragma("unroll") for (int k = 0; k < 2; k++) { \
    bf4 lo_, hi_; PJ_TR(lo_, n ? b1_ : bo, (h) * 16384 + k * 8192); PJ_TR(hi_, n ? b1_ : bo, (h) * 16384 + k * 8192 + 1024); D[n][k] = cat8(lo_, hi_); } } while (0)
#define PJ_CV(ai, bj, q) ((u32x4){pk2(acc[ai][bj][(q) & 2][(q) & 1][0], acc[ai][bj][(q) & 2][(q) & 1][1]), pk2(acc[ai][bj][(q) & 2][(q) & 1][2], acc[ai][bj][(q) & 2][(q) & 1][3]), \
                                  pk2(acc[ai][bj][((q) & 2) + 1][(q) & 1][0], acc[ai][bj][((q) & 2) + 1][(q) & 1][1]), pk2(acc[ai][bj][((q) & 2) + 1][(q) & 1][2], acc[ai][bj][((q) & 2) + 1][(q) & 1][3])})
#define PJ_SW(v) pair16(v[0], v[1], v[2], v[3])
#define PJ_MMA(Z, ai, bj, D, F) do { PJ_SCHED; __builtin_amdgcn_s_setprio(1); \
    _Pragma("unroll") for (int k = 0; k < 2; k++) _Pragma("unroll") for (int m = 0; m < 4; m++) _Pragma("unroll") for (int n = 0; n < 2; n++) { \
      if (k == 0 && (Z)) asm volatile("v_mfma_f32_16x16x32_bf16 %0, %1, %2, 0" : "+v"(acc[ai][bj][m][n]) : "v"(D[n][k]), "v"(At[m][k])); \
      else asm volatile("v_mfma_f32_16x16x32_bf16 %0, %1, %2, %0" : "+v"(acc[ai][bj][m][n]) : "v"(D[n][k]), "v"(At[m][k])); \
      F(k * 8 + m * 2 + n); } \
    __builtin_amdgcn_s_setprio(0); if (PJ_PAD) __builtin_amdgcn_s_sleep(PJ_PAD); PJ_SCHED; } while (0)
#define PJ_SWI(a, b) asm volatile("v_permlane16_swap_b32 %0, %1" : "+v"(a), "+v"(b))
#define PJ_CVI(d, s, e) asm volatile("v_cvt_pk_bf16_f32 %0, %1, %2" : "=v"(d) : "v"(s[e]), "v"(s[(e) + 1]))
#define PJ_PACK(P, ai, bj, i) do { const int q_ = (i) >> 2; \
    if (((i) & 3) < 2) { PJ_CVI(P[q_][(i) & 1], acc[ai][bj][q_ & 2][q_ & 1], ((i) & 1) * 2); PJ_CVI(P[q_][2 + ((i) & 1)], acc[ai][bj][(q_ & 2) + 1][q_ & 1], ((i) & 1) * 2); } \
    else PJ_SWI(P[q_][(i) & 1], P[q_][2 + ((i) & 1)]); } while (0)
#define PJ_WV(n) asm volatile("s_waitcnt vmcnt(" #n ")" ::: "memory")
#define PJ_WL(n) asm volatile("s_waitcnt lgkmcnt(" #n ")" ::: "memory")
#define PJ_WL0 do { __builtin_amdgcn_s_waitcnt(0xC07F); asm volatile("" ::: "memory"); } while (0)
#define PJ_BAR __builtin_amdgcn_s_barrier()
#define PJ_SCHED __builtin_amdgcn_sched_barrier(0)
#define PJ_NOP do { PJ_SCHED; asm volatile("s_nop 7"); PJ_SCHED; } while (0)
#define PJ_DS(v, ai, bj, q) do { PJ_SCHED; __builtin_amdgcn_raw_buffer_store_b128(v, rc, vD + ((q) & 1) * 1024, dco + ((ai) * 4 + ((q) >> 1)) * 262144 + (bj) * 8192, 16); \
    asm volatile("s_nop 1"); PJ_SCHED; } while (0)
#define PJ_PARK(q) (*(u32x4*)(smem + C_PARK + wid * 4096 + vD + (q) * 1024))
#define PJ_PUB(ai, n) do { if (wid == 4) __builtin_amdgcn_raw_buffer_store_b32(FULL, rc, vD < (n) * 16 ? vD >> 2 : PJ_OOB, dfl + (ai) * 16, 16); } while (0)
#define PJ_ST()
#define PJ_TOUCH(ptr, bytes, vo, so) __builtin_amdgcn_raw_ptr_buffer_load_lds(RSRC(ptr, bytes), (LDSP void*)(smem + C_PARK + wid * 4096), 4, vo, so, 0, 0)
#define PJ_SL 0x1E
#define PJ_PT 0
  static_assert(XS0 >= 46 && PJ_SL == 0x1E && PJ_PT == 0, "");
  int emp = 128;
  u32 old = 0u;
#define PJ_WARM(k) do { \
    if (u == 0 && (j & 3) != 3) { \
      int vt_ = vD; asm volatile("" : "+v"(vt_)); vt_ <<= 3; \
      const int i_ = (((x & 1) * 8 + (j >> 2)) * 2 + (j & 1)) * 8 + wid + (k) * 256; \
      const int nw_ = x < 2 ? 576 : 512, na_ = x < 2 ? 288 : x < 4 ? 256 : 144, no_ = (x >> 1) * 96 + 96; \
      const u64 w_ = (u64)(pB1 - cb * 256), a_ = x < 4 ? (u64)pA1 : (u64)(pA0 + 1024 * 576), o_ = (u64)p.Awo + (x < 2 ? 0 : x < 4 ? 96 : 288) * 8192; \
      PJ_TOUCH(i_ < nw_ ? w_ : i_ < nw_ + na_ ? a_ : o_, i_ < nw_ + na_ ? WOB : no_ * 8192, vt_, (i_ < nw_ ? i_ : i_ < nw_ + na_ ? i_ - nw_ : i_ - nw_ - na_) * 8192); \
    } else { \
      const int e_ = emp + (u * 4 + (k)) * 3; \
      __builtin_amdgcn_raw_buffer_store_b128(ones4(), rc, vD, e_ < 128 ? O_OB + (e_ >> 1) * 262144 + cb * 16384 + (e_ & 1) * 8192 + wid * 1024 : PJ_OOB, 16); \
    } } while (0)
#define PJ_F0(i)
#define PJ_F1(i) do { if (S == 8) PJ_PACK(pa, 0, 0, i); } while (0)
#define PJ_F2(i) do { if (S == 8) PJ_PACK(pb, 0, 1, i); if (S == 0) PJ_PACK(pb, 1, 1, i); } while (0)
#define PJ_F3(i) do { if (S == 8) PJ_PACK(pa, 1, 0, i); if (S == 5 && (i) == 0) { PJ_SCHED; PJ_PREP(PJ_NEXT); PJ_SCHED; } } while (0)
#define PJ_STEP(SS) do { constexpr int S = SS; PJ_ST(); \
    PJ_LDB(B0, 0); PJ_SCHED; PJ_LDA(0); PJ_SA((bsel ^ 32768) + 16384, sa + 128 * k2); \
    PJ_WL(8); PJ_BAR; PJ_WL0; PJ_MMA(S == 0, 0, 0, B0, PJ_F0); PJ_BAR; PJ_SCHED; \
    if (S == 7) PJ_UNIT(PJ_NEXT); \
    PJ_LDB(B1, 1); PJ_SB(65536 + bsel, sb); \
    if (S == 0) { PJ_DS(pb[2], 0, 1, 2); PJ_DS(pb[3], 0, 1, 3); } \
    if ((PJ_SL >> S) & 1) PJ_WARM(PJ_PT + __builtin_popcount(PJ_SL & ((1 << S) - 1))); \
    PJ_BAR; PJ_WL0; PJ_MMA(S == 0, 0, 1, B1, PJ_F1); PJ_BAR; \
    PJ_LDA(16384); sa += 128; PJ_SA(bsel, sa); \
    if (S == 8) { PJ_SCHED; PJ_DEST(u, j & 3); _Pragma("unroll") for (int q = 0; q < 4; q++) PJ_PARK(q) = pa[q]; PJ_SCHED; } \
    PJ_BAR; PJ_WL0; PJ_MMA(S == 0, 1, 0, B0, PJ_F2); PJ_BAR; PJ_SCHED; \
    { u32x4 c0_, c1_; \
      if (S < 2) { c0_ = PJ_PARK(2 * S); c1_ = PJ_PARK(2 * S + 1); PJ_SCHED; } \
      PJ_SB(65536 + bsel + 16384, sb + 256); sb += 64 * 8192; \
      if (S == 8) { PJ_DS(pb[0], 0, 1, 0); PJ_DS(pb[1], 0, 1, 1); } \
      else if (S < 2) { PJ_DS(c0_, 0, 0, (2 * S) & 3); PJ_DS(c1_, 0, 0, (2 * S + 1) & 3); } \
      else if (S < 4) { PJ_DS(pa[(2 * S) & 3], 1, 0, (2 * S) & 3); PJ_DS(pa[(2 * S + 1) & 3], 1, 0, (2 * S + 1) & 3); } \
      else if (S < 6) { PJ_DS(pb[(2 * S) & 3], 1, 1, (2 * S) & 3); PJ_DS(pb[(2 * S + 1) & 3], 1, 1, (2 * S + 1) & 3); } } \
      \
    asm volatile("s_waitcnt vmcnt(%0)" :: "i"((S == 0 ? 10 : S < 6 || S == 8 ? 8 : 6) + ((PJ_SL >> S) & 1)) : "memory"); \
    PJ_BAR; \
    if (S == 2) PJ_PUB(0, 4); \
    if (S == 7) PJ_PUB(1, 4); \
    PJ_MMA(S == 0, 1, 1, B1, PJ_F3); PJ_BAR; \
    bsel ^= 32768; ao ^= 32768; bo ^= 32768; } while (0)
  f4 acc[2][2][4][2];
  bf8 At[4][2], B0[2][2], B1[2][2];
  u32x4 pa[4], pb[4];
  int bsel = 0, dco = PJ_OOB, dfl = PJ_OOB, u = 0;
  auto quick = [&](auto NBC, const int hf, const int ch) __attribute__((always_inline)) {
    constexpr int NB = decltype(NBC)::value, NS = 5 - NB;
#define PJ_FS(t) do { PJ_SA(((t) % NS) * 16384, hf * 128 * 1152 + (t) * 128); \
      _Pragma("unroll") for (int h_ = 0; h_ < NB; h_++) PJ_SB(NS * 16384 + (((t) % NS) * NB + h_) * 16384, (t) * 64 * 8192 + (ch + h_) * 256); PJ_SCHED; } while (0)
#pragma unroll
    for (int t = 0; t < NS - 1; t++) PJ_FS(t);
    bo += NS * 16384 - 65536;
#pragma unroll
    for (int t = 0; t < 9; t++) {
      PJ_ST();
      if (t == 8) PJ_WV(0); else if (NB == 2) PJ_WV(6); else if (t == 7) PJ_WV(4); else PJ_WV(8);
      PJ_BAR; PJ_SCHED;
      if (wr && t + NS - 1 < 9) PJ_FS(t + NS - 1);
      PJ_LDB(B0, 0); PJ_LDA((t % NS) * 16384);
      if (NB == 2) PJ_LDB(B1, 1);
      PJ_SCHED;
      bo += t == 8 ? 65536 - NS * 16384 - (8 % NS) * NB * 16384 : (t % NS == NS - 1 ? -(NS - 1) * NB * 16384 : NB * 16384);
      PJ_WL0;
      PJ_MMA(t == 0, 0, 0, B0, PJ_F0);
      if (NB == 2) PJ_MMA(t == 0, 0, 1, B1, PJ_F0);
      if (!wr && t + NS - 1 < 9) PJ_FS(t + NS - 1);
    }
    PJ_NOP;
    u32 b1;
    if (NB == 1 && job >= 0 && wid == 0) b1 = BEAT_LD(job);
#pragma unroll
    for (int q = 0; q < 4 * NB; q++) { const u32x4 v = PJ_CV(0, q >> 2, q & 3); PJ_DS(PJ_SW(v), hf, ch + (q >> 2), q & 3); }
    if (NB == 2 && tid == 0) { *(u32*)(smem + C_B0 + 4) = old; PJ_WL0; }
    PJ_WV(0); PJ_BAR;
    if (NB == 1 && job >= 0 && wid == 0) old = confirm(p, smem, job, b1, wv);
    if (tid < 4) __hip_atomic_fetch_or(WSP(u32, dfl) + hf * 4 + tid, NB == 2 ? FULL : 4u << ch, __ATOMIC_RELAXED, __HIP_MEMORY_SCOPE_AGENT);
  };
  if (((j + 1) & 3) < 2) {
    PJ_PREP(0); PJ_UNIT(0); PJ_DEST(0, 0);
    quick(std::integral_constant<int, 1>{}, 0, j & 1);
    if ((j & 3) == 0) {
      quick(std::integral_constant<int, 2>{}, 1, 0);
      emp = (__builtin_amdgcn_readfirstlane(*(const u32*)(smem + C_B0 + 4)) >> (job & 31)) & 1 ? 129 : XS0 * 2 - 12 + (x >> 1);
    }
    if ((j & 3) == 0 || PJ_ORD != 0x1312) u = 2;
    dco = PJ_OOB; dfl = PJ_OOB; job = -1;
  }
#pragma unroll
  for (int a = 0; a < 16; a++) asm volatile("" : "=v"(acc[a >> 3][(a >> 2) & 1][a & 3][0]), "=v"(acc[a >> 3][(a >> 2) & 1][a & 3][1]));
#pragma unroll
  for (int a = 0; a < 4; a++) asm volatile("" : "=v"(pa[a]), "=v"(pb[a]));
  PJ_PREP(u); PJ_UNIT(u);
  const int cs = x < 4 && (j & 3) == 2 ? (x * 8 + (j >> 2)) * 128 : -1;
#pragma unroll
  for (int i = 0; i < 2; i++) pa[i] = __builtin_amdgcn_raw_buffer_load_b128(RSRC(p.BWo, WOB), (tid >> 3) * 1152 + (tid & 7) * 16, cs >= 0 ? (cs + i * 64) * 1152 + 1024 : PJ_OOB, 0);
  PJ_SCHED;
  PJ_SB(65536, 0); PJ_SA(0, sa + 128);
  PJ_SB(65536 + 16384, 256); PJ_SA(16384, sa + 128 + 128 * k2);
  PJ_SCHED;
#pragma unroll
  for (int i = 0; i < 2; i++) __builtin_amdgcn_raw_buffer_store_b128(pa[i], rc, (tid & 7) * 65536 + (tid >> 3) * 16, cs >= 0 ? O_WX + (cs + i * 64) * 16 : PJ_OOB, 16);
  PJ_SCHED;
  if (wr == 1) PJ_BAR;
  PJ_WV(6); PJ_BAR;
  u32 b1;
  if (job >= 0 && wid == 0) b1 = BEAT_LD(job);
  PJ_SCHED;
  PJ_SB(65536 + 32768, 64 * 8192); PJ_SA(32768, sa + 256);
  PJ_SB(65536 + 32768 + 16384, 64 * 8192 + 256);
  PJ_WV(6);
  if (job >= 0 && wid == 0) confirm(p, smem, job, b1, wv);
  PJ_BAR;
  if (tid == 256 && cs >= 0) AOR(SY_WX, 1u << (cs >> 7));
#pragma unroll
  for (int i = 0; i < PJ_PT; i++) PJ_WARM(i);
  sa += 256; sb = 128 * 8192;
#pragma unroll 1
  for (;; u = PJ_NEXT) {
    PJ_STEP(0); PJ_STEP(1); PJ_STEP(2); PJ_STEP(3); PJ_STEP(4); PJ_STEP(5);
    int more = k2 - 1024; asm volatile("" : "+s"(more));
    if (more) PJ_STEP(6);
    PJ_STEP(7); PJ_STEP(8);
    if (u == 1) break;
  }
  PJ_ST();
  PJ_NOP;
  const u32 left = __builtin_amdgcn_raw_buffer_load_b32(rc, wid ? PJ_OOB : vD < 128 ? SY_JOB(0) * 4 + vD * 4 : SY_INIT(0) * 4, 0, 16);
#define FSL 0
#define FWN 15
#define PJ_FD(v, ai, bj, q) do { PJ_DS(v, ai, bj, q); if (FSL) __builtin_amdgcn_s_sleep(FSL); if (FWN < 15) asm volatile("s_waitcnt vmcnt(%0)" :: "i"(FWN) : "memory"); } while (0)
  PJ_FD(pb[2], 0, 1, 2); PJ_FD(pb[3], 0, 1, 3);
#pragma unroll
  for (int q = 0; q < 4; q++) pb[q] = PJ_PARK(q);
#pragma unroll
  for (int q = 0; q < 4; q++) PJ_FD(pa[q], 1, 0, q);
#pragma unroll
  for (int q = 0; q < 4; q++) PJ_FD(pb[q], 0, 0, q);
#pragma unroll
  for (int q = 0; q < 4; q++) { const u32x4 v = PJ_CV(1, 1, q); PJ_FD(PJ_SW(v), 1, 1, q); }
  pre = -1;
  const int xp = __builtin_amdgcn_readlane(left, 8);
  if (__all(left >= (vD < 128 ? ~0u : 1u)) && vD == 0) pre = (int)__hip_atomic_fetch_add(HOTP(xp) + SY_QHEAD, 1u, __ATOMIC_RELAXED, __HIP_MEMORY_SCOPE_AGENT) + (xp & 0x7000);
  PJ_WV(0);
  if (wr == 0) PJ_BAR;
  PJ_BAR;
  if (emp != 129) PJ_PUB(0, 8);
  PJ_ST();
}

namespace fa {
constexpr float SCALE = 0.08838834764831845f;
constexpr int NW = 8, QBLK = 32, KVBLK = 64, QB = NW * QBLK, D = 128;
constexpr int SHM_V = KVBLK * D * 2, SHM_K = KVBLK * D * 2;
#define SBAR() __builtin_amdgcn_sched_barrier(0)
#define MF32(a, b, c) __builtin_amdgcn_mfma_f32_32x32x16_bf16(a, b, c, 0, 0, 0)
#define FMX3(a, b, c) __builtin_elementwise_maximum(__builtin_elementwise_maximum(a, b), c)
#define AT_PAD 0
#define EBAR 14
#define MX0 3
#define ATHR 8.f
#define fadd1(a, b) ((a) + (b))
#define fmul1(a, b) ((a) * (b))
__device__ __forceinline__ void attn_block(const char* __restrict__ Qp, const char* __restrict__ Kh, const char* __restrict__ Vh,
                                           char* __restrict__ Op, int P0, char* lds, const int wv, u32* qh, int& pre) {
  const int tid = TIDX, wid = wv, lane = tid & 63, r32 = lane & 31, hi = lane >> 5, grp = wid >> 2;
  const int NT = (P0 + QB) / KVBLK;
  const int rb = wid ^ 4;
  const int TL = NT - 4 + (rb >> 1);
  const int rem = 2 * (NT - TL) - 2 - grp;
  float l_reg = 0.f;
  f32x16 o[4] = {}, s0, s1, nm = {};
  int ko = lane * 16;
  int vo = 4 * SHM_K + ((lane >> 4) & 1) * (1024 + 128) + ((lane >> 1) & 1) * 512 + ((hi ^ ((lane >> 1) & 1)) << 6) + ((lane >> 2) & 3) * 16 + (lane & 1) * 8;
  const __amdgpu_buffer_rsrc_t rk = RSRC(Kh, (2 * NT - 1) * 262144 + 8192), rv = RSRC(Vh, (2 * NT - 1) * 262144 + 8192);
  const int gk = tid * 16, gv = gk ^ (((tid >> 5) & 3) << 6);
#define DMA(rs, go, dst, so) __builtin_amdgcn_raw_ptr_buffer_load_lds(rs, (LDSP void*)(lds + (dst) + wid * 1024), 16, go, so, 0, 0)
#define ISSUE_K(t) do { const int s_ = (t) * 524288, d_ = ((t) & 3) * SHM_K; DMA(rk, gk, d_, s_); DMA(rk, gk, d_ + 8192, s_ + 262144); } while (0)
#define ISSUE_V(t) do { const int s_ = (t) * 524288, d_ = 4 * SHM_K + ((t) & 3) * SHM_V; DMA(rv, gv, d_, s_); DMA(rv, gv, d_ + 8192, s_ + 262144); } while (0)
#define PHASE(n) do { SBAR(); asm volatile("s_waitcnt vmcnt(" #n ")" ::: "memory"); __builtin_amdgcn_s_barrier(); asm volatile("" ::: "memory"); SBAR(); } while (0)
#define LDK(d0) do { kf[(d0) % 3][0] = *(const bf8*)(lds + ko + (d0) * 1024); kf[(d0) % 3][1] = *(const bf8*)(lds + ko + (d0) * 1024 + 8192); } while (0)
#define LDV(i) do { const int o_ = ((i) & 3) * 2048 + (((i) >> 2) & 1) * 256 + ((i) >> 3) * 8192; vf[(i) & 3] = cat8(trrd(lds + vo + o_), trrd(lds + vo1 + o_)); } while (0)
#define AST(i)
#define AP(i)
  AP(0);
#define QK(NEXT) do { _Pragma("unroll") for (int d0 = 0; d0 < 8; ++d0) { \
      if (d0 + 2 < 8) LDK(d0 + 2); else { NEXT(2 * (d0 - 6)); NEXT(2 * (d0 - 6) + 1); } \
      SBAR(); \
      if (d0) { s0 = MF32(kf[d0 % 3][0], qr[d0], s0); s1 = MF32(kf[d0 % 3][1], qr[d0], s1); } \
      else { asm volatile("v_mfma_f32_32x32x16_bf16 %0, %1, %2, %3" : "+v"(s0) : "v"(kf[0][0]), "v"(qr[0]), "v"(nm)); \
             asm volatile("v_mfma_f32_32x32x16_bf16 %0, %1, %2, %3" : "+v"(s1) : "v"(kf[0][1]), "v"(qr[0]), "v"(nm)); } \
      SBAR(); } } while (0)
#define NOV(i)
#define MAXS(i) do { \
      if ((i) == 0) { mx = FMX3(s0[0], s0[1], s0[2]); my = FMX3(s1[0], s1[1], s1[2]); } \
      else if ((i) < 7) { mx = FMX3(mx, s0[2 * (i) + 1], s0[2 * (i) + 2]); my = FMX3(my, s1[2 * (i) + 1], s1[2 * (i) + 2]); } \
      else { mx = FMX3(mx, s0[15], s1[15]); mx = __builtin_elementwise_maximum(mx, my); asm volatile("" : "+v"(mx)); } } while (0)
  float mx, my;
  constexpr float C2 = 1.4426950408889634f * SCALE;
  bf8 qr[8], kf[3][2], vf[4], pa[4];
#pragma unroll
  for (int d0 = 0; d0 < 8; ++d0) qr[d0] = *(const bf8*)(Qp + rb * 262144 + d0 * 1024 + lane * 16);
  SBAR();
  ISSUE_K(0); ISSUE_K(1); ISSUE_V(0); ISSUE_K(2); ISSUE_V(1);
  SBAR();
  asm volatile("" : "=v"(s0), "=v"(s1), "+v"(nm), "+v"(o[0]), "+v"(o[1]), "+v"(o[2]), "+v"(o[3]));
  SBAR();
#pragma unroll
  for (int d0 = 0; d0 < 8; ++d0) {
    u32x4 q = __builtin_bit_cast(u32x4, qr[d0]);
#pragma unroll
    for (int i = 0; i < 4; i++) q[i] = pk2(__uint_as_float(q[i] << 16) * C2, __uint_as_float(q[i] & 0xFFFF0000u) * C2);
    qr[d0] = __builtin_bit_cast(bf8, q);
    asm volatile("" : "+v"(qr[d0]));
  }
  AP(8);
  PHASE(8);
  AP(1);
  if (grp) PHASE(6);
  LDK(0); LDK(1);
  QK(NOV);
#pragma unroll
  for (int i = 0; i < 8; ++i) MAXS(i);
  AP(2);
  PHASE(4);
  AP(3);
  auto tile = [&](const int t, auto LASTC) __attribute__((always_inline)) {
    constexpr bool LAST = decltype(LASTC)::value;
    AST(0);
    ISSUE_K(t + 3); ISSUE_V(t + 2);
    SBAR();
    if constexpr (LAST) {
      const int dq = r32 - 4 * hi;
      if (rb & 1) {
#pragma unroll
        for (int r = 0; r < 16; ++r) { if (dq < (r & 3) + 8 * (r >> 2)) s1[r] = -__builtin_inff(); SBAR(); }
      } else {
#pragma unroll
        for (int r = 0; r < 16; ++r) { if (dq < (r & 3) + 8 * (r >> 2)) s0[r] = -__builtin_inff(); s1[r] = -__builtin_inff(); SBAR(); }
      }
#pragma unroll
      for (int i = 0; i < 8; ++i) MAXS(i);
    }
    {
      if (__builtin_expect(t == 0 || __any(mx > ATHR), 0)) {
        auto rr = __builtin_amdgcn_permlane32_swap(__float_as_uint(mx), __float_as_uint(mx), false, false);
        mx = __builtin_elementwise_maximum(__uint_as_float(rr[0]), __uint_as_float(rr[1]));
        if (t) mx = __builtin_elementwise_maximum(mx, 0.f);
        const float alpha = t ? __builtin_amdgcn_exp2f(-mx) : 1.f, nn = nm[0] - mx;
        mx = -mx;
#pragma unroll
        for (int r = 0; r < 16; ++r) { s0[r] = fadd1(s0[r], mx); s1[r] = fadd1(s1[r], mx); nm[r] = nn; }
#pragma unroll
        for (int d = 0; d < 4; ++d)
#pragma unroll
          for (int r = 0; r < 16; ++r) o[d][r] = fmul1(o[d][r], alpha);
        l_reg *= alpha;
        asm volatile("" : "+v"(nm));
      }
    }
    AST(1);
    float x[32], sb = 0.f;
    u32 pw[16];
#pragma unroll
    for (int r = 0; r < 18; ++r) {
      SBAR();
      if (r < 16) { x[2 * r] = __builtin_amdgcn_exp2f(r < 8 ? s0[2 * r] : s1[2 * r - 16]); x[2 * r + 1] = __builtin_amdgcn_exp2f(r < 8 ? s0[2 * r + 1] : s1[2 * r - 15]); }
      if (r >= 2) {
        l_reg = fadd1(l_reg, x[2 * r - 4]);
        sb = r == 2 ? x[1] : fadd1(sb, x[2 * r - 3]);
        pw[r - 2] = pk2(x[2 * r - 4], x[2 * r - 3]); }
    }
    SBAR();
    l_reg = fadd1(l_reg, sb);
#pragma unroll
    for (int i = 0; i < 4; ++i) pa[i] = __builtin_bit_cast(bf8, (u32x4){pw[4 * i], pw[4 * i + 1], pw[4 * i + 2], pw[4 * i + 3]});
    asm volatile("" : "+v"(pa[0]), "+v"(pa[1]), "+v"(pa[2]), "+v"(pa[3]), "+v"(l_reg));
    ko ^= (t & 1) ? 3 * SHM_K : SHM_K;
    const int vo1 = vo ^ 128;
    SBAR();
    AST(3);
    if constexpr (!LAST) { LDK(0); LDK(1); } else { LDV(0); LDV(1); LDV(2); LDV(3); }
    AST(4);
    PHASE(4);
    AST(5);
    __builtin_amdgcn_s_setprio(1);
    if constexpr (!LAST) QK(LDV);
    AST(6);
#pragma unroll
    for (int i = 0; i < 16; ++i) {
      SBAR();
      o[i & 3] = MF32(vf[i & 3], pa[i >> 2], o[i & 3]);
      SBAR();
      if (i + 4 < 16) LDV(i + 4);
      if constexpr (!LAST) if (i >= MX0 && i < MX0 + 8) MAXS(i - MX0);
      if (AT_PAD && i == EBAR) __builtin_amdgcn_s_sleep(AT_PAD);
      if (i == EBAR && (!LAST || rem)) PHASE(4);
    }
    __builtin_amdgcn_s_setprio(0);
    vo ^= (t & 1) ? 3 * SHM_V : SHM_V;
    AST(7);
  };
#pragma unroll 1
  for (int t = 0; t < TL; t++) {
    tile(t, std::false_type{});
  }
  AP(4);
  tile(TL, std::true_type{});
  AP(5);
  ISSUE_V(TL + 3);
  SBAR();
  {
    const float inv = __builtin_amdgcn_rcpf(swadd(l_reg, l_reg));
    const __amdgpu_buffer_rsrc_t ro = RSRC(Op + rb * 262144, 8192);
#pragma unroll
    for (int i = 0; i < 8; ++i) {
      const int g = (i & 1) * 8;
#define ON(k) fmul1(o[i >> 1][g + (k)], inv)
      auto r0 = __builtin_amdgcn_permlane32_swap(pk2(ON(0), ON(1)), pk2(ON(4), ON(5)), false, false);
      auto r1 = __builtin_amdgcn_permlane32_swap(pk2(ON(2), ON(3)), pk2(ON(6), ON(7)), false, false);
#undef ON
      __builtin_amdgcn_raw_buffer_store_b128((u32x4){r0[0], r1[0], r0[1], r1[1]}, ro, r32 * 32 + hi * 16 + (i & 3) * 1024, (i >> 2) * 4096, 16);
      if (i == 3 && rem > 1) PHASE(6);
    }
  }
  AP(6);
  if (rem > 2) PHASE(10);
  if (rem > 3) { PHASE(8); PHASE(8); }
  CLAIM();
  VMW0();
  AP(7);
  __syncthreads();
}
#undef DMA
#undef ISSUE_K
#undef ISSUE_V
#undef PHASE
#undef LDK
#undef LDV
#undef QK
#undef NOV
#undef MAXS
}

__device__ __forceinline__ void wof_task(const Params& p, int kc, char* smem, const int wv, u32* qh, int& pre) {
  const int tid = TIDX, lane = tid & 63, wid = wv;
  const int l15 = lane & 15, lq = lane >> 4, q4 = l15 >> 2, p4 = lane & 3;
  const __amdgpu_buffer_rsrc_t rw = RSRC(WSP(u16, O_WOF), 2 * WOB);
  bf8 st[2][9];
#pragma unroll
  for (int m = 0; m < 2; m++)
#pragma unroll
    for (int i = 0; i < 9; i++) st[m][i] = *(const bf8*)((m ? p.BWo : p.Awo) + (size_t)kc * 64 * 576 + (tid + 512 * i) * 8);
  CLAIM();
#pragma unroll
  for (int m = 0; m < 2; m++) {
#pragma unroll
    for (int i = 0; i < 9; i++) {
      const int id = tid + 512 * i, row = id / 72, ch = id % 72;
      *(bf8*)(smem + row * 1280 + (((ch >> 1) ^ fsw(row)) << 5) + ((ch & 1) << 4)) = st[m][i];
    }
    asm volatile("s_waitcnt lgkmcnt(0)" ::: "memory"); __builtin_amdgcn_s_barrier(); asm volatile("" ::: "memory");
#pragma unroll
    for (int i = 0; i < 9; i++) {
      const int f = wid * 9 + i, nt = f >> 1, k2 = f & 1;
      const int r0 = k2 * 32 + lq * 8 + q4, r1 = r0 + 4;
      bf4 lo = trrd(smem + r0 * 1280 + ((nt ^ fsw(r0)) << 5) + p4 * 8);
      bf4 hi = trrd(smem + r1 * 1280 + ((nt ^ fsw(r1)) << 5) + p4 * 8);
      __builtin_amdgcn_raw_buffer_store_b128(__builtin_bit_cast(u32x4, cat8(lo, hi)), rw, ((nt * 128 + kc * 2 + k2) * 64 + lane) * 16, m * WOB, 16);
    }
    if (m == 0) { asm volatile("s_waitcnt lgkmcnt(0)" ::: "memory"); __builtin_amdgcn_s_barrier(); asm volatile("" ::: "memory"); }
  }
  VMW0();
  __syncthreads();
}

template <int MT>
__device__ __forceinline__ void out_tile(const char* A, const char* W, u16* C, char* smem, const int wv, u32* qh, int& pre) {
  constexpr int U = 32 / MT, SC = U / 2, DP = MT / 4, NCH = 4 * MT, CW = 32768 / MT;
  const int tid = TIDX, lane = tid & 63, wid = wv;
  const int l15 = lane & 15, lq = lane >> 4, kh = wid >> 2, wc = wid & 3;
  const __amdgpu_buffer_rsrc_t ra = RSRC(A, MT / 2 * 262144), rw = RSRC(W + wc * 3 * 131072 + kh * 1024, 3 * 131072 - kh * 1024);
  const int va = tid * 16, vw = lane * 16;
  int ao = kh * 2048 + (lq >> 1) * 1024 + l15 * 32 + (lq & 1) * 16;
  f4 acc[MT][3];
  bf8 wf[U][3], af[2][MT];
#define OT_DMA(i, buf, so) __builtin_amdgcn_raw_ptr_buffer_load_lds(ra, (LDSP void*)(smem + (buf) + (i) * 8192 + wid * 1024), 16, va, \
                                                                    (so) + (MT == 4 ? ((i) >> 1) * 262144 + ((i) & 1) * 8192 : (i) * 262144), 0, 0)
#define OT_LDA(d, base, off) do { _Pragma("unroll") for (int m = 0; m < MT; m++) af[d][m] = *(const bf8*)(smem + (base) + (off) + (m >> 1) * 2 * CW + (m & 1) * 512); } while (0)
#define OT_VM(n) __builtin_amdgcn_s_waitcnt(0x0F70 | ((n) & 15) | (((n) >> 4) << 14))
#define OT_PAIR(buf, so) do { _Pragma("unroll") for (int i = 0; i < 8; i++) OT_DMA(i & 3, (buf) + (i >> 2) * 32768, (so) + (i >> 2) * 2 * CW); SB0(); } while (0)
  if constexpr (MT == 4) {
  OT_PAIR(0, 0);
#pragma unroll
  for (int u = 0; u < U; u++) {
    if (u == U - 1) OT_PAIR(65536, 4 * CW);
#pragma unroll
    for (int j = 0; j < 3; j++) wf[u][j] = __builtin_bit_cast(bf8, __builtin_amdgcn_raw_buffer_load_b128(rw, vw, u * 2048 + j * 131072, 0));
    SB0();
  }
#pragma unroll
  for (int m = 0; m < MT; m++)
#pragma unroll
    for (int j = 0; j < 3; j++) acc[m][j] = (f4){0, 0, 0, 0};
  asm volatile("s_waitcnt vmcnt(%0)" :: "i"(U * 3 + 8) : "memory"); __builtin_amdgcn_s_barrier(); asm volatile("" ::: "memory");
  OT_LDA(0, ao, 0);
  int bs = 0;
#pragma unroll 1
  for (int c = 0; c < NCH; c += 2) {
    const int sw = c < NCH - 2 ? (c + 2) * CW : PJ_OOB, sd = c < NCH - 4 ? (c + 4) * 2 * CW : PJ_OOB;
#pragma unroll
    for (int u = 0; u < U; u++) {
      SB0();
      if (u < U - 1) OT_VM(3 * (U - 1) + 8); else OT_VM(3 * (U - 1));
      if (u == U - 1) {
        asm volatile("s_waitcnt lgkmcnt(0)" ::: "memory"); __builtin_amdgcn_s_barrier(); asm volatile("" ::: "memory");
        ao ^= 65536;
      }
      OT_LDA((u + 1) & 1, ao, u == U - 1 ? 0 : ((u + 1) / SC) * 32768 + ((u + 1) % SC) * 4096);
      SB0();
      if (u == U - 1) OT_PAIR(bs, sd);
#pragma unroll
      for (int j = 0; j < 3; j++) {
#pragma unroll
        for (int m = 0; m < MT; m++) acc[m][j] = MFMA(wf[u][j], af[u & 1][m], acc[m][j]);
        SB0();
        wf[u][j] = __builtin_bit_cast(bf8, __builtin_amdgcn_raw_buffer_load_b128(rw, vw, sw + u * 2048 + j * 131072, 0));
        SB0();
      }
    }
    bs ^= 65536;
  }
  } else {
#pragma unroll
  for (int i = 0; i < 4; i++) OT_DMA(i, 0, 0);
  SB0();
#pragma unroll
  for (int u = 0; u < U; u++) {
#pragma unroll
    for (int d = 0; d < DP; d++) OT_DMA((u % SC) * DP + d, 32768 + (u / SC) * 32768, (1 + u / SC) * 2 * CW);
#pragma unroll
    for (int j = 0; j < 3; j++) wf[u][j] = __builtin_bit_cast(bf8, __builtin_amdgcn_raw_buffer_load_b128(rw, vw, u * 2048 + j * 131072, 0));
    SB0();
  }
#pragma unroll
  for (int m = 0; m < MT; m++)
#pragma unroll
    for (int j = 0; j < 3; j++) acc[m][j] = (f4){0, 0, 0, 0};
  asm volatile("s_waitcnt vmcnt(%0)" :: "i"(U * (DP + 3)) : "memory"); __builtin_amdgcn_s_barrier(); asm volatile("" ::: "memory");
  OT_LDA(0, ao, 0);
  int bs = 0;
#pragma unroll 1
  for (int c = 0; c < NCH; c += 2) {
    const int sw = c < NCH - 2 ? (c + 2) * CW : PJ_OOB;
#pragma unroll
    for (int u = 0; u < U; u++) {
      SB0();
      __builtin_amdgcn_s_waitcnt(MT == 4 ? 0x4F7C : 0x0F7F);
      if (u % SC == SC - 1) { asm volatile("s_waitcnt lgkmcnt(0)" ::: "memory"); __builtin_amdgcn_s_barrier(); asm volatile("" ::: "memory"); }
      if (u == U - 1) ao ^= 65536;
      OT_LDA((u + 1) & 1, ao, u == U - 1 ? 0 : ((u + 1) / SC) * 32768 + ((u + 1) % SC) * 4096);
      SB0();
      const int cn = c + 3 + u / SC;
#pragma unroll
      for (int j = 0; j < 3; j++) {
#pragma unroll
        for (int m = 0; m < MT; m++) {
          acc[m][j] = MFMA(wf[u][j], af[u & 1][m], acc[m][j]);
          if (j == 0 && (m & 3) == 1) { SB0(); OT_DMA((u % SC) * DP + (m >> 2), u < SC ? (bs ^ 65536) + 32768 : bs, cn < NCH ? cn * 2 * CW : PJ_OOB); SB0(); }
        }
        SB0();
        wf[u][j] = __builtin_bit_cast(bf8, __builtin_amdgcn_raw_buffer_load_b128(rw, vw, sw + u * 2048 + j * 131072, 0));
        SB0();
      }
    }
    bs ^= 65536;
  }
  }
  CLAIM();
  VMW0C();
  asm volatile("s_waitcnt lgkmcnt(0)" ::: "memory"); __builtin_amdgcn_s_barrier(); asm volatile("" ::: "memory");
#pragma unroll
  for (int m = 0; m < MT / 2; m++)
#pragma unroll
    for (int j = 0; j < 3; j++) *(f4*)(smem + (m * 3 + j) * 8192 + va) = kh ? acc[m][j] : acc[MT / 2 + m][j];
  __syncthreads();
#pragma unroll
  for (int m = 0; m < MT / 2; m++) {
    f4 t[3];
#pragma unroll
    for (int j = 0; j < 3; j++) { const f4 o = *(const f4*)(smem + (m * 3 + j) * 8192 + (va ^ 4096)); t[j] = kh ? o + acc[MT / 2 + m][j] : acc[m][j] + o; }
    u16* c = C + (size_t)(kh * MT * 8 + m * 16 + l15) * 1152 + wc * 48;
    *(u32x4*)(c + (lq & 1) * 16 + (lq >> 1) * 8) = pair16(pk2(t[0][0], t[0][1]), pk2(t[0][2], t[0][3]), pk2(t[1][0], t[1][1]), pk2(t[1][2], t[1][3]));
    *(u32x2*)(c + 32 + lq * 4) = (u32x2){pk2(t[2][0], t[2][1]), pk2(t[2][2], t[2][3])};
  }
  __syncthreads();
#undef OT_DMA
#undef OT_LDA
}

#define CLAIMS() do { const int l_ = TIDX; if (l_ < 64) { \
    const u32 v_ = ld4_sc1(hot + SY_OQ(l_ < 6 ? l_ : 0)); \
    if (l_ == 0) pre = (int)__hip_atomic_fetch_add(hot + SY_OQ(6), 1u, __ATOMIC_RELAXED, __HIP_MEMORY_SCOPE_AGENT); \
    pre |= (int)((u32)__ballot(l_ < 6 && v_ < ((l_ & 1) ? NOB : NOA)) << 16); } } while (0)
#define CLS 51
#define KDUE 112
#define KDL 84
#define TPS 2
__device__ __forceinline__ void small_out_task(const Params& p, int rq, int cp, char* smem, const int wv, u32* hot, int& pre) {
  const int tid = TIDX, lane = tid & 63, wid = wv;
  const int l15 = lane & 15, lq = lane >> 4;
  const __amdgpu_buffer_rsrc_t rw = RSRC(WSP(char, O_WOF) + WOB, WOB), ro = RSRC(WSP(u16, O_OB), 2048 * 4096 * 2);
  if (tid == 0) {
    int it = 0;
    while (ld4_sc1(hot + SY_CW) < 64u && ++it < SPIN_CAP) __builtin_amdgcn_s_sleep(8);
  }
  __syncthreads();
  bf8 wf[8][4];
#pragma unroll
  for (int k = 0; k < 8; k++)
#pragma unroll
    for (int j = 0; j < 4; j++)
      wf[k][j] = __builtin_bit_cast(bf8, __builtin_amdgcn_raw_buffer_load_b128(rw, (l15 >> 3) * 1024 + (lq * 16 + (l15 & 7)) * 16 + (j & 1) * 128,
                                                                              ((cp * 2 + (j >> 1)) * 128 + wid * 16 + k * 2) * 1024, 16));
  const int va = OB_OFF(rq * 8 + (l15 & 7), wid * 512 + (l15 >> 3) * 32 + lq * 8);
  const int wo = (lq >> 1) == (l15 >> 3) ? (wid * 2 + (lq >> 1)) * 272 + (l15 & 7) * 32 + (lq & 1) * 16 : 4 * 4352 + lane * 16;
  const int rd = (lane >> 4) * 4352 + (lane & 7) * 544 + wid * 32 + ((lane >> 3) & 1) * 16;
  u32 left = 0u, due = 0u, last = 0u, per = 240u * 64u;
  bool have = false;
  auto step = [&](u32x4 (&af)[8], u32x4 (&nf)[8], const int s) __attribute__((always_inline)) {
    if (due == 0u) {
      if (tid < 64) {
        u32 v;
        int it = 0;
        while (v = ld4_sc1(CSP(s - 2 - (lane & 1))), __any(v == 0u) && ++it < SPIN_CAP) __builtin_amdgcn_s_sleep(SPS);
        *(u32*)(smem + 40960 + (lane & 1) * 4) = v;
      }
      asm volatile("s_waitcnt lgkmcnt(0)" ::: "memory"); __builtin_amdgcn_s_barrier(); asm volatile("" ::: "memory");
      due = __builtin_amdgcn_readfirstlane(*(const u32*)(smem + 40960));
      last = __builtin_amdgcn_readfirstlane(*(const u32*)(smem + 40964));
    }
    per = umin(per, due - last);
    last = due;
    int n = 400, late;
    while (late = (int)(((u32)wall_clock64() << 6) - due - per) - (s == 63 ? KDL : KDUE) * 64, late < 0 && --n > 0) {}
    if (s == 63) TLS(1);
    const u32 v = __builtin_amdgcn_raw_buffer_load_b32(RSRC(CSP(0), wid ? 0 : 4096), 0, (s - 1) * 64, 16);
    if (s == CLS) left = __builtin_amdgcn_raw_buffer_load_b32(RSRC(hot, wid ? 0 : 4096), SY_OQ(lane < 6 ? lane : 6) * 4, 0, 16);
    SB0();
    auto rows = [&](u32x4 (&f)[8], const int so) __attribute__((always_inline)) {
#pragma unroll
      for (int k = 0; k < 8; k++) f[k] = __builtin_amdgcn_raw_buffer_load_b128(ro, va, so + k * 4096, 16);
      SB0();
    };
    auto bad = [&]() __attribute__((always_inline)) {
      u32 t = 0u;
#pragma unroll
      for (int k = 0; k < 8; k++) t = umax(umax(t, af[k][0]), af[k][2]);
      return __any(t == ~0u);
    };
    if (!have) rows(af, s * 262144);
    have = late >= (int)per && s < 63;
    rows(nf, have ? (s + 1) * 262144 : PJ_OOB);
    if (wid == 0) *(u32*)(smem + 40968 + (s & 1) * 4) = v;
    if (__builtin_expect(bad(), 0)) {
      int it = 0;
      do { __builtin_amdgcn_s_sleep(TPS); CFENCE(); rows(af, s * 262144); } while (bad() && ++it < SPIN_CAP);
    }
    SB0();
    f4 acc[4];
#pragma unroll
    for (int k = 0; k < 8; k++)
#pragma unroll
      for (int j = 0; j < 4; j++) acc[j] = MFMA(wf[k][j], __builtin_bit_cast(bf8, af[k]), k ? acc[j] : ((f4){0, 0, 0, 0}));
    char* const sm = smem + (s & 1) * 20480;
#pragma unroll
    for (int j = 0; j < 4; j++) *(f4*)(sm + wo + ((lq >> 1) == (l15 >> 3) ? j * 4352 : 0)) = acc[j];
    asm volatile("s_waitcnt lgkmcnt(0)" ::: "memory"); __builtin_amdgcn_s_barrier(); asm volatile("" ::: "memory");
    if (s == 63) TLS(2);
    due = __builtin_amdgcn_readfirstlane(*(const u32*)(smem + 40968 + (s & 1) * 4));
    f4 t = *(const f4*)(sm + rd) + *(const f4*)(sm + rd + 272);
#pragma unroll
    for (int r = 0; r < 4; r++) {
      t[r] += __uint_as_float(__builtin_amdgcn_mov_dpp(__float_as_uint(t[r]), 0xB1, 0xf, 0xf, true));
      t[r] += __uint_as_float(__builtin_amdgcn_mov_dpp(__float_as_uint(t[r]), 0x4E, 0xf, 0xf, true));
      t[r] += __uint_as_float(__builtin_amdgcn_mov_dpp(__float_as_uint(t[r]), 0x141, 0xf, 0xf, true));
    }
    __builtin_amdgcn_raw_buffer_store_b64((u32x2){pk2(t[0], t[1]), pk2(t[2], t[3])}, RSRC(p.out, 2048 * 2304), (lane & 7) == 0 ? lane : PJ_OOB,
                                          (s * 32 + rq * 8 + wid) * 2304 + 1152 + cp * 64, 0);
  };
  u32x4 fa[8], fb[8];
#pragma unroll
  for (int k = 0; k < 8; k++) asm volatile("" : "=v"(fa[k]));
#pragma unroll 1
  for (int s = XS0; s < 64; s += 2) { step(fa, fb, s); step(fb, fa, s + 1); }
  pre = 72;
  if (__any(left < (lane > 5 ? 72u : (lane & 1) ? NOB : NOA))) CLAIMS();
}
#define PSTAMP()
#define STAMP(i)
#define YEND()
#define ANS 16
#define ANX(slot) (X_ANN ^ xp ^ ((slot) * X_HS))
#define KSL 1
#define DSH 2
#define K1 14
#define K3 11
#define DRL 0
#define CSL 5
#define CSC 0
#define KA 0
#define KB 0
#define KS0 0
#define KMASK 0x00FFFE000FE00000ull
#define KXT(k) ((k) ? (int)((KMASK >> s) & 1) * (k) : 0)
#define DOMAX 90
#define DO0 80
#define DH0 54
#define DOCAP 6
#define C_IMG 0
#define C_QT 9216
#define C_KT (C_QT + 3072)
#define C_P (C_KT + 4096)
#define C_PX (C_P + 768)
#define C_KX (C_PX + 2560)
#define C_VX (C_KX + 8192)
#define C_QX (C_VX + 8192)
#define C_XP 161024
#define OBE 0
#define CALR 2
#define GPS 6
#define C_WL 49920
__device__ __forceinline__ float ror8(float v) {
  return __uint_as_float(__builtin_amdgcn_mov_dpp(__float_as_uint(v), 0x128, 0xf, 0xf, true));
}
__device__ __forceinline__ float rb1(float f) { return __uint_as_float(pk2(0.f, f)); }
#define FMX(a, b) __builtin_elementwise_maximum(a, b)
__device__ __forceinline__ float tmax(float mx) {
  mx = FMX(mx, ror8(mx));
  { auto t = __builtin_amdgcn_permlane16_swap(__float_as_uint(mx), __float_as_uint(mx), false, false);
    mx = FMX(__uint_as_float(t[0]), __uint_as_float(t[1])); }
  { auto t = __builtin_amdgcn_permlane32_swap(__float_as_uint(mx), __float_as_uint(mx), false, false);
    mx = FMX(__uint_as_float(t[0]), __uint_as_float(t[1])); }
  return mx;
}
__device__ __forceinline__ int hmax(int x) {
  x = max(x, __builtin_amdgcn_mov_dpp(x, 0x128, 0xf, 0xf, true));
  x = max(x, __builtin_amdgcn_mov_dpp(x, 0x124, 0xf, 0xf, true));
  x = max(x, __builtin_amdgcn_mov_dpp(x, 0x4E, 0xf, 0xf, true));
  x = max(x, __builtin_amdgcn_mov_dpp(x, 0xB1, 0xf, 0xf, true));
  { auto t = __builtin_amdgcn_permlane16_swap((u32)x, (u32)x, false, false); x = max((int)t[0], (int)t[1]); }
  return x;
}
#define BAR() do { asm volatile("s_waitcnt lgkmcnt(0)" ::: "memory"); __builtin_amdgcn_s_barrier(); asm volatile("" ::: "memory"); } while (0)
#define BARC() do { asm volatile("" ::: "memory"); __builtin_amdgcn_s_waitcnt(0xC07F); __builtin_amdgcn_s_barrier(); asm volatile("" ::: "memory"); } while (0)
#define BARN() do { asm volatile("" ::: "memory"); __builtin_amdgcn_s_barrier(); asm volatile("" ::: "memory"); } while (0)
#define IDX() const int tid = TIDX, lane = tid & 63, l15 = lane & 15, lq = lane >> 4, q4 = l15 >> 2, p4 = lane & 3, hi8 = l15 >> 3; \
    (void)q4; (void)p4; (void)hi8; (void)lq
#define AD_LD(HEAD) __builtin_amdgcn_raw_buffer_load_b32(rsX, (TIDX & (HEAD ? 63 : 31)) * ANS, ANX(s & 1), 16)
#define AD_GET(HEAD, K, CAP) do { int n_ = 0; u32 an = AD_LD(HEAD); \
    while ((u32)__ballot((an & 63u) != (u32)s) != 0u && ++n_ < (CAP)) { __builtin_amdgcn_s_sleep(1); CFENCE(); an = AD_LD(HEAD); } \
    const u32 r_ = (u32)__builtin_amdgcn_readfirstlane((int)an); \
    const int m_ = hmax((TIDX & 32) ? (int)umin(an, DOMAX * 64u) : (int)(an - r_)); \
    dl = ((r_ + (u32)__builtin_amdgcn_readlane(m_, 0)) & ~63u) + (u32)(K) * 64u + (HEAD ? (u32)__builtin_amdgcn_readlane(m_, 32) : 0u); \
    if (!HEAD && !CSL && c == CSC && s > 0 && n_ < (CAP)) __builtin_amdgcn_raw_buffer_store_b32(dl | 1u, rsX, 0, (X_HOT(xp) ^ X_CS) + (s - 1) * 64, 16); } while (0)
#define AD_SPIN() do { int n_ = 160; while ((int)(((u32)wall_clock64() << 6) - dl) < 0 && --n_ > 0) {} } while (0)
#define STG_AUX 0
#define OSTG_AUX 0
#define STG_D 2
#define STG_POLL 7
#define STS(c, i)

#define DPR(OP, N) OP " %3, %3, %3 row_ror:" #N " row_mask:0xf bank_mask:0xf\n\t" OP " %4, %4, %4 row_ror:" #N " row_mask:0xf bank_mask:0xf\n\t"
#define DPROW(OP) "s_nop 0\n\t" DPR(OP, 8) "s_nop 0\n\t" DPR(OP, 4) "s_nop 0\n\t" DPR(OP, 2) "s_nop 0\n\t" DPR(OP, 1)
#define NXA 0
#define NXE 0
#define ADE 1
#define NXH 0
#define NXV 1
#define NXB 4
#define NXS 3
#define BJ 2
#define KSP 0
#define SP1 2
#define SP2 0
#define KP 3
#define GX 1
#define GXO (GX == 1 ? 4096 : 1024)
#define ZE 0x0D
#define KD 0
#define QSL 0
#define DJ 9
#define VSL 0
#define VSL1 4
#define KV0 1
#define PVP 0
#define IPR 3
#define DSLOT 0x9876E1CBAD0ull
#define F_K (44288 - C_KT)
#define F_Q (47872 - 768 - C_QT)
#define HAL 2
#define OAL 2
#define ALIGN(n) asm volatile(".p2align %0" :: "i"(n))
#define QXP 4
#define RORD 0
#define PRIO(n) __builtin_amdgcn_s_setprio(n)
#define LSL 6
__device__ __forceinline__ int chain_head(const Params& p, char* smem, const int h, const int wv, const bool spec) {
  STS(h, 1);
  TLS(12);
  const int wid = wv;
  u32x8 w[2][9], wx[3];
  const __amdgpu_buffer_rsrc_t rsX = RSRC(XWIN(p), X_END);
  const __amdgpu_buffer_rsrc_t rsB = RSRC(WSP(u16, O_QB), 4 * ACB);
  const int cb = h >> 1;
  const int nvt = (wid & 3) == 0 ? 0 : (wid < 3 ? 2 : 1);
  const int vt = wid < 3 ? wid * 2 + 2 : (wid == 3 ? 0 : wid - 4);
  const int wl = C_WL + (nvt == 1 ? vt * 12288 : 49152 + (nvt == 2 ? wid - 1 : wid >> 2) * 30720);
  int xp;
  {
    IDX();
    u32x4 st[54], nk, nv, nq;
    u32 fl, fk;
    const int vw = ((tid >> 4) * 4096 + h * 128 + (tid & 15) * 8) * 2;
#define NTL 64
#define AH 1
    u32 tc[9], b1, old = 0u;
#define TEC 0
    const u16 *pq = p.BWq, *pk = p.BWk, *pv = p.BWv;
    asm volatile("" : "+s"(pq), "+s"(pk), "+s"(pv));
    const bool te = wid < 3 && (h & 3) == TEC;
    const int tm = te ? wid : (h & 3) - 1;
    const u64 pt = tm == 1 ? (u64)pk : tm == 2 ? (u64)pv : (u64)pq;
    const __amdgpu_buffer_rsrc_t rt = RSRC(pt, te || (tm && h != 31 && wid == 7) ? 576 * 4096 * 2 : 0);
    const int tv = lane < NTL ? lane * 8192 : PJ_OOB, ts_ = te ? 8064 : (h >> 2) * 1024 + 384;
    const __amdgpu_buffer_rsrc_t rq = RSRC(pq, 576 * 4096 * 2), rk = RSRC(pk, 576 * 4096 * 2), rv = RSRC(pv, 576 * 4096 * 2);
    auto req = [&](int kc) {
#pragma unroll
      for (int i = 0; i < 6; i++) {
        st[kc * 6 + i] = __builtin_amdgcn_raw_buffer_load_b128(i < 2 ? rq : (i < 4 ? rk : rv), vw, (kc * 64 + (i & 1) * 32) * 8192, STG_AUX);
        if (NTL && kc + AH < 9 && i == (kc ? 0 : 4)) { SB0(); tc[kc] = __builtin_amdgcn_raw_buffer_load_b32(rt, tv, te && (h >> 2) != kc ? PJ_OOB : (kc + AH) * 64 * 8192 + ts_, 0); }
      }
      SB0();
    };
#pragma unroll
    for (int kc = 0; kc < STG_D; kc++) { if (kc == 0) TLS(13); req(kc); __builtin_amdgcn_s_barrier(); SB0(); }
    TLS(14);
    auto cell = [](int row, int slot) { return (((row * 2 + slot) ^ ((((row >> 2) ^ (row >> 4)) & 1) << 2) ^ ((row >> 5) & 1)) << 5); };
    auto put = [&](int unit, int row, const u32x4& v) { *(u32x4*)(smem + unit * 4160 + cell(row, (tid >> 1) & 1) + (tid & 1) * 16) = v; };
    auto frag = [&](int tile) {
      bf4 tt[4];
#pragma unroll
      for (int kk = 0; kk < 4; kk++) {
        const int r0 = (kk >> 1) * 32 + (lq >> 1) * 16 + (q4 >> 1) * 8 + (lq & 1) * 4 + (kk & 1) * 2 + (q4 & 1);
        tt[kk] = trrd(smem + (tile >> 1) * 4160 + cell(r0, tile & 1) + p4 * 8);
      }
      return ilv(tt[0], tt[1], tt[2], tt[3]);
    };
    auto park = [&](int slot, const u32x8& f) {
      char* d = smem + wl + slot * 2048 + lane * 16;
      *(u32x4*)d = (u32x4){f[0], f[1], f[2], f[3]};
      *(u32x4*)(d + 1024) = (u32x4){f[4], f[5], f[6], f[7]};
    };
    const __amdgpu_buffer_rsrc_t rs0 = RSRC(p.ws, wid ? 0 : O_QA);
    auto poll = [&]() {
      fl = __builtin_amdgcn_raw_buffer_load_b32(rs0, (lane < 32 ? SY_INIT(lane) : SY_ARR) * 4, 0, 16);
      fk = __builtin_amdgcn_raw_buffer_load_b32(rs0, SY_BLK(lane < 12 ? lane >> 2 : 0, cb, lane & 3) * 4, 0, 16);
    };
    auto rows = [&](int vo) {
      nq = __builtin_amdgcn_raw_buffer_load_b128(rsB, vo, h * 8192, 16);
      nk = __builtin_amdgcn_raw_buffer_load_b128(rsB, vo, h * 8192 + ACB, 16);
      nv = __builtin_amdgcn_raw_buffer_load_b128(rsB, vo, h * 8192 + 2 * ACB, 16);
    };
    bool early = false;
    int lost = 0;
#pragma unroll
    for (int kc = 0; kc < 9; kc++) {
      if (kc == STG_D - 1) { if (spec && wid == 0) old = confirm(p, smem, h, b1, wv); SB0(); }
      if (kc == 10 - STG_D) { early = __all(fk >= FULL); CFENCE(); rows(early ? tid * 16 : PJ_OOB); poll(); SB0(); }
#pragma unroll
      for (int i = 0; i < 6; i++) put((i >> 1) * 4 + ((tid & 15) >> 2), (i & 1) * 32 + (tid >> 4), st[kc * 6 + i]);
      if (NTL && kc + AH < 9) asm volatile("" :: "v"(tc[kc]));
      if (kc == 2 * STG_D - 1) lost = (__builtin_amdgcn_readfirstlane(old) >> h) & 1;
      BAR();
      if (kc == 0) TLS(15);
      if (kc == 0) { b1 = __builtin_amdgcn_raw_buffer_load_b32(rs0, 0, SY_BEAT(h & 7) * 4, 16); SB0(); }
      if (kc + STG_D == 8) { fk = ld4_sc1(WSP(u32, 0) + SY_BLK(lane < 12 ? lane >> 2 : 0, cb, lane & 3)); SB0(); }
      if (kc + STG_D < 9) req(kc + STG_D);
      w[0][kc] = frag(wid * 2);
      w[1][kc] = frag(nvt == 2 && kc < 4 ? 16 + vt : wid * 2 + 1);
      const u32x8 tp = frag(nvt == 0 ? 21 + (wid >> 1) : (nvt == 2 && kc < 4 ? wid * 2 + 1 : 16 + vt));
      if (kc >= 4 && kc < 7) { wx[kc - 4] = tp; if (nvt == 0) park(6 + kc, tp); }
      else park(nvt == 0 ? 6 + kc : (kc >= 7 ? kc - 7 : 2 + kc), tp);
      BAR();
      if (kc == 0 || kc == 8) STS(h, kc + 3);
    }
    TLS(6);
    if (wid == 0) {
      int it = 0;
      while (!lost && !__all(fl >= (lane < 32 ? 1u : 256u) && fk >= FULL) && ++it < SPIN_CAP) { BEAT_ON(it); __builtin_amdgcn_s_sleep(GPS); CFENCE(); poll(); }
      if (lane == 0) *(int*)(smem + C_XP) = lost ? -1 : fl & 0x7800;
    }
    BAR();
    if (!early) rows(tid * 16);
    xp = __builtin_amdgcn_readfirstlane(*(const int*)(smem + C_XP));
    if (xp < 0) return xp;
    TLS(7);
    SB0();
    *(u32x4*)(smem + C_QX + tid * 16) = nq;
    *(u32x4*)(smem + C_KX + tid * 16) = nk;
    *(u32x4*)(smem + C_VX + ((tid * 16) ^ ((tid & 32) << 2))) = nv;
  }
  STS(h, 14);
  constexpr float CE = 0.08838834764831845f * 1.4426950408889634f;
  auto run = [&](auto RC) __attribute__((always_inline)) {
  constexpr int R = decltype(RC)::value;
  constexpr int NV = R < 2 ? 0 : (R < 4 ? 1 : 2);
  constexpr bool QW = !(R & 1);
  int known = 0;
  bf8 pre[4][2], vx[NV ? NV : 1];
  int sidx, mo, io, eo, gvo, wvo = 0, so = 0, po = 0, qo = 0, ko = 0, ko1 = 0, fo = 0, fq = 0, fk = 0;
  float mk[4], mm = 0.f, cv = CE;
#define LDW(dst, slot) do { dst[0] = *(const bf8*)(smem + wvo + (slot) * 2048); dst[1] = *(const bf8*)(smem + wvo + (slot) * 2048 + 1024); } while (0)
  {
    IDX();
    if constexpr (R == 1) {
      po = (tid * 16) ^ ((tid & 32) << 2);
      so = (lane >> 5) * 131072 + (lane & 31) * 16;
      asm volatile("" : "+v"(po), "+v"(so));
    } else if constexpr (R == 0) {
      qo = C_QT + lq * 384 + q4 * 32 + p4 * 8; ko = C_KT + ((((lq * 8 + q4) * 32) ^ ((lq & 1) << 7)) + p4 * 8); ko1 = C_KT + ((((lq * 8 + 4 + q4) * 32) ^ ((lq & 1) << 7)) + p4 * 8);
      po = C_P + l15 * 16 + lq * 4;
      fo = lq * 512 + l15 * 16;
#pragma unroll
      for (int r = 0; r < 4; r++) { mk[r] = lq * 4 + r <= l15 ? 0.f : -__builtin_inff(); asm volatile("" : "+v"(mk[r])); }
      mm = (l15 & 2) ? -__builtin_inff() : 0.f;
      asm volatile("" : "+v"(mm), "+v"(cv));
      fq = C_QT + F_Q + (lq ? lq : 1) * 768 + lq * 8; fk = C_KT + F_K + lq * 1032;
      asm volatile("" : "+v"(qo), "+v"(ko), "+v"(ko1), "+v"(po), "+v"(fo), "+v"(fq), "+v"(fk));
      *(u32*)(smem + fq) = 0u; *(u32*)(smem + fk) = 0u;
    } else {
      wvo = wl + lane * 16;
      po = C_P + (lq * 4 + q4) * 16 + (p4 & 1) * 8;
      so = ((l15 >> 2) & 1) * 131072 + (((vt & 3) + (NV == 2 ? hi8 : 0)) * 8 + ((l15 >> 1) & 1) * 4 + (lq >> 1) * 2 + (l15 & 1)) * 16 + (lq & 1) * 8;
      if (NV == 1 && hi8) so = PJ_OOB;
      asm volatile("" : "+v"(wvo), "+v"(po), "+v"(so));
      if constexpr (NV == 2) { qo = po ^ 32; asm volatile("" : "+v"(qo)); }
#pragma unroll
      for (int j = 0; j < 4; j++) LDW(pre[j], NV == 2 ? 2 + j : j);
    }
    eo = QW ? C_QT + wid * 768 + ((l15 >> 2) * 2 + (l15 & 1)) * 32 + lq * 8 + ((l15 >> 1) & 1) * 4
            : C_KT + (wid & 3) * 1024 + ((l15 * 32) ^ (hi8 << 7)) + lq * 8;
    sidx = (l15 & 2) ? 0xEEEE : 0x4444;
    mo = C_IMG + lane * 16;
    io = C_IMG + ((tid & ~30) | ((tid & 10) << 1) | ((tid & 20) >> 1)) * 16;
    gvo = tid * 16;
    asm volatile("" : "+v"(sidx), "+v"(mo), "+v"(io), "+v"(eo));
    if constexpr (R < 2) asm volatile("" : "+v"(gvo));
    if constexpr (!QW) { *(u32*)(smem + eo + 4) = 0u; *(u32*)(smem + eo + 516) = 0u; }
  }
  f4 a0, a1;
  u32x4 vm = {0u, 0u, 0u, 0u};
  if constexpr (NV > 0) asm volatile("" : "+v"(vm));
  u32 ts = 0;
  u64 tq = 0;
  int dur = DH0 * 64;
  const int ob = ((h >> 3) * 32768 + (h & 7) * 512) ^ xp ^ (NV == 1 ? X_HS : 0);
  const int ib = X_M ^ xp ^ ((wid >> 2) * 32768 + (wid & 3) * 1024);
  int b3 = 0;
  PRIO(QW ? 2 : 1);
#pragma unroll 1
  for (int s = 0;; s++) {
    int mb = ib + b3 * 65536, mbx = X_M ^ xp ^ (196608 + b3 * 1024);
    if constexpr (R == GX) asm volatile("" : "+s"(mb), "+s"(mbx));
    u32x4 g, gx;
    if constexpr (R != 1) ALIGN(HAL);
    BAR();
    STAMP(13);
    if constexpr (R == 1 && NXE) {
      asm volatile("buffer_load_dwordx4 %0, %2, %3, %4 offen sc1\n\tbuffer_load_dwordx4 %1, %2, %3, %5 offen sc1"
                   : "=&v"(g), "=&v"(gx) : "v"(mo), "s"(rsX), "s"(mb), "s"(mbx) : "memory");
    } else {
      g = __builtin_amdgcn_raw_buffer_load_b128(rsX, mo, mb, 16);
      if constexpr (R == GX) gx = __builtin_amdgcn_raw_buffer_load_b128(rsX, mo, mbx, 16);
    }
    SB0();
    if constexpr (R == 0) {
#pragma unroll
      for (int j = 0; j < 4; j++) { const char* kx_ = smem + C_KX + fo + j * 2048;
        pre[j >> 1][j & 1] = *(const bf8*)kx_; pre[2 + (j >> 1)][j & 1] = *(const bf8*)(kx_ + 256); }
    } else if constexpr (R > 1) {
      IDX();
#pragma unroll
      for (int d = 0; d < NV; d++) { const char* vb_ = smem + C_VX + (vt + d) * 1024 + (p4 >> 1) * 512 + (((lq * 4 + q4) ^ ((p4 >> 1) * 8)) << 4) + (p4 & 1) * 8;
        vx[d] = cat8(trrd(vb_), trrd(vb_ + 256)); }
    }
    SB0();
    a0 = zero4(); a1 = zero4();
    f4 c0, c1, a2;
#define A2INI() do { u64 z_; float m0_, m1_; asm volatile("v_mov_b32 %0, %3\n\tv_mov_b32 %1, %3\n\tv_mov_b64 %2, 0\n\ts_nop 1" : "=&v"(m0_), "=&v"(m1_), "=v"(z_) : "v"(mm)); \
                     const f2v zz_ = __builtin_bit_cast(f2v, z_); a2 = (f4){m0_, m1_, zz_[0], zz_[1]}; } while (0)
    if constexpr ((ZE >> R) & 1) {
      c1 = zero4();
      if constexpr (NV != 1) c0 = zero4();
      if constexpr (R == 0) { A2INI(); asm volatile("" : "+v"(a2)); }
    }
    int so_ = X_O(b3) ^ ob, an_ = ANX(s & 1) + h * ANS;
    int nb = s < 63 ? (s + 1) * 262144 + h * 8192 : PJ_OOB;
    asm volatile("" : "+s"(so_), "+s"(an_), "+s"(nb));
    u32x4 nx[2];
#define NXL1(g, i) __builtin_amdgcn_raw_ptr_buffer_load_lds(rsB, (LDSP void*)(smem + C_KX + (g) * 4096), 16, (g) < 2 ? gvo : po, \
                                                             nb + (1 + ((g) >> 1)) * ACB + ((g) & 1) * 4096 - 4096, (i) * 1024, 16)
#define NXL(g) do { NXL1(g, 0); NXL1(g, 1); NXL1(g, 2); NXL1(g, 3); } while (0)
    SB0();
    if (NXE) {
      BARC();
      if constexpr (R == 1) {
        if (NXE > 1) __builtin_amdgcn_s_sleep(NXE - 1);
#pragma unroll
        for (int i = 0; i < 4; i++)
          asm volatile("s_mov_b32 m0, %3\n\ts_nop 0\n\tbuffer_load_dwordx4 %0, %1, %2 offen sc1 lds\n\tbuffer_load_dwordx4 %0, %1, %2 offen offset:1024 sc1 lds\n\t"
                       "buffer_load_dwordx4 %0, %1, %2 offen offset:2048 sc1 lds\n\tbuffer_load_dwordx4 %0, %1, %2 offen offset:3072 sc1 lds"
                       :: "v"(i < 2 ? gvo : po), "s"(rsB), "s"(nb + (1 + (i >> 1)) * ACB + (i & 1) * 4096 - 4096), "i"(C_KX + i * 4096) : "memory", "m0");
      }
      SB0();
    }
    if constexpr (!QW) { PRIO(IPR); BARN(); }
    {
      if constexpr (R == 1 && NXE) asm volatile("s_waitcnt vmcnt(17)" : "+v"(g));
      else __builtin_amdgcn_s_waitcnt(R == GX ? 0x0F71 : 0x0F70);
      CFENCE();
      *(u32x4*)(smem + io) = g;
      SB0();
      u32 top = umax(umax(umax(g[0], g[1]), g[2]), g[3]);
      if constexpr (R == GX) {
        asm volatile("" : "+v"(top));
        SB0();
        if constexpr (R == 1 && NXE) asm volatile("s_waitcnt vmcnt(16)" : "+v"(gx));
        *(u32x4*)(smem + GXO + io) = gx;
        SB0();
        top = umax(umax(umax(umax(top, gx[0]), gx[1]), gx[2]), gx[3]);
      }
#define G_BAD() (umax(umax(umax(g[0], g[1]), umax(g[2], g[3])), R != GX ? 0u : umax(umax(gx[0], gx[1]), umax(gx[2], gx[3]))) == ~0u)
      if (__builtin_expect(__any(top == ~0u), 0)) {
        int it = 0;
        do {
          CFENCE();
          if (G_BAD()) {
            g = __builtin_amdgcn_raw_buffer_load_b128(rsX, mo, mb, 16);
            if constexpr (R == GX) gx = __builtin_amdgcn_raw_buffer_load_b128(rsX, mo, mbx, 16);
          }
          VMW0C();
        } while (__any(G_BAD()) && ++it < SPIN_CAP);
        *(u32x4*)(smem + io) = g;
        if constexpr (R == GX) *(u32x4*)(smem + GXO + io) = gx;
      }
    }
    SB0();
    STAMP(15);
    if constexpr (!QW) PRIO(1);
    BARC();
    STAMP(0);
#define LDM(j) *(const bf8*)(smem + mo + (j) * 1024)
    constexpr int NB = R == 1 ? 9 : 5;
    bf8 b[NB];
    if constexpr (R == 1) {
      STAMP(6);
      asm volatile("s_memrealtime %0\n\ts_waitcnt lgkmcnt(0)" : "=s"(tq));
      ts = (u32)tq;
      __builtin_amdgcn_raw_buffer_store_b32((((ts << 6) + (u32)dur) & ~63u) | (u32)s, rsX, 0, an_, 16);
      SB0();
#pragma unroll
      for (int j = 0; j < 9; j++) { b[j] = LDM(j); SB0(); }
#pragma unroll
      for (int i = 0; i < NXA; i++) { NXL(i); SB0(); }
    } else {
      if (!QW && KSP) __builtin_amdgcn_s_sleep(KSP);
#pragma unroll
      for (int j = 0; j < 4; j++) { b[j] = LDM(j); SB0(); }
    }
#pragma unroll
    for (int j = 0; j < 9; j++) {
      if constexpr (QW) {
        if (j == BJ) {
          BARN();
#pragma unroll
          for (int i = 4; i < 4 + BJ; i++) { b[i % 5] = LDM(i); SB0(); }
        }
        if (j >= BJ && j + 4 < 9) b[(j + 4) % 5] = LDM(j + 4);
      } else if (R != 1 && j + 4 < 9) b[(j + 4) % 5] = LDM(j + 4);
      SB0();
      a0 = SMF(b[j % NB], w[0][j], a0, sidx);
      if (NV < 2 || j >= 4) a1 = SMF(b[j % NB], w[1][j], a1, sidx);
      else a1 = SMF(b[j % 5], cat16(pre[j & 3][0], pre[j & 3][1]), a1, sidx);
      SB0();
      if (j == 0) STAMP(10);
      if (j == 4) STAMP(11);
      if (j == 8) STAMP(12);
      if (NV == 2 && j >= DJ && j < DJ + 4) LDW(pre[j - DJ], (DSLOT >> ((j - DJ) * 4)) & 15);
    }
    {
      const u32 e0 = pk2(a0[0] + a0[2], a0[1] + a0[3]), e1 = pk2(a1[0] + a1[2], a1[1] + a1[3]);
      asm volatile("ds_write2_b32 %0, %1, %2 offset1:%3" :: "v"(eo), "v"(e0), "v"(e1), "i"(QW ? 96 : 128) : "memory");
      if constexpr (R != 0) asm volatile("ds_write_b32 %0, %1 offset:%2" :: "v"(eo), "v"(sidx), "i"(QW ? F_Q : F_K) : "memory");
    }
    SB0();
    STAMP(1);
    if constexpr (QW) PRIO(R ? 0 : SP1);
    else if (KV0) PRIO(0);
    if constexpr ((ZE >> R) & 1) { a0 = c0; a1 = c1; }
    else if constexpr (R != 1) { a1 = zero4(); if constexpr (NV != 1) a0 = zero4(); if constexpr (R == 0) A2INI(); }
    if constexpr (NV == 0) {
      u32 dl = 0;
      const int c = 1;
      bf8 km[4];
      if constexpr (R == 0) {
        if (QSL) __builtin_amdgcn_s_sleep(QSL);
        bf8 qa[2];
        u32 fl;
        do {
          CFENCE();
          fl = *(const volatile LDSP u32*)(smem + fq);
          CFENCE(); SB0();
#pragma unroll
          for (int i = 0; i < 2; i++) { qa[i] = cat8(trrd(smem + qo + i * 1536), trrd(smem + qo + i * 1536 + 128)); SB0(); }
        } while (__builtin_expect(__any(fl == 0u), 0));
        STAMP(2);
        auto rdk = [&]() __attribute__((always_inline)) {
          CFENCE(); SB0();
          fl = *(const volatile LDSP u32*)(smem + fk);
          CFENCE(); SB0();
#pragma unroll
          for (int i = 0; i < 4; i++) { km[i] = cat8(trrd(smem + ko + i * 1024), trrd(smem + ko1 + i * 1024)); SB0(); }
        };
        auto mulk = [&]() __attribute__((always_inline)) {
#pragma unroll
          for (int i = 0; i < 2; i++) { a2 = SMF(qa[i], cat16(km[2 * i], km[2 * i + 1]), a2, sidx); asm volatile("" : "+v"(a2)); SB0(); }
        };
#pragma unroll
        for (int i = 0; i < 2; i++) {
          a0 = SMF(qa[i], cat16(pre[i][0], pre[i][1]), a0, sidx); asm volatile("" : "+v"(a0)); SB0();
          a1 = SMF(qa[i], cat16(pre[2 + i][0], pre[2 + i][1]), a1, sidx); asm volatile("" : "+v"(a1)); SB0();
        }
        if (SP2 != SP1) PRIO(SP2);
        if (KP == 0) rdk();
        float e0 = a0[0] + a0[2], e1 = a0[1] + a0[3];
        asm volatile("" : "+v"(e0), "+v"(e1));
        SB0();
        float e2 = a1[0] + a1[2], e3 = a1[1] + a1[3], x0, x1, y0, y1, f0, f1, f2, f3, f4_, f5;
        asm volatile("v_mul_f32_dpp %0, %2, %4 row_newbcast:0 row_mask:0xf bank_mask:0xf\n\tv_mul_f32_dpp %1, %3, %4 row_newbcast:0 row_mask:0xf bank_mask:0xf"
                     : "=&v"(x0), "=&v"(x1) : "v"(e0), "v"(e1), "v"(cv), "v"(e2), "v"(e3));
        if (KP == 1) rdk();
        asm volatile("v_fma_f32 %0, %9, %6, -%7\n\tv_fma_f32 %2, %11, %6, -%8\n\tv_exp_f32 %0, %0\n\tv_exp_f32 %2, %2\n\t"
                     "v_fma_f32 %1, %10, %6, -%7\n\tv_fma_f32 %3, %12, %6, -%8\n\tv_exp_f32 %1, %1\n\tv_exp_f32 %3, %3\n\t"
                     "v_add_f32 %4, %0, %1\n\tv_add_f32 %5, %2, %3"
                     : "=&v"(f0), "=&v"(f2), "=&v"(f1), "=&v"(f3), "=&v"(y0), "=&v"(y1) : "v"(cv), "v"(x0), "v"(x1), "v"(e0), "v"(e2), "v"(e1), "v"(e3));
        if (KP == 2) rdk();
        STAMP(3);
        PRIO(2);
        SB0();
        if (KP == 3) {
          if (KD) asm volatile("s_nop %0" :: "i"(KD));
          do {
            rdk();
          } while (__any(fl == 0u));
        }
        mulk();
        if (KP < 3 && __builtin_expect(__any(fl == 0u), 0)) {
          do rdk(); while (__any(fl == 0u));
          A2INI();
          mulk();
        }
        STAMP(4);
        float e4 = a2[0] + a2[2], e5 = a2[1] + a2[3];
#define P_OUT "v_rcp_f32 %3, %3\n\tv_rcp_f32 %4, %4\n\t" \
              "v_mul_f32 %0, %0, %3\n\tv_mul_f32 %5, %5, %4\n\tv_mul_f32 %1, %1, %3\n\tv_cvt_pk_bf16_f32 %0, %0, %5\n\t" \
              "v_mul_f32 %6, %6, %4\n\tv_mul_f32 %2, %2, %3\n\tv_cvt_pk_bf16_f32 %1, %1, %6\n\tv_mul_f32 %7, %7, %4\n\t" \
              "ds_write2st64_b32 %9, %0, %1 offset1:1\n\tv_cvt_pk_bf16_f32 %2, %2, %7\n\tds_write_b32 %9, %2 offset:512"
        asm volatile("v_fma_f32 %2, %12, %8, -%10\n\tv_fma_f32 %7, %13, %8, -%11\n\tv_exp_f32 %2, %2\n\tv_exp_f32 %7, %7\n\t"
                     "v_add_f32 %3, %3, %2\n\tv_add_f32 %4, %4, %7\n\t" DPROW("v_add_f32_dpp") P_OUT "\n\tv_min_f32 %3, %3, %4"
                     : "+v"(f0), "+v"(f2), "=&v"(f4_), "+v"(y0), "+v"(y1), "+v"(f1), "+v"(f3), "=&v"(f5)
                     : "v"(cv), "v"(po), "v"(x0), "v"(x1), "v"(e4), "v"(e5) : "memory");
        SB0();
        if (__builtin_expect(__any(y0 < 0x1p-64f), 0)) {
          asm volatile("v_max3_f32 %3, %0, %1, %2\n\tv_max3_f32 %4, %5, %6, %7\n\t" DPROW("v_max_f32_dpp")
                       "v_mul_f32 %3, %8, %3\n\tv_mul_f32 %4, %8, %4\n\t"
                       "v_fma_f32 %0, %0, %8, -%3\n\tv_fma_f32 %5, %5, %8, -%4\n\tv_exp_f32 %0, %0\n\tv_exp_f32 %5, %5\n\t"
                       "v_fma_f32 %1, %1, %8, -%3\n\tv_fma_f32 %6, %6, %8, -%4\n\tv_exp_f32 %1, %1\n\tv_exp_f32 %6, %6\n\t"
                       "v_fma_f32 %2, %2, %8, -%3\n\tv_fma_f32 %7, %7, %8, -%4\n\tv_exp_f32 %2, %2\n\tv_exp_f32 %7, %7\n\t"
                       "v_add_f32 %3, %0, %1\n\tv_add_f32 %4, %5, %6\n\tv_add_f32 %3, %3, %2\n\tv_add_f32 %4, %4, %7\n\t" DPROW("v_add_f32_dpp") P_OUT
                       : "+v"(e0), "+v"(e2), "+v"(e4), "=&v"(x0), "=&v"(x1), "+v"(e1), "+v"(e3), "+v"(e5) : "v"(cv), "v"(po) : "memory");
        }
        SB0();
      } else {
#pragma unroll
        for (int i = NXA; i < (NXV ? NXH : NXB) && !NXE; i++) NXL(i);
        SB0();
        const int rb = X_O(b3 == 0 ? 2 : b3 - 1) ^ ob;
        const u32x4 e = ones4();
        __builtin_amdgcn_raw_buffer_store_b128(e, rsX, so, rb, 16);
        __builtin_amdgcn_raw_buffer_store_b128(e, rsX, so, rb ^ X_HS, 16);
        PSTAMP();
      }
      STAMP(5);
      BARC();
      if constexpr (R == 0) STAMP(6);
      if constexpr (R == 1) {
        const u32 tp = (u32)wall_clock64(); dur += ((int)umin((tp - ts) << 6, 200u * 64u) - dur) >> DSH;
        SB0();
        if (ADE && s < 63) AD_GET(true, K1 + K3 + KXT(KA + KB) + (s ? 0 : KS0), 4096);
        if (NXB < 4) {
          __builtin_amdgcn_s_sleep(NXS);
#pragma unroll
          for (int i = NXB; i < 4; i++) NXL(i);
          BARN();
        }
        ALIGN(HAL);
        VMW0C();
        STAMP(8);
        if (NXB == 4) BARN();
        if (s == 63) break;
      } else {
        *(u32*)(smem + fq) = 0u; *(u32*)(smem + fk) = 0u;
        bf8 qx[2][4];
#pragma unroll
        for (int ks = 0; ks < 8; ks++) { qx[ks >> 2][ks & 3] = *(const bf8*)(smem + C_QX + (ks >> 2) * 256 + fo + (ks & 3) * 2048); if (ks < QXP) SB0(); }
#pragma unroll
        for (int NT = 1; NT <= 2; NT++) {
          f4 sc[2], sm;
          const bf8* qf = qx[NT - 1];
#pragma unroll
          for (int ks = 0; ks < 4; ks++) {
#pragma unroll
            for (int j = 0; j < NT; j++) sc[j] = MFMA(pre[2 * j + (ks >> 1)][ks & 1], qf[ks], ks ? sc[j] : (j == NT - 1 ? ((f4){mk[0], mk[1], mk[2], mk[3]}) : ((f4){0, 0, 0, 0})));
            sm = MFMA(km[ks], qf[ks], ks ? sm : ((f4){0, 0, 0, 0}));
          }
          float mx = FMX3(FMX3(sc[0][0], sc[0][1], sc[0][2]), sc[0][3], FMX(sm[0], sm[1]));
          if (NT == 2) mx = FMX3(FMX3(mx, sc[1][0], sc[1][1]), sc[1][2], sc[1][3]);
          { auto t = __builtin_amdgcn_permlane16_swap(__float_as_uint(mx), __float_as_uint(mx), false, false);
            mx = FMX(__uint_as_float(t[0]), __uint_as_float(t[1])); }
          { auto t = __builtin_amdgcn_permlane32_swap(__float_as_uint(mx), __float_as_uint(mx), false, false);
            mx = FMX(__uint_as_float(t[0]), __uint_as_float(t[1])); }
          mx *= -CE;
#pragma unroll
          for (int j = 0; j < NT; j++)
#pragma unroll
            for (int r = 0; r < 4; r++) sc[j][r] = __builtin_amdgcn_exp2f(__builtin_fmaf(sc[j][r], CE, mx));
          sm[0] = __builtin_amdgcn_exp2f(__builtin_fmaf(sm[0], CE, mx)); sm[1] = __builtin_amdgcn_exp2f(__builtin_fmaf(sm[1], CE, mx));
          float sum = ((sc[0][0] + sc[0][1]) + (sc[0][2] + sc[0][3])) + (sm[0] + sm[1]);
          if (NT == 2) sum += (sc[1][0] + sc[1][1]) + (sc[1][2] + sc[1][3]);
          { auto t = __builtin_amdgcn_permlane16_swap(__float_as_uint(sum), __float_as_uint(sum), false, false);
            sum = __uint_as_float(t[0]) + __uint_as_float(t[1]); }
          sum = swadd(sum, sum);
          const float inv = __builtin_amdgcn_rcpf(sum);
          *(u32x4*)(smem + C_PX + (NT - 1) * 1024 + gvo) = (u32x4){pk2(sc[0][0] * inv, sc[0][1] * inv), pk2(sc[0][2] * inv, sc[0][3] * inv),
                                                                  NT == 2 ? pk2(sc[1][0] * inv, sc[1][1] * inv) : 0u, NT == 2 ? pk2(sc[1][2] * inv, sc[1][3] * inv) : 0u};
          *(u32*)(smem + C_PX + 2048 + (NT - 1) * 256 + (gvo >> 2)) = pk2(sm[0] * inv, sm[1] * inv);
        }
        STAMP(8);
        BAR();
        if (s == 63) break;
        if (known < (s < 61 ? s + 3 : 64)) {
          int n = 0;
          while (true) {
            u32 bf[3];
#pragma unroll
            for (int i = 0; i < 3; i++) bf[i] = ld4_sc1(WSP(u32, 0) + SY_BLK(i, cb, gvo >> 4));
            const u64 miss = __ballot(!(bf[0] >= FULL && bf[1] >= FULL && bf[2] >= FULL));
            known = miss ? __builtin_ctzll(miss) : 64;
            if (known >= (s < 61 ? s + 3 : 64) || ++n >= SPIN_CAP) break;
            __builtin_amdgcn_s_sleep(KSL);
          }
        }
      }
      STAMP(9);
      YEND();
      if constexpr (R == 1) { if (!ADE) AD_GET(true, K1 + K3, 4096); AD_SPIN(); }
    } else {
      if (NV == 2 && DJ > 5) {
#pragma unroll
        for (int j = 0; j < 4; j++) LDW(pre[j], (DSLOT >> (j * 4)) & 15);
        SB0();
      }
      if (QW && (NV == 2 ? VSL : VSL1)) __builtin_amdgcn_s_sleep(NV == 2 ? VSL : VSL1);
      if constexpr (NV == 2) {
        int u = 0;
#pragma unroll
        for (int i = 0; i < 9; i++) {
          const int bs = (0x31042 >> ((i % 5) * 4)) & 7;
#define RING(a) do { a = SMF(b[bs], cat16(pre[u & 3][0], pre[u & 3][1]), a, sidx); SB0(); \
                     if (u < 7) LDW(pre[u & 3], (DSLOT >> (u * 4 + 16)) & 15); u++; } while (0)
          SB0();
          if (i == 0 || i == 4) RING(a0);
          else a0 = SMF(b[bs], i < 4 ? wx[i - 1] : w[1][i - 5], a0, sidx);
          SB0();
          RING(a1);
          if (i < 4) b[bs] = LDM(i);
        }
      } else {
#pragma unroll
        for (int i = 0; i < 9; i++) {
          SB0();
          if (i < 3) a1 = SMF(b[(i + 4) % 5], wx[i], a1, sidx);
          else a1 = SMF(b[(i + 4) % 5], cat16(pre[(i + 1) & 3][0], pre[(i + 1) & 3][1]), a1, sidx);
          SB0();
          if (i < 4) b[(i + 4) % 5] = LDM(i);
          if (i == 3 || i == 4) LDW(pre[(i + 1) & 3], 1 + i);
        }
      }
      vm[NV - 1] = pk2(a1[0] + a1[2], a1[1] + a1[3]);
      if constexpr (NV == 2) vm[0] = pk2(a0[0] + a0[2], a0[1] + a0[3]);
      asm volatile("" : "+v"(vm));
      const bool hi = so & 128;
      if (QW && PVP) PRIO(2);
      if (!QW && KV0) PRIO(1);
      SB0();
      STAMP(5);
      BARN();
      {
        const bf4 p0 = trrd(smem + po), p1 = trrd(smem + po + 256);
        SB0();
        const bf4 m1 = trrd(smem + (NV == 2 ? qo : po) + 512);
        SB0();
        bf4 m0;
        if constexpr (NV == 2) m0 = trrd(smem + po + 512);
        SB0();
        const u32x8 pp = ilv(m0, m1, p0, p1);
        f4 o[NV];
#pragma unroll
        for (int d = NV - 1; d >= 0; d--) {
          o[d] = MFMA(vx[d], __builtin_bit_cast(bf8, __builtin_shufflevector(pp, pp, 4, 5, 6, 7)), ((f4){0, 0, 0, 0})); asm volatile("" : "+v"(o[d])); SB0();
        }
        o[NV - 1] = MFMA(__builtin_bit_cast(bf8, vm), __builtin_bit_cast(bf8, __builtin_shufflevector(pp, pp, 2, 3, 4, 5)), o[NV - 1]);
        asm volatile("" : "+v"(o[NV - 1]));
        SB0();
        if constexpr (NV == 2) {
          o[0] = MFMA(__builtin_bit_cast(bf8, vm), __builtin_bit_cast(bf8, __builtin_shufflevector(pp, pp, 0, 1, 2, 3)), o[0]);
          asm volatile("" : "+v"(o[0]));
        }
        asm volatile("" :: "v"(pp));
        SB0();
        u32x2 ov = {pk2(o[0][0], o[0][1]), pk2(o[0][2], o[0][3])};
        if constexpr (NV == 2) {
          const u32x2 ow = {pk2(o[1][0], o[1][1]), pk2(o[1][2], o[1][3])};
          if (hi) ov = ow;
        }
        ov[0] = umin(ov[0], 0xFFFFFFFEu);
        __builtin_amdgcn_raw_buffer_store_b64(ov, rsX, so, so_, 16);
        SB0();
      }
      STAMP(7);
      if (QW && !PVP) PRIO(2);
      int xo, lo, pa = 0;
      {
        IDX();
        if constexpr (NV == 1) {
          __builtin_amdgcn_s_sleep(LSL);
          if (NXV && vt >= NXH) {
            asm volatile("s_mov_b32 m0, %3\n\ts_nop 0\n\tbuffer_load_dwordx4 %0, %1, %2 offen sc1 lds\n\tbuffer_load_dwordx4 %0, %1, %2 offen offset:1024 sc1 lds\n\t"
                         "buffer_load_dwordx4 %0, %1, %2 offen offset:2048 sc1 lds\n\tbuffer_load_dwordx4 %0, %1, %2 offen offset:3072 sc1 lds"
                         :: "v"((lane * 16) ^ ((lane & (vt * 16) & 32) << 2)), "s"(rsB), "s"(nb + (1 + (vt >> 1)) * ACB + (vt & 1) * 4096), "s"(C_KX + vt * 4096) : "memory", "m0");
            SB0();
          }
#pragma unroll
          for (int i = 0; i < 2; i++) nx[i] = __builtin_amdgcn_raw_buffer_load_b128(rsB, lane * 16 + i * 1024, nb + vt * 2048, 16);
          pa = C_QX + vt * 2048 + lane * 16;
          asm volatile("" : "+v"(pa));
        }
        xo = OB_OFF((lq & 1) * 16 + l15, (lq >> 1) * 8);
        lo = lane * 4;
        asm volatile("" : "+v"(xo), "+v"(lo));
#pragma unroll
        for (int j = 0; j < 4; j++) LDW(pre[j], NV == 2 ? 2 + j : j);
      }
      SB0();
      BARN();
      STAMP(8);
      {
        f4 o[2][NV];
#pragma unroll
        for (int it = 0; it < 2; it++) {
          const bf8 pf = *(const bf8*)(smem + C_PX + it * 1024 + lo * 4);
          const u32 pm = *(const u32*)(smem + C_PX + 2048 + it * 256 + lo);
#pragma unroll
          for (int d = 0; d < NV; d++) {
            o[it][d] = MFMA(vx[d], pf, ((f4){0, 0, 0, 0}));
            o[it][d] = MFMA(__builtin_bit_cast(bf8, vm), __builtin_bit_cast(bf8, d ? (u32x4){0u, pm, 0u, 0u} : (u32x4){pm, 0u, 0u, 0u}), o[it][d]);
          }
        }
#pragma unroll
        for (int d = 0; d < NV; d++)
          __builtin_amdgcn_raw_buffer_store_b128(pair16(umin(pk2(o[0][d][0], o[0][d][1]), 0xFFFFFFFEu), pk2(o[0][d][2], o[0][d][3]),
                                                        umin(pk2(o[1][d][0], o[1][d][1]), 0xFFFFFFFEu), pk2(o[1][d][2], o[1][d][3])), rsB,
              xo, 3 * ACB + OB_OFF(s * 32, h * 128 + (vt + d) * 16), 16);
        SB0();
        if (s == 63) break;
        if constexpr (NV == 1) { *(u32x4*)(smem + pa) = nx[0]; *(u32x4*)(smem + pa + 1024) = nx[1]; }
      }
      STAMP(9);
      YEND();
    }
    b3 = b3 == 2 ? 0 : b3 + 1;
    STAMP(14);
  }
  };
  if (wid == 0) run(std::integral_constant<int, 0>{});
  else if (wid == 4) run(std::integral_constant<int, 1>{});
  else if (wid > 4) run(std::integral_constant<int, 3>{});
  else if (wid == 3) run(std::integral_constant<int, 2>{});
  else run(std::integral_constant<int, 4>{});
  PRIO(0);
  TLS(0);
  return xp;
}

__device__ __forceinline__ int chain_out(const Params& p, char* smem, const int c, const int wv, const bool spec) {
  STS(32 + c, 1);
  constexpr int R = 5; (void)R;
  const int wid = wv, tq = c & 1, cg = c >> 1;
  u32x8 w[4][5];
  const __amdgpu_buffer_rsrc_t rsX = RSRC(XWIN(p), X_END);
  int xp = 0, mo, wo, ro;
  const int ms = wid < 4 ? 65536 : 1024;
#define OST 0
  const int r1 = tq & OST, r2 = cg & (OST >> 1);
  {
    IDX();
    mo = wid < 4 ? (cg >> 3) * 32768 + (cg & 7) * 512 + tq * 256 + wid * 64 : 196608 + (cg >> 2) * 512 + tq * 256 + (cg & 3) * 64;
    if (wid > 4 || (wid == 4 && cg >= 8)) mo = PJ_OOB;
    mo ^= X_M;
    u32 m0[2], old = 0u;
    if (wid == 0) {
#pragma unroll
      for (int i = 0; i < 2; i++) m0[i] = *(const GLB u32*)(p.Bm + (tq * 4 + q4) * 576 + (i ? 512 + (cg & 7) * 8 : cg * 32 + lq * 8) + p4 * 2);
      const int cal = X_CAL + (cg & 7) * 32768 + (cg >> 3) * 1024 + tq * 512;
      __builtin_amdgcn_raw_buffer_store_b32(0u, rsX, lane < 8 ? ((2048 << p4) & ~2048) + q4 * 2304 : PJ_OOB, cal, 16);
      asm volatile("" :: "v"(m0[0]), "v"(m0[1]));
      VMW0();
      TLS(8);
      u32 d[4] = {0u, 0u, 0u, 0u};
#pragma unroll 1
      for (int r = 0; r < CALR; r++) {
#pragma unroll
        for (int k = 0; k < 4; k++) {
          const u32 t0 = (u32)__builtin_readcyclecounter();
          __builtin_amdgcn_raw_buffer_store_b32(1u, rsX, lane ? PJ_OOB : 0, cal + ((2048 << k) & ~2048) + (r ^ tq) * 2304, 16);
          VMW0();
          d[k] += (u32)__builtin_readcyclecounter() - t0;
        }
      }
      if (OBE) BARN();
      if (spec) old = confirm(p, smem, 32 + c, BEAT_LD(c), wv);
      if (lane == 0) st4_sc1(WSP(u32, 0) + SY_VOTE(c), 8u + (((d[1] < d[0] ? 1u : 0u) + (d[3] < d[0] ? 4u : 0u)) ^ (d[2] < d[0] ? 3u : 0u)));
      TLS(11);
    }
    if (!OBE || wid) BARN();
    if (wid == 0) {
      u32 fv;
      int it = 0;
      while (fv = ld4_sc1(WSP(u32, 0) + SY_VOTE(lane & 31)), xp = -((__builtin_amdgcn_readfirstlane(old) >> c) & 1), !(xp || __all(fv != 0u)) && ++it < SPIN_CAP) {
        BEAT_ON(it); __builtin_amdgcn_s_sleep(2);
      }
#pragma unroll
      for (int j = 0; j < 3; j++) xp |= __popcll(__ballot((fv >> j) & 1u)) > 32 ? 4096 << j : 0;
      if (xp >= 0) {
#pragma unroll
        for (int i = 0; i < 3; i++) __builtin_amdgcn_raw_buffer_store_b32(i ? ~0u : umin(m0[0], 0xFFFFFFFEu), rsX, lane * 4, mo ^ xp ^ (i * 65536), 16);
        __builtin_amdgcn_raw_buffer_store_b32(lq ? ~0u : umin(m0[1], 0xFFFFFFFEu), rsX, lq < 3 && cg < 8 ? lq * 1024 + l15 * 4 : PJ_OOB,
                                              (196608 + (cg >> 2) * 512 + tq * 256 + (cg & 3) * 64) ^ X_M ^ xp, 16);
#pragma unroll
        for (int i = 0; i < 6; i++)
          __builtin_amdgcn_raw_buffer_store_b128(ones4(), rsX, (lane >> 5) * 131072 + (lane & 31) * 16, X_O(i >> 1) ^ ((c >> 3) * 32768 + (c & 7) * 512) ^ xp ^ ((i & 1) * X_HS), 16);
        if (c == 0) {
#pragma unroll
          for (int i = 0; i < 6; i++) __builtin_amdgcn_raw_buffer_store_b128((u32x4){0u, 0u, 0u, 0u}, rsX, lane * 16 + (i & 3) * 1024, X_HOT(xp) ^ (i < 4 ? X_CS : 0), 16);
        }
        __builtin_amdgcn_raw_buffer_store_b32(lane & 2 ? DO0 * 64u : ~0u, rsX, lane < 4 ? ANX(lane & 1) + ((lane & 2) * 16 + c) * ANS : PJ_OOB, 0, 16);
      }
    }
    SB0();
    const __amdgpu_buffer_rsrc_t rw = RSRC(p.BWo, 576 * 4096 * 2);
    u32x4 st[4][8];
    u32 fl;
    bool early = false;
    const int va = ((lane >> 2) * 576 + cg * 32 + p4 * 8) * 2;
#pragma unroll
    for (int i = 0; i < 4; i++) {
#pragma unroll
      for (int k = 0; k < 8; k++) st[i][k] = __builtin_amdgcn_raw_buffer_load_b128(rw, va, ((((i & 1) ^ r1) * 16 + wid * 2 + ((k >> 2) ^ r2)) * 128 + (i >> 1) * 64 + (k & 3) * 16) * 1152, OSTG_AUX);
      SB0();
      if (i == 1 && wid == 0 && xp >= 0) {
        asm volatile("s_waitcnt vmcnt(16)" ::: "memory");
        if (lane == 0) st4_sc1(WSP(u32, 0) + SY_INIT(c), (u32)xp + 1u);
        TLS(10);
      }
      if (i == 2) fl = ld4_sc1(WSP(u32, 0) + SY_WX);
      SB0();
    }
    auto wx = [&](int vo) {
#pragma unroll
      for (int k = 0; k < 8; k++)
        st[0][k] = __builtin_amdgcn_raw_buffer_load_b128(RSRC(WSP(char, O_WX), 524288), vo, cg * 65536 + ((((k & 2) ^ (r1 * 2)) * 8 + wid * 2 + (k & 1)) * 128 + (k >> 2) * 64) * 16, 16);
    };
    auto poll = [&]() { fl = __builtin_amdgcn_raw_buffer_load_b32(RSRC(p.ws, wid ? 0 : O_QA), (lane < 32 ? SY_INIT(lane) : lane < 48 ? SY_ARR : SY_WX) * 4, 0, 16); };
    char* sc = smem + wid * 10240;
    auto frag = [&](const char* b, int rs) {
      bf4 tt[4];
#pragma unroll
      for (int kk = 0; kk < 4; kk++) {
        const int r0 = ((kk >> 1) * 2 + (lq >> 1)) * 32 + (p4 >> 1) * 16 + (q4 >> 1) * 8 + (lq & 1) * 4 + (kk & 1) * 2 + (q4 & 1);
        tt[kk] = trrd(b + r0 * rs + (p4 & 1) * 8);
      }
      return ilv(tt[0], tt[1], tt[2], tt[3]);
    };
#pragma unroll
    for (int i = 0; i < 4; i++) {
#pragma unroll
      for (int k = 0; k < 8; k++) *(u32x4*)(sc + ((k ^ (r2 * 4)) * 16 + (lane >> 2)) * 80 + p4 * 16) = st[i][k];
      __builtin_amdgcn_fence(__ATOMIC_RELEASE, "wavefront");
      __builtin_amdgcn_wave_barrier();
#pragma unroll
      for (int t = 0; t < 4; t++) w[i][t] = frag(sc + t * 16, 80);
      asm volatile("s_waitcnt lgkmcnt(0)" ::: "memory");
      __builtin_amdgcn_wave_barrier();
      if (i == 2) { early = __all(fl == ~0u); CFENCE(); poll(); wx(early ? lane * 16 : PJ_OOB); }
      SB0();
    }
    if (wid == 0) {
      int it = 0;
      while (xp >= 0 && !__all(fl >= (lane < 32 ? 1u : lane < 48 ? 256u : ~0u)) && ++it < SPIN_CAP) { BEAT_ON(it); __builtin_amdgcn_s_sleep(GPS); CFENCE(); poll(); }
      *(int*)(smem + C_XP) = xp;
    }
    asm volatile("s_waitcnt lgkmcnt(0)" ::: "memory");
    BARN();
    xp = __builtin_amdgcn_readfirstlane(*(const int*)(smem + C_XP));
    if (xp < 0) return xp;
    {
      if (!early) wx(lane * 16);
#pragma unroll
      for (int k = 0; k < 8; k++) *(u32x4*)(sc + k * 1024 + lane * 16) = st[0][k];
      __builtin_amdgcn_fence(__ATOMIC_RELEASE, "wavefront");
      __builtin_amdgcn_wave_barrier();
#pragma unroll
      for (int i = 0; i < 4; i++) w[i][4] = frag(sc + i * 2048, 16);
    }
    mo = __builtin_amdgcn_readfirstlane(mo ^ xp);
    const int row = (lq & 1) * 4 + ((l15 >> 1) & 3);
    wo = ((lq >> 1) == hi8 ? row * 128 : 1024) + (((wid * 2 + hi8) ^ (row * 2)) << 3) + (l15 & 1) * 4;
    ro = wid * 2560 + (lane >> 5) * 1280 + ((lane >> 2) & 7) * 128 + (lane & 8) * 8 + p4 * 16;
  }
  __syncthreads();
  STS(32 + c, 14);
  TLS(9);
  int go, sidx, po;
  { IDX(); go = lane * 16; sidx = (l15 & 2) ? 0xEEEE : 0x4444;
    po = p4 == 0 ? (lq & 1) * 32 + (lq >> 1) * 16 + q4 * 4 : PJ_OOB; }
  int ro1 = ro ^ 64;
  asm volatile("" : "+v"(ro), "+v"(ro1), "+v"(wo));
#define PUTP(t) asm volatile("ds_write2st64_b32 %0, %1, %2 offset0:%3 offset1:%4" :: "v"(wo), "v"(d[t][0] + d[t][2]), "v"(d[t][1] + d[t][3]), "i"((t) * 10), "i"((t) * 10 + 5) : "memory")
#define APS 18
#define ONOP 7
  int b3 = 0, dur = DO0 * 64;
#pragma unroll 1
  for (int s = 0; s < 63; s++) {
    f4 d[5];
    int gb[4], pb = mo ^ ((b3 == 2 ? 0 : b3 + 1) * ms);
#pragma unroll
    for (int i = 0; i < 4; i++) { gb[i] = X_O(b3) ^ xp ^ (tq * 131072 + (((i & 1) ^ r1) * 2 + (wid >> 2)) * 32768 + (wid & 3) * 1024 + (i < 2 ? X_HS : 0)); asm volatile("" : "+s"(gb[i])); }
    asm volatile("" : "+s"(pb));
    u32x4 g[4];
    u32 bad;
    auto mult = [&]() __attribute__((always_inline)) {
      CFENCE();
#pragma unroll
      for (int i = 0; i < 4; i++) {
        g[i] = __builtin_amdgcn_raw_buffer_load_b128(rsX, go, gb[i], 16);
        SB0();
        if (ONOP >= 0 && i < 3) asm volatile("s_nop %0" :: "i"(ONOP >= 0 ? ONOP : 0));
        SB0();
      }
      bad = 0u;
#pragma unroll
      for (int i = 0; i < 4; i++) {
        SB0();
#pragma unroll
        for (int t = 0; t < 5; t++) {
          d[t] = SMF(g[i], w[i][t], d[t], sidx);
          if (t == 0) bad = umax(umax(bad, g[i][0]), g[i][2]);
          if (i == 3 && t >= 2) { SB0(); PUTP(t - 2); SB0(); }
        }
      }
      SB0();
#pragma unroll
      for (int t = 3; t < 5; t++) PUTP(t);
      SB0();
    };
#pragma unroll
    for (int t = 0; t < 5; t++) d[t] = zero4();
    SB0();
    u32 dl = 0;
    ALIGN(OAL);
    if (wid == 7) { __builtin_amdgcn_s_sleep(APS); STAMP(4); AD_GET(false, K1 + KXT(KA), SPIN_CAP); STAMP(5); AD_SPIN(); }
    STAMP(15);
    BARN();
    STAMP(0);
    mult();
    if (__builtin_expect(__any(bad == ~0u), 0)) {
      int it = 0;
      do {
#pragma unroll
        for (int t = 0; t < 5; t++) d[t] = zero4();
        mult();
      } while (__any(bad == ~0u) && ++it < SPIN_CAP);
    }
    STAMP(1);
    BARC();
    STAMP(2);
    if (__builtin_expect(wid < 5, 1)) {
      const f4 t0 = *(const f4*)(smem + ro), t1 = *(const f4*)(smem + ro1);
      float a = (t0[0] + t0[2]) + (t1[0] + t1[2]), b = (t0[1] + t0[3]) + (t1[1] + t1[3]);
      a += __uint_as_float(__builtin_amdgcn_mov_dpp(__float_as_uint(a), 0xB1, 0xf, 0xf, true));
      b += __uint_as_float(__builtin_amdgcn_mov_dpp(__float_as_uint(b), 0xB1, 0xf, 0xf, true));
      a += __uint_as_float(__builtin_amdgcn_mov_dpp(__float_as_uint(a), 0x4E, 0xf, 0xf, true));
      b += __uint_as_float(__builtin_amdgcn_mov_dpp(__float_as_uint(b), 0x4E, 0xf, 0xf, true));
      __builtin_amdgcn_raw_buffer_store_b32(umin(pk2(a, b), 0xFFFFFFFEu), rsX, po, pb, 16);
      SB0();
      STAMP(3);
      __builtin_amdgcn_s_sleep(4);
      __builtin_amdgcn_raw_buffer_store_b32(~0u, rsX, po, mo ^ (b3 * ms), 16);
    } else if (wid == 7) {
      dur += (min((int)(((u32)wall_clock64() << 6) - dl), dur + DOCAP * 64) - dur) >> DSH;
      if (DRL) __builtin_amdgcn_s_sleep(DRL);
      __builtin_amdgcn_raw_buffer_store_b32((u32)dur, rsX, 0, ANX(~s & 1) + (32 + c) * ANS, 16);
      if (CSL && c == CSC && s > 0) { if (CSL > DRL) __builtin_amdgcn_s_sleep(CSL > DRL ? CSL - DRL : 0); __builtin_amdgcn_raw_buffer_store_b32(dl | 1u, rsX, 0, (X_HOT(xp) ^ X_CS) + (s - 1) * 64, 16); }
    }
    VMW0C();
    YEND();
    b3 = b3 == 2 ? 0 : b3 + 1;
  }
  __syncthreads();
  return xp;
}

__device__ __forceinline__ void wait_ge(const u32* w, u32 val, const int wv) {
  if (TIDX == 0) {
    int it = 0;
    while (ld4_sc1(w) < val && ++it < SPIN_CAP) __builtin_amdgcn_s_sleep(WPS);
  }
}
__device__ __forceinline__ void acquire_all(const int wv) {
  if (TIDX == 0) {
    __builtin_amdgcn_fence(__ATOMIC_ACQUIRE, "agent");
    VMW0();
  }
  __syncthreads();
}

#define T_KV 0
#define T_Q 0
#define T_A 192
#define T_W (T_A + 64)
#define T_S (T_W + 256)
#define T_L (T_S + 72)
#define T_O T_L
#define T_CHAIN (1 << 20)
#define CODE_KB 84
#define OWARM 2
#define BXW 1
#define AORD 0x01234567u
#define OORD 0x67014523u

__device__ __forceinline__ int next_task(const Params& p, int* s_task, const int wv, const int pt, const int pre) {
  __syncthreads();
  if (wv == 0) {
    int t = T_A + pre;
    if (__builtin_amdgcn_readfirstlane(pre) < 0) {
      t = -1;
      int xp = 0;
      for (int it = 0; it < SPIN_CAP; it++) {
        const int l = TIDX & 15;
        const u32 v = ld4_sc1(WSP(u32, 0) + (l < 8 ? SY_JOB(l) : l == 8 ? SY_ARR : SY_INIT(0)));
        const u32 open = (u32)__ballot(v != ~0u) & 255u;
        xp = __builtin_amdgcn_readlane(v, 9);
        if (!open && xp) break;
        if (!open || __builtin_amdgcn_readlane(v, 8) < 256u) { BEAT_ON(it); __builtin_amdgcn_s_sleep(8); continue; }
        const int x = __builtin_ctz(open), k = __builtin_ctz(~(u32)__builtin_amdgcn_readlane(v, x));
        u32 old = 0u;
        if (TIDX == 0) old = AOR(SY_JOB(x), 1u << k);
        if (!((__builtin_amdgcn_readfirstlane(old) >> k) & 1)) { t = x * 32 + k + (x < 2 ? T_CHAIN : -64); break; }
      }
      if (t < 0 && TIDX == 0) t = T_A + (int)__hip_atomic_fetch_add(HOTP(xp) + SY_QHEAD, 1u, __ATOMIC_RELAXED, __HIP_MEMORY_SCOPE_AGENT) + (xp & 0x7000);
    }
    if (TIDX == 0) *s_task = t;
  }
  __syncthreads();
  return __builtin_amdgcn_readfirstlane(*s_task);
}

__device__ __forceinline__ int take_task(int* s_task, const int pre, const int wv, const int pb, const int pl, const int pt) {
  if (TIDX == 0) {
    *s_task = pre;
  }
  __syncthreads();
  const int t = __builtin_amdgcn_readfirstlane(*s_task);
  __syncthreads();
  return t;
}
#if defined(__HIP_DEVICE_COMPILE__) && !defined(KEEP_PK)
#define NOPK __attribute__((target("no-packed-fp32-ops")))
#else
#define NOPK
#endif
__device__ __forceinline__ void mega_body(const Params& p0, char* smem) {
  int* const s_task = (int*)(smem + LDS_TOTAL);
  int wv = __builtin_amdgcn_readfirstlane(threadIdx.x >> 6), t, xs;
#define tid TIDX
  {
    const Params& p = p0;
    u64 pc0; asm volatile("s_getpc_b64 %0" : "=s"(pc0));
#define ZB 1
    const int xcc = (int)(__builtin_amdgcn_s_getreg(20 | (3 << 11)) & 7u), xq = xcc >= 2 ? xcc - 2 : xcc, idx = (blockIdx.x >> 3) & 31;
    t = xq * 32 + (xcc < 2 ? T_CHAIN + idx : (idx + 31) & 31);
    xs = xcc + 8;
#define WARM(ptr, bytes, vo, so, sc) asm volatile("s_mov_b32 m0, %3\n\ts_nop 0\n\tbuffer_load_dword %0, %1, %2 offen" sc " lds" \
                                                   :: "v"(vo), "s"(RSRC(ptr, bytes)), "s"(so), "s"(C_B0 + wv * 256) : "memory", "m0")
    if (wv == 0) WARM(p.ws, O_QA, 0, SY_BEAT(t & 7) * 4, " sc1");
    if (blockIdx.x == ZB) {
#pragma unroll
      for (int i = 0; i < 4; i++) __builtin_amdgcn_raw_buffer_store_b128((u32x4){0u, 0u, 0u, 0u}, RSRC(p.ws, SY_WORDS * 4), tid * 16 + i * 8192, 0, 16);
      VMW0();
      BARN();
      if (tid < 8) AADD(SY_BEAT(tid), 1u);
    }
    if (wv) {
      const int g = idx * 7 + wv - 1, vo = (tid & 63) * 64;
      if (g < CODE_KB / 4) WARM(pc0, CODE_KB * 1024, vo, g * 4096, "");
      if (xcc > 1) {
        if (OWARM == 1 && g >= 32) WARM(p.BWo, WOB, vo, (xq * 192 + g - 32) * 4096, "");
        if (OWARM == 2 && g >= 128) WARM(p.BWo, WOB, vo * 2, (xq * 96 + g - 128) * 8192, "");
        if (BXW) WARM(p.Bx, 256 * 1152, vo, (223 - g) * 4096, "");
      }
    }
  }
  u64 pp = (u64)__builtin_amdgcn_kernarg_segment_ptr();
  SB0();
  asm volatile(".Lmega_again:" : "+{s[40:41]}"(pp), "+{s42}"(wv), "+{s43}"(t), "+{s44}"(xs) :: "memory");
  SB0();
  const Params& p = *(const Params*)(const __attribute__((address_space(4))) Params*)pp;
  const int xcc = xs & 7, xq = xcc >= 2 ? xcc - 2 : xcc;
  const bool spec = xs > 7;
  int pre = -1;
  if (t < T_A) {
    const int x = t >> 5, j = (t & 7) * 4 + ((0x9C >> ((t >> 2) & 6)) & 3);
    proj_slot(p, x, j, smem, wv, pre, spec ? t + 64 : -1);
    VMW0C();
  } else {
    int own;
    if (t < T_CHAIN + 32) { own = chain_head(p, smem, t - T_CHAIN, wv, spec); VMW0C(); }
    else { own = chain_out(p, smem, t - T_CHAIN - 32, wv, spec); VMW0C(); asm volatile("s_nop 0"); }
    if (own >= 0) {
      const int l = tid & 7;
      const u32 v = ld4_sc1(HOTP(own) + (l < 7 ? SY_OQ(l) : SY_QHEAD));
      if (tid == 0) *s_task = 0;
      __syncthreads();
      if (v < (l == 7 ? T_S - T_A : l == 6 ? 72 : (l & 1) ? NOB : NOA)) *s_task = 1;
      __syncthreads();
      if (__builtin_amdgcn_readfirstlane(*s_task) == 0) {
        if (xcc == 0) TLS(3); else TLS(4);
        return;
      }
    }
  }
  t = next_task(p, s_task, wv, t, pre);
  if (t < T_A || t >= T_CHAIN) {
    asm volatile("s_waitcnt vmcnt(0) lgkmcnt(0)\n\ts_barrier\n\ts_branch .Lmega_again" :: "{s[40:41]}"(pp), "{s42}"(wv), "{s43}"(t), "{s44}"(xcc) : "memory");
    return;
  }
  u32* const hot = HOTP(t);
  u32* const qh = hot + SY_QHEAD;
  t &= ~0x7000;
#define WPOS 0
  while (t < T_S) {
    if ((u32)(t - T_A - WPOS) < 64u) {
      wof_task(p, t - T_A - WPOS, smem, wv, qh, pre);
      if (tid == 0) __hip_atomic_fetch_add(hot + SY_CW, 1u, __ATOMIC_RELAXED, __HIP_MEMORY_SCOPE_AGENT);
      t = T_A + take_task(s_task, pre, wv, T_A, T_S - T_A, t);
      continue;
    }
    const int qb = (AORD >> (((t - (t < T_A + WPOS ? T_A : T_W)) >> 5) * 4)) & 7, h = t & 31;
    if (tid < 64) {
      int it = 0;
      while (true) {
        const u32 f0 = ld4_sc1(WSP(u32, 0) + SY_BLK(3, h >> 1, tid)), f1 = ld4_sc1(WSP(u32, 0) + SY_BLK(4, h >> 1, tid)), f2 = ld4_sc1(WSP(u32, 0) + SY_BLK(5, h >> 1, tid));
        if (__all(tid >= qb * 8 + 8 || ((tid < qb * 8 || f0 >= FULL) && f1 >= FULL && f2 >= FULL)) || ++it >= SPIN_CAP) break;
        __builtin_amdgcn_s_sleep(8);
      }
    }
    acquire_all(wv);
    fa::attn_block(WSP(char, O_QA) + FR_OFF(qb * 256, h * 128), WSP(char, O_KA) + h * 8192, WSP(char, O_VA) + h * 8192,
                   WSP(char, O_CA) + OB_OFF(qb * 256, h * 128), qb * 256, smem, wv, qh, pre);
    if (tid == 0) __hip_atomic_fetch_add(hot + SY_CC(qb), 1u, __ATOMIC_RELAXED, __HIP_MEMORY_SCOPE_AGENT);
    t = T_A + take_task(s_task, pre, wv, T_A, T_S - T_A, t);
  }
  int rest = 64 | (1 << xq);
#pragma unroll 1
  while (rest) {
    const int x = __builtin_ctz(rest);
    rest &= rest - 1;
    if (x == 6) {
      CLAIMS();
      t = take_task(s_task, pre, wv, T_S, 72, -1);
      while ((t & 0xFFFF) < 72) {
        t &= 0xFFFF;
        small_out_task(p, t & 3, t >> 2, smem, wv, hot, pre);
        t = take_task(s_task, pre, wv, T_S, 72, T_S + t);
      }
      rest = t >> 16;
      continue;
    }
    const int part = x & 1, cg = x >> 1, n = part ? NOB : NOA;
    u32* const qh = hot + SY_OQ(x);
    CLAIM();
    t = take_task(s_task, pre, wv, T_O + x * 32, n, -1);
    while (t < n) {
      const int nb = part ? NBB : NAB, big = t < nb, e = big ? t * 2 : t + nb;
      const int rb = part ? e : ((OORD >> ((e >> 2) * 4)) & 7) * 4 + (e & 3);
      if (tid == 0) {
        const u32* w = part ? CSP((rb + big) * 2 + 1) : hot + SY_CC(rb >> 2);
        int it = 0;
        while ((ld4_sc1(w) < (part ? 1u : 32u) || ld4_sc1(hot + SY_CW) < 64u) && ++it < SPIN_CAP) __builtin_amdgcn_s_sleep(WPS);
      }
      acquire_all(wv);
      const char *pa = WSP(char, part ? O_OB : O_CA) + rb * 2 * 262144, *pw = WSP(char, O_WOF) + part * WOB + cg * 12 * 131072;
      u16* const pc = p.out + (size_t)rb * 64 * 1152 + part * 576 + cg * 192;
      if (big) out_tile<8>(pa, pw, pc, smem, wv, qh, pre);
      else out_tile<4>(pa, pw, pc, smem, wv, qh, pre);
      t = take_task(s_task, pre, wv, T_O + x * 32, n, T_O + x * 32 + t);
    }
  }
  if (xcc == 0) TLS(3); else if (xcc == 1) TLS(4); else TLS(5);
}

#undef tid
__global__ NOPK void __launch_bounds__(512, 2) mega(Params p) {
  __shared__ __attribute__((aligned(16))) char smem[LDS_ALL];
  mega_body(p, smem);
}
extern "C" void kernel_launch(void* const* d_in, const int* in_sizes, int n_in, void* d_out, int out_size, void* d_ws,
                              size_t ws_size, hipStream_t stream) {
  Params p;
  p.Ax = (const u16*)d_in[0]; p.Ac = (const u16*)d_in[1]; p.Awq = (const u16*)d_in[2]; p.Awk = (const u16*)d_in[3];
  p.Awv = (const u16*)d_in[4]; p.Awo = (const u16*)d_in[5]; p.Bx = (const u16*)d_in[6]; p.Bm = (const u16*)d_in[7];
  p.BWq = (const u16*)d_in[8]; p.BWk = (const u16*)d_in[9]; p.BWv = (const u16*)d_in[10]; p.BWo = (const u16*)d_in[11];
  p.out = (u16*)d_out;
  p.ws = (char*)d_ws;
  mega<<<256, 512, 0, stream>>>(p);
}
```

```cpp
#include <hip/hip_runtime.h>
#include <hip/hip_bf16.h>
#include <stdint.h>
#include <type_traits>

typedef unsigned short u16;
typedef unsigned int u32;
typedef unsigned long long u64;
typedef __attribute__((ext_vector_type(8))) short bf8;
typedef __attribute__((ext_vector_type(4))) short bf4;
typedef __attribute__((ext_vector_type(4))) float f4;
typedef __attribute__((ext_vector_type(2))) float f2v;
typedef __attribute__((ext_vector_type(2))) __bf16 bf2v;
typedef __attribute__((ext_vector_type(4))) unsigned u32x4;
typedef __attribute__((ext_vector_type(2))) unsigned u32x2;
typedef float f32x16 __attribute__((ext_vector_type(16)));
typedef __attribute__((ext_vector_type(8))) unsigned u32x8;
typedef __attribute__((ext_vector_type(8))) __bf16 bh8;
typedef __attribute__((ext_vector_type(16))) __bf16 bh16;
#define LDSP __attribute__((address_space(3)))

__device__ __forceinline__ u32 pk2(float a, float b) {
  f2v x = {a, b};
  bf2v y = __builtin_convertvector(x, bf2v);
  return __builtin_bit_cast(u32, y);
}
__device__ __forceinline__ u32 umin(u32 a, u32 b) { return a < b ? a : b; }
__device__ __forceinline__ u32 umax(u32 a, u32 b) { return a > b ? a : b; }
__device__ __forceinline__ float rbf(float f) { return __uint_as_float(pk2(f, f) << 16); }
#define MFMA(a, b, c) __builtin_amdgcn_mfma_f32_16x16x32_bf16(a, b, c, 0, 0, 0)
#define MFMA16(a, b, c) __builtin_amdgcn_mfma_f32_16x16x16bf16_1k(a, b, c, 0, 0, 0)
#define SMF(a, b, c, idx) __builtin_amdgcn_smfmac_f32_16x16x64_bf16(__builtin_bit_cast(bh8, a), __builtin_bit_cast(bh16, b), c, idx, 0, 0)
__device__ __forceinline__ u32x8 ilv(bf4 a, bf4 b, bf4 c, bf4 d) {
  const u32x2 x = __builtin_bit_cast(u32x2, a), y = __builtin_bit_cast(u32x2, b), z = __builtin_bit_cast(u32x2, c), w = __builtin_bit_cast(u32x2, d);
  return (u32x8){x[0], x[1], y[0], y[1], z[0], z[1], w[0], w[1]};
}
__device__ __forceinline__ u32x8 cat16(bf8 a, bf8 b) {
  const u32x4 x = __builtin_bit_cast(u32x4, a), y = __builtin_bit_cast(u32x4, b);
  return (u32x8){x[0], x[1], x[2], x[3], y[0], y[1], y[2], y[3]};
}
__device__ __forceinline__ float swadd(float a, float b) {
  auto x = __builtin_amdgcn_permlane32_swap(__float_as_uint(a), __float_as_uint(b), false, false);
  return __uint_as_float(x[0]) + __uint_as_float(x[1]);
}
__device__ __forceinline__ bf4 trrd(const char* p) {
  return __builtin_amdgcn_ds_read_tr16_b64_v4i16((LDSP bf4*)(p));
}
__device__ __forceinline__ bf8 cat8(bf4 a, bf4 b) { return __builtin_shufflevector(a, b, 0, 1, 2, 3, 4, 5, 6, 7); }
__device__ __forceinline__ void st8_sc1(void* p, u64 v) { __hip_atomic_store((u64*)p, v, __ATOMIC_RELAXED, __HIP_MEMORY_SCOPE_AGENT); }
__device__ __forceinline__ u32 ld4_sc1(const u32* p) { return __hip_atomic_load(p, __ATOMIC_RELAXED, __HIP_MEMORY_SCOPE_AGENT); }
__device__ __forceinline__ void st4_sc1(u32* p, u32 v) { __hip_atomic_store(p, v, __ATOMIC_RELAXED, __HIP_MEMORY_SCOPE_AGENT); }
#define VMW0() asm volatile("s_waitcnt vmcnt(0)" ::: "memory")
#define CFENCE() asm volatile("" ::: "memory")
#define VMW0C() do { __builtin_amdgcn_s_waitcnt(0x0F70); asm volatile("" ::: "memory"); } while (0)
#define SPIN_CAP (1 << 22)
#define XPS 4
#define SPS 4
#define WPS 8
__device__ __forceinline__ int tid_now(int wv) {
  int l; asm volatile("v_mbcnt_lo_u32_b32 %0, -1, 0\n\tv_mbcnt_hi_u32_b32 %0, -1, %0" : "=v"(l));
  return wv * 64 + l;
}
#define TIDX tid_now(wv)
#define CLAIM() do { if (TIDX == 0) pre = (int)__hip_atomic_fetch_add(qh, 1u, __ATOMIC_RELAXED, __HIP_MEMORY_SCOPE_AGENT); } while (0)

#define TLS(i)
struct Params {
  const u16 *Ax, *Ac, *Awq, *Awk, *Awv, *Awo, *Bx, *Bm, *BWq, *BWk, *BWv, *BWo;
  u16* out;
  char* ws;
};
#define SY_QHEAD 0
#define SY_CC(rb) (64 + (rb) * 16)
#define SY_CW (64 + 8 * 16)
#define SY_OQ(x) (64 + 9 * 16 + (x) * 16)
#define SY_B0 (64 + 80 * 16)
#define SY_INIT(c) (SY_B0 + 64 + (c))
#define SY_VOTE(i) (SY_B0 + 128 + (i))
#define SY_JOB(x) (SY_B0 + 256 + (x) * 16)
#define SY_ARR (SY_B0 + 384)
#define SY_WX (SY_ARR + 32)
#define SY_BLK(mi, cb, g) (SY_ARR + 64 + ((mi) * 16 + (cb)) * 64 + (g))
#define FULL 12u
#define SY_WORDS (SY_ARR + 64 + 6 * 16 * 64)
#define SY_BEAT(r) (SY_WORDS + (r) * 32)

#define ACB (2048 * 4096 * 2)
#define O_PAD 0
#define O_QA (SY_BEAT(8) * 4 + O_PAD)
#define O_KA (O_QA + ACB)
#define O_VA (O_KA + ACB)
#define O_CA (O_VA + ACB)
#define O_QB (O_CA + ACB)
#define O_KB (O_QB + ACB)
#define O_VB (O_KB + ACB)
#define O_OB (O_VB + ACB)
#define O_WOF (O_OB + ACB)
#define WOB (576 * 4096 * 2)
#define O_WX (O_WOF + 2 * WOB)
#define O_XW (O_WX + 524288)
#define X_HS 0x4000
#define X_O(b) ((b) * 0x80000 + ((0x140 >> ((b) * 4)) & 15) * 4096)
#define X_MS X_HS
#define X_M (0x185000 ^ X_MS)
#define X_ANN (0x185000 + 7 * 32768)
#define X_CAL 0x40000
#define HFL 0x2000
#define CFL 0x1000
#define X_HOT(xp) ((0xC4000 ^ HFL) ^ ((xp) & 0x7000))
#define X_CS (HFL ^ CFL ^ 0x8000)
#define CSP(s) ((const u32*)(const GLB u32*)((uintptr_t)hot ^ X_CS) + (s) * 16)
#define X_END 0x200000
#define OB_OFF(r, c) (((r) >> 5) * 262144 + ((c) >> 4) * 1024 + ((r) & 31) * 32 + ((c) & 15) * 2)
#define FR_OFF(r, c) (((r) >> 5) * 262144 + ((c) >> 3) * 512 + ((r) & 31) * 16 + ((c) & 7) * 2)
#define GLB __attribute__((address_space(1)))
#define WSP(T, off) ((T*)((GLB char*)p.ws + (off)))
#define XWIN(p) (((uintptr_t)(p).ws + O_XW + 0x1FFFFFu) & ~(uintptr_t)0x1FFFFFu)

#define HOTP(xp) ((u32*)(GLB u32*)(XWIN(p) + X_HOT(xp)))

#define XS0 48
#define NAB 8
#define NBB 10
#define NOA (32 - NAB)
#define NOB (XS0 / 2 - NBB)
#define LDS_TOTAL (131072 + 8192 + 16384)
#define LDS_ALL 163840

#define SB0() __builtin_amdgcn_sched_barrier(0)
#define PJ_OOB 0x7F000000
#define RSRC(ptr, bytes) __builtin_amdgcn_make_buffer_rsrc((void*)(ptr), 0, (bytes), 0x00020000)
__device__ __forceinline__ bf8 ldsc1(__amdgpu_buffer_rsrc_t r, int off) {
  return __builtin_bit_cast(bf8, __builtin_amdgcn_raw_buffer_load_b128(r, off, 0, 16));
}
__device__ __forceinline__ u32x4 pair16(u32 a0, u32 a1, u32 b0, u32 b1) {
  auto r0 = __builtin_amdgcn_permlane16_swap(a0, b0, false, false);
  auto r1 = __builtin_amdgcn_permlane16_swap(a1, b1, false, false);
  return (u32x4){r0[0], r1[0], r0[1], r1[1]};
}
__device__ __forceinline__ int fsw(int k) { return (k & 3) | (((k >> 3) & 1) << 2); }

#define AADD(w, v) __hip_atomic_fetch_add(WSP(u32, 0) + (w), v, __ATOMIC_RELAXED, __HIP_MEMORY_SCOPE_AGENT)
#define AOR(w, v) __hip_atomic_fetch_or(WSP(u32, 0) + (w), v, __ATOMIC_RELAXED, __HIP_MEMORY_SCOPE_AGENT)
#define C_B0 (LDS_ALL - 2048)
#define BEAT_LD(job) ld4_sc1(WSP(u32, 0) + SY_BEAT((job) & 7))
#define BEAT_ON(it) do { if (((it) & 31) == 31 && (TIDX & 63) < 8) AADD(SY_BEAT(TIDX & 63), 1u); } while (0)
__device__ __forceinline__ u32 confirm(const Params& p, const char* smem, const int job, u32 b1, const int wv) {
  asm volatile("" :: "v"(b1) : "memory");
  const u32 b0 = *(const u32*)(smem + C_B0);
  if (__builtin_expect(__any(b1 == b0), 0)) {
    int it = 0;
    while (__any(BEAT_LD(job) == b0) && ++it < SPIN_CAP) __builtin_amdgcn_s_sleep(2);
  }
  u32 old = 0u;
  if (TIDX == 0) old = AOR(SY_JOB(job >> 5) + TIDX, 1u << (job & 31));
  if (TIDX < 2) AADD(TIDX ? SY_BEAT(job & 7) : SY_ARR, 1u);
  return old;
}

typedef __attribute__((ext_vector_type(2))) unsigned long long u64x2;
__device__ __forceinline__ f4 zero4() {
  u64 a, b; asm volatile("v_mov_b64 %0, 0\n\tv_mov_b64 %1, 0\n\ts_nop 1" : "=v"(a), "=v"(b));
  return __builtin_bit_cast(f4, (u64x2){a, b});
}
__device__ __forceinline__ u32x4 ones4() {
  u64 a, b; asm volatile("v_mov_b64 %0, -1\n\tv_mov_b64 %1, -1" : "=v"(a), "=v"(b));
  return __builtin_bit_cast(u32x4, (u64x2){a, b});
}
#define C_PARK 131072
#define PJ_PAD 0
__device__ __forceinline__ void proj_slot(const Params& p, int x, int j, char* smem, const int wv, int& pre, int job) {
  const int tid = TIDX, lane = tid & 63, wid = wv;
  const int l15 = lane & 15, lq = lane >> 4, q4 = l15 >> 2, p4 = lane & 3, wr = wid >> 2, wc = wid & 3;
  const __amdgpu_buffer_rsrc_t rc = RSRC(p.ws, O_XW);
  const int vB = ((wid * 4 + lq) * 4096 + (((l15 >> 1) ^ (lq | (((wid >> 1) & 1) << 2))) * 16 + (lane & 1) * 8)) * 2, vB1 = vB + 32 * 8192;
  int ao = (wr * 64 + l15) * 128 + ((lq ^ (l15 & 7)) << 4);
  int bo = (int)(uintptr_t)smem + 65536 + (lq * 8 + q4) * 256 + (((wc * 2) ^ (q4 | ((lq & 1) << 2))) << 5) + p4 * 8;
  const int vD = lane * 16;
#define PJ_ORD 0x1312
  const int ord = (j & 3) == 3 ? PJ_ORD : 0x1312;
#define PJ_NEXT ((ord >> (u * 4)) & 3)
  const int cb = (x & 1) * 8 + (j >> 2);
  __amdgpu_buffer_rsrc_t rsA, rsB;
  const u16 *pA0 = p.Bx, *pB0 = (x < 2 ? p.BWk : x < 4 ? p.BWv : p.BWq) + cb * 256;
  const u16 *pA1 = x < 2 ? p.Ax : p.Ac, *pB1 = (x < 2 ? p.Awq : x < 4 ? p.Awk : p.Awv) + cb * 256;
  asm volatile("" : "+s"(pA0), "+s"(pB0), "+s"(pA1), "+s"(pB1));
  int k2, vA, vA1, sa, sb, nvA;
#define PJ_K2(u) ((u) < 2 || x < 2 ? 1152 : 1024)
#define PJ_PREP(u) do { int ln_ = vD; asm volatile("" : "+v"(ln_)); \
    nvA = (wid * 8 + (ln_ >> 7)) * PJ_K2(u) + ((ln_ ^ (ln_ >> 3)) & 0x70); \
    asm volatile("" : "+v"(nvA)); } while (0)
#define PJ_UNIT(u) do { k2 = PJ_K2(u); rsA = RSRC((u) < 2 ? pA0 : pA1, 2048 * k2); rsB = RSRC((u) < 2 ? pB0 : pB1, k2 * 4096 - cb * 512); vA = nvA; vA1 = nvA + 64 * k2; \
    sa = ((j & 3) + ((u) & 1) * 4) * 256 * k2 - 128; sb = 0; } while (0)
#define PJ_DEST(u, rt) do { const int mi_ = ((u) >> 1) * 3 + (x >> 1), g_ = ((rt) + ((u) & 1) * 4) * 8; \
    dco = O_QA + (mi_ == 2 ? 4 : (mi_ + 5) & 7) * ACB + cb * 16384 + wc * 2048 + (g_ + wr * 2) * 262144; \
    dfl = SY_BLK(mi_, cb, g_) * 4; } while (0)
#define PJ_STAGE(dst, rs, v0, v1, so) do { const int so_ = (so); \
    __builtin_amdgcn_raw_ptr_buffer_load_lds(rs, (LDSP void*)(smem + (dst) + wid * 1024), 16, v0, so_, 0, 0); \
    __builtin_amdgcn_raw_ptr_buffer_load_lds(rs, (LDSP void*)(smem + (dst) + wid * 1024 + 8192), 16, v1, so_, 0, 0); } while (0)
#define PJ_SA(dst, so) PJ_STAGE(dst, rsA, vA, vA1, so)
#define PJ_SB(dst, so) PJ_STAGE(dst, rsB, vB, vB1, so)
#define PJ_LDA(off) do { _Pragma("unroll") for (int m = 0; m < 4; m++) _Pragma("unroll") for (int k = 0; k < 2; k++) \
    At[m][k] = *(const bf8*)(smem + (ao ^ (k * 64)) + (off) + m * 2048); } while (0)
#define PJ_TR(dst, a, off) asm volatile("ds_read_b64_tr_b16 %0, %1 offset:%2" : "=&v"(dst) : "v"(a), "i"(off) : "memory")
#define PJ_LDB(D, h) do { const int b1_ = bo ^ 32; _Pragma("unroll") for (int n = 0; n < 2; n++) _Pragma("unroll") for (int k = 0; k < 2; k++) { \
    bf4 lo_, hi_; PJ_TR(lo_, n ? b1_ : bo, (h) * 16384 + k * 8192); PJ_TR(hi_, n ? b1_ : bo, (h) * 16384 + k * 8192 + 1024); D[n][k] = cat8(lo_, hi_); } } while (0)
#define PJ_CV(ai, bj, q) ((u32x4){pk2(acc[ai][bj][(q) & 2][(q) & 1][0], acc[ai][bj][(q) & 2][(q) & 1][1]), pk2(acc[ai][bj][(q) & 2][(q) & 1][2], acc[ai][bj][(q) & 2][(q) & 1][3]), \
                                  pk2(acc[ai][bj][((q) & 2) + 1][(q) & 1][0], acc[ai][bj][((q) & 2) + 1][(q) & 1][1]), pk2(acc[ai][bj][((q) & 2) + 1][(q) & 1][2], acc[ai][bj][((q) & 2) + 1][(q) & 1][3])})
#define PJ_SW(v) pair16(v[0], v[1], v[2], v[3])
#define PJ_MMA(Z, ai, bj, D, F) do { PJ_SCHED; __builtin_amdgcn_s_setprio(1); \
    _Pragma("unroll") for (int k = 0; k < 2; k++) _Pragma("unroll") for (int m = 0; m < 4; m++) _Pragma("unroll") for (int n = 0; n < 2; n++) { \
      if (k == 0 && (Z)) asm volatile("v_mfma_f32_16x16x32_bf16 %0, %1, %2, 0" : "+v"(acc[ai][bj][m][n]) : "v"(D[n][k]), "v"(At[m][k])); \
      else asm volatile("v_mfma_f32_16x16x32_bf16 %0, %1, %2, %0" : "+v"(acc[ai][bj][m][n]) : "v"(D[n][k]), "v"(At[m][k])); \
      F(k * 8 + m * 2 + n); } \
    __builtin_amdgcn_s_setprio(0); if (PJ_PAD) __builtin_amdgcn_s_sleep(PJ_PAD); PJ_SCHED; } while (0)
#define PJ_SWI(a, b) asm volatile("v_permlane16_swap_b32 %0, %1" : "+v"(a), "+v"(b))
#define PJ_CVI(d, s, e) asm volatile("v_cvt_pk_bf16_f32 %0, %1, %2" : "=v"(d) : "v"(s[e]), "v"(s[(e) + 1]))
#define PJ_PACK(P, ai, bj, i) do { const int q_ = (i) >> 2; \
    if (((i) & 3) < 2) { PJ_CVI(P[q_][(i) & 1], acc[ai][bj][q_ & 2][q_ & 1], ((i) & 1) * 2); PJ_CVI(P[q_][2 + ((i) & 1)], acc[ai][bj][(q_ & 2) + 1][q_ & 1], ((i) & 1) * 2); } \
    else PJ_SWI(P[q_][(i) & 1], P[q_][2 + ((i) & 1)]); } while (0)
#define PJ_WV(n) asm volatile("s_waitcnt vmcnt(" #n ")" ::: "memory")
#define PJ_WL(n) asm volatile("s_waitcnt lgkmcnt(" #n ")" ::: "memory")
#define PJ_WL0 do { __builtin_amdgcn_s_waitcnt(0xC07F); asm volatile("" ::: "memory"); } while (0)
#define PJ_BAR __builtin_amdgcn_s_barrier()
#define PJ_SCHED __builtin_amdgcn_sched_barrier(0)
#define PJ_NOP do { PJ_SCHED; asm volatile("s_nop 7"); PJ_SCHED; } while (0)
#define PJ_DS(v, ai, bj, q) do { PJ_SCHED; __builtin_amdgcn_raw_buffer_store_b128(v, rc, vD + ((q) & 1) * 1024, dco + ((ai) * 4 + ((q) >> 1)) * 262144 + (bj) * 8192, 16); \
    asm volatile("s_nop 1"); PJ_SCHED; } while (0)
#define PJ_PARK(q) (*(u32x4*)(smem + C_PARK + wid * 4096 + vD + (q) * 1024))
#define PJ_PUB(ai, n) do { if (wid == 4) __builtin_amdgcn_raw_buffer_store_b32(FULL, rc, vD < (n) * 16 ? vD >> 2 : PJ_OOB, dfl + (ai) * 16, 16); } while (0)
#define PJ_ST()
#define PJ_TOUCH(ptr, bytes, vo, so) __builtin_amdgcn_raw_ptr_buffer_load_lds(RSRC(ptr, bytes), (LDSP void*)(smem + C_PARK + wid * 4096), 4, vo, so, 0, 0)
#define PJ_SL 0x1E
#define PJ_PT 0
  static_assert(XS0 >= 46 && PJ_SL == 0x1E && PJ_PT == 0, "");
  int emp = 128;
  u32 old = 0u;
#define PJ_WARM(k) do { \
    if (u == 0 && (j & 3) != 3) { \
      int vt_ = vD; asm volatile("" : "+v"(vt_)); vt_ <<= 3; \
      const int i_ = (((x & 1) * 8 + (j >> 2)) * 2 + (j & 1)) * 8 + wid + (k) * 256; \
      const int nw_ = x < 2 ? 576 : 512, na_ = x < 2 ? 288 : x < 4 ? 256 : 144, no_ = (x >> 1) * 96 + 96; \
      const u64 w_ = (u64)(pB1 - cb * 256), a_ = x < 4 ? (u64)pA1 : (u64)(pA0 + 1024 * 576), o_ = (u64)p.Awo + (x < 2 ? 0 : x < 4 ? 96 : 288) * 8192; \
      PJ_TOUCH(i_ < nw_ ? w_ : i_ < nw_ + na_ ? a_ : o_, i_ < nw_ + na_ ? WOB : no_ * 8192, vt_, (i_ < nw_ ? i_ : i_ < nw_ + na_ ? i_ - nw_ : i_ - nw_ - na_) * 8192); \
    } else { \
      const int e_ = emp + (u * 4 + (k)) * 3; \
      __builtin_amdgcn_raw_buffer_store_b128(ones4(), rc, vD, e_ < 128 ? O_OB + (e_ >> 1) * 262144 + cb * 16384 + (e_ & 1) * 8192 + wid * 1024 : PJ_OOB, 16); \
    } } while (0)
#define PJ_F0(i)
#define PJ_F1(i) do { if (S == 8) PJ_PACK(pa, 0, 0, i); } while (0)
#define PJ_F2(i) do { if (S == 8) PJ_PACK(pb, 0, 1, i); if (S == 0) PJ_PACK(pb, 1, 1, i); } while (0)
#define PJ_F3(i) do { if (S == 8) PJ_PACK(pa, 1, 0, i); if (S == 5 && (i) == 0) { PJ_SCHED; PJ_PREP(PJ_NEXT); PJ_SCHED; } } while (0)
#define PJ_STEP(SS) do { constexpr int S = SS; PJ_ST(); \
    PJ_LDB(B0, 0); PJ_SCHED; PJ_LDA(0); PJ_SA((bsel ^ 32768) + 16384, sa + 128 * k2); \
    PJ_WL(8); PJ_BAR; PJ_WL0; PJ_MMA(S == 0, 0, 0, B0, PJ_F0); PJ_BAR; PJ_SCHED; \
    if (S == 7) PJ_UNIT(PJ_NEXT); \
    PJ_LDB(B1, 1); PJ_SB(65536 + bsel, sb); \
    if (S == 0) { PJ_DS(pb[2], 0, 1, 2); PJ_DS(pb[3], 0, 1, 3); } \
    if ((PJ_SL >> S) & 1) PJ_WARM(PJ_PT + __builtin_popcount(PJ_SL & ((1 << S) - 1))); \
    PJ_BAR; PJ_WL0; PJ_MMA(S == 0, 0, 1, B1, PJ_F1); PJ_BAR; \
    PJ_LDA(16384); sa += 128; PJ_SA(bsel, sa); \
    if (S == 8) { PJ_SCHED; PJ_DEST(u, j & 3); _Pragma("unroll") for (int q = 0; q < 4; q++) PJ_PARK(q) = pa[q]; PJ_SCHED; } \
    PJ_BAR; PJ_WL0; PJ_MMA(S == 0, 1, 0, B0, PJ_F2); PJ_BAR; PJ_SCHED; \
    { u32x4 c0_, c1_; \
      if (S < 2) { c0_ = PJ_PARK(2 * S); c1_ = PJ_PARK(2 * S + 1); PJ_SCHED; } \
      PJ_SB(65536 + bsel + 16384, sb + 256); sb += 64 * 8192; \
      if (S == 8) { PJ_DS(pb[0], 0, 1, 0); PJ_DS(pb[1], 0, 1, 1); } \
      else if (S < 2) { PJ_DS(c0_, 0, 0, (2 * S) & 3); PJ_DS(c1_, 0, 0, (2 * S + 1) & 3); } \
      else if (S < 4) { PJ_DS(pa[(2 * S) & 3], 1, 0, (2 * S) & 3); PJ_DS(pa[(2 * S + 1) & 3], 1, 0, (2 * S + 1) & 3); } \
      else if (S < 6) { PJ_DS(pb[(2 * S) & 3], 1, 1, (2 * S) & 3); PJ_DS(pb[(2 * S + 1) & 3], 1, 1, (2 * S + 1) & 3); } } \
      \
    asm volatile("s_waitcnt vmcnt(%0)" :: "i"((S == 0 ? 10 : S < 6 || S == 8 ? 8 : 6) + ((PJ_SL >> S) & 1)) : "memory"); \
    PJ_BAR; \
    if (S == 2) PJ_PUB(0, 4); \
    if (S == 7) PJ_PUB(1, 4); \
    PJ_MMA(S == 0, 1, 1, B1, PJ_F3); PJ_BAR; \
    bsel ^= 32768; ao ^= 32768; bo ^= 32768; } while (0)
  f4 acc[2][2][4][2];
  bf8 At[4][2], B0[2][2], B1[2][2];
  u32x4 pa[4], pb[4];
  int bsel = 0, dco = PJ_OOB, dfl = PJ_OOB, u = 0;
  auto quick = [&](auto NBC, const int hf, const int ch) __attribute__((always_inline)) {
    constexpr int NB = decltype(NBC)::value, NS = 5 - NB;
#define PJ_FS(t) do { PJ_SA(((t) % NS) * 16384, hf * 128 * 1152 + (t) * 128); \
      _Pragma("unroll") for (int h_ = 0; h_ < NB; h_++) PJ_SB(NS * 16384 + (((t) % NS) * NB + h_) * 16384, (t) * 64 * 8192 + (ch + h_) * 256); PJ_SCHED; } while (0)
#pragma unroll
    for (int t = 0; t < NS - 1; t++) PJ_FS(t);
    bo += NS * 16384 - 65536;
#pragma unroll
    for (int t = 0; t < 9; t++) {
      PJ_ST();
      if (t == 8) PJ_WV(0); else if (NB == 2) PJ_WV(6); else if (t == 7) PJ_WV(4); else PJ_WV(8);
      PJ_BAR; PJ_SCHED;
      if (wr && t + NS - 1 < 9) PJ_FS(t + NS - 1);
      PJ_LDB(B0, 0); PJ_LDA((t % NS) * 16384);
      if (NB == 2) PJ_LDB(B1, 1);
      PJ_SCHED;
      bo += t == 8 ? 65536 - NS * 16384 - (8 % NS) * NB * 16384 : (t % NS == NS - 1 ? -(NS - 1) * NB * 16384 : NB * 16384);
      PJ_WL0;
      PJ_MMA(t == 0, 0, 0, B0, PJ_F0);
      if (NB == 2) PJ_MMA(t == 0, 0, 1, B1, PJ_F0);
      if (!wr && t + NS - 1 < 9) PJ_FS(t + NS - 1);
    }
    PJ_NOP;
    u32 b1;
    if (NB == 1 && job >= 0 && wid == 0) b1 = BEAT_LD(job);
#pragma unroll
    for (int q = 0; q < 4 * NB; q++) { const u32x4 v = PJ_CV(0, q >> 2, q & 3); PJ_DS(PJ_SW(v), hf, ch + (q >> 2), q & 3); }
    if (NB == 2 && tid == 0) { *(u32*)(smem + C_B0 + 4) = old; PJ_WL0; }
    PJ_WV(0); PJ_BAR;
    if (NB == 1 && job >= 0 && wid == 0) old = confirm(p, smem, job, b1, wv);
    if (tid < 4) __hip_atomic_fetch_or(WSP(u32, dfl) + hf * 4 + tid, NB == 2 ? FULL : 4u << ch, __ATOMIC_RELAXED, __HIP_MEMORY_SCOPE_AGENT);
  };
  if (((j + 1) & 3) < 2) {
    PJ_PREP(0); PJ_UNIT(0); PJ_DEST(0, 0);
    quick(std::integral_constant<int, 1>{}, 0, j & 1);
    if ((j & 3) == 0) {
      quick(std::integral_constant<int, 2>{}, 1, 0);
      emp = (__builtin_amdgcn_readfirstlane(*(const u32*)(smem + C_B0 + 4)) >> (job & 31)) & 1 ? 129 : XS0 * 2 - 12 + (x >> 1);
    }
    if ((j & 3) == 0 || PJ_ORD != 0x1312) u = 2;
    dco = PJ_OOB; dfl = PJ_OOB; job = -1;
  }
#pragma unroll
  for (int a = 0; a < 16; a++) asm volatile("" : "=v"(acc[a >> 3][(a >> 2) & 1][a & 3][0]), "=v"(acc[a >> 3][(a >> 2) & 1][a & 3][1]));
#pragma unroll
  for (int a = 0; a < 4; a++) asm volatile("" : "=v"(pa[a]), "=v"(pb[a]));
  PJ_PREP(u); PJ_UNIT(u);
  const int cs = x < 4 && (j & 3) == 2 ? (x * 8 + (j >> 2)) * 128 : -1;
#pragma unroll
  for (int i = 0; i < 2; i++) pa[i] = __builtin_amdgcn_raw_buffer_load_b128(RSRC(p.BWo, WOB), (tid >> 3) * 1152 + (tid & 7) * 16, cs >= 0 ? (cs + i * 64) * 1152 + 1024 : PJ_OOB, 0);
  PJ_SCHED;
  PJ_SB(65536, 0); PJ_SA(0, sa + 128);
  PJ_SB(65536 + 16384, 256); PJ_SA(16384, sa + 128 + 128 * k2);
  PJ_SCHED;
#pragma unroll
  for (int i = 0; i < 2; i++) __builtin_amdgcn_raw_buffer_store_b128(pa[i], rc, (tid & 7) * 65536 + (tid >> 3) * 16, cs >= 0 ? O_WX + (cs + i * 64) * 16 : PJ_OOB, 16);
  PJ_SCHED;
  if (wr == 1) PJ_BAR;
  PJ_WV(6); PJ_BAR;
  u32 b1;
  if (job >= 0 && wid == 0) b1 = BEAT_LD(job);
  PJ_SCHED;
  PJ_SB(65536 + 32768, 64 * 8192); PJ_SA(32768, sa + 256);
  PJ_SB(65536 + 32768 + 16384, 64 * 8192 + 256);
  PJ_WV(6);
  if (job >= 0 && wid == 0) confirm(p, smem, job, b1, wv);
  PJ_BAR;
  if (tid == 256 && cs >= 0) AOR(SY_WX, 1u << (cs >> 7));
#pragma unroll
  for (int i = 0; i < PJ_PT; i++) PJ_WARM(i);
  sa += 256; sb = 128 * 8192;
#pragma unroll 1
  for (;; u = PJ_NEXT) {
    PJ_STEP(0); PJ_STEP(1); PJ_STEP(2); PJ_STEP(3); PJ_STEP(4); PJ_STEP(5);
    int more = k2 - 1024; asm volatile("" : "+s"(more));
    if (more) PJ_STEP(6);
    PJ_STEP(7); PJ_STEP(8);
    if (u == 1) break;
  }
  PJ_ST();
  PJ_NOP;
  const u32 left = __builtin_amdgcn_raw_buffer_load_b32(rc, wid ? PJ_OOB : vD < 128 ? SY_JOB(0) * 4 + vD * 4 : SY_INIT(0) * 4, 0, 16);
#define FSL 0
#define FWN 15
#define PJ_FD(v, ai, bj, q) do { PJ_DS(v, ai, bj, q); if (FSL) __builtin_amdgcn_s_sleep(FSL); if (FWN < 15) asm volatile("s_waitcnt vmcnt(%0)" :: "i"(FWN) : "memory"); } while (0)
  PJ_FD(pb[2], 0, 1, 2); PJ_FD(pb[3], 0, 1, 3);
#pragma unroll
  for (int q = 0; q < 4; q++) pb[q] = PJ_PARK(q);
#pragma unroll
  for (int q = 0; q < 4; q++) PJ_FD(pa[q], 1, 0, q);
#pragma unroll
  for (int q = 0; q < 4; q++) PJ_FD(pb[q], 0, 0, q);
#pragma unroll
  for (int q = 0; q < 4; q++) { const u32x4 v = PJ_CV(1, 1, q); PJ_FD(PJ_SW(v), 1, 1, q); }
  pre = -1;
  const int xp = __builtin_amdgcn_readlane(left, 8);
  if (__all(left >= (vD < 128 ? ~0u : 1u)) && vD == 0) pre = (int)__hip_atomic_fetch_add(HOTP(xp) + SY_QHEAD, 1u, __ATOMIC_RELAXED, __HIP_MEMORY_SCOPE_AGENT) + (xp & 0x7000);
  PJ_WV(0);
  if (wr == 0) PJ_BAR;
  PJ_BAR;
  if (emp != 129) PJ_PUB(0, 8);
  PJ_ST();
}

namespace fa {
constexpr float SCALE = 0.08838834764831845f;
constexpr int NW = 8, QBLK = 32, KVBLK = 64, QB = NW * QBLK, D = 128;
constexpr int SHM_V = KVBLK * D * 2, SHM_K = KVBLK * D * 2;
#define SBAR() __builtin_amdgcn_sched_barrier(0)
#define MF32(a, b, c) __builtin_amdgcn_mfma_f32_32x32x16_bf16(a, b, c, 0, 0, 0)
#define FMX3(a, b, c) __builtin_elementwise_maximum(__builtin_elementwise_maximum(a, b), c)
#define AT_PAD 0
#define EBAR 14
#define MX0 3
#define ATHR 8.f
#define fadd1(a, b) ((a) + (b))
#define fmul1(a, b) ((a) * (b))
__device__ __forceinline__ void attn_block(const char* __restrict__ Qp, const char* __restrict__ Kh, const char* __restrict__ Vh,
                                           char* __restrict__ Op, int P0, char* lds, const int wv, u32* qh, int& pre) {
  const int tid = TIDX, wid = wv, lane = tid & 63, r32 = lane & 31, hi = lane >> 5, grp = wid >> 2;
  const int NT = (P0 + QB) / KVBLK;
  const int rb = wid ^ 4;
  const int TL = NT - 4 + (rb >> 1);
  const int rem = 2 * (NT - TL) - 2 - grp;
  float l_reg = 0.f;
  f32x16 o[4] = {}, s0, s1, nm = {};
  int ko = lane * 16;
  int vo = 4 * SHM_K + ((lane >> 4) & 1) * (1024 + 128) + ((lane >> 1) & 1) * 512 + ((hi ^ ((lane >> 1) & 1)) << 6) + ((lane >> 2) & 3) * 16 + (lane & 1) * 8;
  const __amdgpu_buffer_rsrc_t rk = RSRC(Kh, (2 * NT - 1) * 262144 + 8192), rv = RSRC(Vh, (2 * NT - 1) * 262144 + 8192);
  const int gk = tid * 16, gv = gk ^ (((tid >> 5) & 3) << 6);
#define DMA(rs, go, dst, so) __builtin_amdgcn_raw_ptr_buffer_load_lds(rs, (LDSP void*)(lds + (dst) + wid * 1024), 16, go, so, 0, 0)
#define ISSUE_K(t) do { const int s_ = (t) * 524288, d_ = ((t) & 3) * SHM_K; DMA(rk, gk, d_, s_); DMA(rk, gk, d_ + 8192, s_ + 262144); } while (0)
#define ISSUE_V(t) do { const int s_ = (t) * 524288, d_ = 4 * SHM_K + ((t) & 3) * SHM_V; DMA(rv, gv, d_, s_); DMA(rv, gv, d_ + 8192, s_ + 262144); } while (0)
#define PHASE(n) do { SBAR(); asm volatile("s_waitcnt vmcnt(" #n ")" ::: "memory"); __builtin_amdgcn_s_barrier(); asm volatile("" ::: "memory"); SBAR(); } while (0)
#define LDK(d0) do { kf[(d0) % 3][0] = *(const bf8*)(lds + ko + (d0) * 1024); kf[(d0) % 3][1] = *(const bf8*)(lds + ko + (d0) * 1024 + 8192); } while (0)
#define LDV(i) do { const int o_ = ((i) & 3) * 2048 + (((i) >> 2) & 1) * 256 + ((i) >> 3) * 8192; vf[(i) & 3] = cat8(trrd(lds + vo + o_), trrd(lds + vo1 + o_)); } while (0)
#define AST(i)
#define AP(i)
  AP(0);
#define QK(NEXT) do { _Pragma("unroll") for (int d0 = 0; d0 < 8; ++d0) { \
      if (d0 + 2 < 8) LDK(d0 + 2); else { NEXT(2 * (d0 - 6)); NEXT(2 * (d0 - 6) + 1); } \
      SBAR(); \
      if (d0) { s0 = MF32(kf[d0 % 3][0], qr[d0], s0); s1 = MF32(kf[d0 % 3][1], qr[d0], s1); } \
      else { asm volatile("v_mfma_f32_32x32x16_bf16 %0, %1, %2, %3" : "+v"(s0) : "v"(kf[0][0]), "v"(qr[0]), "v"(nm)); \
             asm volatile("v_mfma_f32_32x32x16_bf16 %0, %1, %2, %3" : "+v"(s1) : "v"(kf[0][1]), "v"(qr[0]), "v"(nm)); } \
      SBAR(); } } while (0)
#define NOV(i)
#define MAXS(i) do { \
      if ((i) == 0) { mx = FMX3(s0[0], s0[1], s0[2]); my = FMX3(s1[0], s1[1], s1[2]); } \
      else if ((i) < 7) { mx = FMX3(mx, s0[2 * (i) + 1], s0[2 * (i) + 2]); my = FMX3(my, s1[2 * (i) + 1], s1[2 * (i) + 2]); } \
      else { mx = FMX3(mx, s0[15], s1[15]); mx = __builtin_elementwise_maximum(mx, my); asm volatile("" : "+v"(mx)); } } while (0)
  float mx, my;
  constexpr float C2 = 1.4426950408889634f * SCALE;
  bf8 qr[8], kf[3][2], vf[4], pa[4];
#pragma unroll
  for (int d0 = 0; d0 < 8; ++d0) qr[d0] = *(const bf8*)(Qp + rb * 262144 + d0 * 1024 + lane * 16);
  SBAR();
  ISSUE_K(0); ISSUE_K(1); ISSUE_V(0); ISSUE_K(2); ISSUE_V(1);
  SBAR();
  asm volatile("" : "=v"(s0), "=v"(s1), "+v"(nm), "+v"(o[0]), "+v"(o[1]), "+v"(o[2]), "+v"(o[3]));
  SBAR();
#pragma unroll
  for (int d0 = 0; d0 < 8; ++d0) {
    u32x4 q = __builtin_bit_cast(u32x4, qr[d0]);
#pragma unroll
    for (int i = 0; i < 4; i++) q[i] = pk2(__uint_as_float(q[i] << 16) * C2, __uint_as_float(q[i] & 0xFFFF0000u) * C2);
    qr[d0] = __builtin_bit_cast(bf8, q);
    asm volatile("" : "+v"(qr[d0]));
  }
  AP(8);
  PHASE(8);
  AP(1);
  if (grp) PHASE(6);
  LDK(0); LDK(1);
  QK(NOV);
#pragma unroll
  for (int i = 0; i < 8; ++i) MAXS(i);
  AP(2);
  PHASE(4);
  AP(3);
  auto tile = [&](const int t, auto LASTC) __attribute__((always_inline)) {
    constexpr bool LAST = decltype(LASTC)::value;
    AST(0);
    ISSUE_K(t + 3); ISSUE_V(t + 2);
    SBAR();
    if constexpr (LAST) {
      const int dq = r32 - 4 * hi;
      if (rb & 1) {
#pragma unroll
        for (int r = 0; r < 16; ++r) { if (dq < (r & 3) + 8 * (r >> 2)) s1[r] = -__builtin_inff(); SBAR(); }
      } else {
#pragma unroll
        for (int r = 0; r < 16; ++r) { if (dq < (r & 3) + 8 * (r >> 2)) s0[r] = -__builtin_inff(); s1[r] = -__builtin_inff(); SBAR(); }
      }
#pragma unroll
      for (int i = 0; i < 8; ++i) MAXS(i);
    }
    {
      if (__builtin_expect(t == 0 || __any(mx > ATHR), 0)) {
        auto rr = __builtin_amdgcn_permlane32_swap(__float_as_uint(mx), __float_as_uint(mx), false, false);
        mx = __builtin_elementwise_maximum(__uint_as_float(rr[0]), __uint_as_float(rr[1]));
        if (t) mx = __builtin_elementwise_maximum(mx, 0.f);
        const float alpha = t ? __builtin_amdgcn_exp2f(-mx) : 1.f, nn = nm[0] - mx;
        mx = -mx;
#pragma unroll
        for (int r = 0; r < 16; ++r) { s0[r] = fadd1(s0[r], mx); s1[r] = fadd1(s1[r], mx); nm[r] = nn; }
#pragma unroll
        for (int d = 0; d < 4; ++d)
#pragma unroll
          for (int r = 0; r < 16; ++r) o[d][r] = fmul1(o[d][r], alpha);
        l_reg *= alpha;
        asm volatile("" : "+v"(nm));
      }
    }
    AST(1);
    float x[32], sb = 0.f;
    u32 pw[16];
#pragma unroll
    for (int r = 0; r < 18; ++r) {
      SBAR();
      if (r < 16) { x[2 * r] = __builtin_amdgcn_exp2f(r < 8 ? s0[2 * r] : s1[2 * r - 16]); x[2 * r + 1] = __builtin_amdgcn_exp2f(r < 8 ? s0[2 * r + 1] : s1[2 * r - 15]); }
      if (r >= 2) {
        l_reg = fadd1(l_reg, x[2 * r - 4]);
        sb = r == 2 ? x[1] : fadd1(sb, x[2 * r - 3]);
        pw[r - 2] = pk2(x[2 * r - 4], x[2 * r - 3]); }
    }
    SBAR();
    l_reg = fadd1(l_reg, sb);
#pragma unroll
    for (int i = 0; i < 4; ++i) pa[i] = __builtin_bit_cast(bf8, (u32x4){pw[4 * i], pw[4 * i + 1], pw[4 * i + 2], pw[4 * i + 3]});
    asm volatile("" : "+v"(pa[0]), "+v"(pa[1]), "+v"(pa[2]), "+v"(pa[3]), "+v"(l_reg));
    ko ^= (t & 1) ? 3 * SHM_K : SHM_K;
    const int vo1 = vo ^ 128;
    SBAR();
    AST(3);
    if constexpr (!LAST) { LDK(0); LDK(1); } else { LDV(0); LDV(1); LDV(2); LDV(3); }
    AST(4);
    PHASE(4);
    AST(5);
    __builtin_amdgcn_s_setprio(1);
    if constexpr (!LAST) QK(LDV);
    AST(6);
#pragma unroll
    for (int i = 0; i < 16; ++i) {
      SBAR();
      o[i & 3] = MF32(vf[i & 3], pa[i >> 2], o[i & 3]);
      SBAR();
      if (i + 4 < 16) LDV(i + 4);
      if constexpr (!LAST) if (i >= MX0 && i < MX0 + 8) MAXS(i - MX0);
      if (AT_PAD && i == EBAR) __builtin_amdgcn_s_sleep(AT_PAD);
      if (i == EBAR && (!LAST || rem)) PHASE(4);
    }
    __builtin_amdgcn_s_setprio(0);
    vo ^= (t & 1) ? 3 * SHM_V : SHM_V;
    AST(7);
  };
#pragma unroll 1
  for (int t = 0; t < TL; t++) {
    tile(t, std::false_type{});
  }
  AP(4);
  tile(TL, std::true_type{});
  AP(5);
  ISSUE_V(TL + 3);
  SBAR();
  {
    const float inv = __builtin_amdgcn_rcpf(swadd(l_reg, l_reg));
    const __amdgpu_buffer_rsrc_t ro = RSRC(Op + rb * 262144, 8192);
#pragma unroll
    for (int i = 0; i < 8; ++i) {
      const int g = (i & 1) * 8;
#define ON(k) fmul1(o[i >> 1][g + (k)], inv)
      auto r0 = __builtin_amdgcn_permlane32_swap(pk2(ON(0), ON(1)), pk2(ON(4), ON(5)), false, false);
      auto r1 = __builtin_amdgcn_permlane32_swap(pk2(ON(2), ON(3)), pk2(ON(6), ON(7)), false, false);
#undef ON
      __builtin_amdgcn_raw_buffer_store_b128((u32x4){r0[0], r1[0], r0[1], r1[1]}, ro, r32 * 32 + hi * 16 + (i & 3) * 1024, (i >> 2) * 4096, 16);
      if (i == 3 && rem > 1) PHASE(6);
    }
  }
  AP(6);
  if (rem > 2) PHASE(10);
  if (rem > 3) { PHASE(8); PHASE(8); }
  CLAIM();
  VMW0();
  AP(7);
  __syncthreads();
}
#undef DMA
#undef ISSUE_K
#undef ISSUE_V
#undef PHASE
#undef LDK
#undef LDV
#undef QK
#undef NOV
#undef MAXS
}

__device__ __forceinline__ void wof_task(const Params& p, int kc, char* smem, const int wv, u32* qh, int& pre) {
  const int tid = TIDX, lane = tid & 63, wid = wv;
  const int l15 = lane & 15, lq = lane >> 4, q4 = l15 >> 2, p4 = lane & 3;
  const __amdgpu_buffer_rsrc_t rw = RSRC(WSP(u16, O_WOF), 2 * WOB);
  bf8 st[2][9];
#pragma unroll
  for (int m = 0; m < 2; m++)
#pragma unroll
    for (int i = 0; i < 9; i++) st[m][i] = *(const bf8*)((m ? p.BWo : p.Awo) + (size_t)kc * 64 * 576 + (tid + 512 * i) * 8);
  CLAIM();
#pragma unroll
  for (int m = 0; m < 2; m++) {
#pragma unroll
    for (int i = 0; i < 9; i++) {
      const int id = tid + 512 * i, row = id / 72, ch = id % 72;
      *(bf8*)(smem + row * 1280 + (((ch >> 1) ^ fsw(row)) << 5) + ((ch & 1) << 4)) = st[m][i];
    }
    asm volatile("s_waitcnt lgkmcnt(0)" ::: "memory"); __builtin_amdgcn_s_barrier(); asm volatile("" ::: "memory");
#pragma unroll
    for (int i = 0; i < 9; i++) {
      const int f = wid * 9 + i, nt = f >> 1, k2 = f & 1;
      const int r0 = k2 * 32 + lq * 8 + q4, r1 = r0 + 4;
      bf4 lo = trrd(smem + r0 * 1280 + ((nt ^ fsw(r0)) << 5) + p4 * 8);
      bf4 hi = trrd(smem + r1 * 1280 + ((nt ^ fsw(r1)) << 5) + p4 * 8);
      __builtin_amdgcn_raw_buffer_store_b128(__builtin_bit_cast(u32x4, cat8(lo, hi)), rw, ((nt * 128 + kc * 2 + k2) * 64 + lane) * 16, m * WOB, 16);
    }
    if (m == 0) { asm volatile("s_waitcnt lgkmcnt(0)" ::: "memory"); __builtin_amdgcn_s_barrier(); asm volatile("" ::: "memory"); }
  }
  VMW0();
  __syncthreads();
}

template <int MT>
__device__ __forceinline__ void out_tile(const char* A, const char* W, u16* C, char* smem, const int wv, u32* qh, int& pre) {
  constexpr int U = 32 / MT, SC = U / 2, DP = MT / 4, NCH = 4 * MT, CW = 32768 / MT;
  const int tid = TIDX, lane = tid & 63, wid = wv;
  const int l15 = lane & 15, lq = lane >> 4, kh = wid >> 2, wc = wid & 3;
  const __amdgpu_buffer_rsrc_t ra = RSRC(A, MT / 2 * 262144), rw = RSRC(W + wc * 3 * 131072 + kh * 1024, 3 * 131072 - kh * 1024);
  const int va = tid * 16, vw = lane * 16;
  int ao = kh * 2048 + (lq >> 1) * 1024 + l15 * 32 + (lq & 1) * 16;
  f4 acc[MT][3];
  bf8 wf[U][3], af[2][MT];
#define OT_DMA(i, buf, so) __builtin_amdgcn_raw_ptr_buffer_load_lds(ra, (LDSP void*)(smem + (buf) + (i) * 8192 + wid * 1024), 16, va, \
                                                                    (so) + (MT == 4 ? ((i) >> 1) * 262144 + ((i) & 1) * 8192 : (i) * 262144), 0, 0)
#define OT_LDA(d, base, off) do { _Pragma("unroll") for (int m = 0; m < MT; m++) af[d][m] = *(const bf8*)(smem + (base) + (off) + (m >> 1) * 2 * CW + (m & 1) * 512); } while (0)
#define OT_VM(n) __builtin_amdgcn_s_waitcnt(0x0F70 | ((n) & 15) | (((n) >> 4) << 14))
#define OT_PAIR(buf, so) do { _Pragma("unroll") for (int i = 0; i < 8; i++) OT_DMA(i & 3, (buf) + (i >> 2) * 32768, (so) + (i >> 2) * 2 * CW); SB0(); } while (0)
  if constexpr (MT == 4) {
  OT_PAIR(0, 0);
#pragma unroll
  for (int u = 0; u < U; u++) {
    if (u == U - 1) OT_PAIR(65536, 4 * CW);
#pragma unroll
    for (int j = 0; j < 3; j++) wf[u][j] = __builtin_bit_cast(bf8, __builtin_amdgcn_raw_buffer_load_b128(rw, vw, u * 2048 + j * 131072, 0));
    SB0();
  }
#pragma unroll
  for (int m = 0; m < MT; m++)
#pragma unroll
    for (int j = 0; j < 3; j++) acc[m][j] = (f4){0, 0, 0, 0};
  asm volatile("s_waitcnt vmcnt(%0)" :: "i"(U * 3 + 8) : "memory"); __builtin_amdgcn_s_barrier(); asm volatile("" ::: "memory");
  OT_LDA(0, ao, 0);
  int bs = 0;
#pragma unroll 1
  for (int c = 0; c < NCH; c += 2) {
    const int sw = c < NCH - 2 ? (c + 2) * CW : PJ_OOB, sd = c < NCH - 4 ? (c + 4) * 2 * CW : PJ_OOB;
#pragma unroll
    for (int u = 0; u < U; u++) {
      SB0();
      if (u < U - 1) OT_VM(3 * (U - 1) + 8); else OT_VM(3 * (U - 1));
      if (u == U - 1) {
        asm volatile("s_waitcnt lgkmcnt(0)" ::: "memory"); __builtin_amdgcn_s_barrier(); asm volatile("" ::: "memory");
        ao ^= 65536;
      }
      OT_LDA((u + 1) & 1, ao, u == U - 1 ? 0 : ((u + 1) / SC) * 32768 + ((u + 1) % SC) * 4096);
      SB0();
      if (u == U - 1) OT_PAIR(bs, sd);
#pragma unroll
      for (int j = 0; j < 3; j++) {
#pragma unroll
        for (int m = 0; m < MT; m++) acc[m][j] = MFMA(wf[u][j], af[u & 1][m], acc[m][j]);
        SB0();
        wf[u][j] = __builtin_bit_cast(bf8, __builtin_amdgcn_raw_buffer_load_b128(rw, vw, sw + u * 2048 + j * 131072, 0));
        SB0();
      }
    }
    bs ^= 65536;
  }
  } else {
#pragma unroll
  for (int i = 0; i < 4; i++) OT_DMA(i, 0, 0);
  SB0();
#pragma unroll
  for (int u = 0; u < U; u++) {
#pragma unroll
    for (int d = 0; d < DP; d++) OT_DMA((u % SC) * DP + d, 32768 + (u / SC) * 32768, (1 + u / SC) * 2 * CW);
#pragma unroll
    for (int j = 0; j < 3; j++) wf[u][j] = __builtin_bit_cast(bf8, __builtin_amdgcn_raw_buffer_load_b128(rw, vw, u * 2048 + j * 131072, 0));
    SB0();
  }
#pragma unroll
  for (int m = 0; m < MT; m++)
#pragma unroll
    for (int j = 0; j < 3; j++) acc[m][j] = (f4){0, 0, 0, 0};
  asm volatile("s_waitcnt vmcnt(%0)" :: "i"(U * (DP + 3)) : "memory"); __builtin_amdgcn_s_barrier(); asm volatile("" ::: "memory");
  OT_LDA(0, ao, 0);
  int bs = 0;
#pragma unroll 1
  for (int c = 0; c < NCH; c += 2) {
    const int sw = c < NCH - 2 ? (c + 2) * CW : PJ_OOB;
#pragma unroll
    for (int u = 0; u < U; u++) {
      SB0();
      __builtin_amdgcn_s_waitcnt(MT == 4 ? 0x4F7C : 0x0F7F);
      if (u % SC == SC - 1) { asm volatile("s_waitcnt lgkmcnt(0)" ::: "memory"); __builtin_amdgcn_s_barrier(); asm volatile("" ::: "memory"); }
      if (u == U - 1) ao ^= 65536;
      OT_LDA((u + 1) & 1, ao, u == U - 1 ? 0 : ((u + 1) / SC) * 32768 + ((u + 1) % SC) * 4096);
      SB0();
      const int cn = c + 3 + u / SC;
#pragma unroll
      for (int j = 0; j < 3; j++) {
#pragma unroll
        for (int m = 0; m < MT; m++) {
          acc[m][j] = MFMA(wf[u][j], af[u & 1][m], acc[m][j]);
          if (j == 0 && (m & 3) == 1) { SB0(); OT_DMA((u % SC) * DP + (m >> 2), u < SC ? (bs ^ 65536) + 32768 : bs, cn < NCH ? cn * 2 * CW : PJ_OOB); SB0(); }
        }
        SB0();
        wf[u][j] = __builtin_bit_cast(bf8, __builtin_amdgcn_raw_buffer_load_b128(rw, vw, sw + u * 2048 + j * 131072, 0));
        SB0();
      }
    }
    bs ^= 65536;
  }
  }
  CLAIM();
  VMW0C();
  asm volatile("s_waitcnt lgkmcnt(0)" ::: "memory"); __builtin_amdgcn_s_barrier(); asm volatile("" ::: "memory");
#pragma unroll
  for (int m = 0; m < MT / 2; m++)
#pragma unroll
    for (int j = 0; j < 3; j++) *(f4*)(smem + (m * 3 + j) * 8192 + va) = kh ? acc[m][j] : acc[MT / 2 + m][j];
  __syncthreads();
#pragma unroll
  for (int m = 0; m < MT / 2; m++) {
    f4 t[3];
#pragma unroll
    for (int j = 0; j < 3; j++) { const f4 o = *(const f4*)(smem + (m * 3 + j) * 8192 + (va ^ 4096)); t[j] = kh ? o + acc[MT / 2 + m][j] : acc[m][j] + o; }
    u16* c = C + (size_t)(kh * MT * 8 + m * 16 + l15) * 1152 + wc * 48;
    *(u32x4*)(c + (lq & 1) * 16 + (lq >> 1) * 8) = pair16(pk2(t[0][0], t[0][1]), pk2(t[0][2], t[0][3]), pk2(t[1][0], t[1][1]), pk2(t[1][2], t[1][3]));
    *(u32x2*)(c + 32 + lq * 4) = (u32x2){pk2(t[2][0], t[2][1]), pk2(t[2][2], t[2][3])};
  }
  __syncthreads();
#undef OT_DMA
#undef OT_LDA
}

#define CLAIMS() do { const int l_ = TIDX; if (l_ < 64) { \
    const u32 v_ = ld4_sc1(hot + SY_OQ(l_ < 6 ? l_ : 0)); \
    if (l_ == 0) pre = (int)__hip_atomic_fetch_add(hot + SY_OQ(6), 1u, __ATOMIC_RELAXED, __HIP_MEMORY_SCOPE_AGENT); \
    pre |= (int)((u32)__ballot(l_ < 6 && v_ < ((l_ & 1) ? NOB : NOA)) << 16); } } while (0)
#define CLS 51
#define KDUE 112
#define KDL 84
#define TPS 2
__device__ __forceinline__ void small_out_task(const Params& p, int rq, int cp, char* smem, const int wv, u32* hot, int& pre) {
  const int tid = TIDX, lane = tid & 63, wid = wv;
  const int l15 = lane & 15, lq = lane >> 4;
  const __amdgpu_buffer_rsrc_t rw = RSRC(WSP(char, O_WOF) + WOB, WOB), ro = RSRC(WSP(u16, O_OB), 2048 * 4096 * 2);
  if (tid == 0) {
    int it = 0;
    while (ld4_sc1(hot + SY_CW) < 64u && ++it < SPIN_CAP) __builtin_amdgcn_s_sleep(8);
  }
  __syncthreads();
  bf8 wf[8][4];
#pragma unroll
  for (int k = 0; k < 8; k++)
#pragma unroll
    for (int j = 0; j < 4; j++)
      wf[k][j] = __builtin_bit_cast(bf8, __builtin_amdgcn_raw_buffer_load_b128(rw, (l15 >> 3) * 1024 + (lq * 16 + (l15 & 7)) * 16 + (j & 1) * 128,
                                                                              ((cp * 2 + (j >> 1)) * 128 + wid * 16 + k * 2) * 1024, 16));
  const int va = OB_OFF(rq * 8 + (l15 & 7), wid * 512 + (l15 >> 3) * 32 + lq * 8);
  const int wo = (lq >> 1) == (l15 >> 3) ? (wid * 2 + (lq >> 1)) * 272 + (l15 & 7) * 32 + (lq & 1) * 16 : 4 * 4352 + lane * 16;
  const int rd = (lane >> 4) * 4352 + (lane & 7) * 544 + wid * 32 + ((lane >> 3) & 1) * 16;
  u32 left = 0u, due = 0u, last = 0u, per = 240u * 64u;
  bool have = false;
  auto step = [&](u32x4 (&af)[8], u32x4 (&nf)[8], const int s) __attribute__((always_inline)) {
    if (due == 0u) {
      if (tid < 64) {
        u32 v;
        int it = 0;
        while (v = ld4_sc1(CSP(s - 2 - (lane & 1))), __any(v == 0u) && ++it < SPIN_CAP) __builtin_amdgcn_s_sleep(SPS);
        *(u32*)(smem + 40960 + (lane & 1) * 4) = v;
      }
      asm volatile("s_waitcnt lgkmcnt(0)" ::: "memory"); __builtin_amdgcn_s_barrier(); asm volatile("" ::: "memory");
      due = __builtin_amdgcn_readfirstlane(*(const u32*)(smem + 40960));
      last = __builtin_amdgcn_readfirstlane(*(const u32*)(smem + 40964));
    }
    per = umin(per, due - last);
    last = due;
    int n = 400, late;
    while (late = (int)(((u32)wall_clock64() << 6) - due - per) - (s == 63 ? KDL : KDUE) * 64, late < 0 && --n > 0) {}
    if (s == 63) TLS(1);
    const u32 v = __builtin_amdgcn_raw_buffer_load_b32(RSRC(CSP(0), wid ? 0 : 4096), 0, (s - 1) * 64, 16);
    if (s == CLS) left = __builtin_amdgcn_raw_buffer_load_b32(RSRC(hot, wid ? 0 : 4096), SY_OQ(lane < 6 ? lane : 6) * 4, 0, 16);
    SB0();
    auto rows = [&](u32x4 (&f)[8], const int so) __attribute__((always_inline)) {
#pragma unroll
      for (int k = 0; k < 8; k++) f[k] = __builtin_amdgcn_raw_buffer_load_b128(ro, va, so + k * 4096, 16);
      SB0();
    };
    auto bad = [&]() __attribute__((always_inline)) {
      u32 t = 0u;
#pragma unroll
      for (int k = 0; k < 8; k++) t = umax(umax(t, af[k][0]), af[k][2]);
      return __any(t == ~0u);
    };
    if (!have) rows(af, s * 262144);
    have = late >= (int)per && s < 63;
    rows(nf, have ? (s + 1) * 262144 : PJ_OOB);
    if (wid == 0) *(u32*)(smem + 40968 + (s & 1) * 4) = v;
    if (__builtin_expect(bad(), 0)) {
      int it = 0;
      do { __builtin_amdgcn_s_sleep(TPS); CFENCE(); rows(af, s * 262144); } while (bad() && ++it < SPIN_CAP);
    }
    SB0();
    f4 acc[4];
#pragma unroll
    for (int k = 0; k < 8; k++)
#pragma unroll
      for (int j = 0; j < 4; j++) acc[j] = MFMA(wf[k][j], __builtin_bit_cast(bf8, af[k]), k ? acc[j] : ((f4){0, 0, 0, 0}));
    char* const sm = smem + (s & 1) * 20480;
#pragma unroll
    for (int j = 0; j < 4; j++) *(f4*)(sm + wo + ((lq >> 1) == (l15 >> 3) ? j * 4352 : 0)) = acc[j];
    asm volatile("s_waitcnt lgkmcnt(0)" ::: "memory"); __builtin_amdgcn_s_barrier(); asm volatile("" ::: "memory");
    if (s == 63) TLS(2);
    due = __builtin_amdgcn_readfirstlane(*(const u32*)(smem + 40968 + (s & 1) * 4));
    f4 t = *(const f4*)(sm + rd) + *(const f4*)(sm + rd + 272);
#pragma unroll
    for (int r = 0; r < 4; r++) {
      t[r] += __uint_as_float(__builtin_amdgcn_mov_dpp(__float_as_uint(t[r]), 0xB1, 0xf, 0xf, true));
      t[r] += __uint_as_float(__builtin_amdgcn_mov_dpp(__float_as_uint(t[r]), 0x4E, 0xf, 0xf, true));
      t[r] += __uint_as_float(__builtin_amdgcn_mov_dpp(__float_as_uint(t[r]), 0x141, 0xf, 0xf, true));
    }
    __builtin_amdgcn_raw_buffer_store_b64((u32x2){pk2(t[0], t[1]), pk2(t[2], t[3])}, RSRC(p.out, 2048 * 2304), (lane & 7) == 0 ? lane : PJ_OOB,
                                          (s * 32 + rq * 8 + wid) * 2304 + 1152 + cp * 64, 0);
  };
  u32x4 fa[8], fb[8];
#pragma unroll
  for (int k = 0; k < 8; k++) asm volatile("" : "=v"(fa[k]));
#pragma unroll 1
  for (int s = XS0; s < 64; s += 2) { step(fa, fb, s); step(fb, fa, s + 1); }
  pre = 72;
  if (__any(left < (lane > 5 ? 72u : (lane & 1) ? NOB : NOA))) CLAIMS();
}
#define PSTAMP()
#define STAMP(i)
#define YEND()
#define ANS 16
#define ANX(slot) (X_ANN ^ xp ^ ((slot) * X_HS))
#define KSL 1
#define DSH 2
#define K1 13
#define K3 8
#define DRL 0
#define CSL 5
#define CSC 0
#define KA 0
#define KB 0
#define KS0 0
#define KMASK 0x00FFFE000FE00000ull
#define KXT(k) ((k) ? (int)((KMASK >> s) & 1) * (k) : 0)
#define DOMAX 90
#define DO0 80
#define DH0 54
#define DOCAP 6
#define C_IMG 0
#define C_QT 9216
#define C_KT (C_QT + 3072)
#define C_P (C_KT + 4096)
#define C_PX (C_P + 768)
#define C_KX (C_PX + 2560)
#define C_VX (C_KX + 8192)
#define C_QX (C_VX + 8192)
#define C_XP 161024
#define OBE 0
#define CALR 2
#define GPS 6
#define C_WL 49920
__device__ __forceinline__ float ror8(float v) {
  return __uint_as_float(__builtin_amdgcn_mov_dpp(__float_as_uint(v), 0x128, 0xf, 0xf, true));
}
__device__ __forceinline__ float rb1(float f) { return __uint_as_float(pk2(0.f, f)); }
#define FMX(a, b) __builtin_elementwise_maximum(a, b)
__device__ __forceinline__ float tmax(float mx) {
  mx = FMX(mx, ror8(mx));
  { auto t = __builtin_amdgcn_permlane16_swap(__float_as_uint(mx), __float_as_uint(mx), false, false);
    mx = FMX(__uint_as_float(t[0]), __uint_as_float(t[1])); }
  { auto t = __builtin_amdgcn_permlane32_swap(__float_as_uint(mx), __float_as_uint(mx), false, false);
    mx = FMX(__uint_as_float(t[0]), __uint_as_float(t[1])); }
  return mx;
}
__device__ __forceinline__ int hmax(int x) {
  x = max(x, __builtin_amdgcn_mov_dpp(x, 0x128, 0xf, 0xf, true));
  x = max(x, __builtin_amdgcn_mov_dpp(x, 0x124, 0xf, 0xf, true));
  x = max(x, __builtin_amdgcn_mov_dpp(x, 0x4E, 0xf, 0xf, true));
  x = max(x, __builtin_amdgcn_mov_dpp(x, 0xB1, 0xf, 0xf, true));
  { auto t = __builtin_amdgcn_permlane16_swap((u32)x, (u32)x, false, false); x = max((int)t[0], (int)t[1]); }
  return x;
}
#define BAR() do { asm volatile("s_waitcnt lgkmcnt(0)" ::: "memory"); __builtin_amdgcn_s_barrier(); asm volatile("" ::: "memory"); } while (0)
#define BARC() do { asm volatile("" ::: "memory"); __builtin_amdgcn_s_waitcnt(0xC07F); __builtin_amdgcn_s_barrier(); asm volatile("" ::: "memory"); } while (0)
#define BARN() do { asm volatile("" ::: "memory"); __builtin_amdgcn_s_barrier(); asm volatile("" ::: "memory"); } while (0)
#define IDX() const int tid = TIDX, lane = tid & 63, l15 = lane & 15, lq = lane >> 4, q4 = l15 >> 2, p4 = lane & 3, hi8 = l15 >> 3; \
    (void)q4; (void)p4; (void)hi8; (void)lq
#define AD_LD(HEAD) __builtin_amdgcn_raw_buffer_load_b32(rsX, (TIDX & (HEAD ? 63 : 31)) * ANS, ANX(s & 1), 16)
#define AD_GET(HEAD, K, CAP) do { int n_ = 0; u32 an = AD_LD(HEAD); \
    while ((u32)__ballot((an & 63u) != (u32)s) != 0u && ++n_ < (CAP)) { __builtin_amdgcn_s_sleep(1); CFENCE(); an = AD_LD(HEAD); } \
    const u32 r_ = (u32)__builtin_amdgcn_readfirstlane((int)an); \
    const int m_ = hmax((TIDX & 32) ? (int)umin(an, DOMAX * 64u) : (int)(an - r_)); \
    dl = ((r_ + (u32)__builtin_amdgcn_readlane(m_, 0)) & ~63u) + (u32)(K) * 64u + (HEAD ? (u32)__builtin_amdgcn_readlane(m_, 32) : 0u); \
    if (!HEAD && !CSL && c == CSC && s > 0 && n_ < (CAP)) __builtin_amdgcn_raw_buffer_store_b32(dl | 1u, rsX, 0, (X_HOT(xp) ^ X_CS) + (s - 1) * 64, 16); } while (0)
#define AD_SPIN() do { int n_ = 160; while ((int)(((u32)wall_clock64() << 6) - dl) < 0 && --n_ > 0) {} } while (0)
#define STG_AUX 0
#define OSTG_AUX 0
#define STG_D 2
#define STG_POLL 7
#define STS(c, i)

#define NXA 1
#define NXE 0
#define ADE 1
#define NXH 1
#define NXV 1
#define NXB 4
#define NXS 3
#define PKA 18
#define PK_(w0, d, a, b, w1) ".long " #w0 " + " #d ", " w1 " + " #a " + " #b " * 512\n\t"
#define PKADD(d, a, b) PK_(0xD3B24000, d, a, b, "0x18020100")
#define PKMUL(d, a, b) PK_(0xD3B14000, d, a, b, "0x18020100")
#define PKFMS(d, a, b, c) PK_(0xD3B04400, d, a, b, "0x9C020100 + " #c " * 262144")
#define CV2(d, a) "v_cvt_pk_bf16_f32 " d ", v[" #a "], v[" #a "+1]\n\t"
#define VBP 0xA9A
#define C_Z 45824
#define SMALL 0x1p-64f
#define EK 0
#define EKN 2
#define EKL 1
#define EKA 1
#define BJ 2
#define KSP 0
#define SP1 2
#define SP2 0
#define KP 3
#define GX 1
#define GXO (GX == 1 ? 4096 : 1024)
#define ZE 0x0D
#define KD 5
#define QSL 0
#define DJ 9
#define VSL 0
#define VSL1 4
#define KV0 1
#define PVP 0
#define IPR 3
#define DSLOT 0x9876E1CBAD0ull
#define F_K (47360 - C_KT)
#define F_Q (47872 - 768 - C_QT)
#define HAL 2
#define OAL 2
#define ALIGN(n) asm volatile(".p2align %0" :: "i"(n))
#define QXP 4
#define RORD 0
#define PRIO(n) __builtin_amdgcn_s_setprio(n)
#define LSL 6
__device__ __forceinline__ int chain_head(const Params& p, char* smem, const int h, const int wv, const bool spec) {
  STS(h, 1);
  TLS(12);
  const int wid = wv;
  u32x8 w[2][9], wx[3];
  const __amdgpu_buffer_rsrc_t rsX = RSRC(XWIN(p), X_END);
  const __amdgpu_buffer_rsrc_t rsB = RSRC(WSP(u16, O_QB), 4 * ACB);
  const int cb = h >> 1;
  const int nvt = (wid & 3) == 0 ? 0 : (wid < 3 ? 2 : 1);
  const int vt = wid < 3 ? wid * 2 + 2 : (wid == 3 ? 0 : wid - 4);
  const int wl = C_WL + (nvt == 1 ? vt * 12288 : 49152 + (nvt == 2 ? wid - 1 : wid >> 2) * 30720);
  int xp;
  {
    IDX();
    u32x4 st[54], nk, nv, nq;
    u32 fl, fk;
    const int vw = ((tid >> 4) * 4096 + h * 128 + (tid & 15) * 8) * 2;
#define NTL 64
#define AH 1
    u32 tc[9], b1, old = 0u;
#define TEC 0
    const u16 *pq = p.BWq, *pk = p.BWk, *pv = p.BWv;
    asm volatile("" : "+s"(pq), "+s"(pk), "+s"(pv));
    const bool te = wid < 3 && (h & 3) == TEC;
    const int tm = te ? wid : (h & 3) - 1;
    const u64 pt = tm == 1 ? (u64)pk : tm == 2 ? (u64)pv : (u64)pq;
    const __amdgpu_buffer_rsrc_t rt = RSRC(pt, te || (tm && h != 31 && wid == 7) ? 576 * 4096 * 2 : 0);
    const int tv = lane < NTL ? lane * 8192 : PJ_OOB, ts_ = te ? 8064 : (h >> 2) * 1024 + 384;
    const __amdgpu_buffer_rsrc_t rq = RSRC(pq, 576 * 4096 * 2), rk = RSRC(pk, 576 * 4096 * 2), rv = RSRC(pv, 576 * 4096 * 2);
    auto req = [&](int kc) {
#pragma unroll
      for (int i = 0; i < 6; i++) {
        st[kc * 6 + i] = __builtin_amdgcn_raw_buffer_load_b128(i < 2 ? rq : (i < 4 ? rk : rv), vw, (kc * 64 + (i & 1) * 32) * 8192, STG_AUX);
        if (NTL && kc + AH < 9 && i == (kc ? 0 : 4)) { SB0(); tc[kc] = __builtin_amdgcn_raw_buffer_load_b32(rt, tv, te && (h >> 2) != kc ? PJ_OOB : (kc + AH) * 64 * 8192 + ts_, 0); }
      }
      SB0();
    };
#pragma unroll
    for (int kc = 0; kc < STG_D; kc++) { if (kc == 0) TLS(13); req(kc); __builtin_amdgcn_s_barrier(); SB0(); }
    TLS(14);
    auto cell = [](int row, int slot) { return (((row * 2 + slot) ^ ((((row >> 2) ^ (row >> 4)) & 1) << 2) ^ ((row >> 5) & 1)) << 5); };
    auto put = [&](int unit, int row, const u32x4& v) { *(u32x4*)(smem + unit * 4160 + cell(row, (tid >> 1) & 1) + (tid & 1) * 16) = v; };
    auto frag = [&](int tile) {
      bf4 tt[4];
#pragma unroll
      for (int kk = 0; kk < 4; kk++) {
        const int r0 = (kk >> 1) * 32 + (lq >> 1) * 16 + (q4 >> 1) * 8 + (lq & 1) * 4 + (kk & 1) * 2 + (q4 & 1);
        tt[kk] = trrd(smem + (tile >> 1) * 4160 + cell(r0, tile & 1) + p4 * 8);
      }
      return ilv(tt[0], tt[1], tt[2], tt[3]);
    };
    auto park = [&](int slot, const u32x8& f) {
      char* d = smem + wl + slot * 2048 + lane * 16;
      *(u32x4*)d = (u32x4){f[0], f[1], f[2], f[3]};
      *(u32x4*)(d + 1024) = (u32x4){f[4], f[5], f[6], f[7]};
    };
    const __amdgpu_buffer_rsrc_t rs0 = RSRC(p.ws, wid ? 0 : O_QA);
    auto poll = [&]() {
      fl = __builtin_amdgcn_raw_buffer_load_b32(rs0, (lane < 32 ? SY_INIT(lane) : SY_ARR) * 4, 0, 16);
      fk = __builtin_amdgcn_raw_buffer_load_b32(rs0, SY_BLK(lane < 12 ? lane >> 2 : 0, cb, lane & 3) * 4, 0, 16);
    };
    auto rows = [&](int vo) {
      nq = __builtin_amdgcn_raw_buffer_load_b128(rsB, vo, h * 8192, 16);
      nk = __builtin_amdgcn_raw_buffer_load_b128(rsB, vo, h * 8192 + ACB, 16);
      nv = __builtin_amdgcn_raw_buffer_load_b128(rsB, vo, h * 8192 + 2 * ACB, 16);
    };
    bool early = false;
    int lost = 0;
#pragma unroll
    for (int kc = 0; kc < 9; kc++) {
      if (kc == STG_D - 1) { if (spec && wid == 0) old = confirm(p, smem, h, b1, wv); SB0(); }
      if (kc == 10 - STG_D) { early = __all(fk >= FULL); CFENCE(); rows(early ? tid * 16 : PJ_OOB); poll(); SB0(); }
#pragma unroll
      for (int i = 0; i < 6; i++) put((i >> 1) * 4 + ((tid & 15) >> 2), (i & 1) * 32 + (tid >> 4), st[kc * 6 + i]);
      if (NTL && kc + AH < 9) asm volatile("" :: "v"(tc[kc]));
      if (kc == 2 * STG_D - 1) lost = (__builtin_amdgcn_readfirstlane(old) >> h) & 1;
      BAR();
      if (kc == 0) TLS(15);
      if (kc == 0) { b1 = __builtin_amdgcn_raw_buffer_load_b32(rs0, 0, SY_BEAT(h & 7) * 4, 16); SB0(); }
      if (kc + STG_D == 8) { fk = ld4_sc1(WSP(u32, 0) + SY_BLK(lane < 12 ? lane >> 2 : 0, cb, lane & 3)); SB0(); }
      if (kc + STG_D < 9) req(kc + STG_D);
      w[0][kc] = frag(wid * 2);
      w[1][kc] = frag(nvt == 2 && kc < 4 ? 16 + vt : wid * 2 + 1);
      const u32x8 tp = frag(nvt == 0 ? 21 + (wid >> 1) : (nvt == 2 && kc < 4 ? wid * 2 + 1 : 16 + vt));
      if (kc >= 4 && kc < 7) { wx[kc - 4] = tp; if (nvt == 0) park(6 + kc, tp); }
      else park(nvt == 0 ? 6 + kc : (kc >= 7 ? kc - 7 : 2 + kc), tp);
      BAR();
      if (kc == 0 || kc == 8) STS(h, kc + 3);
    }
    TLS(6);
    if (wid == 0) {
      int it = 0;
      while (!lost && !__all(fl >= (lane < 32 ? 1u : 256u) && fk >= FULL) && ++it < SPIN_CAP) { BEAT_ON(it); __builtin_amdgcn_s_sleep(GPS); CFENCE(); poll(); }
      if (lane == 0) *(int*)(smem + C_XP) = lost ? -1 : fl & 0x7800;
    }
    BAR();
    if (!early) rows(tid * 16);
    xp = __builtin_amdgcn_readfirstlane(*(const int*)(smem + C_XP));
    if (xp < 0) return xp;
    TLS(7);
    SB0();
    *(u32x4*)(smem + C_QX + tid * 16) = nq;
    *(u32x4*)(smem + C_KX + tid * 16) = nk;
    *(u32x4*)(smem + C_VX + ((tid * 16) ^ ((tid & 32) << 2))) = nv;
  }
  STS(h, 14);
  constexpr float CE = 0.08838834764831845f * 1.4426950408889634f;
  auto run = [&](auto RC) __attribute__((always_inline)) {
  constexpr int R = decltype(RC)::value;
  constexpr int NV = R < 2 ? 0 : (R < 4 ? 1 : 2);
  constexpr bool QW = !(R & 1);
  int known = 0;
  bf8 pre[4][2], vx[NV ? NV : 1];
  int sidx, mo, io, eo, gvo, wvo = 0, so = 0, po = 0, qo = 0, ko = 0, fo = 0, fq = 0, fk = 0;
  float mk[4], mm = 0.f;
  f2v cw = {CE, CE};
#define LDW(dst, slot) do { dst[0] = *(const bf8*)(smem + wvo + (slot) * 2048); dst[1] = *(const bf8*)(smem + wvo + (slot) * 2048 + 1024); } while (0)
  {
    IDX();
    if constexpr (R == 1) {
      po = (tid * 16) ^ ((tid & 32) << 2);
      so = (lane >> 5) * 131072 + (lane & 31) * 16;
      asm volatile("" : "+v"(po), "+v"(so));
    } else if constexpr (R == 0) {
      qo = C_QT + lq * 384 + q4 * 32 + p4 * 8;
      po = C_P + l15 * 16 + lq * 4;
      fo = lq * 512 + l15 * 16;
#pragma unroll
      for (int r = 0; r < 4; r++) { mk[r] = lq * 4 + r <= l15 ? 0.f : -__builtin_inff(); asm volatile("" : "+v"(mk[r])); }
      mm = (l15 & 2) ? -__builtin_inff() : 0.f;
      asm volatile("" : "+v"(mm), "+{v[230:231]}"(cw));
      fq = C_QT + F_Q + (lq ? lq : 1) * 768 + lq * 8; fk = C_KT + F_K + lq * 776;
      asm volatile("" : "+v"(qo), "+v"(po), "+v"(fo), "+v"(fq), "+v"(fk));
      *(u32*)(smem + fq) = 0u; *(u32*)(smem + fk) = 0u;
    } else {
      wvo = wl + lane * 16;
      po = C_P + (lq * 4 + q4) * 16 + (p4 & 1) * 8;
      so = ((l15 >> 2) & 1) * 131072 + (((vt & 3) + (NV == 2 ? hi8 : 0)) * 8 + ((l15 >> 1) & 1) * 4 + (lq >> 1) * 2 + (l15 & 1)) * 16 + (lq & 1) * 8;
      if (NV == 1 && hi8) so = PJ_OOB;
      asm volatile("" : "+v"(wvo), "+v"(po), "+v"(so));
      if constexpr (NV == 2) {
        qo = po ^ ((p4 & 2) << 4); ko = (p4 & 2) ? po : C_Z; po = (p4 & 2) ? C_Z : po;
        *(u64*)(smem + C_Z + hi8 * 256) = 0ull;
        asm volatile("" : "+v"(ko), "+v"(po), "+v"(qo));
      }
#pragma unroll
      for (int j = 0; j < 4; j++) LDW(pre[j], NV == 2 ? 2 + j : j);
    }
    eo = C_QT + wid * 768 + ((l15 >> 2) * 2 + (l15 & 1)) * 32 + lq * 8 + ((l15 >> 1) & 1) * 4;
    sidx = (l15 & 2) ? 0xEEEE : 0x4444;
    mo = C_IMG + lane * 16;
    io = C_IMG + ((tid & ~30) | ((tid & 10) << 1) | ((tid & 20) >> 1)) * 16;
    gvo = tid * 16;
    asm volatile("" : "+v"(sidx), "+v"(mo), "+v"(io), "+v"(eo));
    if constexpr (R < 2) asm volatile("" : "+v"(gvo));
  }
  f4 a0, a1;
  u32x4 vm = {0u, 0u, 0u, 0u};
  if constexpr (NV > 0) asm volatile("" : "+v"(vm));
  u32 ts = 0;
  u64 tq = 0;
  int dur = DH0 * 64;
  const int ob = ((h >> 3) * 32768 + (h & 7) * 512) ^ xp ^ (NV == 1 ? X_HS : 0);
  const int ib = X_M ^ xp ^ ((wid >> 2) * 32768 + (wid & 3) * 1024);
  int b3 = 0;
  PRIO(QW ? 2 : 1);
#pragma unroll 1
  for (int s = 0;; s++) {
    int mb = ib + b3 * 65536, mbx = X_M ^ xp ^ (196608 + b3 * 1024);
    if constexpr (R == GX) asm volatile("" : "+s"(mb), "+s"(mbx));
    u32x4 g, gx;
    if constexpr (R != 1) ALIGN(HAL);
    BAR();
    STAMP(13);
    if constexpr (R == 1 && NXE) {
      asm volatile("buffer_load_dwordx4 %0, %2, %3, %4 offen sc1\n\tbuffer_load_dwordx4 %1, %2, %3, %5 offen sc1"
                   : "=&v"(g), "=&v"(gx) : "v"(mo), "s"(rsX), "s"(mb), "s"(mbx) : "memory");
    } else {
      g = __builtin_amdgcn_raw_buffer_load_b128(rsX, mo, mb, 16);
      if constexpr (R == GX) gx = __builtin_amdgcn_raw_buffer_load_b128(rsX, mo, mbx, 16);
    }
    SB0();
    if constexpr (R == 0) {
#pragma unroll
      for (int j = 0; j < 4; j++) { const char* kx_ = smem + C_KX + fo + j * 2048;
        pre[j >> 1][j & 1] = *(const bf8*)kx_; pre[2 + (j >> 1)][j & 1] = *(const bf8*)(kx_ + 256); }
    } else if constexpr (R > 1) {
      IDX();
#pragma unroll
      for (int d = 0; d < NV; d++) { const char* vb_ = smem + C_VX + (vt + d) * 1024 + (p4 >> 1) * 512 + (((lq * 4 + q4) ^ ((p4 >> 1) * 8)) << 4) + (p4 & 1) * 8;
        vx[d] = cat8(trrd(vb_), trrd(vb_ + 256)); }
    }
    SB0();
    a0 = zero4(); a1 = zero4();
    f4 c0, c1, a2;
#define A2INI() do { u64 z_; float m0_, m1_; asm volatile("v_mov_b32 %0, %3\n\tv_mov_b32 %1, %3\n\tv_mov_b64 %2, 0\n\ts_nop 1" : "=&v"(m0_), "=&v"(m1_), "=v"(z_) : "v"(mm)); \
                     const f2v zz_ = __builtin_bit_cast(f2v, z_); a2 = (f4){m0_, m1_, zz_[0], zz_[1]}; } while (0)
    if constexpr ((ZE >> R) & 1) {
      c1 = zero4();
      if constexpr (NV != 1) c0 = zero4();
      if constexpr (R == 0) { A2INI(); asm volatile("" : "+v"(a2)); }
    }
    int so_ = X_O(b3) ^ ob, an_ = ANX(s & 1) + h * ANS;
    int nb = s < 63 ? (s + 1) * 262144 + h * 8192 : PJ_OOB;
    asm volatile("" : "+s"(so_), "+s"(an_), "+s"(nb));
    u32x4 nx[2];
#define NXL1(g, i) __builtin_amdgcn_raw_ptr_buffer_load_lds(rsB, (LDSP void*)(smem + C_KX + (g) * 4096), 16, (g) < 2 ? gvo : po, \
                                                             nb + (1 + ((g) >> 1)) * ACB + ((g) & 1) * 4096 - 4096, (i) * 1024, 16)
#define NXL(g) do { NXL1(g, 0); NXL1(g, 1); NXL1(g, 2); NXL1(g, 3); } while (0)
    SB0();
    if (NXE) {
      BARC();
      if constexpr (R == 1) {
        if (NXE > 1) __builtin_amdgcn_s_sleep(NXE - 1);
#pragma unroll
        for (int i = 0; i < 4; i++)
          asm volatile("s_mov_b32 m0, %3\n\ts_nop 0\n\tbuffer_load_dwordx4 %0, %1, %2 offen sc1 lds\n\tbuffer_load_dwordx4 %0, %1, %2 offen offset:1024 sc1 lds\n\t"
                       "buffer_load_dwordx4 %0, %1, %2 offen offset:2048 sc1 lds\n\tbuffer_load_dwordx4 %0, %1, %2 offen offset:3072 sc1 lds"
                       :: "v"(i < 2 ? gvo : po), "s"(rsB), "s"(nb + (1 + (i >> 1)) * ACB + (i & 1) * 4096 - 4096), "i"(C_KX + i * 4096) : "memory", "m0");
      }
      SB0();
    }
#define LDM(j) *(const bf8*)(smem + mo + (j) * 1024)
    constexpr int NB = R == 1 ? 9 : 5;
    bf8 b[NB];
    if constexpr (!QW) {
      PRIO(IPR); BARN();
      if (((EK >> R) & 1) && !EKL) {
#pragma unroll
        for (int j = 0; j < EKN; j++) { b[j] = LDM(j); SB0(); }
      }
    }
    {
      if constexpr (R == 1 && NXE) asm volatile("s_waitcnt vmcnt(17)" : "+v"(g));
      else __builtin_amdgcn_s_waitcnt(R == GX ? 0x0F71 : 0x0F70);
      CFENCE();
      *(u32x4*)(smem + io) = g;
      SB0();
      u32 top = umax(umax(umax(g[0], g[1]), g[2]), g[3]);
      if constexpr (R == GX) {
        asm volatile("" : "+v"(top));
        SB0();
        if constexpr (R == 1 && NXE) asm volatile("s_waitcnt vmcnt(16)" : "+v"(gx));
        *(u32x4*)(smem + GXO + io) = gx;
        SB0();
        top = umax(umax(umax(umax(top, gx[0]), gx[1]), gx[2]), gx[3]);
      }
#define G_BAD() (umax(umax(umax(g[0], g[1]), umax(g[2], g[3])), R != GX ? 0u : umax(umax(gx[0], gx[1]), umax(gx[2], gx[3]))) == ~0u)
      if (__builtin_expect(__any(top == ~0u), 0)) {
        int it = 0;
        do {
          CFENCE();
          if (G_BAD()) {
            g = __builtin_amdgcn_raw_buffer_load_b128(rsX, mo, mb, 16);
            if constexpr (R == GX) gx = __builtin_amdgcn_raw_buffer_load_b128(rsX, mo, mbx, 16);
          }
          VMW0C();
        } while (__any(G_BAD()) && ++it < SPIN_CAP);
        *(u32x4*)(smem + io) = g;
        if constexpr (R == GX) *(u32x4*)(smem + GXO + io) = gx;
      }
    }
    SB0();
    STAMP(15);
    if constexpr (!QW) PRIO(1);
    if (((EK >> R) & 1) && EKL) {
#pragma unroll
      for (int j = 0; j < EKN; j++) { b[j] = LDM(j); SB0(); }
      CFENCE(); __builtin_amdgcn_s_waitcnt(0xC07F | (EKN << 8)); BARN();
    } else
    BARC();
    STAMP(0);
#define ANNC() do { ts = (u32)tq; __builtin_amdgcn_raw_buffer_store_b32((((ts << 6) + (u32)dur) & ~63u) | (u32)s, rsX, 0, an_, 16); SB0(); } while (0)
    if constexpr (R == 1) {
      STAMP(6);
      if (EK & 2) asm volatile("s_memrealtime %0" : "=s"(tq));
      else { asm volatile("s_memrealtime %0\n\ts_waitcnt lgkmcnt(0)" : "=s"(tq)); ANNC(); }
      SB0();
#pragma unroll
      for (int j = (EK & 2) ? EKN : 0; j < 9; j++) { b[j] = LDM(j); SB0(); }
#pragma unroll
      for (int i = 0; i < NXA; i++) { NXL(i); SB0(); }
    } else {
      if (!QW && KSP) __builtin_amdgcn_s_sleep(KSP);
#pragma unroll
      for (int j = ((EK >> R) & 1) ? EKN : 0; j < 4; j++) { b[j] = LDM(j); SB0(); }
    }
#pragma unroll
    for (int j = 0; j < 9; j++) {
      if constexpr (QW) {
        if (j == BJ) {
          BARN();
#pragma unroll
          for (int i = 4; i < 4 + BJ; i++) {
            b[i % 5] = LDM(i); SB0();
          }
        }
        if (j >= BJ && j + 4 < 9) b[(j + 4) % 5] = LDM(j + 4);
      } else if (R != 1 && j + 4 < 9) b[(j + 4) % 5] = LDM(j + 4);
      SB0();
      a0 = SMF(b[j % NB], w[0][j], a0, sidx);
      if (NV < 2 || j >= 4) a1 = SMF(b[j % NB], w[1][j], a1, sidx);
      else a1 = SMF(b[j % 5], cat16(pre[j & 3][0], pre[j & 3][1]), a1, sidx);
      SB0();
      if (R == 1 && (EK & 2) && j == EKA) { asm volatile("s_waitcnt lgkmcnt(0)" : "+s"(tq)); ANNC(); }
      if (j == 0) STAMP(10);
      if (j == 4) STAMP(11);
      if (j == 8) STAMP(12);
      if (NV == 2 && j >= DJ && j < DJ + 4) LDW(pre[j - DJ], (DSLOT >> ((j - DJ) * 4)) & 15);
    }
    {
      {
        const u32 e0 = pk2(a0[0] + a0[2], a0[1] + a0[3]), e1 = pk2(a1[0] + a1[2], a1[1] + a1[3]);
        asm volatile("ds_write2_b32 %0, %1, %2 offset1:%3" :: "v"(eo), "v"(e0), "v"(e1), "i"(96) : "memory");
      }
      if constexpr (R != 0) asm volatile("ds_write_b32 %0, %1 offset:%2" :: "v"(eo), "v"(sidx), "i"(QW ? F_Q : F_K) : "memory");
    }
    SB0();
    STAMP(1);
    if constexpr (QW) PRIO(R ? 0 : SP1);
    else if (KV0) PRIO(0);
    if constexpr ((ZE >> R) & 1) { a0 = c0; a1 = c1; }
    else if constexpr (R != 1) { a1 = zero4(); if constexpr (NV != 1) a0 = zero4(); if constexpr (R == 0) A2INI(); }
    if constexpr (NV == 0) {
      u32 dl = 0;
      const int c = 1;
      bf8 km[2];
      if constexpr (R == 0) {
        if (QSL) __builtin_amdgcn_s_sleep(QSL);
        bf8 qa[2];
        u32 fl;
        do {
          CFENCE();
          fl = *(const volatile LDSP u32*)(smem + fq);
          CFENCE(); SB0();
#pragma unroll
          for (int i = 0; i < 2; i++) { qa[i] = cat8(trrd(smem + qo + i * 1536), trrd(smem + qo + i * 1536 + 128)); SB0(); }
        } while (__builtin_expect(__any(fl == 0u), 0));
        STAMP(2);
        auto rdk = [&]() __attribute__((always_inline)) {
          CFENCE(); SB0();
          fl = *(const volatile LDSP u32*)(smem + fk);
          CFENCE(); SB0();
#pragma unroll
          for (int i = 0; i < 2; i++) { km[i] = cat8(trrd(smem + qo + 3072 + i * 1536), trrd(smem + qo + 3200 + i * 1536)); SB0(); }
        };
        auto mulk = [&]() __attribute__((always_inline)) {
#pragma unroll
          for (int i = 0; i < 2; i++) { a2 = MFMA(qa[i], km[i], a2); if (!i) asm volatile("" : "+{v[240:243]}"(a2)); SB0(); }
        };
#pragma unroll
        for (int i = 0; i < 2; i++) {
          a0 = SMF(qa[i], cat16(pre[i][0], pre[i][1]), a0, sidx); asm volatile("" : "+v"(a0)); SB0();
          a1 = SMF(qa[i], cat16(pre[2 + i][0], pre[2 + i][1]), a1, sidx); if (!i) asm volatile("" : "+v"(a1)); SB0();
        }
#define EX2(d) "v_exp_f32 v[" #d "], v[" #d "]\n\tv_exp_f32 v[" #d "+1], v[" #d "+1]\n\t"
#define DP2(OP, d, N) OP " v[" #d "], v[" #d "], v[" #d "] row_ror:" #N " row_mask:0xf bank_mask:0xf\n\t" OP " v[" #d "+1], v[" #d "+1], v[" #d "+1] row_ror:" #N " row_mask:0xf bank_mask:0xf\n\t"
#define ROW2(OP, d) DP2(OP, d, 8) "s_nop 0\n\t" DP2(OP, d, 4) "s_nop 0\n\t" DP2(OP, d, 2) "s_nop 0\n\t" DP2(OP, d, 1)
#define S_F PKFMS(234, 232, 230, 244) PKFMS(238, 236, 230, 244)
#define S_E EX2(234) EX2(238)
#define S_IO "+{v[232:235]}"(a0), "+{v[236:239]}"(a1)
        f4 z;
#define S_0 "s_setprio %[pr]\n\ts_nop 5\n\t" PKADD(232, 232, 234) PKADD(236, 236, 238) "s_nop 0\n\t" \
            "v_mul_f32_dpp v244, v232, v230 row_newbcast:0 row_mask:0xf bank_mask:0xf\n\tv_mul_f32_dpp v245, v233, v230 row_newbcast:0 row_mask:0xf bank_mask:0xf\n\t" S_F
        if (KP == 3) asm volatile(S_0 S_E "s_setprio 2" : S_IO, "=&{v[244:247]}"(z) : "{v[230:231]}"(cw), [pr] "i"(SP2));
        if (KP == 2) asm volatile(S_0 EX2(234) "s_setprio 2" : S_IO, "=&{v[244:247]}"(z) : "{v[230:231]}"(cw), [pr] "i"(SP2));
        if (KP == 1) asm volatile(S_0 "s_setprio 2" : S_IO, "=&{v[244:247]}"(z) : "{v[230:231]}"(cw), [pr] "i"(SP2));
        STAMP(3);
        if (KD) asm volatile("s_nop %0" :: "i"(KD - 1));
        SB0();
        rdk();
#define S_X PKADD(246, 234, 238)
#define S_M PKFMS(242, 240, 230, 244) EX2(242) "s_nop 0\n\t" PKADD(246, 246, 242) "s_nop 1\n\t" ROW2("v_add_f32_dpp", 246) "v_rcp_f32 v246, v246\n\tv_rcp_f32 v247, v247\n\ts_nop 0\n\t" \
            PKMUL(234, 234, 246) PKMUL(238, 238, 246) PKMUL(242, 242, 246) \
            CV2("v234", 234) CV2("v238", 238) CV2("v242", 242) "ds_write2st64_b32 %[po], v234, v238 offset1:1\n\tds_write_b32 %[po], v242 offset:512\n\tv_min_f32 v246, v246, v247"
        if (KP == 3) asm volatile(S_X : S_IO, "+{v[244:247]}"(z));
        if (KP == 2) asm volatile(EX2(238) "s_nop 0\n\t" S_X : S_IO, "+{v[244:247]}"(z));
        if (KP == 1) asm volatile(S_E "s_nop 0\n\t" S_X : S_IO, "+{v[244:247]}"(z));
        SB0();
        if (__builtin_expect(__any(fl == 0u), 0)) {
          asm volatile("s_nop 0");
          do {
            rdk();
          } while (__any(fl == 0u));
        }
        mulk();
        STAMP(4);
#define S_IO2 S_IO, "+{v[240:243]}"(a2), "+{v[244:247]}"(z) : "{v[230:231]}"(cw), [po] "v"(po) : "memory"
        asm volatile("s_nop 7\n\tv_add_f32_dpp v240, v242, v240 quad_perm:[2,3,0,1] row_mask:0xf bank_mask:0xf\n\tv_add_f32_dpp v241, v243, v241 quad_perm:[2,3,0,1] row_mask:0xf bank_mask:0xf\n\t" S_M : S_IO2);
        SB0();
        if (__builtin_expect(__any(z[2] < SMALL), 0)) {
          asm volatile("v_max3_f32 v244, v232, v236, v240\n\tv_max3_f32 v245, v233, v237, v241\n\ts_nop 0\n\t" ROW2("v_max_f32_dpp", 244) PKMUL(244, 244, 230) S_F S_E "s_nop 0\n\t" S_X S_M : S_IO2);
        }
        SB0();
      } else {
#pragma unroll
        for (int i = NXA; i < (NXV ? NXH : NXB) && !NXE; i++) NXL(i);
        SB0();
        const int rb = X_O(b3 == 0 ? 2 : b3 - 1) ^ ob;
        const u32x4 e = ones4();
        __builtin_amdgcn_raw_buffer_store_b128(e, rsX, so, rb, 16);
        __builtin_amdgcn_raw_buffer_store_b128(e, rsX, so, rb ^ X_HS, 16);
        PSTAMP();
      }
      STAMP(5);
      BARC();
      if constexpr (R == 0) STAMP(6);
      if constexpr (R == 1) {
        const u32 tp = (u32)wall_clock64(); dur += ((int)umin((tp - ts) << 6, 200u * 64u) - dur) >> DSH;
        SB0();
        if (ADE && s < 63) AD_GET(true, K1 + K3 + KXT(KA + KB) + (s ? 0 : KS0), 4096);
        if (NXB < 4) {
          __builtin_amdgcn_s_sleep(NXS);
#pragma unroll
          for (int i = NXB; i < 4; i++) NXL(i);
          BARN();
        }
        ALIGN(HAL);
        VMW0C();
        STAMP(8);
        if (NXB == 4) BARN();
        if (s == 63) break;
      } else {
        *(u32*)(smem + fq) = 0u; *(u32*)(smem + fk) = 0u;
        bf8 qx[2][4];
#pragma unroll
        for (int ks = 0; ks < 8; ks++) { qx[ks >> 2][ks & 3] = *(const bf8*)(smem + C_QX + (ks >> 2) * 256 + fo + (ks & 3) * 2048); if (ks < QXP) SB0(); }
#pragma unroll
        for (int NT = 1; NT <= 2; NT++) {
          f4 sc[2], sm = zero4();
          const bf8* qf = qx[NT - 1];
#pragma unroll
          for (int ks = 0; ks < 4; ks++) {
#pragma unroll
            for (int j = 0; j < NT; j++) sc[j] = MFMA(pre[2 * j + (ks >> 1)][ks & 1], qf[ks], ks ? sc[j] : (j == NT - 1 ? ((f4){mk[0], mk[1], mk[2], mk[3]}) : ((f4){0, 0, 0, 0})));
            if (ks & 1) sm = SMF(km[ks >> 1], cat16(qf[ks - 1], qf[ks]), sm, sidx);
          }
          sm[0] += sm[2]; sm[1] += sm[3];
          float mx = FMX3(FMX3(sc[0][0], sc[0][1], sc[0][2]), sc[0][3], FMX(sm[0], sm[1]));
          if (NT == 2) mx = FMX3(FMX3(mx, sc[1][0], sc[1][1]), sc[1][2], sc[1][3]);
          { auto t = __builtin_amdgcn_permlane16_swap(__float_as_uint(mx), __float_as_uint(mx), false, false);
            mx = FMX(__uint_as_float(t[0]), __uint_as_float(t[1])); }
          { auto t = __builtin_amdgcn_permlane32_swap(__float_as_uint(mx), __float_as_uint(mx), false, false);
            mx = FMX(__uint_as_float(t[0]), __uint_as_float(t[1])); }
          mx *= -CE;
#pragma unroll
          for (int j = 0; j < NT; j++)
#pragma unroll
            for (int r = 0; r < 4; r++) sc[j][r] = __builtin_amdgcn_exp2f(__builtin_fmaf(sc[j][r], CE, mx));
          sm[0] = __builtin_amdgcn_exp2f(__builtin_fmaf(sm[0], CE, mx)); sm[1] = __builtin_amdgcn_exp2f(__builtin_fmaf(sm[1], CE, mx));
          float sum = ((sc[0][0] + sc[0][1]) + (sc[0][2] + sc[0][3])) + (sm[0] + sm[1]);
          if (NT == 2) sum += (sc[1][0] + sc[1][1]) + (sc[1][2] + sc[1][3]);
          { auto t = __builtin_amdgcn_permlane16_swap(__float_as_uint(sum), __float_as_uint(sum), false, false);
            sum = __uint_as_float(t[0]) + __uint_as_float(t[1]); }
          sum = swadd(sum, sum);
          const float inv = __builtin_amdgcn_rcpf(sum);
          *(u32x4*)(smem + C_PX + (NT - 1) * 1024 + gvo) = (u32x4){pk2(sc[0][0] * inv, sc[0][1] * inv), pk2(sc[0][2] * inv, sc[0][3] * inv),
                                                                  NT == 2 ? pk2(sc[1][0] * inv, sc[1][1] * inv) : 0u, NT == 2 ? pk2(sc[1][2] * inv, sc[1][3] * inv) : 0u};
          *(u32*)(smem + C_PX + 2048 + (NT - 1) * 256 + (gvo >> 2)) = pk2(sm[0] * inv, sm[1] * inv);
        }
        STAMP(8);
        BAR();
        if (s == 63) break;
        if (known < (s < 61 ? s + 3 : 64)) {
          int n = 0;
          while (true) {
            u32 bf[3];
#pragma unroll
            for (int i = 0; i < 3; i++) bf[i] = ld4_sc1(WSP(u32, 0) + SY_BLK(i, cb, gvo >> 4));
            const u64 miss = __ballot(!(bf[0] >= FULL && bf[1] >= FULL && bf[2] >= FULL));
            known = miss ? __builtin_ctzll(miss) : 64;
            if (known >= (s < 61 ? s + 3 : 64) || ++n >= SPIN_CAP) break;
            __builtin_amdgcn_s_sleep(KSL);
          }
        }
      }
      STAMP(9);
      YEND();
      if constexpr (R == 1) { if (!ADE) AD_GET(true, K1 + K3, 4096); AD_SPIN(); }
    } else {
      if (NV == 2 && DJ > 5) {
#pragma unroll
        for (int j = 0; j < 4; j++) LDW(pre[j], (DSLOT >> (j * 4)) & 15);
        SB0();
      }
      if (QW && (NV == 2 ? VSL : VSL1)) __builtin_amdgcn_s_sleep(NV == 2 ? VSL : VSL1);
      bf4 p0, p1, p2, p3, m0, m1;
      auto getp = [&]() __attribute__((always_inline)) {
        if (QW && PVP) PRIO(2);
        if (!QW && KV0) PRIO(1);
        SB0();
        STAMP(5);
        BARN();
        p0 = trrd(smem + po); p1 = trrd(smem + po + 256);
        SB0();
        if constexpr (NV == 2) { p2 = trrd(smem + ko); p3 = trrd(smem + ko + 256); }
        SB0();
        m1 = trrd(smem + (NV == 2 ? qo : po) + 512);
        SB0();
      };
      constexpr int VB = (VBP >> (R * 4 - 8)) & 15;
      if constexpr (NV == 2) {
        int u = 0;
#pragma unroll
        for (int i = 0; i < 9; i++) {
          const int bs = (0x31042 >> ((i % 5) * 4)) & 7;
#define RING(a) do { a = SMF(b[bs], cat16(pre[u & 3][0], pre[u & 3][1]), a, sidx); SB0(); \
                     if (u < 7) LDW(pre[u & 3], (DSLOT >> (u * 4 + 16)) & 15); u++; } while (0)
          SB0();
          if (i == VB) getp();
          if (i == 0 || i == 4) RING(a0);
          else a0 = SMF(b[bs], i < 4 ? wx[i - 1] : w[1][i - 5], a0, sidx);
          SB0();
          RING(a1);
          if (i < 4) b[bs] = LDM(i);
        }
      } else {
#pragma unroll
        for (int i = 0; i < 9; i++) {
          SB0();
          if (i == VB) getp();
          if (i < 3) a1 = SMF(b[(i + 4) % 5], wx[i], a1, sidx);
          else a1 = SMF(b[(i + 4) % 5], cat16(pre[(i + 1) & 3][0], pre[(i + 1) & 3][1]), a1, sidx);
          SB0();
          if (i < 4) b[(i + 4) % 5] = LDM(i);
          if (i == 3 || i == 4) LDW(pre[(i + 1) & 3], 1 + i);
        }
      }
      if (VB == 9) getp();
      if constexpr (NV == 2) { vm[0] = pk2(a0[0] + a0[2], a0[1] + a0[3]); SB0(); }
      vm[NV - 1] = pk2(a1[0] + a1[2], a1[1] + a1[3]);
      asm volatile("" : "+v"(vm));
      SB0();
      if (VB > 9) getp();
      {
        const u32x8 pp = ilv(m0, m1, p0, p1);
        const bf8 pq = cat8(p2, p3);
        f4 o = MFMA(vx[0], __builtin_bit_cast(bf8, __builtin_shufflevector(pp, pp, 4, 5, 6, 7)), ((f4){0, 0, 0, 0})); asm volatile("" : "+v"(o)); SB0();
        if constexpr (NV == 2) { o = MFMA(vx[1], pq, o); asm volatile("" : "+v"(o)); SB0(); }
        o = MFMA(__builtin_bit_cast(bf8, vm), __builtin_bit_cast(bf8, __builtin_shufflevector(pp, pp, 2, 3, 4, 5)), o);
        asm volatile("" : "+v"(o));
        asm volatile("" :: "v"(pp));
        if constexpr (NV == 2) asm volatile("" :: "v"(pq));
        SB0();
        __builtin_amdgcn_raw_buffer_store_b64((u32x2){umin(pk2(o[0], o[1]), 0xFFFFFFFEu), pk2(o[2], o[3])}, rsX, so, so_, 16);
        SB0();
      }
      STAMP(7);
      if (QW && !PVP) PRIO(2);
      int xo, lo, pa = 0;
      {
        IDX();
        if constexpr (NV == 1) {
          __builtin_amdgcn_s_sleep(LSL);
          if (NXV && vt >= NXH) {
            asm volatile("s_mov_b32 m0, %3\n\ts_nop 0\n\tbuffer_load_dwordx4 %0, %1, %2 offen sc1 lds\n\tbuffer_load_dwordx4 %0, %1, %2 offen offset:1024 sc1 lds\n\t"
                         "buffer_load_dwordx4 %0, %1, %2 offen offset:2048 sc1 lds\n\tbuffer_load_dwordx4 %0, %1, %2 offen offset:3072 sc1 lds"
                         :: "v"((lane * 16) ^ ((lane & (vt * 16) & 32) << 2)), "s"(rsB), "s"(nb + (1 + (vt >> 1)) * ACB + (vt & 1) * 4096), "s"(C_KX + vt * 4096) : "memory", "m0");
            SB0();
          }
#pragma unroll
          for (int i = 0; i < 2; i++) nx[i] = __builtin_amdgcn_raw_buffer_load_b128(rsB, lane * 16 + i * 1024, nb + vt * 2048, 16);
          pa = C_QX + vt * 2048 + lane * 16;
          asm volatile("" : "+v"(pa));
        }
        xo = OB_OFF((lq & 1) * 16 + l15, (lq >> 1) * 8);
        lo = lane * 4;
        asm volatile("" : "+v"(xo), "+v"(lo));
#pragma unroll
        for (int j = 0; j < 4; j++) LDW(pre[j], NV == 2 ? 2 + j : j);
      }
      SB0();
      BARN();
      STAMP(8);
      {
        f4 o[2][NV];
#pragma unroll
        for (int it = 0; it < 2; it++) {
          const bf8 pf = *(const bf8*)(smem + C_PX + it * 1024 + lo * 4);
          const u32 pm = *(const u32*)(smem + C_PX + 2048 + it * 256 + lo);
#pragma unroll
          for (int d = 0; d < NV; d++) {
            o[it][d] = MFMA(vx[d], pf, ((f4){0, 0, 0, 0}));
            o[it][d] = MFMA(__builtin_bit_cast(bf8, vm), __builtin_bit_cast(bf8, d ? (u32x4){0u, pm, 0u, 0u} : (u32x4){pm, 0u, 0u, 0u}), o[it][d]);
          }
        }
#pragma unroll
        for (int d = 0; d < NV; d++)
          __builtin_amdgcn_raw_buffer_store_b128(pair16(umin(pk2(o[0][d][0], o[0][d][1]), 0xFFFFFFFEu), pk2(o[0][d][2], o[0][d][3]),
                                                        umin(pk2(o[1][d][0], o[1][d][1]), 0xFFFFFFFEu), pk2(o[1][d][2], o[1][d][3])), rsB,
              xo, 3 * ACB + OB_OFF(s * 32, h * 128 + (vt + d) * 16), 16);
        SB0();
        if (s == 63) break;
        if constexpr (NV == 1) { *(u32x4*)(smem + pa) = nx[0]; *(u32x4*)(smem + pa + 1024) = nx[1]; }
      }
      STAMP(9);
      YEND();
    }
    b3 = b3 == 2 ? 0 : b3 + 1;
    STAMP(14);
  }
  };
  if (wid == 0) run(std::integral_constant<int, 0>{});
  else if (wid == 4) run(std::integral_constant<int, 1>{});
  else if (wid > 4) run(std::integral_constant<int, 3>{});
  else if (wid == 3) run(std::integral_constant<int, 2>{});
  else run(std::integral_constant<int, 4>{});
  PRIO(0);
  TLS(0);
  return xp;
}

__device__ __forceinline__ int chain_out(const Params& p, char* smem, const int c, const int wv, const bool spec) {
  STS(32 + c, 1);
  constexpr int R = 5; (void)R;
  const int wid = wv, tq = c & 1, cg = c >> 1;
  u32x8 w[4][5];
  const __amdgpu_buffer_rsrc_t rsX = RSRC(XWIN(p), X_END);
  int xp = 0, mo, wo, ro;
  const int ms = wid < 4 ? 65536 : 1024;
#define OST 0
  const int r1 = tq & OST, r2 = cg & (OST >> 1);
  {
    IDX();
    mo = wid < 4 ? (cg >> 3) * 32768 + (cg & 7) * 512 + tq * 256 + wid * 64 : 196608 + (cg >> 2) * 512 + tq * 256 + (cg & 3) * 64;
    if (wid > 4 || (wid == 4 && cg >= 8)) mo = PJ_OOB;
    mo ^= X_M;
    u32 m0[2], old = 0u;
    if (wid == 0) {
#pragma unroll
      for (int i = 0; i < 2; i++) m0[i] = *(const GLB u32*)(p.Bm + (tq * 4 + q4) * 576 + (i ? 512 + (cg & 7) * 8 : cg * 32 + lq * 8) + p4 * 2);
      const int cal = X_CAL + (cg & 7) * 32768 + (cg >> 3) * 1024 + tq * 512;
      __builtin_amdgcn_raw_buffer_store_b32(0u, rsX, lane < 8 ? ((2048 << p4) & ~2048) + q4 * 2304 : PJ_OOB, cal, 16);
      asm volatile("" :: "v"(m0[0]), "v"(m0[1]));
      VMW0();
      TLS(8);
      u32 d[4] = {0u, 0u, 0u, 0u};
#pragma unroll 1
      for (int r = 0; r < CALR; r++) {
#pragma unroll
        for (int k = 0; k < 4; k++) {
          const u32 t0 = (u32)__builtin_readcyclecounter();
          __builtin_amdgcn_raw_buffer_store_b32(1u, rsX, lane ? PJ_OOB : 0, cal + ((2048 << k) & ~2048) + (r ^ tq) * 2304, 16);
          VMW0();
          d[k] += (u32)__builtin_readcyclecounter() - t0;
        }
      }
      if (OBE) BARN();
      if (spec) old = confirm(p, smem, 32 + c, BEAT_LD(c), wv);
      if (lane == 0) st4_sc1(WSP(u32, 0) + SY_VOTE(c), 8u + (((d[1] < d[0] ? 1u : 0u) + (d[3] < d[0] ? 4u : 0u)) ^ (d[2] < d[0] ? 3u : 0u)));
      TLS(11);
    }
    if (!OBE || wid) BARN();
    if (wid == 0) {
      u32 fv;
      int it = 0;
      while (fv = ld4_sc1(WSP(u32, 0) + SY_VOTE(lane & 31)), xp = -((__builtin_amdgcn_readfirstlane(old) >> c) & 1), !(xp || __all(fv != 0u)) && ++it < SPIN_CAP) {
        BEAT_ON(it); __builtin_amdgcn_s_sleep(2);
      }
#pragma unroll
      for (int j = 0; j < 3; j++) xp |= __popcll(__ballot((fv >> j) & 1u)) > 32 ? 4096 << j : 0;
      if (xp >= 0) {
#pragma unroll
        for (int i = 0; i < 3; i++) __builtin_amdgcn_raw_buffer_store_b32(i ? ~0u : umin(m0[0], 0xFFFFFFFEu), rsX, lane * 4, mo ^ xp ^ (i * 65536), 16);
        __builtin_amdgcn_raw_buffer_store_b32(lq ? ~0u : umin(m0[1], 0xFFFFFFFEu), rsX, lq < 3 && cg < 8 ? lq * 1024 + l15 * 4 : PJ_OOB,
                                              (196608 + (cg >> 2) * 512 + tq * 256 + (cg & 3) * 64) ^ X_M ^ xp, 16);
#pragma unroll
        for (int i = 0; i < 6; i++)
          __builtin_amdgcn_raw_buffer_store_b128(ones4(), rsX, (lane >> 5) * 131072 + (lane & 31) * 16, X_O(i >> 1) ^ ((c >> 3) * 32768 + (c & 7) * 512) ^ xp ^ ((i & 1) * X_HS), 16);
        if (c == 0) {
#pragma unroll
          for (int i = 0; i < 6; i++) __builtin_amdgcn_raw_buffer_store_b128((u32x4){0u, 0u, 0u, 0u}, rsX, lane * 16 + (i & 3) * 1024, X_HOT(xp) ^ (i < 4 ? X_CS : 0), 16);
        }
        __builtin_amdgcn_raw_buffer_store_b32(lane & 2 ? DO0 * 64u : ~0u, rsX, lane < 4 ? ANX(lane & 1) + ((lane & 2) * 16 + c) * ANS : PJ_OOB, 0, 16);
      }
    }
    SB0();
    const __amdgpu_buffer_rsrc_t rw = RSRC(p.BWo, 576 * 4096 * 2);
    u32x4 st[4][8];
    u32 fl;
    bool early = false;
    const int va = ((lane >> 2) * 576 + cg * 32 + p4 * 8) * 2;
#pragma unroll
    for (int i = 0; i < 4; i++) {
#pragma unroll
      for (int k = 0; k < 8; k++) st[i][k] = __builtin_amdgcn_raw_buffer_load_b128(rw, va, ((((i & 1) ^ r1) * 16 + wid * 2 + ((k >> 2) ^ r2)) * 128 + (i >> 1) * 64 + (k & 3) * 16) * 1152, OSTG_AUX);
      SB0();
      if (i == 1 && wid == 0 && xp >= 0) {
        asm volatile("s_waitcnt vmcnt(16)" ::: "memory");
        if (lane == 0) st4_sc1(WSP(u32, 0) + SY_INIT(c), (u32)xp + 1u);
        TLS(10);
      }
      if (i == 2) fl = ld4_sc1(WSP(u32, 0) + SY_WX);
      SB0();
    }
    auto wx = [&](int vo) {
#pragma unroll
      for (int k = 0; k < 8; k++)
        st[0][k] = __builtin_amdgcn_raw_buffer_load_b128(RSRC(WSP(char, O_WX), 524288), vo, cg * 65536 + ((((k & 2) ^ (r1 * 2)) * 8 + wid * 2 + (k & 1)) * 128 + (k >> 2) * 64) * 16, 16);
    };
    auto poll = [&]() { fl = __builtin_amdgcn_raw_buffer_load_b32(RSRC(p.ws, wid ? 0 : O_QA), (lane < 32 ? SY_INIT(lane) : lane < 48 ? SY_ARR : SY_WX) * 4, 0, 16); };
    char* sc = smem + wid * 10240;
    auto frag = [&](const char* b, int rs) {
      bf4 tt[4];
#pragma unroll
      for (int kk = 0; kk < 4; kk++) {
        const int r0 = ((kk >> 1) * 2 + (lq >> 1)) * 32 + (p4 >> 1) * 16 + (q4 >> 1) * 8 + (lq & 1) * 4 + (kk & 1) * 2 + (q4 & 1);
        tt[kk] = trrd(b + r0 * rs + (p4 & 1) * 8);
      }
      return ilv(tt[0], tt[1], tt[2], tt[3]);
    };
#pragma unroll
    for (int i = 0; i < 4; i++) {
#pragma unroll
      for (int k = 0; k < 8; k++) *(u32x4*)(sc + ((k ^ (r2 * 4)) * 16 + (lane >> 2)) * 80 + p4 * 16) = st[i][k];
      __builtin_amdgcn_fence(__ATOMIC_RELEASE, "wavefront");
      __builtin_amdgcn_wave_barrier();
#pragma unroll
      for (int t = 0; t < 4; t++) w[i][t] = frag(sc + t * 16, 80);
      asm volatile("s_waitcnt lgkmcnt(0)" ::: "memory");
      __builtin_amdgcn_wave_barrier();
      if (i == 2) { early = __all(fl == ~0u); CFENCE(); poll(); wx(early ? lane * 16 : PJ_OOB); }
      SB0();
    }
    if (wid == 0) {
      int it = 0;
      while (xp >= 0 && !__all(fl >= (lane < 32 ? 1u : lane < 48 ? 256u : ~0u)) && ++it < SPIN_CAP) { BEAT_ON(it); __builtin_amdgcn_s_sleep(GPS); CFENCE(); poll(); }
      *(int*)(smem + C_XP) = xp;
    }
    asm volatile("s_waitcnt lgkmcnt(0)" ::: "memory");
    BARN();
    xp = __builtin_amdgcn_readfirstlane(*(const int*)(smem + C_XP));
    if (xp < 0) return xp;
    {
      if (!early) wx(lane * 16);
#pragma unroll
      for (int k = 0; k < 8; k++) *(u32x4*)(sc + k * 1024 + lane * 16) = st[0][k];
      __builtin_amdgcn_fence(__ATOMIC_RELEASE, "wavefront");
      __builtin_amdgcn_wave_barrier();
#pragma unroll
      for (int i = 0; i < 4; i++) w[i][4] = frag(sc + i * 2048, 16);
    }
    mo = __builtin_amdgcn_readfirstlane(mo ^ xp);
    const int row = (lq & 1) * 4 + ((l15 >> 1) & 3);
    wo = ((lq >> 1) == hi8 ? row * 128 : 1024) + (((wid * 2 + hi8) ^ (row * 2)) << 3) + (l15 & 1) * 4;
    ro = wid > 4 ? 0 : wid * 2560 + (lane >> 5) * 1280 + ((lane >> 2) & 7) * 128 + (lane & 8) * 8 + p4 * 16;
  }
  __syncthreads();
  STS(32 + c, 14);
  TLS(9);
  int go, sidx, po;
  { IDX(); go = lane * 16; sidx = (l15 & 2) ? 0xEEEE : 0x4444;
    po = p4 == 0 ? (lq & 1) * 32 + (lq >> 1) * 16 + q4 * 4 : PJ_OOB; }
  int ro1 = ro ^ 64;
  asm volatile("" : "+v"(ro), "+v"(ro1), "+v"(wo));
#define PUTK(t, r, e, nop) asm volatile(nop PKADD(r, r, (r + 2)) "ds_write2st64_b32 %1, v[" #r "], v[" #r "+1] offset0:%2 offset1:%3" \
                                  : "+{v[" #r ":" #e "]}"(d[t]) : "v"(wo), "i"((t) * 10), "i"((t) * 10 + 5) : "memory")
#define PUTP(t) do { if (PKA & 8) { if (t == 0) PUTK(0, 236, 239, "s_nop 5\n\t"); if (t == 1) PUTK(1, 240, 243, ""); if (t == 2) PUTK(2, 244, 247, ""); \
                                    if (t == 3) PUTK(3, 248, 251, "s_nop 2\n\t"); if (t == 4) PUTK(4, 252, 255, "s_nop 0\n\t"); } \
    else asm volatile("ds_write2st64_b32 %0, %1, %2 offset0:%3 offset1:%4" :: "v"(wo), "v"(d[t][0] + d[t][2]), "v"(d[t][1] + d[t][3]), "i"((t) * 10), "i"((t) * 10 + 5) : "memory"); } while (0)
#define APS 18
#define ONOP 7
  int b3 = 0, dur = DO0 * 64;
#pragma unroll 1
  for (int s = 0; s < 63; s++) {
    f4 d[5];
    int gb[4], pb = mo ^ ((b3 == 2 ? 0 : b3 + 1) * ms);
#pragma unroll
    for (int i = 0; i < 4; i++) { gb[i] = X_O(b3) ^ xp ^ (tq * 131072 + (((i & 1) ^ r1) * 2 + (wid >> 2)) * 32768 + (wid & 3) * 1024 + (i < 2 ? X_HS : 0)); asm volatile("" : "+s"(gb[i])); }
    asm volatile("" : "+s"(pb));
    u32x4 g[4];
    u32 bad;
    auto mult = [&]() __attribute__((always_inline)) {
      CFENCE();
#pragma unroll
      for (int i = 0; i < 4; i++) {
        g[i] = __builtin_amdgcn_raw_buffer_load_b128(rsX, go, gb[i], 16);
        SB0();
        if (ONOP >= 0 && i < 3) asm volatile("s_nop %0" :: "i"(ONOP >= 0 ? ONOP : 0));
        SB0();
      }
      bad = 0u;
#pragma unroll
      for (int i = 0; i < 4; i++) {
        SB0();
#pragma unroll
        for (int t = 0; t < 5; t++) {
          d[t] = SMF(g[i], w[i][t], d[t], sidx);
          if (t == 0) bad = umax(umax(bad, g[i][0]), g[i][2]);
          if (i == 3 && t >= 2) { SB0(); PUTP(t - 2); SB0(); }
        }
      }
      SB0();
#pragma unroll
      for (int t = 3; t < 5; t++) PUTP(t);
      SB0();
    };
#pragma unroll
    for (int t = 0; t < 5; t++) d[t] = zero4();
    SB0();
    u32 dl = 0;
    ALIGN(OAL);
    if (wid == 7) { __builtin_amdgcn_s_sleep(APS); STAMP(4); AD_GET(false, K1 + KXT(KA), SPIN_CAP); STAMP(5); AD_SPIN(); }
    STAMP(15);
    BARN();
    STAMP(0);
    mult();
    if (__builtin_expect(__any(bad == ~0u), 0)) {
      int it = 0;
      do {
#pragma unroll
        for (int t = 0; t < 5; t++) d[t] = zero4();
        mult();
      } while (__any(bad == ~0u) && ++it < SPIN_CAP);
    }
    STAMP(1);
    BARC();
    STAMP(2);
#define OALL 1
    f4 t0, t1;
    if (OALL) { t0 = *(const f4*)(smem + ro); t1 = *(const f4*)(smem + ro1); SB0(); }
    if (__builtin_expect(wid < 5, 1)) {
      if (!OALL) { t0 = *(const f4*)(smem + ro); t1 = *(const f4*)(smem + ro1); }
      if (PKA & 16) {
#define QP2(q) "v_add_f32_dpp v228, v228, v228 quad_perm:" q " row_mask:0xf bank_mask:0xf\n\tv_add_f32_dpp v229, v229, v229 quad_perm:" q " row_mask:0xf bank_mask:0xf\n\t"
        asm volatile(PKADD(228, 228, 230) PKADD(232, 232, 234) PKADD(228, 228, 232) "s_nop 1\n\t"
                     QP2("[1,0,3,2]") "s_nop 0\n\t" QP2("[2,3,0,1]") CV2("v228", 228) "v_min_u32 v228, -2, v228\n\tbuffer_store_dword v228, %2, %3, %4 offen sc1"
                     : "+{v[228:231]}"(t0), "+{v[232:235]}"(t1) : "v"(po), "s"(rsX), "s"(pb) : "memory");
      } else {
      float a = (t0[0] + t0[2]) + (t1[0] + t1[2]), b = (t0[1] + t0[3]) + (t1[1] + t1[3]);
      a += __uint_as_float(__builtin_amdgcn_mov_dpp(__float_as_uint(a), 0xB1, 0xf, 0xf, true));
      b += __uint_as_float(__builtin_amdgcn_mov_dpp(__float_as_uint(b), 0xB1, 0xf, 0xf, true));
      a += __uint_as_float(__builtin_amdgcn_mov_dpp(__float_as_uint(a), 0x4E, 0xf, 0xf, true));
      b += __uint_as_float(__builtin_amdgcn_mov_dpp(__float_as_uint(b), 0x4E, 0xf, 0xf, true));
      __builtin_amdgcn_raw_buffer_store_b32(umin(pk2(a, b), 0xFFFFFFFEu), rsX, po, pb, 16);
      }
      SB0();
      STAMP(3);
      __builtin_amdgcn_s_sleep(4);
      __builtin_amdgcn_raw_buffer_store_b32(~0u, rsX, po, mo ^ (b3 * ms), 16);
    } else if (wid == 7) {
      dur += (min((int)(((u32)wall_clock64() << 6) - dl), dur + DOCAP * 64) - dur) >> DSH;
      if (DRL) __builtin_amdgcn_s_sleep(DRL);
      __builtin_amdgcn_raw_buffer_store_b32((u32)dur, rsX, 0, ANX(~s & 1) + (32 + c) * ANS, 16);
      if (CSL && c == CSC && s > 0) { if (CSL > DRL) __builtin_amdgcn_s_sleep(CSL > DRL ? CSL - DRL : 0); __builtin_amdgcn_raw_buffer_store_b32(dl | 1u, rsX, 0, (X_HOT(xp) ^ X_CS) + (s - 1) * 64, 16); }
    }
    VMW0C();
    YEND();
    b3 = b3 == 2 ? 0 : b3 + 1;
  }
  __syncthreads();
  return xp;
}

__device__ __forceinline__ void wait_ge(const u32* w, u32 val, const int wv) {
  if (TIDX == 0) {
    int it = 0;
    while (ld4_sc1(w) < val && ++it < SPIN_CAP) __builtin_amdgcn_s_sleep(WPS);
  }
}
__device__ __forceinline__ void acquire_all(const int wv) {
  if (TIDX == 0) {
    __builtin_amdgcn_fence(__ATOMIC_ACQUIRE, "agent");
    VMW0();
  }
  __syncthreads();
}

#define T_KV 0
#define T_Q 0
#define T_A 192
#define T_W (T_A + 64)
#define T_S (T_W + 256)
#define T_L (T_S + 72)
#define T_O T_L
#define T_CHAIN (1 << 20)
#define CODE_KB 84
#define OWARM 2
#define BXW 1
#define AORD 0x01234567u
#define OORD 0x67014523u

__device__ __forceinline__ int next_task(const Params& p, int* s_task, const int wv, const int pt, const int pre) {
  __syncthreads();
  if (wv == 0) {
    int t = T_A + pre;
    if (__builtin_amdgcn_readfirstlane(pre) < 0) {
      t = -1;
      int xp = 0;
      for (int it = 0; it < SPIN_CAP; it++) {
        const int l = TIDX & 15;
        const u32 v = ld4_sc1(WSP(u32, 0) + (l < 8 ? SY_JOB(l) : l == 8 ? SY_ARR : SY_INIT(0)));
        const u32 open = (u32)__ballot(v != ~0u) & 255u;
        xp = __builtin_amdgcn_readlane(v, 9);
        if (!open && xp) break;
        if (!open || __builtin_amdgcn_readlane(v, 8) < 256u) { BEAT_ON(it); __builtin_amdgcn_s_sleep(8); continue; }
        const int x = __builtin_ctz(open), k = __builtin_ctz(~(u32)__builtin_amdgcn_readlane(v, x));
        u32 old = 0u;
        if (TIDX == 0) old = AOR(SY_JOB(x), 1u << k);
        if (!((__builtin_amdgcn_readfirstlane(old) >> k) & 1)) { t = x * 32 + k + (x < 2 ? T_CHAIN : -64); break; }
      }
      if (t < 0 && TIDX == 0) t = T_A + (int)__hip_atomic_fetch_add(HOTP(xp) + SY_QHEAD, 1u, __ATOMIC_RELAXED, __HIP_MEMORY_SCOPE_AGENT) + (xp & 0x7000);
    }
    if (TIDX == 0) *s_task = t;
  }
  __syncthreads();
  return __builtin_amdgcn_readfirstlane(*s_task);
}

__device__ __forceinline__ int take_task(int* s_task, const int pre, const int wv, const int pb, const int pl, const int pt) {
  if (TIDX == 0) {
    *s_task = pre;
  }
  __syncthreads();
  const int t = __builtin_amdgcn_readfirstlane(*s_task);
  __syncthreads();
  return t;
}
#if defined(__HIP_DEVICE_COMPILE__) && !defined(KEEP_PK)
#define NOPK __attribute__((target("no-packed-fp32-ops")))
#else
#define NOPK
#endif
__device__ __forceinline__ void mega_body(const Params& p0, char* smem) {
  int* const s_task = (int*)(smem + LDS_TOTAL);
  int wv = __builtin_amdgcn_readfirstlane(threadIdx.x >> 6), t, xs;
#define tid TIDX
  {
    const Params& p = p0;
    u64 pc0; asm volatile("s_getpc_b64 %0" : "=s"(pc0));
#define ZB 1
    const int xcc = (int)(__builtin_amdgcn_s_getreg(20 | (3 << 11)) & 7u), xq = xcc >= 2 ? xcc - 2 : xcc, idx = (blockIdx.x >> 3) & 31;
    t = xq * 32 + (xcc < 2 ? T_CHAIN + idx : (idx + 31) & 31);
    xs = xcc + 8;
#define WARM(ptr, bytes, vo, so, sc) asm volatile("s_mov_b32 m0, %3\n\ts_nop 0\n\tbuffer_load_dword %0, %1, %2 offen" sc " lds" \
                                                   :: "v"(vo), "s"(RSRC(ptr, bytes)), "s"(so), "s"(C_B0 + wv * 256) : "memory", "m0")
    if (wv == 0) WARM(p.ws, O_QA, 0, SY_BEAT(t & 7) * 4, " sc1");
    if (blockIdx.x == ZB) {
#pragma unroll
      for (int i = 0; i < 4; i++) __builtin_amdgcn_raw_buffer_store_b128((u32x4){0u, 0u, 0u, 0u}, RSRC(p.ws, SY_WORDS * 4), tid * 16 + i * 8192, 0, 16);
      VMW0();
      BARN();
      if (tid < 8) AADD(SY_BEAT(tid), 1u);
    }
    if (wv) {
      const int g = idx * 7 + wv - 1, vo = (tid & 63) * 64;
      if (g < CODE_KB / 4) WARM(pc0, CODE_KB * 1024, vo, g * 4096, "");
      if (xcc > 1) {
        if (OWARM == 1 && g >= 32) WARM(p.BWo, WOB, vo, (xq * 192 + g - 32) * 4096, "");
        if (OWARM == 2 && g >= 128) WARM(p.BWo, WOB, vo * 2, (xq * 96 + g - 128) * 8192, "");
        if (BXW) WARM(p.Bx, 256 * 1152, vo, (223 - g) * 4096, "");
      }
    }
  }
  u64 pp = (u64)__builtin_amdgcn_kernarg_segment_ptr();
  SB0();
  asm volatile(".Lmega_again:" : "+{s[40:41]}"(pp), "+{s42}"(wv), "+{s43}"(t), "+{s44}"(xs) :: "memory");
  SB0();
  const Params& p = *(const Params*)(const __attribute__((address_space(4))) Params*)pp;
  const int xcc = xs & 7, xq = xcc >= 2 ? xcc - 2 : xcc;
  const bool spec = xs > 7;
  int pre = -1;
  if (t < T_A) {
    const int x = t >> 5, j = (t & 7) * 4 + ((0x9C >> ((t >> 2) & 6)) & 3);
    proj_slot(p, x, j, smem, wv, pre, spec ? t + 64 : -1);
    VMW0C();
  } else {
    int own;
    if (t < T_CHAIN + 32) { own = chain_head(p, smem, t - T_CHAIN, wv, spec); VMW0C(); }
    else { own = chain_out(p, smem, t - T_CHAIN - 32, wv, spec); VMW0C(); asm volatile("s_nop 0"); }
    if (own >= 0) {
      const int l = tid & 7;
      const u32 v = ld4_sc1(HOTP(own) + (l < 7 ? SY_OQ(l) : SY_QHEAD));
      if (tid == 0) *s_task = 0;
      __syncthreads();
      if (v < (l == 7 ? T_S - T_A : l == 6 ? 72 : (l & 1) ? NOB : NOA)) *s_task = 1;
      __syncthreads();
      if (__builtin_amdgcn_readfirstlane(*s_task) == 0) {
        if (xcc == 0) TLS(3); else TLS(4);
        return;
      }
    }
  }
  t = next_task(p, s_task, wv, t, pre);
  if (t < T_A || t >= T_CHAIN) {
    asm volatile("s_waitcnt vmcnt(0) lgkmcnt(0)\n\ts_barrier\n\ts_branch .Lmega_again" :: "{s[40:41]}"(pp), "{s42}"(wv), "{s43}"(t), "{s44}"(xcc) : "memory");
    return;
  }
  u32* const hot = HOTP(t);
  u32* const qh = hot + SY_QHEAD;
  t &= ~0x7000;
#define WPOS 0
  while (t < T_S) {
    if ((u32)(t - T_A - WPOS) < 64u) {
      wof_task(p, t - T_A - WPOS, smem, wv, qh, pre);
      if (tid == 0) __hip_atomic_fetch_add(hot + SY_CW, 1u, __ATOMIC_RELAXED, __HIP_MEMORY_SCOPE_AGENT);
      t = T_A + take_task(s_task, pre, wv, T_A, T_S - T_A, t);
      continue;
    }
    const int qb = (AORD >> (((t - (t < T_A + WPOS ? T_A : T_W)) >> 5) * 4)) & 7, h = t & 31;
    if (tid < 64) {
      int it = 0;
      while (true) {
        const u32 f0 = ld4_sc1(WSP(u32, 0) + SY_BLK(3, h >> 1, tid)), f1 = ld4_sc1(WSP(u32, 0) + SY_BLK(4, h >> 1, tid)), f2 = ld4_sc1(WSP(u32, 0) + SY_BLK(5, h >> 1, tid));
        if (__all(tid >= qb * 8 + 8 || ((tid < qb * 8 || f0 >= FULL) && f1 >= FULL && f2 >= FULL)) || ++it >= SPIN_CAP) break;
        __builtin_amdgcn_s_sleep(8);
      }
    }
    acquire_all(wv);
    fa::attn_block(WSP(char, O_QA) + FR_OFF(qb * 256, h * 128), WSP(char, O_KA) + h * 8192, WSP(char, O_VA) + h * 8192,
                   WSP(char, O_CA) + OB_OFF(qb * 256, h * 128), qb * 256, smem, wv, qh, pre);
    if (tid == 0) __hip_atomic_fetch_add(hot + SY_CC(qb), 1u, __ATOMIC_RELAXED, __HIP_MEMORY_SCOPE_AGENT);
    t = T_A + take_task(s_task, pre, wv, T_A, T_S - T_A, t);
  }
  int rest = 64 | (1 << xq);
#pragma unroll 1
  while (rest) {
    const int x = __builtin_ctz(rest);
    rest &= rest - 1;
    if (x == 6) {
      CLAIMS();
      t = take_task(s_task, pre, wv, T_S, 72, -1);
      while ((t & 0xFFFF) < 72) {
        t &= 0xFFFF;
        small_out_task(p, t & 3, t >> 2, smem, wv, hot, pre);
        t = take_task(s_task, pre, wv, T_S, 72, T_S + t);
      }
      rest = t >> 16;
      continue;
    }
    const int part = x & 1, cg = x >> 1, n = part ? NOB : NOA;
    u32* const qh = hot + SY_OQ(x);
    CLAIM();
    t = take_task(s_task, pre, wv, T_O + x * 32, n, -1);
    while (t < n) {
      const int nb = part ? NBB : NAB, big = t < nb, e = big ? t * 2 : t + nb;
      const int rb = part ? e : ((OORD >> ((e >> 2) * 4)) & 7) * 4 + (e & 3);
      if (tid == 0) {
        const u32* w = part ? CSP((rb + big) * 2 + 1) : hot + SY_CC(rb >> 2);
        int it = 0;
        while ((ld4_sc1(w) < (part ? 1u : 32u) || ld4_sc1(hot + SY_CW) < 64u) && ++it < SPIN_CAP) __builtin_amdgcn_s_sleep(WPS);
      }
      acquire_all(wv);
      const char *pa = WSP(char, part ? O_OB : O_CA) + rb * 2 * 262144, *pw = WSP(char, O_WOF) + part * WOB + cg * 12 * 131072;
      u16* const pc = p.out + (size_t)rb * 64 * 1152 + part * 576 + cg * 192;
      if (big) out_tile<8>(pa, pw, pc, smem, wv, qh, pre);
      else out_tile<4>(pa, pw, pc, smem, wv, qh, pre);
      t = take_task(s_task, pre, wv, T_O + x * 32, n, T_O + x * 32 + t);
    }
  }
  if (xcc == 0) TLS(3); else if (xcc == 1) TLS(4); else TLS(5);
}

#undef tid
__global__ NOPK void __launch_bounds__(512, 2) mega(Params p) {
  __shared__ __attribute__((aligned(16))) char smem[LDS_ALL];
  mega_body(p, smem);
}
extern "C" void kernel_launch(void* const* d_in, const int* in_sizes, int n_in, void* d_out, int out_size, void* d_ws,
                              size_t ws_size, hipStream_t stream) {
  Params p;
  p.Ax = (const u16*)d_in[0]; p.Ac = (const u16*)d_in[1]; p.Awq = (const u16*)d_in[2]; p.Awk = (const u16*)d_in[3];
  p.Awv = (const u16*)d_in[4]; p.Awo = (const u16*)d_in[5]; p.Bx = (const u16*)d_in[6]; p.Bm = (const u16*)d_in[7];
  p.BWq = (const u16*)d_in[8]; p.BWk = (const u16*)d_in[9]; p.BWv = (const u16*)d_in[10]; p.BWo = (const u16*)d_in[11];
  p.out = (u16*)d_out;
  p.ws = (char*)d_ws;
  mega<<<256, 512, 0, stream>>>(p);
}
```

```cpp
#include <hip/hip_runtime.h>
#include <hip/hip_bf16.h>
#include <stdint.h>
#include <type_traits>

typedef unsigned short u16;
typedef unsigned int u32;
typedef unsigned long long u64;
typedef __attribute__((ext_vector_type(8))) short bf8;
typedef __attribute__((ext_vector_type(4))) short bf4;
typedef __attribute__((ext_vector_type(4))) float f4;
typedef __attribute__((ext_vector_type(2))) float f2v;
typedef __attribute__((ext_vector_type(2))) __bf16 bf2v;
typedef __attribute__((ext_vector_type(4))) unsigned u32x4;
typedef __attribute__((ext_vector_type(2))) unsigned u32x2;
typedef float f32x16 __attribute__((ext_vector_type(16)));
typedef __attribute__((ext_vector_type(8))) unsigned u32x8;
typedef __attribute__((ext_vector_type(8))) __bf16 bh8;
typedef __attribute__((ext_vector_type(16))) __bf16 bh16;
#define LDSP __attribute__((address_space(3)))

__device__ __forceinline__ u32 pk2(float a, float b) {
  f2v x = {a, b};
  bf2v y = __builtin_convertvector(x, bf2v);
  return __builtin_bit_cast(u32, y);
}
__device__ __forceinline__ u32 umin(u32 a, u32 b) { return a < b ? a : b; }
__device__ __forceinline__ u32 umax(u32 a, u32 b) { return a > b ? a : b; }
__device__ __forceinline__ float rbf(float f) { return __uint_as_float(pk2(f, f) << 16); }
#define MFMA(a, b, c) __builtin_amdgcn_mfma_f32_16x16x32_bf16(a, b, c, 0, 0, 0)
#define MFMA16(a, b, c) __builtin_amdgcn_mfma_f32_16x16x16bf16_1k(a, b, c, 0, 0, 0)
#define SMF(a, b, c, idx) __builtin_amdgcn_smfmac_f32_16x16x64_bf16(__builtin_bit_cast(bh8, a), __builtin_bit_cast(bh16, b), c, idx, 0, 0)
__device__ __forceinline__ u32x8 ilv(bf4 a, bf4 b, bf4 c, bf4 d) {
  const u32x2 x = __builtin_bit_cast(u32x2, a), y = __builtin_bit_cast(u32x2, b), z = __builtin_bit_cast(u32x2, c), w = __builtin_bit_cast(u32x2, d);
  return (u32x8){x[0], x[1], y[0], y[1], z[0], z[1], w[0], w[1]};
}
__device__ __forceinline__ u32x8 cat16(bf8 a, bf8 b) {
  const u32x4 x = __builtin_bit_cast(u32x4, a), y = __builtin_bit_cast(u32x4, b);
  return (u32x8){x[0], x[1], x[2], x[3], y[0], y[1], y[2], y[3]};
}
__device__ __forceinline__ float swadd(float a, float b) {
  auto x = __builtin_amdgcn_permlane32_swap(__float_as_uint(a), __float_as_uint(b), false, false);
  return __uint_as_float(x[0]) + __uint_as_float(x[1]);
}
__device__ __forceinline__ bf4 trrd(const char* p) {
  return __builtin_amdgcn_ds_read_tr16_b64_v4i16((LDSP bf4*)(p));
}
__device__ __forceinline__ bf8 cat8(bf4 a, bf4 b) { return __builtin_shufflevector(a, b, 0, 1, 2, 3, 4, 5, 6, 7); }
__device__ __forceinline__ void st8_sc1(void* p, u64 v) { __hip_atomic_store((u64*)p, v, __ATOMIC_RELAXED, __HIP_MEMORY_SCOPE_AGENT); }
__device__ __forceinline__ u32 ld4_sc1(const u32* p) { return __hip_atomic_load(p, __ATOMIC_RELAXED, __HIP_MEMORY_SCOPE_AGENT); }
__device__ __forceinline__ void st4_sc1(u32* p, u32 v) { __hip_atomic_store(p, v, __ATOMIC_RELAXED, __HIP_MEMORY_SCOPE_AGENT); }
#define VMW0() asm volatile("s_waitcnt vmcnt(0)" ::: "memory")
#define CFENCE() asm volatile("" ::: "memory")
#define VMW0C() do { __builtin_amdgcn_s_waitcnt(0x0F70); asm volatile("" ::: "memory"); } while (0)
#define SPIN_CAP (1 << 22)
#define XPS 4
#define SPS 4
#define WPS 8
__device__ __forceinline__ int tid_now(int wv) {
  int l; asm volatile("v_mbcnt_lo_u32_b32 %0, -1, 0\n\tv_mbcnt_hi_u32_b32 %0, -1, %0" : "=v"(l));
  return wv * 64 + l;
}
#define TIDX tid_now(wv)
#define CLAIM() do { if (TIDX == 0) pre = (int)__hip_atomic_fetch_add(qh, 1u, __ATOMIC_RELAXED, __HIP_MEMORY_SCOPE_AGENT); } while (0)

#define TLS(i)
struct Params {
  const u16 *Ax, *Ac, *Awq, *Awk, *Awv, *Awo, *Bx, *Bm, *BWq, *BWk, *BWv, *BWo;
  u16* out;
  char* ws;
};
#define SY_QHEAD 0
#define SY_CC(rb) (64 + (rb) * 16)
#define SY_CW (64 + 8 * 16)
#define SY_OQ(x) (64 + 9 * 16 + (x) * 16)
#define SY_B0 (64 + 80 * 16)
#define SY_INIT(c) (SY_B0 + 64 + (c))
#define SY_VOTE(i) (SY_B0 + 128 + (i))
#define SY_JOB(x) (SY_B0 + 256 + (x) * 16)
#define SY_ARR (SY_B0 + 384)
#define SY_WX (SY_ARR + 32)
#define SY_BLK(mi, cb, g) (SY_ARR + 64 + ((mi) * 16 + (cb)) * 64 + (g))
#define FULL 12u
#define SY_WORDS (SY_ARR + 64 + 6 * 16 * 64)
#define SY_BEAT(r) (SY_WORDS + (r) * 32)

#define ACB (2048 * 4096 * 2)
#define O_PAD 0
#define O_QA (SY_BEAT(8) * 4 + O_PAD)
#define O_KA (O_QA + ACB)
#define O_VA (O_KA + ACB)
#define O_CA (O_VA + ACB)
#define O_QB (O_CA + ACB)
#define O_KB (O_QB + ACB)
#define O_VB (O_KB + ACB)
#define O_OB (O_VB + ACB)
#define O_WOF (O_OB + ACB)
#define WOB (576 * 4096 * 2)
#define O_WX (O_WOF + 2 * WOB)
#define O_XW (O_WX + 524288)
#define X_HS 0x4000
#define X_O(b) ((b) * 0x80000 + ((0x140 >> ((b) * 4)) & 15) * 4096)
#define X_MS X_HS
#define X_M (0x185000 ^ X_MS)
#define X_ANN (0x185000 + 7 * 32768)
#define X_CAL 0x40000
#define HFL 0x2000
#define CFL 0x1000
#define X_HOT(xp) ((0xC4000 ^ HFL) ^ ((xp) & 0x7000))
#define X_CS (HFL ^ CFL ^ 0x8000)
#define CSP(s) ((const u32*)(const GLB u32*)((uintptr_t)hot ^ X_CS) + (s) * 16)
#define X_END 0x200000
#define OB_OFF(r, c) (((r) >> 5) * 262144 + ((c) >> 4) * 1024 + ((r) & 31) * 32 + ((c) & 15) * 2)
#define FR_OFF(r, c) (((r) >> 5) * 262144 + ((c) >> 3) * 512 + ((r) & 31) * 16 + ((c) & 7) * 2)
#define GLB __attribute__((address_space(1)))
#define WSP(T, off) ((T*)((GLB char*)p.ws + (off)))
#define XWIN(p) (((uintptr_t)(p).ws + O_XW + 0x1FFFFFu) & ~(uintptr_t)0x1FFFFFu)

#define HOTP(xp) ((u32*)(GLB u32*)(XWIN(p) + X_HOT(xp)))

#define XS0 48
#define NAB 8
#define NBB 10
#define NOA (32 - NAB)
#define NOB (XS0 / 2 - NBB)
#define PEEK 0
#define OXIT 1
#define LDS_TOTAL (131072 + 8192 + 16384)
#define LDS_ALL 163840

#define SB0() __builtin_amdgcn_sched_barrier(0)
#define PJ_OOB 0x7F000000
#define RSRC(ptr, bytes) __builtin_amdgcn_make_buffer_rsrc((void*)(ptr), 0, (bytes), 0x00020000)
__device__ __forceinline__ bf8 ldsc1(__amdgpu_buffer_rsrc_t r, int off) {
  return __builtin_bit_cast(bf8, __builtin_amdgcn_raw_buffer_load_b128(r, off, 0, 16));
}
__device__ __forceinline__ u32x4 pair16(u32 a0, u32 a1, u32 b0, u32 b1) {
  auto r0 = __builtin_amdgcn_permlane16_swap(a0, b0, false, false);
  auto r1 = __builtin_amdgcn_permlane16_swap(a1, b1, false, false);
  return (u32x4){r0[0], r1[0], r0[1], r1[1]};
}
__device__ __forceinline__ int fsw(int k) { return (k & 3) | (((k >> 3) & 1) << 2); }

#define AADD(w, v) __hip_atomic_fetch_add(WSP(u32, 0) + (w), v, __ATOMIC_RELAXED, __HIP_MEMORY_SCOPE_AGENT)
#define AOR(w, v) __hip_atomic_fetch_or(WSP(u32, 0) + (w), v, __ATOMIC_RELAXED, __HIP_MEMORY_SCOPE_AGENT)
#define C_B0 (LDS_ALL - 2048)
#define BEAT_LD(job) ld4_sc1(WSP(u32, 0) + SY_BEAT((job) & 7))
#define BEAT_ON(it) do { if (((it) & 31) == 31 && (TIDX & 63) < 8) AADD(SY_BEAT(TIDX & 63), 1u); } while (0)
__device__ __forceinline__ u32 confirm(const Params& p, const char* smem, const int job, u32 b1, const int wv) {
  asm volatile("" :: "v"(b1) : "memory");
  const u32 b0 = *(const u32*)(smem + C_B0);
  if (__builtin_expect(__any(b1 == b0), 0)) {
    int it = 0;
    while (__any(BEAT_LD(job) == b0) && ++it < SPIN_CAP) __builtin_amdgcn_s_sleep(2);
  }
  u32 old = 0u;
  if (TIDX == 0) old = AOR(SY_JOB(job >> 5) + TIDX, 1u << (job & 31));
  if (TIDX < 2) AADD(TIDX ? SY_BEAT(job & 7) : SY_ARR, 1u);
  return old;
}

typedef __attribute__((ext_vector_type(2))) unsigned long long u64x2;
__device__ __forceinline__ f4 zero4() {
  u64 a, b; asm volatile("v_mov_b64 %0, 0\n\tv_mov_b64 %1, 0\n\ts_nop 1" : "=v"(a), "=v"(b));
  return __builtin_bit_cast(f4, (u64x2){a, b});
}
__device__ __forceinline__ u32x4 ones4() {
  u64 a, b; asm volatile("v_mov_b64 %0, -1\n\tv_mov_b64 %1, -1" : "=v"(a), "=v"(b));
  return __builtin_bit_cast(u32x4, (u64x2){a, b});
}
#define C_PARK 131072
#define PJ_PAD 0
__device__ __forceinline__ void proj_slot(const Params& p, int x, int j, char* smem, const int wv, int& pre, int job) {
  const int tid = TIDX, lane = tid & 63, wid = wv;
  const int l15 = lane & 15, lq = lane >> 4, q4 = l15 >> 2, p4 = lane & 3, wr = wid >> 2, wc = wid & 3;
  const __amdgpu_buffer_rsrc_t rc = RSRC(p.ws, O_XW);
  const int vB = ((wid * 4 + lq) * 4096 + (((l15 >> 1) ^ (lq | (((wid >> 1) & 1) << 2))) * 16 + (lane & 1) * 8)) * 2, vB1 = vB + 32 * 8192;
  int ao = (wr * 64 + l15) * 128 + ((lq ^ (l15 & 7)) << 4);
  int bo = (int)(uintptr_t)smem + 65536 + (lq * 8 + q4) * 256 + (((wc * 2) ^ (q4 | ((lq & 1) << 2))) << 5) + p4 * 8;
  const int vD = lane * 16;
#define PJ_ORD 0x1312
  const int ord = (j & 3) == 3 ? PJ_ORD : 0x1312;
#define PJ_NEXT ((ord >> (u * 4)) & 3)
  const int cb = (x & 1) * 8 + (j >> 2);
  __amdgpu_buffer_rsrc_t rsA, rsB;
  const u16 *pA0 = p.Bx, *pB0 = (x < 2 ? p.BWk : x < 4 ? p.BWv : p.BWq) + cb * 256;
  const u16 *pA1 = x < 2 ? p.Ax : p.Ac, *pB1 = (x < 2 ? p.Awq : x < 4 ? p.Awk : p.Awv) + cb * 256;
  asm volatile("" : "+s"(pA0), "+s"(pB0), "+s"(pA1), "+s"(pB1));
  int k2, vA, vA1, sa, sb, nvA;
#define PJ_K2(u) ((u) < 2 || x < 2 ? 1152 : 1024)
#define PJ_PREP(u) do { int ln_ = vD; asm volatile("" : "+v"(ln_)); \
    nvA = (wid * 8 + (ln_ >> 7)) * PJ_K2(u) + ((ln_ ^ (ln_ >> 3)) & 0x70); \
    asm volatile("" : "+v"(nvA)); } while (0)
#define PJ_UNIT(u) do { k2 = PJ_K2(u); rsA = RSRC((u) < 2 ? pA0 : pA1, 2048 * k2); rsB = RSRC((u) < 2 ? pB0 : pB1, k2 * 4096 - cb * 512); vA = nvA; vA1 = nvA + 64 * k2; \
    sa = ((j & 3) + ((u) & 1) * 4) * 256 * k2 - 128; sb = 0; } while (0)
#define PJ_DEST(u, rt) do { const int mi_ = ((u) >> 1) * 3 + (x >> 1), g_ = ((rt) + ((u) & 1) * 4) * 8; \
    dco = O_QA + (mi_ == 2 ? 4 : (mi_ + 5) & 7) * ACB + cb * 16384 + wc * 2048 + (g_ + wr * 2) * 262144; \
    dfl = SY_BLK(mi_, cb, g_) * 4; } while (0)
#define PJ_STAGE(dst, rs, v0, v1, so) do { const int so_ = (so); \
    __builtin_amdgcn_raw_ptr_buffer_load_lds(rs, (LDSP void*)(smem + (dst) + wid * 1024), 16, v0, so_, 0, 0); \
    __builtin_amdgcn_raw_ptr_buffer_load_lds(rs, (LDSP void*)(smem + (dst) + wid * 1024 + 8192), 16, v1, so_, 0, 0); } while (0)
#define PJ_SA(dst, so) PJ_STAGE(dst, rsA, vA, vA1, so)
#define PJ_SB(dst, so) PJ_STAGE(dst, rsB, vB, vB1, so)
#define PJ_LDA(off) do { _Pragma("unroll") for (int m = 0; m < 4; m++) _Pragma("unroll") for (int k = 0; k < 2; k++) \
    At[m][k] = *(const bf8*)(smem + (ao ^ (k * 64)) + (off) + m * 2048); } while (0)
#define PJ_TR(dst, a, off) asm volatile("ds_read_b64_tr_b16 %0, %1 offset:%2" : "=&v"(dst) : "v"(a), "i"(off) : "memory")
#define PJ_LDB(D, h) do { const int b1_ = bo ^ 32; _Pragma("unroll") for (int n = 0; n < 2; n++) _Pragma("unroll") for (int k = 0; k < 2; k++) { \
    bf4 lo_, hi_; PJ_TR(lo_, n ? b1_ : bo, (h) * 16384 + k * 8192); PJ_TR(hi_, n ? b1_ : bo, (h) * 16384 + k * 8192 + 1024); D[n][k] = cat8(lo_, hi_); } } while (0)
#define PJ_CV(ai, bj, q) ((u32x4){pk2(acc[ai][bj][(q) & 2][(q) & 1][0], acc[ai][bj][(q) & 2][(q) & 1][1]), pk2(acc[ai][bj][(q) & 2][(q) & 1][2], acc[ai][bj][(q) & 2][(q) & 1][3]), \
                                  pk2(acc[ai][bj][((q) & 2) + 1][(q) & 1][0], acc[ai][bj][((q) & 2) + 1][(q) & 1][1]), pk2(acc[ai][bj][((q) & 2) + 1][(q) & 1][2], acc[ai][bj][((q) & 2) + 1][(q) & 1][3])})
#define PJ_SW(v) pair16(v[0], v[1], v[2], v[3])
#define PJ_MMA(Z, ai, bj, D, F) do { PJ_SCHED; __builtin_amdgcn_s_setprio(1); \
    _Pragma("unroll") for (int k = 0; k < 2; k++) _Pragma("unroll") for (int m = 0; m < 4; m++) _Pragma("unroll") for (int n = 0; n < 2; n++) { \
      if (k == 0 && (Z)) asm volatile("v_mfma_f32_16x16x32_bf16 %0, %1, %2, 0" : "+v"(acc[ai][bj][m][n]) : "v"(D[n][k]), "v"(At[m][k])); \
      else asm volatile("v_mfma_f32_16x16x32_bf16 %0, %1, %2, %0" : "+v"(acc[ai][bj][m][n]) : "v"(D[n][k]), "v"(At[m][k])); \
      F(k * 8 + m * 2 + n); } \
    __builtin_amdgcn_s_setprio(0); if (PJ_PAD) __builtin_amdgcn_s_sleep(PJ_PAD); PJ_SCHED; } while (0)
#define PJ_SWI(a, b) asm volatile("v_permlane16_swap_b32 %0, %1" : "+v"(a), "+v"(b))
#define PJ_CVI(d, s, e) asm volatile("v_cvt_pk_bf16_f32 %0, %1, %2" : "=v"(d) : "v"(s[e]), "v"(s[(e) + 1]))
#define PJ_PACK(P, ai, bj, i) do { const int q_ = (i) >> 2; \
    if (((i) & 3) < 2) { PJ_CVI(P[q_][(i) & 1], acc[ai][bj][q_ & 2][q_ & 1], ((i) & 1) * 2); PJ_CVI(P[q_][2 + ((i) & 1)], acc[ai][bj][(q_ & 2) + 1][q_ & 1], ((i) & 1) * 2); } \
    else PJ_SWI(P[q_][(i) & 1], P[q_][2 + ((i) & 1)]); } while (0)
#define PJ_WV(n) asm volatile("s_waitcnt vmcnt(" #n ")" ::: "memory")
#define PJ_WL(n) asm volatile("s_waitcnt lgkmcnt(" #n ")" ::: "memory")
#define PJ_WL0 do { __builtin_amdgcn_s_waitcnt(0xC07F); asm volatile("" ::: "memory"); } while (0)
#define PJ_BAR __builtin_amdgcn_s_barrier()
#define PJ_SCHED __builtin_amdgcn_sched_barrier(0)
#define PJ_NOP do { PJ_SCHED; asm volatile("s_nop 7"); PJ_SCHED; } while (0)
#define PJ_DS(v, ai, bj, q) do { PJ_SCHED; __builtin_amdgcn_raw_buffer_store_b128(v, rc, vD + ((q) & 1) * 1024, dco + ((ai) * 4 + ((q) >> 1)) * 262144 + (bj) * 8192, 16); \
    asm volatile("s_nop 1"); PJ_SCHED; } while (0)
#define PJ_PARK(q) (*(u32x4*)(smem + C_PARK + wid * 4096 + vD + (q) * 1024))
#define PJ_PUB(ai, n) do { if (wid == 4) __builtin_amdgcn_raw_buffer_store_b32(FULL, rc, vD < (n) * 16 ? vD >> 2 : PJ_OOB, dfl + (ai) * 16, 16); } while (0)
#define PJ_ST()
#define PJ_TOUCH(ptr, bytes, vo, so) __builtin_amdgcn_raw_ptr_buffer_load_lds(RSRC(ptr, bytes), (LDSP void*)(smem + C_PARK + wid * 4096), 4, vo, so, 0, 0)
#define PJ_SL 0x1E
#define PJ_PT 0
  static_assert(XS0 >= 46 && PJ_SL == 0x1E && PJ_PT == 0, "");
  int emp = 128;
  u32 old = 0u;
#define PJ_WARM(k) do { \
    if (u == 0 && (j & 3) != 3) { \
      int vt_ = vD; asm volatile("" : "+v"(vt_)); vt_ <<= 3; \
      const int i_ = (((x & 1) * 8 + (j >> 2)) * 2 + (j & 1)) * 8 + wid + (k) * 256; \
      const int nw_ = x < 2 ? 576 : 512, na_ = x < 2 ? 288 : x < 4 ? 256 : 144, no_ = (x >> 1) * 96 + 96; \
      const u64 w_ = (u64)(pB1 - cb * 256), a_ = x < 4 ? (u64)pA1 : (u64)(pA0 + 1024 * 576), o_ = (u64)p.Awo + (x < 2 ? 0 : x < 4 ? 96 : 288) * 8192; \
      PJ_TOUCH(i_ < nw_ ? w_ : i_ < nw_ + na_ ? a_ : o_, i_ < nw_ + na_ ? WOB : no_ * 8192, vt_, (i_ < nw_ ? i_ : i_ < nw_ + na_ ? i_ - nw_ : i_ - nw_ - na_) * 8192); \
    } else { \
      const int e_ = emp + (u * 4 + (k)) * 3; \
      __builtin_amdgcn_raw_buffer_store_b128(ones4(), rc, vD, e_ < 128 ? O_OB + (e_ >> 1) * 262144 + cb * 16384 + (e_ & 1) * 8192 + wid * 1024 : PJ_OOB, 16); \
    } } while (0)
#define PJ_F0(i)
#define PJ_F1(i) do { if (S == 8) PJ_PACK(pa, 0, 0, i); } while (0)
#define PJ_F2(i) do { if (S == 8) PJ_PACK(pb, 0, 1, i); if (S == 0) PJ_PACK(pb, 1, 1, i); } while (0)
#define PJ_F3(i) do { if (S == 8) PJ_PACK(pa, 1, 0, i); if (S == 5 && (i) == 0) { PJ_SCHED; PJ_PREP(PJ_NEXT); PJ_SCHED; } } while (0)
#define PJ_STEP(SS) do { constexpr int S = SS; PJ_ST(); \
    PJ_LDB(B0, 0); PJ_SCHED; PJ_LDA(0); PJ_SA((bsel ^ 32768) + 16384, sa + 128 * k2); \
    PJ_WL(8); PJ_BAR; PJ_WL0; PJ_MMA(S == 0, 0, 0, B0, PJ_F0); PJ_BAR; PJ_SCHED; \
    if (S == 7) PJ_UNIT(PJ_NEXT); \
    PJ_LDB(B1, 1); PJ_SB(65536 + bsel, sb); \
    if (S == 0) { PJ_DS(pb[2], 0, 1, 2); PJ_DS(pb[3], 0, 1, 3); } \
    if ((PJ_SL >> S) & 1) PJ_WARM(PJ_PT + __builtin_popcount(PJ_SL & ((1 << S) - 1))); \
    PJ_BAR; PJ_WL0; PJ_MMA(S == 0, 0, 1, B1, PJ_F1); PJ_BAR; \
    PJ_LDA(16384); sa += 128; PJ_SA(bsel, sa); \
    if (S == 8) { PJ_SCHED; PJ_DEST(u, j & 3); _Pragma("unroll") for (int q = 0; q < 4; q++) PJ_PARK(q) = pa[q]; PJ_SCHED; } \
    PJ_BAR; PJ_WL0; PJ_MMA(S == 0, 1, 0, B0, PJ_F2); PJ_BAR; PJ_SCHED; \
    { u32x4 c0_, c1_; \
      if (S < 2) { c0_ = PJ_PARK(2 * S); c1_ = PJ_PARK(2 * S + 1); PJ_SCHED; } \
      PJ_SB(65536 + bsel + 16384, sb + 256); sb += 64 * 8192; \
      if (S == 8) { PJ_DS(pb[0], 0, 1, 0); PJ_DS(pb[1], 0, 1, 1); } \
      else if (S < 2) { PJ_DS(c0_, 0, 0, (2 * S) & 3); PJ_DS(c1_, 0, 0, (2 * S + 1) & 3); } \
      else if (S < 4) { PJ_DS(pa[(2 * S) & 3], 1, 0, (2 * S) & 3); PJ_DS(pa[(2 * S + 1) & 3], 1, 0, (2 * S + 1) & 3); } \
      else if (S < 6) { PJ_DS(pb[(2 * S) & 3], 1, 1, (2 * S) & 3); PJ_DS(pb[(2 * S + 1) & 3], 1, 1, (2 * S + 1) & 3); } } \
      \
    asm volatile("s_waitcnt vmcnt(%0)" :: "i"((S == 0 ? 10 : S < 6 || S == 8 ? 8 : 6) + ((PJ_SL >> S) & 1)) : "memory"); \
    PJ_BAR; \
    if (S == 2) PJ_PUB(0, 4); \
    if (S == 7) PJ_PUB(1, 4); \
    PJ_MMA(S == 0, 1, 1, B1, PJ_F3); PJ_BAR; \
    bsel ^= 32768; ao ^= 32768; bo ^= 32768; } while (0)
  f4 acc[2][2][4][2];
  bf8 At[4][2], B0[2][2], B1[2][2];
  u32x4 pa[4], pb[4];
  int bsel = 0, dco = PJ_OOB, dfl = PJ_OOB, u = 0;
  auto quick = [&](auto NBC, const int hf, const int ch) __attribute__((always_inline)) {
    constexpr int NB = decltype(NBC)::value, NS = 5 - NB;
#define PJ_FS(t) do { PJ_SA(((t) % NS) * 16384, hf * 128 * 1152 + (t) * 128); \
      _Pragma("unroll") for (int h_ = 0; h_ < NB; h_++) PJ_SB(NS * 16384 + (((t) % NS) * NB + h_) * 16384, (t) * 64 * 8192 + (ch + h_) * 256); PJ_SCHED; } while (0)
#pragma unroll
    for (int t = 0; t < NS - 1; t++) PJ_FS(t);
    bo += NS * 16384 - 65536;
#pragma unroll
    for (int t = 0; t < 9; t++) {
      PJ_ST();
      if (t == 8) PJ_WV(0); else if (NB == 2) PJ_WV(6); else if (t == 7) PJ_WV(4); else PJ_WV(8);
      PJ_BAR; PJ_SCHED;
      if (wr && t + NS - 1 < 9) PJ_FS(t + NS - 1);
      PJ_LDB(B0, 0); PJ_LDA((t % NS) * 16384);
      if (NB == 2) PJ_LDB(B1, 1);
      PJ_SCHED;
      bo += t == 8 ? 65536 - NS * 16384 - (8 % NS) * NB * 16384 : (t % NS == NS - 1 ? -(NS - 1) * NB * 16384 : NB * 16384);
      PJ_WL0;
      PJ_MMA(t == 0, 0, 0, B0, PJ_F0);
      if (NB == 2) PJ_MMA(t == 0, 0, 1, B1, PJ_F0);
      if (!wr && t + NS - 1 < 9) PJ_FS(t + NS - 1);
    }
    PJ_NOP;
    u32 b1;
    if (NB == 1 && job >= 0 && wid == 0) b1 = BEAT_LD(job);
#pragma unroll
    for (int q = 0; q < 4 * NB; q++) { const u32x4 v = PJ_CV(0, q >> 2, q & 3); PJ_DS(PJ_SW(v), hf, ch + (q >> 2), q & 3); }
    if (NB == 2 && tid == 0) { *(u32*)(smem + C_B0 + 4) = old; PJ_WL0; }
    PJ_WV(0); PJ_BAR;
    if (NB == 1 && job >= 0 && wid == 0) old = confirm(p, smem, job, b1, wv);
    if (tid < 4) __hip_atomic_fetch_or(WSP(u32, dfl) + hf * 4 + tid, NB == 2 ? FULL : 4u << ch, __ATOMIC_RELAXED, __HIP_MEMORY_SCOPE_AGENT);
  };
  if (((j + 1) & 3) < 2) {
    PJ_PREP(0); PJ_UNIT(0); PJ_DEST(0, 0);
    quick(std::integral_constant<int, 1>{}, 0, j & 1);
    if ((j & 3) == 0) {
      quick(std::integral_constant<int, 2>{}, 1, 0);
      emp = (__builtin_amdgcn_readfirstlane(*(const u32*)(smem + C_B0 + 4)) >> (job & 31)) & 1 ? 129 : XS0 * 2 - 12 + (x >> 1);
    }
    if ((j & 3) == 0 || PJ_ORD != 0x1312) u = 2;
    dco = PJ_OOB; dfl = PJ_OOB; job = -1;
  }
#pragma unroll
  for (int a = 0; a < 16; a++) asm volatile("" : "=v"(acc[a >> 3][(a >> 2) & 1][a & 3][0]), "=v"(acc[a >> 3][(a >> 2) & 1][a & 3][1]));
#pragma unroll
  for (int a = 0; a < 4; a++) asm volatile("" : "=v"(pa[a]), "=v"(pb[a]));
  PJ_PREP(u); PJ_UNIT(u);
  const int cs = x < 4 && (j & 3) == 2 ? (x * 8 + (j >> 2)) * 128 : -1;
#pragma unroll
  for (int i = 0; i < 2; i++) pa[i] = __builtin_amdgcn_raw_buffer_load_b128(RSRC(p.BWo, WOB), (tid >> 3) * 1152 + (tid & 7) * 16, cs >= 0 ? (cs + i * 64) * 1152 + 1024 : PJ_OOB, 0);
  PJ_SCHED;
  PJ_SB(65536, 0); PJ_SA(0, sa + 128);
  PJ_SB(65536 + 16384, 256); PJ_SA(16384, sa + 128 + 128 * k2);
  PJ_SCHED;
#pragma unroll
  for (int i = 0; i < 2; i++) __builtin_amdgcn_raw_buffer_store_b128(pa[i], rc, (tid & 7) * 65536 + (tid >> 3) * 16, cs >= 0 ? O_WX + (cs + i * 64) * 16 : PJ_OOB, 16);
  PJ_SCHED;
  if (wr == 1) PJ_BAR;
  PJ_WV(6); PJ_BAR;
  u32 b1;
  if (job >= 0 && wid == 0) b1 = BEAT_LD(job);
  PJ_SCHED;
  PJ_SB(65536 + 32768, 64 * 8192); PJ_SA(32768, sa + 256);
  PJ_SB(65536 + 32768 + 16384, 64 * 8192 + 256);
  PJ_WV(6);
  if (job >= 0 && wid == 0) confirm(p, smem, job, b1, wv);
  PJ_BAR;
  if (tid == 256 && cs >= 0) AOR(SY_WX, 1u << (cs >> 7));
#pragma unroll
  for (int i = 0; i < PJ_PT; i++) PJ_WARM(i);
  sa += 256; sb = 128 * 8192;
#pragma unroll 1
  for (;; u = PJ_NEXT) {
    PJ_STEP(0); PJ_STEP(1); PJ_STEP(2); PJ_STEP(3); PJ_STEP(4); PJ_STEP(5);
    int more = k2 - 1024; asm volatile("" : "+s"(more));
    if (more) PJ_STEP(6);
    PJ_STEP(7); PJ_STEP(8);
    if (u == 1) break;
  }
  PJ_ST();
  PJ_NOP;
  const u32 left = __builtin_amdgcn_raw_buffer_load_b32(rc, wid ? PJ_OOB : vD < 128 ? SY_JOB(0) * 4 + vD * 4 : SY_INIT(0) * 4, 0, 16);
#define FSL 0
#define FWN 15
#define PJ_FD(v, ai, bj, q) do { PJ_DS(v, ai, bj, q); if (FSL) __builtin_amdgcn_s_sleep(FSL); if (FWN < 15) asm volatile("s_waitcnt vmcnt(%0)" :: "i"(FWN) : "memory"); } while (0)
  PJ_FD(pb[2], 0, 1, 2); PJ_FD(pb[3], 0, 1, 3);
#pragma unroll
  for (int q = 0; q < 4; q++) pb[q] = PJ_PARK(q);
#pragma unroll
  for (int q = 0; q < 4; q++) PJ_FD(pa[q], 1, 0, q);
#pragma unroll
  for (int q = 0; q < 4; q++) PJ_FD(pb[q], 0, 0, q);
#pragma unroll
  for (int q = 0; q < 4; q++) { const u32x4 v = PJ_CV(1, 1, q); PJ_FD(PJ_SW(v), 1, 1, q); }
  pre = -1;
  const int xp = __builtin_amdgcn_readlane(left, 8);
  if (__all(left >= (vD < 128 ? ~0u : 1u)) && vD == 0) pre = (int)__hip_atomic_fetch_add(HOTP(xp) + SY_QHEAD, 1u, __ATOMIC_RELAXED, __HIP_MEMORY_SCOPE_AGENT) + (xp & 0x7000);
  PJ_WV(0);
  if (wr == 0) PJ_BAR;
  PJ_BAR;
  if (emp != 129) PJ_PUB(0, 8);
  PJ_ST();
}

namespace fa {
constexpr float SCALE = 0.08838834764831845f;
constexpr int NW = 8, QBLK = 32, KVBLK = 64, QB = NW * QBLK, D = 128;
constexpr int SHM_V = KVBLK * D * 2, SHM_K = KVBLK * D * 2;
#define SBAR() __builtin_amdgcn_sched_barrier(0)
#define FMX3(a, b, c) __builtin_elementwise_maximum(__builtin_elementwise_maximum(a, b), c)
#define AT_PAD 0
#define EBAR 15
#define LSUM 1
#define LSP 0x80
#define EPR 0
#define DPOS 0x0C080400
#define MX0 -1
#define ATHR 24.f
__device__ __forceinline__ void attn_block(const char* __restrict__ Qp, const char* __restrict__ Kh, const char* __restrict__ Vh,
                                           char* __restrict__ Op, int P0, char* lds, const int wv, u32* qh, const int oq, int& pre) {
  const int tid = TIDX, wid = wv, lane = tid & 63, l15 = lane & 15, lq = lane >> 4, grp = wid >> 2;
  const int NT = (P0 + QB) / KVBLK;
  const int rb = wid ^ 4;
  const int TL = NT - 4 + (rb >> 1);
  const int rem = 2 * (NT - TL) - 2 - grp;
  float l0 = 0.f, l1 = 0.f;
  f4 ls[2] = {};
  u32x4 one = {0x3F803F80u, 0x3F803F80u, 0x3F803F80u, 0x3F803F80u};
  f4 o[8][2] = {}, sc[4][2], nm[2] = {};
  int ko = lq * 512 + l15 * 16;
  int vo = 4 * SHM_K + ((lane >> 1) & 1) * 512 + (((lane >> 2) ^ ((lane & 2) * 4)) << 4) + (lane & 1) * 8;
  const __amdgpu_buffer_rsrc_t rk = RSRC(Kh, (2 * NT - 1) * 262144 + 8192), rv = RSRC(Vh, (2 * NT - 1) * 262144 + 8192);
  const int gk = tid * 16, gv = gk ^ ((tid & 32) << 2);
#define DMA(rs, go, dst, so) __builtin_amdgcn_raw_ptr_buffer_load_lds(rs, (LDSP void*)(lds + (dst) + wid * 1024), 16, go, so, 0, 0)
#define ISSUE_K(t) do { const int s_ = (t) * 524288, d_ = ((t) & 3) * SHM_K; DMA(rk, gk, d_, s_); DMA(rk, gk, d_ + 8192, s_ + 262144); } while (0)
#define ISSUE_V(t) do { const int s_ = (t) * 524288, d_ = 4 * SHM_K + ((t) & 3) * SHM_V; DMA(rv, gv, d_, s_); DMA(rv, gv, d_ + 8192, s_ + 262144); } while (0)
#define PHASE(n) do { SBAR(); asm volatile("s_waitcnt vmcnt(" #n ")" ::: "memory"); __builtin_amdgcn_s_barrier(); asm volatile("" ::: "memory"); SBAR(); } while (0)
#define LDK(i) kf[(i) % 6] = *(const bf8*)(lds + ko + ((i) >> 2) * 2048 + ((i) & 1) * 256 + ((i) & 2) * 4096)
#define LDV0(i) vl[(i) & 3] = trrd(lds + vo + ((i) & 7) * 1024 + ((i) >> 3) * 8192)
#define LDV1(i) vh[(i) & 3] = trrd(lds + vo + ((i) & 7) * 1024 + ((i) >> 3) * 8192 + 256)
#define LDV(i) do { LDV0(i); LDV1(i); } while (0)
#define AST(i)
#define AP(i)
  AP(0);
#define QK(NEXT) do { _Pragma("unroll") for (int i = 0; i < 16; ++i) { \
      _Pragma("unroll") for (int h = 0; h < 2; ++h) { \
        SBAR(); \
        if (i > 3) sc[i & 3][h] = MFMA(kf[i % 6], qr[i >> 2][h], sc[i & 3][h]); \
        else asm volatile("v_mfma_f32_16x16x32_bf16 %0, %1, %2, %3" : "+v"(sc[i][h]) : "v"(kf[i]), "v"(qr[0][h]), "v"(nm[h])); \
        SBAR(); \
        if (i + 4 < 16) { if (h == 0) LDK(i + 4); } else if (NEXT) { if (h == 0) LDV0(i - 12); else LDV1(i - 12); } } } } while (0)
#define SCX(k) sc[(k) >> 3][((k) >> 2) & 1][(k) & 3]
#define MAXS(i) do { \
      if ((i) == 0) mx = FMX3(SCX(0), SCX(1), SCX(2)); else if ((i) == 1) my = FMX3(SCX(3), SCX(4), SCX(5)); \
      else if ((i) < 15) { if ((i) & 1) my = FMX3(my, SCX(2 * (i) + 2), SCX(2 * (i) + 3)); else mx = FMX3(mx, SCX(2 * (i) + 2), SCX(2 * (i) + 3)); } \
      else { mx = __builtin_elementwise_maximum(mx, my); asm volatile("" : "+v"(mx)); } } while (0)
  float mx, my;
  constexpr float C2 = 1.4426950408889634f * SCALE;
  bf8 qr[4][2], kf[6], pa[2][2];
  bf4 vl[4], vh[4];
#pragma unroll
  for (int d0 = 0; d0 < 8; ++d0) qr[d0 >> 1][d0 & 1] = *(const bf8*)(Qp + rb * 262144 + (d0 >> 1) * 2048 + (d0 & 1) * 256 + ko);
  SBAR();
  ISSUE_K(0); ISSUE_K(1); ISSUE_V(0); ISSUE_K(2); ISSUE_V(1);
  SBAR();
#pragma unroll
  for (int i = 0; i < 8; ++i) asm volatile("" : "=v"(sc[i >> 1][i & 1]), "+v"(o[i][0]), "+v"(o[i][1]));
  asm volatile("" : "+v"(nm[0]), "+v"(nm[1]));
  if (LSUM) asm volatile("" : "+v"(ls[0]), "+v"(ls[1]), "+v"(one));
  SBAR();
#pragma unroll
  for (int d0 = 0; d0 < 8; ++d0) {
    u32x4 q = __builtin_bit_cast(u32x4, qr[d0 >> 1][d0 & 1]);
#pragma unroll
    for (int i = 0; i < 4; i++) q[i] = pk2(__uint_as_float(q[i] << 16) * C2, __uint_as_float(q[i] & 0xFFFF0000u) * C2);
    qr[d0 >> 1][d0 & 1] = __builtin_bit_cast(bf8, q);
    asm volatile("" : "+v"(qr[d0 >> 1][d0 & 1]));
  }
  AP(8);
  PHASE(8);
  AP(1);
  if (grp) PHASE(6);
  LDK(0); LDK(1); LDK(2); LDK(3);
  QK(0);
  mx = 0.f;
  AP(2);
  PHASE(4);
  AP(3);
  auto tile = [&](const int t, auto LASTC) __attribute__((always_inline)) {
    constexpr bool LAST = decltype(LASTC)::value;
    AST(0);
#define ISSUE1(i) do { if ((i) < 2) DMA(rk, gk, ((t + 3) & 3) * SHM_K + (i) * 8192, (t + 3) * 524288 + (i) * 262144); \
                       else DMA(rv, gv, 4 * SHM_K + ((t + 2) & 3) * SHM_V + ((i) & 1) * 8192, (t + 2) * 524288 + ((i) & 1) * 262144); SBAR(); } while (0)
#pragma unroll
    for (int i = 0; i < 4; ++i) if (((DPOS >> (i * 8)) & 255) == 0) ISSUE1(i);
    SBAR();
    if constexpr (LAST) {
      const int dq = l15 - 4 * lq;
      if (rb & 1) {
#pragma unroll
        for (int r = 0; r < 4; ++r) { if (dq < r) { sc[2][0][r] = -__builtin_inff(); sc[3][1][r] = -__builtin_inff(); } sc[3][0][r] = -__builtin_inff(); SBAR(); }
      } else {
#pragma unroll
        for (int r = 0; r < 4; ++r) {
          if (dq < r) { sc[0][0][r] = -__builtin_inff(); sc[1][1][r] = -__builtin_inff(); }
          sc[1][0][r] = sc[2][0][r] = sc[2][1][r] = sc[3][0][r] = sc[3][1][r] = -__builtin_inff(); SBAR();
        }
      }
    }
    if (LAST || MX0 < 0) {
#pragma unroll
      for (int i = 0; i < 16; ++i) MAXS(i);
    }
    if (__builtin_expect(t == 0 || __any(mx > ATHR), 0)) {
#pragma unroll
      for (int h = 0; h < 2; ++h) {
        float m = FMX3(FMX3(sc[0][h][0], sc[0][h][1], sc[0][h][2]), sc[0][h][3], t ? 0.f : sc[0][h][3]);
#pragma unroll
        for (int k = 1; k < 4; ++k) m = FMX3(FMX3(m, sc[k][h][0], sc[k][h][1]), sc[k][h][2], sc[k][h][3]);
        { auto rr = __builtin_amdgcn_permlane16_swap(__float_as_uint(m), __float_as_uint(m), false, false);
          m = __builtin_elementwise_maximum(__uint_as_float(rr[0]), __uint_as_float(rr[1])); }
        { auto rr = __builtin_amdgcn_permlane32_swap(__float_as_uint(m), __float_as_uint(m), false, false);
          m = __builtin_elementwise_maximum(__uint_as_float(rr[0]), __uint_as_float(rr[1])); }
        const float alpha = t ? __builtin_amdgcn_exp2f(-m) : 1.f, nn = nm[h][0] - m;
#pragma unroll
        for (int r = 0; r < 4; ++r) {
          nm[h][r] = nn;
#pragma unroll
          for (int k = 0; k < 4; ++k) sc[k][h][r] -= m;
#pragma unroll
          for (int d = 0; d < 8; ++d) o[d][h][r] *= alpha;
        }
        if (LSUM) ls[h][0] *= alpha; else if (h) l1 *= alpha; else l0 *= alpha;
        asm volatile("" : "+v"(nm[h]));
      }
    }
    AST(1);
    float x[32];
    u32 pw[16];
#define SCP(r, e) sc[(r) >> 2][(r) & 1][((r) & 2) + (e)]
#pragma unroll
    for (int r = 0; r < 18; ++r) {
      SBAR();
      if (r < 16) { x[2 * r] = __builtin_amdgcn_exp2f(SCP(r, 0)); x[2 * r + 1] = __builtin_amdgcn_exp2f(SCP(r, 1)); }
      if (r >= 2) {
        if (LSUM) {} else if (r & 1) { l0 += x[2 * r - 5]; l1 += x[2 * r - 4]; } else { if (r > 2) l1 += x[2 * r - 5]; l0 += x[2 * r - 4]; }
        pw[r - 2] = pk2(x[2 * r - 4], x[2 * r - 3]); }
      SBAR();
#pragma unroll
      for (int i = 0; i < 4; ++i) if (((DPOS >> (i * 8)) & 255) == r + 1) ISSUE1(i);
    }
    SBAR();
    if (!LSUM) l1 += x[31];
#pragma unroll
    for (int i = 0; i < 4; ++i) pa[i >> 1][i & 1] = __builtin_bit_cast(bf8, (u32x4){pw[(i >> 1) * 8 + (i & 1)], pw[(i >> 1) * 8 + (i & 1) + 2], pw[(i >> 1) * 8 + (i & 1) + 4], pw[(i >> 1) * 8 + (i & 1) + 6]});
    asm volatile("" : "+v"(pa[0][0]), "+v"(pa[0][1]), "+v"(pa[1][0]), "+v"(pa[1][1]));
    if (!LSUM) asm volatile("" : "+v"(l0), "+v"(l1));
    ko ^= (t & 1) ? 3 * SHM_K : SHM_K;
    SBAR();
    AST(3);
    if constexpr (!LAST) { LDK(0); LDK(1); LDK(2); LDK(3); } else { LDV(0); LDV(1); LDV(2); LDV(3); }
    AST(4);
    PHASE(4);
    AST(5);
    __builtin_amdgcn_s_setprio(1);
    if constexpr (!LAST) QK(1);
    AST(6);
#pragma unroll
    for (int i = 0; i < 16; ++i) {
      SBAR();
      if (LSUM && (i == (LSP & 15) || i == (LSP >> 4))) {
#pragma unroll
        for (int h = 0; h < 2; ++h) { asm volatile("v_mfma_f32_16x16x32_bf16 %0, %1, %2, %0" : "+v"(ls[h]) : "v"(one), "v"(pa[i == (LSP >> 4)][h])); SBAR(); }
      }
      o[i & 7][0] = MFMA(cat8(vl[i & 3], vh[i & 3]), pa[i >> 3][0], o[i & 7][0]);
      SBAR();
      if (i > 0 && i + 3 < 16) LDV1(i + 3);
      if constexpr (!LAST) if (MX0 >= 0 && i >= MX0 && i < MX0 + 8) MAXS(2 * (i - MX0));
      SBAR();
      o[i & 7][1] = MFMA(cat8(vl[i & 3], vh[i & 3]), pa[i >> 3][1], o[i & 7][1]);
      SBAR();
      if (i + 4 < 16) LDV0(i + 4);
      if constexpr (!LAST) if (MX0 >= 0 && i >= MX0 && i < MX0 + 8) MAXS(2 * (i - MX0) + 1);
      if (i == EBAR) AST(2);
      if (EPR && i == EBAR) __builtin_amdgcn_s_setprio(EPR);
      if (i == EBAR && (!LAST || rem)) PHASE(4);
    }
    __builtin_amdgcn_s_setprio(0);
    vo ^= (t & 1) ? 3 * SHM_V : SHM_V;
    AST(7);
  };
#pragma unroll 1
  for (int t = 0; t < TL; t++) {
    tile(t, std::false_type{});
  }
  AP(4);
  const u32 peek = __builtin_amdgcn_raw_buffer_load_b32(RSRC(qh, wid ? 0 : 4), 0, 0, 16);
  tile(TL, std::true_type{});
  AP(5);
  ISSUE_V(TL + 3);
  SBAR();
  {
    { auto rr = __builtin_amdgcn_permlane16_swap(__float_as_uint(l0), __float_as_uint(l0), false, false); l0 = __uint_as_float(rr[0]) + __uint_as_float(rr[1]); }
    { auto rr = __builtin_amdgcn_permlane16_swap(__float_as_uint(l1), __float_as_uint(l1), false, false); l1 = __uint_as_float(rr[0]) + __uint_as_float(rr[1]); }
    const float i0 = __builtin_amdgcn_rcpf(LSUM ? ls[0][0] : swadd(l0, l0)), i1 = __builtin_amdgcn_rcpf(LSUM ? ls[1][0] : swadd(l1, l1));
    const __amdgpu_buffer_rsrc_t ro = RSRC(Op + rb * 262144, 8192);
#pragma unroll
    for (int i = 0; i < 8; ++i) {
      __builtin_amdgcn_raw_buffer_store_b128(pair16(pk2(o[i][0][0] * i0, o[i][0][1] * i0), pk2(o[i][0][2] * i0, o[i][0][3] * i0), pk2(o[i][1][0] * i1, o[i][1][1] * i1), pk2(o[i][1][2] * i1, o[i][1][3] * i1)),
                                             ro, OB_OFF((lq & 1) * 16 + l15, (lq >> 1) * 8) + (i & 3) * 1024, (i >> 2) * 4096, 16);
      if (i == 3 && rem > 1) PHASE(6);
    }
  }
  AP(6);
  if (rem > 2) PHASE(10);
  if (rem > 3) { PHASE(8); PHASE(8); }
  if (TIDX == 0) {
    const bool out = PEEK && peek >= 320u;
    pre = (int)__hip_atomic_fetch_add(qh + (out ? oq : 0), 1u, __ATOMIC_RELAXED, __HIP_MEMORY_SCOPE_AGENT) + (out ? 0x10000 : 0);
  }
  VMW0();
  AP(7);
  __syncthreads();
}
#undef DMA
#undef ISSUE_K
#undef ISSUE_V
#undef PHASE
#undef LDK
#undef LDV
#undef QK
#undef MAXS
}

__device__ __forceinline__ void wof_task(const Params& p, int kc, char* smem, const int wv, u32* qh, int& pre) {
  const int tid = TIDX, lane = tid & 63, wid = wv;
  const int l15 = lane & 15, lq = lane >> 4, q4 = l15 >> 2, p4 = lane & 3;
  const __amdgpu_buffer_rsrc_t rw = RSRC(WSP(u16, O_WOF), 2 * WOB);
  bf8 st[2][9];
#pragma unroll
  for (int m = 0; m < 2; m++)
#pragma unroll
    for (int i = 0; i < 9; i++) st[m][i] = *(const bf8*)((m ? p.BWo : p.Awo) + (size_t)kc * 64 * 576 + (tid + 512 * i) * 8);
  CLAIM();
#pragma unroll
  for (int m = 0; m < 2; m++) {
#pragma unroll
    for (int i = 0; i < 9; i++) {
      const int id = tid + 512 * i, row = id / 72, ch = id % 72;
      *(bf8*)(smem + row * 1280 + (((ch >> 1) ^ fsw(row)) << 5) + ((ch & 1) << 4)) = st[m][i];
    }
    asm volatile("s_waitcnt lgkmcnt(0)" ::: "memory"); __builtin_amdgcn_s_barrier(); asm volatile("" ::: "memory");
#pragma unroll
    for (int i = 0; i < 9; i++) {
      const int f = wid * 9 + i, nt = f >> 1, k2 = f & 1;
      const int r0 = k2 * 32 + lq * 8 + q4, r1 = r0 + 4;
      bf4 lo = trrd(smem + r0 * 1280 + ((nt ^ fsw(r0)) << 5) + p4 * 8);
      bf4 hi = trrd(smem + r1 * 1280 + ((nt ^ fsw(r1)) << 5) + p4 * 8);
      __builtin_amdgcn_raw_buffer_store_b128(__builtin_bit_cast(u32x4, cat8(lo, hi)), rw, ((nt * 128 + kc * 2 + k2) * 64 + lane) * 16, m * WOB, 16);
    }
    if (m == 0) { asm volatile("s_waitcnt lgkmcnt(0)" ::: "memory"); __builtin_amdgcn_s_barrier(); asm volatile("" ::: "memory"); }
  }
  VMW0();
  __syncthreads();
}

#define OT_T(i)
#define OTI 2
#define OTD 0x1511
#define OTI4 2
template <int MT>
__device__ __forceinline__ void out_tile(const char* A, const char* W, u16* C, char* smem, const int wv, u32* hot, u32* qh, int& pre) {
  constexpr int U = 32 / MT, SC = U / 2, DP = MT / 4, NCH = 4 * MT, CW = 32768 / MT;
  const int tid = TIDX, lane = tid & 63, wid = wv;
  const int l15 = lane & 15, lq = lane >> 4, kh = wid >> 2, wc = wid & 3;
#define OTX 0
  const __amdgpu_buffer_rsrc_t ra = RSRC(A, (OTX & 1) ? 0 : MT / 2 * 262144), rw = RSRC(W + wc * 3 * 131072 + kh * 1024, (OTX & 2) ? 0 : 3 * 131072 - kh * 1024);
  const int va = tid * 16, vw = lane * 16;
  int ao = kh * 2048 + (lq >> 1) * 1024 + l15 * 32 + (lq & 1) * 16;
  f4 acc[MT][3];
  bf8 wf[U][3], af[2][MT];
#define OT_DMA(i, buf, so) __builtin_amdgcn_raw_ptr_buffer_load_lds(ra, (LDSP void*)(smem + (buf) + (i) * 8192 + wid * 1024), 16, va, \
                                                                    (so) + (MT == 4 ? ((i) >> 1) * 262144 + ((i) & 1) * 8192 : (i) * 262144), 0, 0)
#define OT_LDA(d, base, off) do { _Pragma("unroll") for (int m = 0; m < MT; m++) af[d][m] = *(const bf8*)(smem + (base) + (off) + (m >> 1) * 2 * CW + (m & 1) * 512); } while (0)
#define OT_LD1(d, m, base, off) af[d][m] = *(const bf8*)(smem + (base) + (off) + ((m) >> 1) * 2 * CW + ((m) & 1) * 512)
#define OT_VM(n) __builtin_amdgcn_s_waitcnt(0x0F70 | ((n) & 15) | (((n) >> 4) << 14))
#define OT_PAIR(buf, so) do { _Pragma("unroll") for (int i = 0; i < 8; i++) OT_DMA(i & 3, (buf) + (i >> 2) * 32768, (so) + (i >> 2) * 2 * CW); SB0(); } while (0)
#define OT_PEEK() do { if (wid == 0) asm volatile("s_mov_b32 m0, %2\n\ts_nop 0\n\tbuffer_load_dword %0, %1, 0 offen offset:%3 sc1 lds" \
                                                  :: "v"(vw * 4), "s"(RSRC(hot, SY_OQ(7) * 4)), "i"(131072), "i"(SY_OQ(0) * 4) : "memory", "m0"); } while (0)
  if constexpr (MT == 4) {
  OT_PAIR(0, 0);
#pragma unroll
  for (int u = 0; u < U; u++) {
    if (u == U - 1) OT_PAIR(65536, 4 * CW);
#pragma unroll
    for (int j = 0; j < 3; j++) wf[u][j] = __builtin_bit_cast(bf8, __builtin_amdgcn_raw_buffer_load_b128(rw, vw, u * 2048 + j * 131072, 0));
    SB0();
  }
#pragma unroll
  for (int m = 0; m < MT; m++)
#pragma unroll
    for (int j = 0; j < 3; j++) acc[m][j] = (f4){0, 0, 0, 0};
  asm volatile("s_waitcnt vmcnt(%0)" :: "i"(U * 3 + 8) : "memory"); __builtin_amdgcn_s_barrier(); asm volatile("" ::: "memory");
  OT_LDA(0, ao, 0);
  OT_T(2);
  int bs = 0;
#pragma unroll 1
  for (int c = 0; c < NCH; c += 2) {
    const int sw = c < NCH - 2 ? (c + 2) * CW : PJ_OOB, sd = c < NCH - 4 ? (c + 4) * 2 * CW : PJ_OOB;
#pragma unroll
    for (int u = 0; u < U; u++) {
      SB0();
      if (u < U - 1) OT_VM(3 * (U - 1) + 8); else OT_VM(3 * (U - 1));
      if (u == U - 1) {
        asm volatile("s_waitcnt lgkmcnt(0)" ::: "memory"); __builtin_amdgcn_s_barrier(); asm volatile("" ::: "memory");
        ao ^= 65536;
      }
      const int nxt = u == U - 1 ? 0 : ((u + 1) / SC) * 32768 + ((u + 1) % SC) * 4096;
      if (!OTI4) OT_LDA((u + 1) & 1, ao, nxt);
      SB0();
      if (u == U - 1) OT_PAIR(bs, sd);
#pragma unroll
      for (int j = 0; j < 3; j++) {
#pragma unroll
        for (int m = 0; m < MT; m++) {
          const int n = j * MT + m;
          acc[m][j] = MFMA(wf[u][j], af[u & 1][m], acc[m][j]);
          if (OTI4 && n % (OTI4 ? OTI4 : 1) == 0 && n / (OTI4 ? OTI4 : 1) < MT) { SB0(); OT_LD1((u + 1) & 1, n / (OTI4 ? OTI4 : 1), ao, nxt); SB0(); }
        }
        SB0();
        wf[u][j] = __builtin_bit_cast(bf8, __builtin_amdgcn_raw_buffer_load_b128(rw, vw, sw + u * 2048 + j * 131072, 0));
        SB0();
      }
    }
    bs ^= 65536;
    if (c == NCH - 6) OT_PEEK();
  }
  } else {
#pragma unroll
  for (int i = 0; i < 4; i++) OT_DMA(i, 0, 0);
  SB0();
#pragma unroll
  for (int u = 0; u < U; u++) {
#pragma unroll
    for (int d = 0; d < DP; d++) OT_DMA((u % SC) * DP + d, 32768 + (u / SC) * 32768, (1 + u / SC) * 2 * CW);
#pragma unroll
    for (int j = 0; j < 3; j++) wf[u][j] = __builtin_bit_cast(bf8, __builtin_amdgcn_raw_buffer_load_b128(rw, vw, u * 2048 + j * 131072, 0));
    SB0();
  }
#pragma unroll
  for (int m = 0; m < MT; m++)
#pragma unroll
    for (int j = 0; j < 3; j++) acc[m][j] = (f4){0, 0, 0, 0};
  asm volatile("s_waitcnt vmcnt(%0)" :: "i"(U * (DP + 3)) : "memory"); __builtin_amdgcn_s_barrier(); asm volatile("" ::: "memory");
  OT_LDA(0, ao, 0);
  OT_T(2);
  int bs = 0;
#pragma unroll 1
  for (int c = 0; c < NCH; c += 2) {
    const int sw = c < NCH - 2 ? (c + 2) * CW : PJ_OOB;
#pragma unroll
    for (int u = 0; u < U; u++) {
      SB0();
      __builtin_amdgcn_s_waitcnt(MT == 4 ? 0x4F7C : 0x0F7F);
      if (u % SC == SC - 1 && !(OTX & 32)) { asm volatile("s_waitcnt lgkmcnt(0)" ::: "memory"); __builtin_amdgcn_s_barrier(); asm volatile("" ::: "memory"); }
      if (u == U - 1) ao ^= 65536;
      const int nxt = u == U - 1 ? 0 : ((u + 1) / SC) * 32768 + ((u + 1) % SC) * 4096;
      if (!OTI) OT_LDA((u + 1) & 1, ao, nxt);
      SB0();
      const int cn = c + 3 + u / SC;
#pragma unroll
      for (int j = 0; j < 3; j++) {
#pragma unroll
        for (int m = 0; m < MT; m++) {
          const int n = j * MT + m;
          acc[m][j] = MFMA(wf[u][j], af[u & 1][m], acc[m][j]);
          if (OTI && n % (OTI ? OTI : 1) == 0 && n / (OTI ? OTI : 1) < MT) { SB0(); OT_LD1((u + 1) & 1, n / (OTI ? OTI : 1), ao, nxt); SB0(); }
          if ((n == (OTD & 255) || n == (OTD >> 8)) && !(OTX & 4)) { SB0(); OT_DMA((u % SC) * DP + (n == (OTD >> 8)), u < SC ? (bs ^ 65536) + 32768 : bs, cn < NCH ? cn * 2 * CW : PJ_OOB); SB0(); }
        }
        SB0();
        if (!(OTX & 8)) wf[u][j] = __builtin_bit_cast(bf8, __builtin_amdgcn_raw_buffer_load_b128(rw, vw, sw + u * 2048 + j * 131072, 0));
        SB0();
      }
    }
    bs ^= 65536;
    if (c == NCH - 6) OT_PEEK();
  }
  }
  OT_T(3);
  VMW0C();
  if (wid == 0) {
    const u32 left = *(const u32*)(smem + 131072 + SY_OQ(0) * 4 + (lane < 6 ? lane : 6) * 4);
    pre = 0x20000;
    if (!OXIT || __any(left < (lane > 5 ? 72u : (lane & 1) ? NOB : NOA))) CLAIM();
  }
  asm volatile("s_waitcnt lgkmcnt(0)" ::: "memory"); __builtin_amdgcn_s_barrier(); asm volatile("" ::: "memory");
#pragma unroll
  for (int m = 0; m < MT / 2; m++)
#pragma unroll
    for (int j = 0; j < 3; j++) *(f4*)(smem + (m * 3 + j) * 8192 + va) = kh ? acc[m][j] : acc[MT / 2 + m][j];
  __syncthreads();
#pragma unroll
  for (int m = 0; m < MT / 2; m++) {
    f4 t[3];
#pragma unroll
    for (int j = 0; j < 3; j++) { const f4 o = *(const f4*)(smem + (m * 3 + j) * 8192 + (va ^ 4096)); t[j] = kh ? o + acc[MT / 2 + m][j] : acc[m][j] + o; }
    u16* c = C + (size_t)(kh * MT * 8 + m * 16 + l15) * 1152 + wc * 48;
    *(u32x4*)(c + (lq & 1) * 16 + (lq >> 1) * 8) = pair16(pk2(t[0][0], t[0][1]), pk2(t[0][2], t[0][3]), pk2(t[1][0], t[1][1]), pk2(t[1][2], t[1][3]));
    *(u32x2*)(c + 32 + lq * 4) = (u32x2){pk2(t[2][0], t[2][1]), pk2(t[2][2], t[2][3])};
  }
  __syncthreads();
#undef OT_DMA
#undef OT_LDA
}

#define CLAIMS() do { const int l_ = TIDX; if (l_ < 64) { \
    const u32 v_ = ld4_sc1(hot + SY_OQ(l_ < 6 ? l_ : 0)); \
    if (l_ == 0) pre = (int)__hip_atomic_fetch_add(hot + SY_OQ(6), 1u, __ATOMIC_RELAXED, __HIP_MEMORY_SCOPE_AGENT); \
    pre |= (int)((u32)__ballot(l_ < 6 && v_ < ((l_ & 1) ? NOB : NOA)) << 16); } } while (0)
#define CLS 51
#define KDUE 112
#define KDL 84
#define TPS 2
__device__ __forceinline__ void small_out_task(const Params& p, int rq, int cp, char* smem, const int wv, u32* hot, int& pre) {
  const int tid = TIDX, lane = tid & 63, wid = wv;
  const int l15 = lane & 15, lq = lane >> 4;
  const __amdgpu_buffer_rsrc_t rw = RSRC(WSP(char, O_WOF) + WOB, WOB), ro = RSRC(WSP(u16, O_OB), 2048 * 4096 * 2);
  if (tid == 0) {
    int it = 0;
    while (ld4_sc1(hot + SY_CW) < 64u && ++it < SPIN_CAP) __builtin_amdgcn_s_sleep(8);
  }
  __syncthreads();
  bf8 wf[8][4];
#pragma unroll
  for (int k = 0; k < 8; k++)
#pragma unroll
    for (int j = 0; j < 4; j++)
      wf[k][j] = __builtin_bit_cast(bf8, __builtin_amdgcn_raw_buffer_load_b128(rw, (l15 >> 3) * 1024 + (lq * 16 + (l15 & 7)) * 16 + (j & 1) * 128,
                                                                              ((cp * 2 + (j >> 1)) * 128 + wid * 16 + k * 2) * 1024, 16));
  const int va = OB_OFF(rq * 8 + (l15 & 7), wid * 512 + (l15 >> 3) * 32 + lq * 8);
  const int wo = (lq >> 1) == (l15 >> 3) ? (wid * 2 + (lq >> 1)) * 272 + (l15 & 7) * 32 + (lq & 1) * 16 : 4 * 4352 + lane * 16;
  const int rd = (lane >> 4) * 4352 + (lane & 7) * 544 + wid * 32 + ((lane >> 3) & 1) * 16;
  u32 left = 0u, due = 0u, last = 0u, per = 240u * 64u;
  bool have = false;
  auto step = [&](u32x4 (&af)[8], u32x4 (&nf)[8], const int s) __attribute__((always_inline)) {
    if (due == 0u) {
      if (tid < 64) {
        u32 v;
        int it = 0;
        while (v = ld4_sc1(CSP(s - 2 - (lane & 1))), __any(v == 0u) && ++it < SPIN_CAP) __builtin_amdgcn_s_sleep(SPS);
        *(u32*)(smem + 40960 + (lane & 1) * 4) = v;
      }
      asm volatile("s_waitcnt lgkmcnt(0)" ::: "memory"); __builtin_amdgcn_s_barrier(); asm volatile("" ::: "memory");
      due = __builtin_amdgcn_readfirstlane(*(const u32*)(smem + 40960));
      last = __builtin_amdgcn_readfirstlane(*(const u32*)(smem + 40964));
    }
    per = umin(per, due - last);
    last = due;
    int n = 400, late;
    while (late = (int)(((u32)wall_clock64() << 6) - due - per) - (s == 63 ? KDL : KDUE) * 64, late < 0 && --n > 0) {}
    if (s == 63) TLS(1);
    const u32 v = __builtin_amdgcn_raw_buffer_load_b32(RSRC(CSP(0), wid ? 0 : 4096), 0, (s - 1) * 64, 16);
    if (s == CLS) left = __builtin_amdgcn_raw_buffer_load_b32(RSRC(hot, wid ? 0 : 4096), SY_OQ(lane < 6 ? lane : 6) * 4, 0, 16);
    SB0();
    auto rows = [&](u32x4 (&f)[8], const int so) __attribute__((always_inline)) {
#pragma unroll
      for (int k = 0; k < 8; k++) f[k] = __builtin_amdgcn_raw_buffer_load_b128(ro, va, so + k * 4096, 16);
      SB0();
    };
    auto bad = [&]() __attribute__((always_inline)) {
      u32 t = 0u;
#pragma unroll
      for (int k = 0; k < 8; k++) t = umax(umax(t, af[k][0]), af[k][2]);
      return __any(t == ~0u);
    };
    if (!have) rows(af, s * 262144);
    have = late >= (int)per && s < 63;
    rows(nf, have ? (s + 1) * 262144 : PJ_OOB);
    if (wid == 0) *(u32*)(smem + 40968 + (s & 1) * 4) = v;
    if (__builtin_expect(bad(), 0)) {
      int it = 0;
      do { __builtin_amdgcn_s_sleep(TPS); CFENCE(); rows(af, s * 262144); } while (bad() && ++it < SPIN_CAP);
    }
    SB0();
    f4 acc[4];
#pragma unroll
    for (int k = 0; k < 8; k++)
#pragma unroll
      for (int j = 0; j < 4; j++) acc[j] = MFMA(wf[k][j], __builtin_bit_cast(bf8, af[k]), k ? acc[j] : ((f4){0, 0, 0, 0}));
    char* const sm = smem + (s & 1) * 20480;
#pragma unroll
    for (int j = 0; j < 4; j++) *(f4*)(sm + wo + ((lq >> 1) == (l15 >> 3) ? j * 4352 : 0)) = acc[j];
    asm volatile("s_waitcnt lgkmcnt(0)" ::: "memory"); __builtin_amdgcn_s_barrier(); asm volatile("" ::: "memory");
    if (s == 63) TLS(2);
    due = __builtin_amdgcn_readfirstlane(*(const u32*)(smem + 40968 + (s & 1) * 4));
    f4 t = *(const f4*)(sm + rd) + *(const f4*)(sm + rd + 272);
#pragma unroll
    for (int r = 0; r < 4; r++) {
      t[r] += __uint_as_float(__builtin_amdgcn_mov_dpp(__float_as_uint(t[r]), 0xB1, 0xf, 0xf, true));
      t[r] += __uint_as_float(__builtin_amdgcn_mov_dpp(__float_as_uint(t[r]), 0x4E, 0xf, 0xf, true));
      t[r] += __uint_as_float(__builtin_amdgcn_mov_dpp(__float_as_uint(t[r]), 0x141, 0xf, 0xf, true));
    }
    __builtin_amdgcn_raw_buffer_store_b64((u32x2){pk2(t[0], t[1]), pk2(t[2], t[3])}, RSRC(p.out, 2048 * 2304), (lane & 7) == 0 ? lane : PJ_OOB,
                                          (s * 32 + rq * 8 + wid) * 2304 + 1152 + cp * 64, 0);
  };
  u32x4 fa[8], fb[8];
#pragma unroll
  for (int k = 0; k < 8; k++) asm volatile("" : "=v"(fa[k]));
#pragma unroll 1
  for (int s = XS0; s < 64; s += 2) { step(fa, fb, s); step(fb, fa, s + 1); }
  pre = 72;
  if (__any(left < (lane > 5 ? 72u : (lane & 1) ? NOB : NOA))) CLAIMS();
}
#define PSTAMP()
#define STAMP(i)
#define YEND()
#define ANS 16
#define ANX(slot) (X_ANN ^ xp ^ ((slot) * X_HS))
#define KSL 1
#define DSH 2
#define K1 13
#define K3 8
#define DRL 0
#define CSL 5
#define CSC 0
#define KA 0
#define KB 0
#define KS0 0
#define KMASK 0x00FFFE000FE00000ull
#define KXT(k) ((k) ? (int)((KMASK >> s) & 1) * (k) : 0)
#define DOMAX 90
#define DO0 80
#define DH0 54
#define DOCAP 6
#define C_IMG 0
#define C_QT 9216
#define C_KT (C_QT + 3072)
#define C_P (C_KT + 4096)
#define C_PX (C_P + 768)
#define C_KX (C_PX + 2560)
#define C_VX (C_KX + 8192)
#define C_QX (C_VX + 8192)
#define C_XP 161024
#define OBE 0
#define CALR 2
#define GPS 6
#define C_WL 49920
__device__ __forceinline__ float ror8(float v) {
  return __uint_as_float(__builtin_amdgcn_mov_dpp(__float_as_uint(v), 0x128, 0xf, 0xf, true));
}
__device__ __forceinline__ float rb1(float f) { return __uint_as_float(pk2(0.f, f)); }
#define FMX(a, b) __builtin_elementwise_maximum(a, b)
__device__ __forceinline__ float tmax(float mx) {
  mx = FMX(mx, ror8(mx));
  { auto t = __builtin_amdgcn_permlane16_swap(__float_as_uint(mx), __float_as_uint(mx), false, false);
    mx = FMX(__uint_as_float(t[0]), __uint_as_float(t[1])); }
  { auto t = __builtin_amdgcn_permlane32_swap(__float_as_uint(mx), __float_as_uint(mx), false, false);
    mx = FMX(__uint_as_float(t[0]), __uint_as_float(t[1])); }
  return mx;
}
__device__ __forceinline__ int hmax(int x) {
  x = max(x, __builtin_amdgcn_mov_dpp(x, 0x128, 0xf, 0xf, true));
  x = max(x, __builtin_amdgcn_mov_dpp(x, 0x124, 0xf, 0xf, true));
  x = max(x, __builtin_amdgcn_mov_dpp(x, 0x4E, 0xf, 0xf, true));
  x = max(x, __builtin_amdgcn_mov_dpp(x, 0xB1, 0xf, 0xf, true));
  { auto t = __builtin_amdgcn_permlane16_swap((u32)x, (u32)x, false, false); x = max((int)t[0], (int)t[1]); }
  return x;
}
#define BAR() do { asm volatile("s_waitcnt lgkmcnt(0)" ::: "memory"); __builtin_amdgcn_s_barrier(); asm volatile("" ::: "memory"); } while (0)
#define BARC() do { asm volatile("" ::: "memory"); __builtin_amdgcn_s_waitcnt(0xC07F); __builtin_amdgcn_s_barrier(); asm volatile("" ::: "memory"); } while (0)
#define BARN() do { asm volatile("" ::: "memory"); __builtin_amdgcn_s_barrier(); asm volatile("" ::: "memory"); } while (0)
#define IDX() const int tid = TIDX, lane = tid & 63, l15 = lane & 15, lq = lane >> 4, q4 = l15 >> 2, p4 = lane & 3, hi8 = l15 >> 3; \
    (void)q4; (void)p4; (void)hi8; (void)lq
#define AD_LD(HEAD) __builtin_amdgcn_raw_buffer_load_b32(rsX, (TIDX & (HEAD ? 63 : 31)) * ANS, ANX(s & 1), 16)
#define AD_GET(HEAD, K, CAP) do { int n_ = 0; u32 an = AD_LD(HEAD); \
    while ((u32)__ballot((an & 63u) != (u32)s) != 0u && ++n_ < (CAP)) { __builtin_amdgcn_s_sleep(1); CFENCE(); an = AD_LD(HEAD); } \
    const u32 r_ = (u32)__builtin_amdgcn_readfirstlane((int)an); \
    const int m_ = hmax((TIDX & 32) ? (int)umin(an, DOMAX * 64u) : (int)(an - r_)); \
    dl = ((r_ + (u32)__builtin_amdgcn_readlane(m_, 0)) & ~63u) + (u32)(K) * 64u + (HEAD ? (u32)__builtin_amdgcn_readlane(m_, 32) : 0u); \
    if (!HEAD && !CSL && c == CSC && s > 0 && n_ < (CAP)) __builtin_amdgcn_raw_buffer_store_b32(dl | 1u, rsX, 0, (X_HOT(xp) ^ X_CS) + (s - 1) * 64, 16); } while (0)
#define AD_SPIN() do { int n_ = 160; while ((int)(((u32)wall_clock64() << 6) - dl) < 0 && --n_ > 0) {} } while (0)
#define STG_AUX 0
#define OSTG_AUX 0
#define STG_D 2
#define STG_POLL 7
#define STS(c, i)

#define NXA 1
#define NXE 0
#define ADE 1
#define NXH 1
#define NXV 1
#define NXB 4
#define NXS 3
#define PKA 18
#define PK_(w0, d, a, b, w1) ".long " #w0 " + " #d ", " w1 " + " #a " + " #b " * 512\n\t"
#define PKADD(d, a, b) PK_(0xD3B24000, d, a, b, "0x18020100")
#define PKMUL(d, a, b) PK_(0xD3B14000, d, a, b, "0x18020100")
#define PKFMS(d, a, b, c) PK_(0xD3B04400, d, a, b, "0x9C020100 + " #c " * 262144")
#define CV2(d, a) "v_cvt_pk_bf16_f32 " d ", v[" #a "], v[" #a "+1]\n\t"
#define VBP 0xA9A
#define C_Z 45824
#define SMALL 0x1p-64f
#define EK 0
#define EKN 2
#define EKL 1
#define EKA 1
#define BJ 2
#define KSP 0
#define SP1 2
#define SP2 0
#define KP 3
#define GX 1
#define GXO (GX == 1 ? 4096 : 1024)
#define ZE 0x0D
#define KD 5
#define QSL 0
#define DJ 9
#define VSL 0
#define VSL1 4
#define KV0 1
#define PVP 0
#define IPR 3
#define DSLOT 0x9876E1CBAD0ull
#define F_K (47360 - C_KT)
#define F_Q (47872 - 768 - C_QT)
#define HAL 2
#define OAL 2
#define ALIGN(n) asm volatile(".p2align %0" :: "i"(n))
#define QXP 4
#define RORD 0
#define PRIO(n) __builtin_amdgcn_s_setprio(n)
#define LSL 6
__device__ __forceinline__ int chain_head(const Params& p, char* smem, const int h, const int wv, const bool spec) {
  STS(h, 1);
  TLS(12);
  const int wid = wv;
  u32x8 w[2][9], wx[3];
  const __amdgpu_buffer_rsrc_t rsX = RSRC(XWIN(p), X_END);
  const __amdgpu_buffer_rsrc_t rsB = RSRC(WSP(u16, O_QB), 4 * ACB);
  const int cb = h >> 1;
  const int nvt = (wid & 3) == 0 ? 0 : (wid < 3 ? 2 : 1);
  const int vt = wid < 3 ? wid * 2 + 2 : (wid == 3 ? 0 : wid - 4);
  const int wl = C_WL + (nvt == 1 ? vt * 12288 : 49152 + (nvt == 2 ? wid - 1 : wid >> 2) * 30720);
  int xp;
  {
    IDX();
    u32x4 st[54], nk, nv, nq;
    u32 fl, fk;
    const int vw = ((tid >> 4) * 4096 + h * 128 + (tid & 15) * 8) * 2;
#define NTL 64
#define AH 1
    u32 tc[9], b1, old = 0u;
#define TEC 0
    const u16 *pq = p.BWq, *pk = p.BWk, *pv = p.BWv;
    asm volatile("" : "+s"(pq), "+s"(pk), "+s"(pv));
    const bool te = wid < 3 && (h & 3) == TEC;
    const int tm = te ? wid : (h & 3) - 1;
    const u64 pt = tm == 1 ? (u64)pk : tm == 2 ? (u64)pv : (u64)pq;
    const __amdgpu_buffer_rsrc_t rt = RSRC(pt, te || (tm && h != 31 && wid == 7) ? 576 * 4096 * 2 : 0);
    const int tv = lane < NTL ? lane * 8192 : PJ_OOB, ts_ = te ? 8064 : (h >> 2) * 1024 + 384;
    const __amdgpu_buffer_rsrc_t rq = RSRC(pq, 576 * 4096 * 2), rk = RSRC(pk, 576 * 4096 * 2), rv = RSRC(pv, 576 * 4096 * 2);
    auto req = [&](int kc) {
#pragma unroll
      for (int i = 0; i < 6; i++) {
        st[kc * 6 + i] = __builtin_amdgcn_raw_buffer_load_b128(i < 2 ? rq : (i < 4 ? rk : rv), vw, (kc * 64 + (i & 1) * 32) * 8192, STG_AUX);
        if (NTL && kc + AH < 9 && i == (kc ? 0 : 4)) { SB0(); tc[kc] = __builtin_amdgcn_raw_buffer_load_b32(rt, tv, te && (h >> 2) != kc ? PJ_OOB : (kc + AH) * 64 * 8192 + ts_, 0); }
      }
      SB0();
    };
#pragma unroll
    for (int kc = 0; kc < STG_D; kc++) { if (kc == 0) TLS(13); req(kc); __builtin_amdgcn_s_barrier(); SB0(); }
    TLS(14);
    auto cell = [](int row, int slot) { return (((row * 2 + slot) ^ ((((row >> 2) ^ (row >> 4)) & 1) << 2) ^ ((row >> 5) & 1)) << 5); };
    auto put = [&](int unit, int row, const u32x4& v) { *(u32x4*)(smem + unit * 4160 + cell(row, (tid >> 1) & 1) + (tid & 1) * 16) = v; };
    auto frag = [&](int tile) {
      bf4 tt[4];
#pragma unroll
      for (int kk = 0; kk < 4; kk++) {
        const int r0 = (kk >> 1) * 32 + (lq >> 1) * 16 + (q4 >> 1) * 8 + (lq & 1) * 4 + (kk & 1) * 2 + (q4 & 1);
        tt[kk] = trrd(smem + (tile >> 1) * 4160 + cell(r0, tile & 1) + p4 * 8);
      }
      return ilv(tt[0], tt[1], tt[2], tt[3]);
    };
    auto park = [&](int slot, const u32x8& f) {
      char* d = smem + wl + slot * 2048 + lane * 16;
      *(u32x4*)d = (u32x4){f[0], f[1], f[2], f[3]};
      *(u32x4*)(d + 1024) = (u32x4){f[4], f[5], f[6], f[7]};
    };
    const __amdgpu_buffer_rsrc_t rs0 = RSRC(p.ws, wid ? 0 : O_QA);
    auto poll = [&]() {
      fl = __builtin_amdgcn_raw_buffer_load_b32(rs0, (lane < 32 ? SY_INIT(lane) : SY_ARR) * 4, 0, 16);
      fk = __builtin_amdgcn_raw_buffer_load_b32(rs0, SY_BLK(lane < 12 ? lane >> 2 : 0, cb, lane & 3) * 4, 0, 16);
    };
    auto rows = [&](int vo) {
      nq = __builtin_amdgcn_raw_buffer_load_b128(rsB, vo, h * 8192, 16);
      nk = __builtin_amdgcn_raw_buffer_load_b128(rsB, vo, h * 8192 + ACB, 16);
      nv = __builtin_amdgcn_raw_buffer_load_b128(rsB, vo, h * 8192 + 2 * ACB, 16);
    };
    bool early = false;
    int lost = 0;
#pragma unroll
    for (int kc = 0; kc < 9; kc++) {
      if (kc == STG_D - 1) { if (spec && wid == 0) old = confirm(p, smem, h, b1, wv); SB0(); }
      if (kc == 10 - STG_D) { early = __all(fk >= FULL); CFENCE(); rows(early ? tid * 16 : PJ_OOB); poll(); SB0(); }
#pragma unroll
      for (int i = 0; i < 6; i++) put((i >> 1) * 4 + ((tid & 15) >> 2), (i & 1) * 32 + (tid >> 4), st[kc * 6 + i]);
      if (NTL && kc + AH < 9) asm volatile("" :: "v"(tc[kc]));
      if (kc == 2 * STG_D - 1) lost = (__builtin_amdgcn_readfirstlane(old) >> h) & 1;
      BAR();
      if (kc == 0) TLS(15);
      if (kc == 0) { b1 = __builtin_amdgcn_raw_buffer_load_b32(rs0, 0, SY_BEAT(h & 7) * 4, 16); SB0(); }
      if (kc + STG_D == 8) { fk = ld4_sc1(WSP(u32, 0) + SY_BLK(lane < 12 ? lane >> 2 : 0, cb, lane & 3)); SB0(); }
      if (kc + STG_D < 9) req(kc + STG_D);
      w[0][kc] = frag(wid * 2);
      w[1][kc] = frag(nvt == 2 && kc < 4 ? 16 + vt : wid * 2 + 1);
      const u32x8 tp = frag(nvt == 0 ? 21 + (wid >> 1) : (nvt == 2 && kc < 4 ? wid * 2 + 1 : 16 + vt));
      if (kc >= 4 && kc < 7) { wx[kc - 4] = tp; if (nvt == 0) park(6 + kc, tp); }
      else park(nvt == 0 ? 6 + kc : (kc >= 7 ? kc - 7 : 2 + kc), tp);
      BAR();
      if (kc == 0 || kc == 8) STS(h, kc + 3);
    }
    TLS(6);
    if (wid == 0) {
      int it = 0;
      while (!lost && !__all(fl >= (lane < 32 ? 1u : 256u) && fk >= FULL) && ++it < SPIN_CAP) { BEAT_ON(it); __builtin_amdgcn_s_sleep(GPS); CFENCE(); poll(); }
      if (lane == 0) *(int*)(smem + C_XP) = lost ? -1 : fl & 0x7800;
    }
    BAR();
    if (!early) rows(tid * 16);
    xp = __builtin_amdgcn_readfirstlane(*(const int*)(smem + C_XP));
    if (xp < 0) return xp;
    TLS(7);
    SB0();
    *(u32x4*)(smem + C_QX + tid * 16) = nq;
    *(u32x4*)(smem + C_KX + tid * 16) = nk;
    *(u32x4*)(smem + C_VX + ((tid * 16) ^ ((tid & 32) << 2))) = nv;
  }
  STS(h, 14);
  constexpr float CE = 0.08838834764831845f * 1.4426950408889634f;
  auto run = [&](auto RC) __attribute__((always_inline)) {
  constexpr int R = decltype(RC)::value;
  constexpr int NV = R < 2 ? 0 : (R < 4 ? 1 : 2);
  constexpr bool QW = !(R & 1);
  int known = 0;
  bf8 pre[4][2], vx[NV ? NV : 1];
  int sidx, mo, io, eo, gvo, wvo = 0, so = 0, po = 0, qo = 0, ko = 0, fo = 0, fq = 0, fk = 0;
  float mk[4], mm = 0.f;
  f2v cw = {CE, CE};
#define LDW(dst, slot) do { dst[0] = *(const bf8*)(smem + wvo + (slot) * 2048); dst[1] = *(const bf8*)(smem + wvo + (slot) * 2048 + 1024); } while (0)
  {
    IDX();
    if constexpr (R == 1) {
      po = (tid * 16) ^ ((tid & 32) << 2);
      so = (lane >> 5) * 131072 + (lane & 31) * 16;
      asm volatile("" : "+v"(po), "+v"(so));
    } else if constexpr (R == 0) {
      qo = C_QT + lq * 384 + q4 * 32 + p4 * 8;
      po = C_P + l15 * 16 + lq * 4;
      fo = lq * 512 + l15 * 16;
#pragma unroll
      for (int r = 0; r < 4; r++) { mk[r] = lq * 4 + r <= l15 ? 0.f : -__builtin_inff(); asm volatile("" : "+v"(mk[r])); }
      mm = (l15 & 2) ? -__builtin_inff() : 0.f;
      asm volatile("" : "+v"(mm), "+{v[230:231]}"(cw));
      fq = C_QT + F_Q + (lq ? lq : 1) * 768 + lq * 8; fk = C_KT + F_K + lq * 776;
      asm volatile("" : "+v"(qo), "+v"(po), "+v"(fo), "+v"(fq), "+v"(fk));
      *(u32*)(smem + fq) = 0u; *(u32*)(smem + fk) = 0u;
    } else {
      wvo = wl + lane * 16;
      po = C_P + (lq * 4 + q4) * 16 + (p4 & 1) * 8;
      so = ((l15 >> 2) & 1) * 131072 + (((vt & 3) + (NV == 2 ? hi8 : 0)) * 8 + ((l15 >> 1) & 1) * 4 + (lq >> 1) * 2 + (l15 & 1)) * 16 + (lq & 1) * 8;
      if (NV == 1 && hi8) so = PJ_OOB;
      asm volatile("" : "+v"(wvo), "+v"(po), "+v"(so));
      if constexpr (NV == 2) {
        qo = po ^ ((p4 & 2) << 4); ko = (p4 & 2) ? po : C_Z; po = (p4 & 2) ? C_Z : po;
        *(u64*)(smem + C_Z + hi8 * 256) = 0ull;
        asm volatile("" : "+v"(ko), "+v"(po), "+v"(qo));
      }
#pragma unroll
      for (int j = 0; j < 4; j++) LDW(pre[j], NV == 2 ? 2 + j : j);
    }
    eo = C_QT + wid * 768 + ((l15 >> 2) * 2 + (l15 & 1)) * 32 + lq * 8 + ((l15 >> 1) & 1) * 4;
    sidx = (l15 & 2) ? 0xEEEE : 0x4444;
    mo = C_IMG + lane * 16;
    io = C_IMG + ((tid & ~30) | ((tid & 10) << 1) | ((tid & 20) >> 1)) * 16;
    gvo = tid * 16;
    asm volatile("" : "+v"(sidx), "+v"(mo), "+v"(io), "+v"(eo));
    if constexpr (R < 2) asm volatile("" : "+v"(gvo));
  }
  f4 a0, a1;
  u32x4 vm = {0u, 0u, 0u, 0u};
  if constexpr (NV > 0) asm volatile("" : "+v"(vm));
  u32 ts = 0;
  u64 tq = 0;
  int dur = DH0 * 64;
  const int ob = ((h >> 3) * 32768 + (h & 7) * 512) ^ xp ^ (NV == 1 ? X_HS : 0);
  const int ib = X_M ^ xp ^ ((wid >> 2) * 32768 + (wid & 3) * 1024);
  int b3 = 0;
  PRIO(QW ? 2 : 1);
#pragma unroll 1
  for (int s = 0;; s++) {
    int mb = ib + b3 * 65536, mbx = X_M ^ xp ^ (196608 + b3 * 1024);
    if constexpr (R == GX) asm volatile("" : "+s"(mb), "+s"(mbx));
    u32x4 g, gx;
    if constexpr (R != 1) ALIGN(HAL);
    BAR();
    STAMP(13);
    if constexpr (R == 1 && NXE) {
      asm volatile("buffer_load_dwordx4 %0, %2, %3, %4 offen sc1\n\tbuffer_load_dwordx4 %1, %2, %3, %5 offen sc1"
                   : "=&v"(g), "=&v"(gx) : "v"(mo), "s"(rsX), "s"(mb), "s"(mbx) : "memory");
    } else {
      g = __builtin_amdgcn_raw_buffer_load_b128(rsX, mo, mb, 16);
      if constexpr (R == GX) gx = __builtin_amdgcn_raw_buffer_load_b128(rsX, mo, mbx, 16);
    }
    SB0();
    if constexpr (R == 0) {
#pragma unroll
      for (int j = 0; j < 4; j++) { const char* kx_ = smem + C_KX + fo + j * 2048;
        pre[j >> 1][j & 1] = *(const bf8*)kx_; pre[2 + (j >> 1)][j & 1] = *(const bf8*)(kx_ + 256); }
    } else if constexpr (R > 1) {
      IDX();
#pragma unroll
      for (int d = 0; d < NV; d++) { const char* vb_ = smem + C_VX + (vt + d) * 1024 + (p4 >> 1) * 512 + (((lq * 4 + q4) ^ ((p4 >> 1) * 8)) << 4) + (p4 & 1) * 8;
        vx[d] = cat8(trrd(vb_), trrd(vb_ + 256)); }
    }
    SB0();
    a0 = zero4(); a1 = zero4();
    f4 c0, c1, a2;
#define A2INI() do { u64 z_; float m0_, m1_; asm volatile("v_mov_b32 %0, %3\n\tv_mov_b32 %1, %3\n\tv_mov_b64 %2, 0\n\ts_nop 1" : "=&v"(m0_), "=&v"(m1_), "=v"(z_) : "v"(mm)); \
                     const f2v zz_ = __builtin_bit_cast(f2v, z_); a2 = (f4){m0_, m1_, zz_[0], zz_[1]}; } while (0)
    if constexpr ((ZE >> R) & 1) {
      c1 = zero4();
      if constexpr (NV != 1) c0 = zero4();
      if constexpr (R == 0) { A2INI(); asm volatile("" : "+v"(a2)); }
    }
    int so_ = X_O(b3) ^ ob, an_ = ANX(s & 1) + h * ANS;
    int nb = s < 63 ? (s + 1) * 262144 + h * 8192 : PJ_OOB;
    asm volatile("" : "+s"(so_), "+s"(an_), "+s"(nb));
    u32x4 nx[2];
#define NXL1(g, i) __builtin_amdgcn_raw_ptr_buffer_load_lds(rsB, (LDSP void*)(smem + C_KX + (g) * 4096), 16, (g) < 2 ? gvo : po, \
                                                             nb + (1 + ((g) >> 1)) * ACB + ((g) & 1) * 4096 - 4096, (i) * 1024, 16)
#define NXL(g) do { NXL1(g, 0); NXL1(g, 1); NXL1(g, 2); NXL1(g, 3); } while (0)
    SB0();
    if (NXE) {
      BARC();
      if constexpr (R == 1) {
        if (NXE > 1) __builtin_amdgcn_s_sleep(NXE - 1);
#pragma unroll
        for (int i = 0; i < 4; i++)
          asm volatile("s_mov_b32 m0, %3\n\ts_nop 0\n\tbuffer_load_dwordx4 %0, %1, %2 offen sc1 lds\n\tbuffer_load_dwordx4 %0, %1, %2 offen offset:1024 sc1 lds\n\t"
                       "buffer_load_dwordx4 %0, %1, %2 offen offset:2048 sc1 lds\n\tbuffer_load_dwordx4 %0, %1, %2 offen offset:3072 sc1 lds"
                       :: "v"(i < 2 ? gvo : po), "s"(rsB), "s"(nb + (1 + (i >> 1)) * ACB + (i & 1) * 4096 - 4096), "i"(C_KX + i * 4096) : "memory", "m0");
      }
      SB0();
    }
#define LDM(j) *(const bf8*)(smem + mo + (j) * 1024)
    constexpr int NB = R == 1 ? 9 : 5;
    bf8 b[NB];
    if constexpr (!QW) {
      PRIO(IPR); BARN();
      if (((EK >> R) & 1) && !EKL) {
#pragma unroll
        for (int j = 0; j < EKN; j++) { b[j] = LDM(j); SB0(); }
      }
    }
    {
      if constexpr (R == 1 && NXE) asm volatile("s_waitcnt vmcnt(17)" : "+v"(g));
      else __builtin_amdgcn_s_waitcnt(R == GX ? 0x0F71 : 0x0F70);
      CFENCE();
      *(u32x4*)(smem + io) = g;
      SB0();
      u32 top = umax(umax(umax(g[0], g[1]), g[2]), g[3]);
      if constexpr (R == GX) {
        asm volatile("" : "+v"(top));
        SB0();
        if constexpr (R == 1 && NXE) asm volatile("s_waitcnt vmcnt(16)" : "+v"(gx));
        *(u32x4*)(smem + GXO + io) = gx;
        SB0();
        top = umax(umax(umax(umax(top, gx[0]), gx[1]), gx[2]), gx[3]);
      }
#define G_BAD() (umax(umax(umax(g[0], g[1]), umax(g[2], g[3])), R != GX ? 0u : umax(umax(gx[0], gx[1]), umax(gx[2], gx[3]))) == ~0u)
      if (__builtin_expect(__any(top == ~0u), 0)) {
        int it = 0;
        do {
          CFENCE();
          if (G_BAD()) {
            g = __builtin_amdgcn_raw_buffer_load_b128(rsX, mo, mb, 16);
            if constexpr (R == GX) gx = __builtin_amdgcn_raw_buffer_load_b128(rsX, mo, mbx, 16);
          }
          VMW0C();
        } while (__any(G_BAD()) && ++it < SPIN_CAP);
        *(u32x4*)(smem + io) = g;
        if constexpr (R == GX) *(u32x4*)(smem + GXO + io) = gx;
      }
    }
    SB0();
    STAMP(15);
    if constexpr (!QW) PRIO(1);
    if (((EK >> R) & 1) && EKL) {
#pragma unroll
      for (int j = 0; j < EKN; j++) { b[j] = LDM(j); SB0(); }
      CFENCE(); __builtin_amdgcn_s_waitcnt(0xC07F | (EKN << 8)); BARN();
    } else
    BARC();
    STAMP(0);
#define ANNC() do { ts = (u32)tq; __builtin_amdgcn_raw_buffer_store_b32((((ts << 6) + (u32)dur) & ~63u) | (u32)s, rsX, 0, an_, 16); SB0(); } while (0)
    if constexpr (R == 1) {
      STAMP(6);
      if (EK & 2) asm volatile("s_memrealtime %0" : "=s"(tq));
      else { asm volatile("s_memrealtime %0\n\ts_waitcnt lgkmcnt(0)" : "=s"(tq)); ANNC(); }
      SB0();
#pragma unroll
      for (int j = (EK & 2) ? EKN : 0; j < 9; j++) { b[j] = LDM(j); SB0(); }
#pragma unroll
      for (int i = 0; i < NXA; i++) { NXL(i); SB0(); }
    } else {
      if (!QW && KSP) __builtin_amdgcn_s_sleep(KSP);
#pragma unroll
      for (int j = ((EK >> R) & 1) ? EKN : 0; j < 4; j++) { b[j] = LDM(j); SB0(); }
    }
#pragma unroll
    for (int j = 0; j < 9; j++) {
      if constexpr (QW) {
        if (j == BJ) {
          BARN();
#pragma unroll
          for (int i = 4; i < 4 + BJ; i++) {
            b[i % 5] = LDM(i); SB0();
          }
        }
        if (j >= BJ && j + 4 < 9) b[(j + 4) % 5] = LDM(j + 4);
      } else if (R != 1 && j + 4 < 9) b[(j + 4) % 5] = LDM(j + 4);
      SB0();
      a0 = SMF(b[j % NB], w[0][j], a0, sidx);
      if (NV < 2 || j >= 4) a1 = SMF(b[j % NB], w[1][j], a1, sidx);
      else a1 = SMF(b[j % 5], cat16(pre[j & 3][0], pre[j & 3][1]), a1, sidx);
      SB0();
      if (R == 1 && (EK & 2) && j == EKA) { asm volatile("s_waitcnt lgkmcnt(0)" : "+s"(tq)); ANNC(); }
      if (j == 0) STAMP(10);
      if (j == 4) STAMP(11);
      if (j == 8) STAMP(12);
      if (NV == 2 && j >= DJ && j < DJ + 4) LDW(pre[j - DJ], (DSLOT >> ((j - DJ) * 4)) & 15);
    }
    {
      {
        const u32 e0 = pk2(a0[0] + a0[2], a0[1] + a0[3]), e1 = pk2(a1[0] + a1[2], a1[1] + a1[3]);
        asm volatile("ds_write2_b32 %0, %1, %2 offset1:%3" :: "v"(eo), "v"(e0), "v"(e1), "i"(96) : "memory");
      }
      if constexpr (R != 0) asm volatile("ds_write_b32 %0, %1 offset:%2" :: "v"(eo), "v"(sidx), "i"(QW ? F_Q : F_K) : "memory");
    }
    SB0();
    STAMP(1);
    if constexpr (QW) PRIO(R ? 0 : SP1);
    else if (KV0) PRIO(0);
    if constexpr ((ZE >> R) & 1) { a0 = c0; a1 = c1; }
    else if constexpr (R != 1) { a1 = zero4(); if constexpr (NV != 1) a0 = zero4(); if constexpr (R == 0) A2INI(); }
    if constexpr (NV == 0) {
      u32 dl = 0;
      const int c = 1;
      bf8 km[2];
      if constexpr (R == 0) {
        if (QSL) __builtin_amdgcn_s_sleep(QSL);
        bf8 qa[2];
        u32 fl;
        do {
          CFENCE();
          fl = *(const volatile LDSP u32*)(smem + fq);
          CFENCE(); SB0();
#pragma unroll
          for (int i = 0; i < 2; i++) { qa[i] = cat8(trrd(smem + qo + i * 1536), trrd(smem + qo + i * 1536 + 128)); SB0(); }
        } while (__builtin_expect(__any(fl == 0u), 0));
        STAMP(2);
        auto rdk = [&]() __attribute__((always_inline)) {
          CFENCE(); SB0();
          fl = *(const volatile LDSP u32*)(smem + fk);
          CFENCE(); SB0();
#pragma unroll
          for (int i = 0; i < 2; i++) { km[i] = cat8(trrd(smem + qo + 3072 + i * 1536), trrd(smem + qo + 3200 + i * 1536)); SB0(); }
        };
        auto mulk = [&]() __attribute__((always_inline)) {
#pragma unroll
          for (int i = 0; i < 2; i++) { a2 = MFMA(qa[i], km[i], a2); if (!i) asm volatile("" : "+{v[240:243]}"(a2)); SB0(); }
        };
#pragma unroll
        for (int i = 0; i < 2; i++) {
          a0 = SMF(qa[i], cat16(pre[i][0], pre[i][1]), a0, sidx); asm volatile("" : "+v"(a0)); SB0();
          a1 = SMF(qa[i], cat16(pre[2 + i][0], pre[2 + i][1]), a1, sidx); if (!i) asm volatile("" : "+v"(a1)); SB0();
        }
#define EX2(d) "v_exp_f32 v[" #d "], v[" #d "]\n\tv_exp_f32 v[" #d "+1], v[" #d "+1]\n\t"
#define DP2(OP, d, N) OP " v[" #d "], v[" #d "], v[" #d "] row_ror:" #N " row_mask:0xf bank_mask:0xf\n\t" OP " v[" #d "+1], v[" #d "+1], v[" #d "+1] row_ror:" #N " row_mask:0xf bank_mask:0xf\n\t"
#define ROW2(OP, d) DP2(OP, d, 8) "s_nop 0\n\t" DP2(OP, d, 4) "s_nop 0\n\t" DP2(OP, d, 2) "s_nop 0\n\t" DP2(OP, d, 1)
#define S_F PKFMS(234, 232, 230, 244) PKFMS(238, 236, 230, 244)
#define S_E EX2(234) EX2(238)
#define S_IO "+{v[232:235]}"(a0), "+{v[236:239]}"(a1)
        f4 z;
#define S_0 "s_setprio %[pr]\n\ts_nop 5\n\t" PKADD(232, 232, 234) PKADD(236, 236, 238) "s_nop 0\n\t" \
            "v_mul_f32_dpp v244, v232, v230 row_newbcast:0 row_mask:0xf bank_mask:0xf\n\tv_mul_f32_dpp v245, v233, v230 row_newbcast:0 row_mask:0xf bank_mask:0xf\n\t" S_F
        if (KP == 3) asm volatile(S_0 S_E "s_setprio 2" : S_IO, "=&{v[244:247]}"(z) : "{v[230:231]}"(cw), [pr] "i"(SP2));
        if (KP == 2) asm volatile(S_0 EX2(234) "s_setprio 2" : S_IO, "=&{v[244:247]}"(z) : "{v[230:231]}"(cw), [pr] "i"(SP2));
        if (KP == 1) asm volatile(S_0 "s_setprio 2" : S_IO, "=&{v[244:247]}"(z) : "{v[230:231]}"(cw), [pr] "i"(SP2));
        STAMP(3);
        if (KD) asm volatile("s_nop %0" :: "i"(KD - 1));
        SB0();
        rdk();
#define S_X PKADD(246, 234, 238)
#define S_M PKFMS(242, 240, 230, 244) EX2(242) "s_nop 0\n\t" PKADD(246, 246, 242) "s_nop 1\n\t" ROW2("v_add_f32_dpp", 246) "v_rcp_f32 v246, v246\n\tv_rcp_f32 v247, v247\n\ts_nop 0\n\t" \
            PKMUL(234, 234, 246) PKMUL(238, 238, 246) PKMUL(242, 242, 246) \
            CV2("v234", 234) CV2("v238", 238) CV2("v242", 242) "ds_write2st64_b32 %[po], v234, v238 offset1:1\n\tds_write_b32 %[po], v242 offset:512\n\tv_min_f32 v246, v246, v247"
        if (KP == 3) asm volatile(S_X : S_IO, "+{v[244:247]}"(z));
        if (KP == 2) asm volatile(EX2(238) "s_nop 0\n\t" S_X : S_IO, "+{v[244:247]}"(z));
        if (KP == 1) asm volatile(S_E "s_nop 0\n\t" S_X : S_IO, "+{v[244:247]}"(z));
        SB0();
        if (__builtin_expect(__any(fl == 0u), 0)) {
          asm volatile("s_nop 0");
          do {
            rdk();
          } while (__any(fl == 0u));
        }
        mulk();
        STAMP(4);
#define S_IO2 S_IO, "+{v[240:243]}"(a2), "+{v[244:247]}"(z) : "{v[230:231]}"(cw), [po] "v"(po) : "memory"
        asm volatile("s_nop 7\n\tv_add_f32_dpp v240, v242, v240 quad_perm:[2,3,0,1] row_mask:0xf bank_mask:0xf\n\tv_add_f32_dpp v241, v243, v241 quad_perm:[2,3,0,1] row_mask:0xf bank_mask:0xf\n\t" S_M : S_IO2);
        SB0();
        if (__builtin_expect(__any(z[2] < SMALL), 0)) {
          asm volatile("v_max3_f32 v244, v232, v236, v240\n\tv_max3_f32 v245, v233, v237, v241\n\ts_nop 0\n\t" ROW2("v_max_f32_dpp", 244) PKMUL(244, 244, 230) S_F S_E "s_nop 0\n\t" S_X S_M : S_IO2);
        }
        SB0();
      } else {
#pragma unroll
        for (int i = NXA; i < (NXV ? NXH : NXB) && !NXE; i++) NXL(i);
        SB0();
        const int rb = X_O(b3 == 0 ? 2 : b3 - 1) ^ ob;
        const u32x4 e = ones4();
        __builtin_amdgcn_raw_buffer_store_b128(e, rsX, so, rb, 16);
        __builtin_amdgcn_raw_buffer_store_b128(e, rsX, so, rb ^ X_HS, 16);
        PSTAMP();
      }
      STAMP(5);
      BARC();
      if constexpr (R == 0) STAMP(6);
      if constexpr (R == 1) {
        const u32 tp = (u32)wall_clock64(); dur += ((int)umin((tp - ts) << 6, 200u * 64u) - dur) >> DSH;
        SB0();
        if (ADE && s < 63) AD_GET(true, K1 + K3 + KXT(KA + KB) + (s ? 0 : KS0), 4096);
        if (NXB < 4) {
          __builtin_amdgcn_s_sleep(NXS);
#pragma unroll
          for (int i = NXB; i < 4; i++) NXL(i);
          BARN();
        }
        ALIGN(HAL);
        VMW0C();
        STAMP(8);
        if (NXB == 4) BARN();
        if (s == 63) break;
      } else {
        *(u32*)(smem + fq) = 0u; *(u32*)(smem + fk) = 0u;
        bf8 qx[2][4];
#pragma unroll
        for (int ks = 0; ks < 8; ks++) { qx[ks >> 2][ks & 3] = *(const bf8*)(smem + C_QX + (ks >> 2) * 256 + fo + (ks & 3) * 2048); if (ks < QXP) SB0(); }
#pragma unroll
        for (int NT = 1; NT <= 2; NT++) {
          f4 sc[2], sm = zero4();
          const bf8* qf = qx[NT - 1];
#pragma unroll
          for (int ks = 0; ks < 4; ks++) {
#pragma unroll
            for (int j = 0; j < NT; j++) sc[j] = MFMA(pre[2 * j + (ks >> 1)][ks & 1], qf[ks], ks ? sc[j] : (j == NT - 1 ? ((f4){mk[0], mk[1], mk[2], mk[3]}) : ((f4){0, 0, 0, 0})));
            if (ks & 1) sm = SMF(km[ks >> 1], cat16(qf[ks - 1], qf[ks]), sm, sidx);
          }
          sm[0] += sm[2]; sm[1] += sm[3];
          float mx = FMX3(FMX3(sc[0][0], sc[0][1], sc[0][2]), sc[0][3], FMX(sm[0], sm[1]));
          if (NT == 2) mx = FMX3(FMX3(mx, sc[1][0], sc[1][1]), sc[1][2], sc[1][3]);
          { auto t = __builtin_amdgcn_permlane16_swap(__float_as_uint(mx), __float_as_uint(mx), false, false);
            mx = FMX(__uint_as_float(t[0]), __uint_as_float(t[1])); }
          { auto t = __builtin_amdgcn_permlane32_swap(__float_as_uint(mx), __float_as_uint(mx), false, false);
            mx = FMX(__uint_as_float(t[0]), __uint_as_float(t[1])); }
          mx *= -CE;
#pragma unroll
          for (int j = 0; j < NT; j++)
#pragma unroll
            for (int r = 0; r < 4; r++) sc[j][r] = __builtin_amdgcn_exp2f(__builtin_fmaf(sc[j][r], CE, mx));
          sm[0] = __builtin_amdgcn_exp2f(__builtin_fmaf(sm[0], CE, mx)); sm[1] = __builtin_amdgcn_exp2f(__builtin_fmaf(sm[1], CE, mx));
          float sum = ((sc[0][0] + sc[0][1]) + (sc[0][2] + sc[0][3])) + (sm[0] + sm[1]);
          if (NT == 2) sum += (sc[1][0] + sc[1][1]) + (sc[1][2] + sc[1][3]);
          { auto t = __builtin_amdgcn_permlane16_swap(__float_as_uint(sum), __float_as_uint(sum), false, false);
            sum = __uint_as_float(t[0]) + __uint_as_float(t[1]); }
          sum = swadd(sum, sum);
          const float inv = __builtin_amdgcn_rcpf(sum);
          *(u32x4*)(smem + C_PX + (NT - 1) * 1024 + gvo) = (u32x4){pk2(sc[0][0] * inv, sc[0][1] * inv), pk2(sc[0][2] * inv, sc[0][3] * inv),
                                                                  NT == 2 ? pk2(sc[1][0] * inv, sc[1][1] * inv) : 0u, NT == 2 ? pk2(sc[1][2] * inv, sc[1][3] * inv) : 0u};
          *(u32*)(smem + C_PX + 2048 + (NT - 1) * 256 + (gvo >> 2)) = pk2(sm[0] * inv, sm[1] * inv);
        }
        STAMP(8);
        BAR();
        if (s == 63) break;
        if (known < (s < 61 ? s + 3 : 64)) {
          int n = 0;
          while (true) {
            u32 bf[3];
#pragma unroll
            for (int i = 0; i < 3; i++) bf[i] = ld4_sc1(WSP(u32, 0) + SY_BLK(i, cb, gvo >> 4));
            const u64 miss = __ballot(!(bf[0] >= FULL && bf[1] >= FULL && bf[2] >= FULL));
            known = miss ? __builtin_ctzll(miss) : 64;
            if (known >= (s < 61 ? s + 3 : 64) || ++n >= SPIN_CAP) break;
            __builtin_amdgcn_s_sleep(KSL);
          }
        }
      }
      STAMP(9);
      YEND();
      if constexpr (R == 1) { if (!ADE) AD_GET(true, K1 + K3, 4096); AD_SPIN(); }
    } else {
      if (NV == 2 && DJ > 5) {
#pragma unroll
        for (int j = 0; j < 4; j++) LDW(pre[j], (DSLOT >> (j * 4)) & 15);
        SB0();
      }
      if (QW && (NV == 2 ? VSL : VSL1)) __builtin_amdgcn_s_sleep(NV == 2 ? VSL : VSL1);
      bf4 p0, p1, p2, p3, m0, m1;
      auto getp = [&]() __attribute__((always_inline)) {
        if (QW && PVP) PRIO(2);
        if (!QW && KV0) PRIO(1);
        SB0();
        STAMP(5);
        BARN();
        p0 = trrd(smem + po); p1 = trrd(smem + po + 256);
        SB0();
        if constexpr (NV == 2) { p2 = trrd(smem + ko); p3 = trrd(smem + ko + 256); }
        SB0();
        m1 = trrd(smem + (NV == 2 ? qo : po) + 512);
        SB0();
      };
      constexpr int VB = (VBP >> (R * 4 - 8)) & 15;
      if constexpr (NV == 2) {
        int u = 0;
#pragma unroll
        for (int i = 0; i < 9; i++) {
          const int bs = (0x31042 >> ((i % 5) * 4)) & 7;
#define RING(a) do { a = SMF(b[bs], cat16(pre[u & 3][0], pre[u & 3][1]), a, sidx); SB0(); \
                     if (u < 7) LDW(pre[u & 3], (DSLOT >> (u * 4 + 16)) & 15); u++; } while (0)
          SB0();
          if (i == VB) getp();
          if (i == 0 || i == 4) RING(a0);
          else a0 = SMF(b[bs], i < 4 ? wx[i - 1] : w[1][i - 5], a0, sidx);
          SB0();
          RING(a1);
          if (i < 4) b[bs] = LDM(i);
        }
      } else {
#pragma unroll
        for (int i = 0; i < 9; i++) {
          SB0();
          if (i == VB) getp();
          if (i < 3) a1 = SMF(b[(i + 4) % 5], wx[i], a1, sidx);
          else a1 = SMF(b[(i + 4) % 5], cat16(pre[(i + 1) & 3][0], pre[(i + 1) & 3][1]), a1, sidx);
          SB0();
          if (i < 4) b[(i + 4) % 5] = LDM(i);
          if (i == 3 || i == 4) LDW(pre[(i + 1) & 3], 1 + i);
        }
      }
      if (VB == 9) getp();
      if constexpr (NV == 2) { vm[0] = pk2(a0[0] + a0[2], a0[1] + a0[3]); SB0(); }
      vm[NV - 1] = pk2(a1[0] + a1[2], a1[1] + a1[3]);
      asm volatile("" : "+v"(vm));
      SB0();
      if (VB > 9) getp();
      {
        const u32x8 pp = ilv(m0, m1, p0, p1);
        const bf8 pq = cat8(p2, p3);
        f4 o = MFMA(vx[0], __builtin_bit_cast(bf8, __builtin_shufflevector(pp, pp, 4, 5, 6, 7)), ((f4){0, 0, 0, 0})); asm volatile("" : "+v"(o)); SB0();
        if constexpr (NV == 2) { o = MFMA(vx[1], pq, o); asm volatile("" : "+v"(o)); SB0(); }
        o = MFMA(__builtin_bit_cast(bf8, vm), __builtin_bit_cast(bf8, __builtin_shufflevector(pp, pp, 2, 3, 4, 5)), o);
        asm volatile("" : "+v"(o));
        asm volatile("" :: "v"(pp));
        if constexpr (NV == 2) asm volatile("" :: "v"(pq));
        SB0();
        __builtin_amdgcn_raw_buffer_store_b64((u32x2){umin(pk2(o[0], o[1]), 0xFFFFFFFEu), pk2(o[2], o[3])}, rsX, so, so_, 16);
        SB0();
      }
      STAMP(7);
      if (QW && !PVP) PRIO(2);
      int xo, lo, pa = 0;
      {
        IDX();
        if constexpr (NV == 1) {
          __builtin_amdgcn_s_sleep(LSL);
          if (NXV && vt >= NXH) {
            asm volatile("s_mov_b32 m0, %3\n\ts_nop 0\n\tbuffer_load_dwordx4 %0, %1, %2 offen sc1 lds\n\tbuffer_load_dwordx4 %0, %1, %2 offen offset:1024 sc1 lds\n\t"
                         "buffer_load_dwordx4 %0, %1, %2 offen offset:2048 sc1 lds\n\tbuffer_load_dwordx4 %0, %1, %2 offen offset:3072 sc1 lds"
                         :: "v"((lane * 16) ^ ((lane & (vt * 16) & 32) << 2)), "s"(rsB), "s"(nb + (1 + (vt >> 1)) * ACB + (vt & 1) * 4096), "s"(C_KX + vt * 4096) : "memory", "m0");
            SB0();
          }
#pragma unroll
          for (int i = 0; i < 2; i++) nx[i] = __builtin_amdgcn_raw_buffer_load_b128(rsB, lane * 16 + i * 1024, nb + vt * 2048, 16);
          pa = C_QX + vt * 2048 + lane * 16;
          asm volatile("" : "+v"(pa));
        }
        xo = OB_OFF((lq & 1) * 16 + l15, (lq >> 1) * 8);
        lo = lane * 4;
        asm volatile("" : "+v"(xo), "+v"(lo));
#pragma unroll
        for (int j = 0; j < 4; j++) LDW(pre[j], NV == 2 ? 2 + j : j);
      }
      SB0();
      BARN();
      STAMP(8);
      {
        f4 o[2][NV];
#pragma unroll
        for (int it = 0; it < 2; it++) {
          const bf8 pf = *(const bf8*)(smem + C_PX + it * 1024 + lo * 4);
          const u32 pm = *(const u32*)(smem + C_PX + 2048 + it * 256 + lo);
#pragma unroll
          for (int d = 0; d < NV; d++) {
            o[it][d] = MFMA(vx[d], pf, ((f4){0, 0, 0, 0}));
            o[it][d] = MFMA(__builtin_bit_cast(bf8, vm), __builtin_bit_cast(bf8, d ? (u32x4){0u, pm, 0u, 0u} : (u32x4){pm, 0u, 0u, 0u}), o[it][d]);
          }
        }
#pragma unroll
        for (int d = 0; d < NV; d++)
          __builtin_amdgcn_raw_buffer_store_b128(pair16(umin(pk2(o[0][d][0], o[0][d][1]), 0xFFFFFFFEu), pk2(o[0][d][2], o[0][d][3]),
                                                        umin(pk2(o[1][d][0], o[1][d][1]), 0xFFFFFFFEu), pk2(o[1][d][2], o[1][d][3])), rsB,
              xo, 3 * ACB + OB_OFF(s * 32, h * 128 + (vt + d) * 16), 16);
        SB0();
        if (s == 63) break;
        if constexpr (NV == 1) { *(u32x4*)(smem + pa) = nx[0]; *(u32x4*)(smem + pa + 1024) = nx[1]; }
      }
      STAMP(9);
      YEND();
    }
    b3 = b3 == 2 ? 0 : b3 + 1;
    STAMP(14);
  }
  };
  if (wid == 0) run(std::integral_constant<int, 0>{});
  else if (wid == 4) run(std::integral_constant<int, 1>{});
  else if (wid > 4) run(std::integral_constant<int, 3>{});
  else if (wid == 3) run(std::integral_constant<int, 2>{});
  else run(std::integral_constant<int, 4>{});
  PRIO(0);
  TLS(0);
  return xp;
}

__device__ __forceinline__ int chain_out(const Params& p, char* smem, const int c, const int wv, const bool spec) {
  STS(32 + c, 1);
  constexpr int R = 5; (void)R;
  const int wid = wv, tq = c & 1, cg = c >> 1;
  u32x8 w[4][5];
  const __amdgpu_buffer_rsrc_t rsX = RSRC(XWIN(p), X_END);
  int xp = 0, mo, wo, ro;
  const int ms = wid < 4 ? 65536 : 1024;
#define OST 0
  const int r1 = tq & OST, r2 = cg & (OST >> 1);
  {
    IDX();
    mo = wid < 4 ? (cg >> 3) * 32768 + (cg & 7) * 512 + tq * 256 + wid * 64 : 196608 + (cg >> 2) * 512 + tq * 256 + (cg & 3) * 64;
    if (wid > 4 || (wid == 4 && cg >= 8)) mo = PJ_OOB;
    mo ^= X_M;
    u32 m0[2], old = 0u;
    if (wid == 0) {
#pragma unroll
      for (int i = 0; i < 2; i++) m0[i] = *(const GLB u32*)(p.Bm + (tq * 4 + q4) * 576 + (i ? 512 + (cg & 7) * 8 : cg * 32 + lq * 8) + p4 * 2);
      const int cal = X_CAL + (cg & 7) * 32768 + (cg >> 3) * 1024 + tq * 512;
      __builtin_amdgcn_raw_buffer_store_b32(0u, rsX, lane < 8 ? ((2048 << p4) & ~2048) + q4 * 2304 : PJ_OOB, cal, 16);
      asm volatile("" :: "v"(m0[0]), "v"(m0[1]));
      VMW0();
      TLS(8);
      u32 d[4] = {0u, 0u, 0u, 0u};
#pragma unroll 1
      for (int r = 0; r < CALR; r++) {
#pragma unroll
        for (int k = 0; k < 4; k++) {
          const u32 t0 = (u32)__builtin_readcyclecounter();
          __builtin_amdgcn_raw_buffer_store_b32(1u, rsX, lane ? PJ_OOB : 0, cal + ((2048 << k) & ~2048) + (r ^ tq) * 2304, 16);
          VMW0();
          d[k] += (u32)__builtin_readcyclecounter() - t0;
        }
      }
      if (OBE) BARN();
      if (spec) old = confirm(p, smem, 32 + c, BEAT_LD(c), wv);
      if (lane == 0) st4_sc1(WSP(u32, 0) + SY_VOTE(c), 8u + (((d[1] < d[0] ? 1u : 0u) + (d[3] < d[0] ? 4u : 0u)) ^ (d[2] < d[0] ? 3u : 0u)));
      TLS(11);
    }
    if (!OBE || wid) BARN();
    if (wid == 0) {
      u32 fv;
      int it = 0;
      while (fv = ld4_sc1(WSP(u32, 0) + SY_VOTE(lane & 31)), xp = -((__builtin_amdgcn_readfirstlane(old) >> c) & 1), !(xp || __all(fv != 0u)) && ++it < SPIN_CAP) {
        BEAT_ON(it); __builtin_amdgcn_s_sleep(2);
      }
#pragma unroll
      for (int j = 0; j < 3; j++) xp |= __popcll(__ballot((fv >> j) & 1u)) > 32 ? 4096 << j : 0;
      if (xp >= 0) {
#pragma unroll
        for (int i = 0; i < 3; i++) __builtin_amdgcn_raw_buffer_store_b32(i ? ~0u : umin(m0[0], 0xFFFFFFFEu), rsX, lane * 4, mo ^ xp ^ (i * 65536), 16);
        __builtin_amdgcn_raw_buffer_store_b32(lq ? ~0u : umin(m0[1], 0xFFFFFFFEu), rsX, lq < 3 && cg < 8 ? lq * 1024 + l15 * 4 : PJ_OOB,
                                              (196608 + (cg >> 2) * 512 + tq * 256 + (cg & 3) * 64) ^ X_M ^ xp, 16);
#pragma unroll
        for (int i = 0; i < 6; i++)
          __builtin_amdgcn_raw_buffer_store_b128(ones4(), rsX, (lane >> 5) * 131072 + (lane & 31) * 16, X_O(i >> 1) ^ ((c >> 3) * 32768 + (c & 7) * 512) ^ xp ^ ((i & 1) * X_HS), 16);
        if (c == 0) {
#pragma unroll
          for (int i = 0; i < 6; i++) __builtin_amdgcn_raw_buffer_store_b128((u32x4){0u, 0u, 0u, 0u}, rsX, lane * 16 + (i & 3) * 1024, X_HOT(xp) ^ (i < 4 ? X_CS : 0), 16);
        }
        __builtin_amdgcn_raw_buffer_store_b32(lane & 2 ? DO0 * 64u : ~0u, rsX, lane < 4 ? ANX(lane & 1) + ((lane & 2) * 16 + c) * ANS : PJ_OOB, 0, 16);
      }
    }
    SB0();
    const __amdgpu_buffer_rsrc_t rw = RSRC(p.BWo, 576 * 4096 * 2);
    u32x4 st[4][8];
    u32 fl;
    bool early = false;
    const int va = ((lane >> 2) * 576 + cg * 32 + p4 * 8) * 2;
#pragma unroll
    for (int i = 0; i < 4; i++) {
#pragma unroll
      for (int k = 0; k < 8; k++) st[i][k] = __builtin_amdgcn_raw_buffer_load_b128(rw, va, ((((i & 1) ^ r1) * 16 + wid * 2 + ((k >> 2) ^ r2)) * 128 + (i >> 1) * 64 + (k & 3) * 16) * 1152, OSTG_AUX);
      SB0();
      if (i == 1 && wid == 0 && xp >= 0) {
        asm volatile("s_waitcnt vmcnt(16)" ::: "memory");
        if (lane == 0) st4_sc1(WSP(u32, 0) + SY_INIT(c), (u32)xp + 1u);
        TLS(10);
      }
      if (i == 2) fl = ld4_sc1(WSP(u32, 0) + SY_WX);
      SB0();
    }
    auto wx = [&](int vo) {
#pragma unroll
      for (int k = 0; k < 8; k++)
        st[0][k] = __builtin_amdgcn_raw_buffer_load_b128(RSRC(WSP(char, O_WX), 524288), vo, cg * 65536 + ((((k & 2) ^ (r1 * 2)) * 8 + wid * 2 + (k & 1)) * 128 + (k >> 2) * 64) * 16, 16);
    };
    auto poll = [&]() { fl = __builtin_amdgcn_raw_buffer_load_b32(RSRC(p.ws, wid ? 0 : O_QA), (lane < 32 ? SY_INIT(lane) : lane < 48 ? SY_ARR : SY_WX) * 4, 0, 16); };
    char* sc = smem + wid * 10240;
    auto frag = [&](const char* b, int rs) {
      bf4 tt[4];
#pragma unroll
      for (int kk = 0; kk < 4; kk++) {
        const int r0 = ((kk >> 1) * 2 + (lq >> 1)) * 32 + (p4 >> 1) * 16 + (q4 >> 1) * 8 + (lq & 1) * 4 + (kk & 1) * 2 + (q4 & 1);
        tt[kk] = trrd(b + r0 * rs + (p4 & 1) * 8);
      }
      return ilv(tt[0], tt[1], tt[2], tt[3]);
    };
#pragma unroll
    for (int i = 0; i < 4; i++) {
#pragma unroll
      for (int k = 0; k < 8; k++) *(u32x4*)(sc + ((k ^ (r2 * 4)) * 16 + (lane >> 2)) * 80 + p4 * 16) = st[i][k];
      __builtin_amdgcn_fence(__ATOMIC_RELEASE, "wavefront");
      __builtin_amdgcn_wave_barrier();
#pragma unroll
      for (int t = 0; t < 4; t++) w[i][t] = frag(sc + t * 16, 80);
      asm volatile("s_waitcnt lgkmcnt(0)" ::: "memory");
      __builtin_amdgcn_wave_barrier();
      if (i == 2) { early = __all(fl == ~0u); CFENCE(); poll(); wx(early ? lane * 16 : PJ_OOB); }
      SB0();
    }
    if (wid == 0) {
      int it = 0;
      while (xp >= 0 && !__all(fl >= (lane < 32 ? 1u : lane < 48 ? 256u : ~0u)) && ++it < SPIN_CAP) { BEAT_ON(it); __builtin_amdgcn_s_sleep(GPS); CFENCE(); poll(); }
      *(int*)(smem + C_XP) = xp;
    }
    asm volatile("s_waitcnt lgkmcnt(0)" ::: "memory");
    BARN();
    xp = __builtin_amdgcn_readfirstlane(*(const int*)(smem + C_XP));
    if (xp < 0) return xp;
    {
      if (!early) wx(lane * 16);
#pragma unroll
      for (int k = 0; k < 8; k++) *(u32x4*)(sc + k * 1024 + lane * 16) = st[0][k];
      __builtin_amdgcn_fence(__ATOMIC_RELEASE, "wavefront");
      __builtin_amdgcn_wave_barrier();
#pragma unroll
      for (int i = 0; i < 4; i++) w[i][4] = frag(sc + i * 2048, 16);
    }
    mo = __builtin_amdgcn_readfirstlane(mo ^ xp);
    const int row = (lq & 1) * 4 + ((l15 >> 1) & 3);
    wo = ((lq >> 1) == hi8 ? row * 128 : 1024) + (((wid * 2 + hi8) ^ (row * 2)) << 3) + (l15 & 1) * 4;
    ro = wid > 4 ? 0 : wid * 2560 + (lane >> 5) * 1280 + ((lane >> 2) & 7) * 128 + (lane & 8) * 8 + p4 * 16;
  }
  __syncthreads();
  STS(32 + c, 14);
  TLS(9);
  int go, sidx, po;
  { IDX(); go = lane * 16; sidx = (l15 & 2) ? 0xEEEE : 0x4444;
    po = p4 == 0 ? (lq & 1) * 32 + (lq >> 1) * 16 + q4 * 4 : PJ_OOB; }
  int ro1 = ro ^ 64;
  asm volatile("" : "+v"(ro), "+v"(ro1), "+v"(wo));
#define PUTK(t, r, e, nop) asm volatile(nop PKADD(r, r, (r + 2)) "ds_write2st64_b32 %1, v[" #r "], v[" #r "+1] offset0:%2 offset1:%3" \
                                  : "+{v[" #r ":" #e "]}"(d[t]) : "v"(wo), "i"((t) * 10), "i"((t) * 10 + 5) : "memory")
#define PUTP(t) do { if (PKA & 8) { if (t == 0) PUTK(0, 236, 239, "s_nop 5\n\t"); if (t == 1) PUTK(1, 240, 243, ""); if (t == 2) PUTK(2, 244, 247, ""); \
                                    if (t == 3) PUTK(3, 248, 251, "s_nop 2\n\t"); if (t == 4) PUTK(4, 252, 255, "s_nop 0\n\t"); } \
    else asm volatile("ds_write2st64_b32 %0, %1, %2 offset0:%3 offset1:%4" :: "v"(wo), "v"(d[t][0] + d[t][2]), "v"(d[t][1] + d[t][3]), "i"((t) * 10), "i"((t) * 10 + 5) : "memory"); } while (0)
#define APS 18
#define ONOP 7
  int b3 = 0, dur = DO0 * 64;
#pragma unroll 1
  for (int s = 0; s < 63; s++) {
    f4 d[5];
    int gb[4], pb = mo ^ ((b3 == 2 ? 0 : b3 + 1) * ms);
#pragma unroll
    for (int i = 0; i < 4; i++) { gb[i] = X_O(b3) ^ xp ^ (tq * 131072 + (((i & 1) ^ r1) * 2 + (wid >> 2)) * 32768 + (wid & 3) * 1024 + (i < 2 ? X_HS : 0)); asm volatile("" : "+s"(gb[i])); }
    asm volatile("" : "+s"(pb));
    u32x4 g[4];
    u32 bad;
    auto mult = [&]() __attribute__((always_inline)) {
      CFENCE();
#pragma unroll
      for (int i = 0; i < 4; i++) {
        g[i] = __builtin_amdgcn_raw_buffer_load_b128(rsX, go, gb[i], 16);
        SB0();
        if (ONOP >= 0 && i < 3) asm volatile("s_nop %0" :: "i"(ONOP >= 0 ? ONOP : 0));
        SB0();
      }
      bad = 0u;
#pragma unroll
      for (int i = 0; i < 4; i++) {
        SB0();
#pragma unroll
        for (int t = 0; t < 5; t++) {
          d[t] = SMF(g[i], w[i][t], d[t], sidx);
          if (t == 0) bad = umax(umax(bad, g[i][0]), g[i][2]);
          if (i == 3 && t >= 2) { SB0(); PUTP(t - 2); SB0(); }
        }
      }
      SB0();
#pragma unroll
      for (int t = 3; t < 5; t++) PUTP(t);
      SB0();
    };
#pragma unroll
    for (int t = 0; t < 5; t++) d[t] = zero4();
    SB0();
    u32 dl = 0;
    ALIGN(OAL);
    if (wid == 7) { __builtin_amdgcn_s_sleep(APS); STAMP(4); AD_GET(false, K1 + KXT(KA), SPIN_CAP); STAMP(5); AD_SPIN(); }
    STAMP(15);
    BARN();
    STAMP(0);
    mult();
    if (__builtin_expect(__any(bad == ~0u), 0)) {
      int it = 0;
      do {
#pragma unroll
        for (int t = 0; t < 5; t++) d[t] = zero4();
        mult();
      } while (__any(bad == ~0u) && ++it < SPIN_CAP);
    }
    STAMP(1);
    BARC();
    STAMP(2);
#define OALL 1
    f4 t0, t1;
    if (OALL) { t0 = *(const f4*)(smem + ro); t1 = *(const f4*)(smem + ro1); SB0(); }
    if (__builtin_expect(wid < 5, 1)) {
      if (!OALL) { t0 = *(const f4*)(smem + ro); t1 = *(const f4*)(smem + ro1); }
      if (PKA & 16) {
#define QP2(q) "v_add_f32_dpp v228, v228, v228 quad_perm:" q " row_mask:0xf bank_mask:0xf\n\tv_add_f32_dpp v229, v229, v229 quad_perm:" q " row_mask:0xf bank_mask:0xf\n\t"
        asm volatile(PKADD(228, 228, 230) PKADD(232, 232, 234) PKADD(228, 228, 232) "s_nop 1\n\t"
                     QP2("[1,0,3,2]") "s_nop 0\n\t" QP2("[2,3,0,1]") CV2("v228", 228) "v_min_u32 v228, -2, v228\n\tbuffer_store_dword v228, %2, %3, %4 offen sc1"
                     : "+{v[228:231]}"(t0), "+{v[232:235]}"(t1) : "v"(po), "s"(rsX), "s"(pb) : "memory");
      } else {
      float a = (t0[0] + t0[2]) + (t1[0] + t1[2]), b = (t0[1] + t0[3]) + (t1[1] + t1[3]);
      a += __uint_as_float(__builtin_amdgcn_mov_dpp(__float_as_uint(a), 0xB1, 0xf, 0xf, true));
      b += __uint_as_float(__builtin_amdgcn_mov_dpp(__float_as_uint(b), 0xB1, 0xf, 0xf, true));
      a += __uint_as_float(__builtin_amdgcn_mov_dpp(__float_as_uint(a), 0x4E, 0xf, 0xf, true));
      b += __uint_as_float(__builtin_amdgcn_mov_dpp(__float_as_uint(b), 0x4E, 0xf, 0xf, true));
      __builtin_amdgcn_raw_buffer_store_b32(umin(pk2(a, b), 0xFFFFFFFEu), rsX, po, pb, 16);
      }
      SB0();
      STAMP(3);
      __builtin_amdgcn_s_sleep(4);
      __builtin_amdgcn_raw_buffer_store_b32(~0u, rsX, po, mo ^ (b3 * ms), 16);
    } else if (wid == 7) {
      dur += (min((int)(((u32)wall_clock64() << 6) - dl), dur + DOCAP * 64) - dur) >> DSH;
      if (DRL) __builtin_amdgcn_s_sleep(DRL);
      __builtin_amdgcn_raw_buffer_store_b32((u32)dur, rsX, 0, ANX(~s & 1) + (32 + c) * ANS, 16);
      if (CSL && c == CSC && s > 0) { if (CSL > DRL) __builtin_amdgcn_s_sleep(CSL > DRL ? CSL - DRL : 0); __builtin_amdgcn_raw_buffer_store_b32(dl | 1u, rsX, 0, (X_HOT(xp) ^ X_CS) + (s - 1) * 64, 16); }
    }
    VMW0C();
    YEND();
    b3 = b3 == 2 ? 0 : b3 + 1;
  }
  __syncthreads();
  return xp;
}

__device__ __forceinline__ void wait_ge(const u32* w, u32 val, const int wv) {
  if (TIDX == 0) {
    int it = 0;
    while (ld4_sc1(w) < val && ++it < SPIN_CAP) __builtin_amdgcn_s_sleep(WPS);
  }
}
__device__ __forceinline__ void acquire_all(const int wv) {
  if (TIDX == 0) {
    __builtin_amdgcn_fence(__ATOMIC_ACQUIRE, "agent");
    VMW0();
  }
  __syncthreads();
}

#define T_KV 0
#define T_Q 0
#define T_A 192
#define T_W (T_A + 64)
#define T_S (T_W + 256)
#define T_L (T_S + 72)
#define T_O T_L
#define T_CHAIN (1 << 20)
#define CODE_KB 84
#define OWARM 2
#define BXW 1
#define AORD 0x01234567u
#define OORD 0x67014523u

__device__ __forceinline__ int next_task(const Params& p, int* s_task, const int wv, const int pt, const int pre) {
  __syncthreads();
  if (wv == 0) {
    int t = T_A + pre;
    if (__builtin_amdgcn_readfirstlane(pre) < 0) {
      t = -1;
      int xp = 0;
      for (int it = 0; it < SPIN_CAP; it++) {
        const int l = TIDX & 15;
        const u32 v = ld4_sc1(WSP(u32, 0) + (l < 8 ? SY_JOB(l) : l == 8 ? SY_ARR : SY_INIT(0)));
        const u32 open = (u32)__ballot(v != ~0u) & 255u;
        xp = __builtin_amdgcn_readlane(v, 9);
        if (!open && xp) break;
        if (!open || __builtin_amdgcn_readlane(v, 8) < 256u) { BEAT_ON(it); __builtin_amdgcn_s_sleep(8); continue; }
        const int x = __builtin_ctz(open), k = __builtin_ctz(~(u32)__builtin_amdgcn_readlane(v, x));
        u32 old = 0u;
        if (TIDX == 0) old = AOR(SY_JOB(x), 1u << k);
        if (!((__builtin_amdgcn_readfirstlane(old) >> k) & 1)) { t = x * 32 + k + (x < 2 ? T_CHAIN : -64); break; }
      }
      if (t < 0 && TIDX == 0) t = T_A + (int)__hip_atomic_fetch_add(HOTP(xp) + SY_QHEAD, 1u, __ATOMIC_RELAXED, __HIP_MEMORY_SCOPE_AGENT) + (xp & 0x7000);
    }
    if (TIDX == 0) *s_task = t;
  }
  __syncthreads();
  return __builtin_amdgcn_readfirstlane(*s_task);
}

__device__ __forceinline__ int take_task(int* s_task, const int pre, const int wv, const int pb, const int pl, const int pt) {
  if (TIDX == 0) {
    *s_task = pre;
  }
  __syncthreads();
  const int t = __builtin_amdgcn_readfirstlane(*s_task);
  __syncthreads();
  return t;
}
#if defined(__HIP_DEVICE_COMPILE__) && !defined(KEEP_PK)
#define NOPK __attribute__((target("no-packed-fp32-ops")))
#else
#define NOPK
#endif
__device__ __forceinline__ void mega_body(const Params& p0, char* smem) {
  int* const s_task = (int*)(smem + LDS_TOTAL);
  int wv = __builtin_amdgcn_readfirstlane(threadIdx.x >> 6), t, xs;
#define tid TIDX
  {
    const Params& p = p0;
    u64 pc0; asm volatile("s_getpc_b64 %0" : "=s"(pc0));
#define ZB 1
    const int xcc = (int)(__builtin_amdgcn_s_getreg(20 | (3 << 11)) & 7u), xq = xcc >= 2 ? xcc - 2 : xcc, idx = (blockIdx.x >> 3) & 31;
    t = xq * 32 + (xcc < 2 ? T_CHAIN + idx : (idx + 31) & 31);
    xs = xcc + 8;
#define WARM(ptr, bytes, vo, so, sc) asm volatile("s_mov_b32 m0, %3\n\ts_nop 0\n\tbuffer_load_dword %0, %1, %2 offen" sc " lds" \
                                                   :: "v"(vo), "s"(RSRC(ptr, bytes)), "s"(so), "s"(C_B0 + wv * 256) : "memory", "m0")
    if (wv == 0) WARM(p.ws, O_QA, 0, SY_BEAT(t & 7) * 4, " sc1");
    if (blockIdx.x == ZB) {
#pragma unroll
      for (int i = 0; i < 4; i++) __builtin_amdgcn_raw_buffer_store_b128((u32x4){0u, 0u, 0u, 0u}, RSRC(p.ws, SY_WORDS * 4), tid * 16 + i * 8192, 0, 16);
      VMW0();
      BARN();
      if (tid < 8) AADD(SY_BEAT(tid), 1u);
    }
    if (wv) {
      const int g = idx * 7 + wv - 1, vo = (tid & 63) * 64;
      if (g < CODE_KB / 4) WARM(pc0, CODE_KB * 1024, vo, g * 4096, "");
      if (xcc > 1) {
        if (OWARM == 1 && g >= 32) WARM(p.BWo, WOB, vo, (xq * 192 + g - 32) * 4096, "");
        if (OWARM == 2 && g >= 128) WARM(p.BWo, WOB, vo * 2, (xq * 96 + g - 128) * 8192, "");
        if (BXW) WARM(p.Bx, 256 * 1152, vo, (223 - g) * 4096, "");
      }
    }
  }
  u64 pp = (u64)__builtin_amdgcn_kernarg_segment_ptr();
  SB0();
  asm volatile(".Lmega_again:" : "+{s[40:41]}"(pp), "+{s42}"(wv), "+{s43}"(t), "+{s44}"(xs) :: "memory");
  SB0();
  const Params& p = *(const Params*)(const __attribute__((address_space(4))) Params*)pp;
  const int xcc = xs & 7, xq = xcc >= 2 ? xcc - 2 : xcc;
  const bool spec = xs > 7;
  int pre = -1;
  if (t < T_A) {
    const int x = t >> 5, j = (t & 7) * 4 + ((0x9C >> ((t >> 2) & 6)) & 3);
    proj_slot(p, x, j, smem, wv, pre, spec ? t + 64 : -1);
    VMW0C();
  } else {
    int own;
    if (t < T_CHAIN + 32) { own = chain_head(p, smem, t - T_CHAIN, wv, spec); VMW0C(); }
    else { own = chain_out(p, smem, t - T_CHAIN - 32, wv, spec); VMW0C(); asm volatile("s_nop 0"); }
    if (own >= 0) {
      const int l = tid & 7;
      const u32 v = ld4_sc1(HOTP(own) + (l < 7 ? SY_OQ(l) : SY_QHEAD));
      if (tid == 0) *s_task = 0;
      __syncthreads();
      if (v < (l == 7 ? T_S - T_A : l == 6 ? 72 : (l & 1) ? NOB : NOA)) *s_task = 1;
      __syncthreads();
      if (__builtin_amdgcn_readfirstlane(*s_task) == 0) {
        if (xcc == 0) TLS(3); else TLS(4);
        return;
      }
    }
  }
  t = next_task(p, s_task, wv, t, pre);
  if (t < T_A || t >= T_CHAIN) {
    asm volatile("s_waitcnt vmcnt(0) lgkmcnt(0)\n\ts_barrier\n\ts_branch .Lmega_again" :: "{s[40:41]}"(pp), "{s42}"(wv), "{s43}"(t), "{s44}"(xcc) : "memory");
    return;
  }
  u32* const hot = HOTP(t);
  u32* const qh = hot + SY_QHEAD;
  t &= ~0x7000;
#define WPOS 0
  while (t < T_S) {
    if ((u32)(t - T_A - WPOS) < 64u) {
      wof_task(p, t - T_A - WPOS, smem, wv, qh, pre);
      if (tid == 0) __hip_atomic_fetch_add(hot + SY_CW, 1u, __ATOMIC_RELAXED, __HIP_MEMORY_SCOPE_AGENT);
      t = T_A + take_task(s_task, pre, wv, T_A, T_S - T_A, t);
      continue;
    }
    const int qb = (AORD >> (((t - (t < T_A + WPOS ? T_A : T_W)) >> 5) * 4)) & 7, h = t & 31;
    if (tid < 64) {
      int it = 0;
      while (true) {
        const u32 f0 = ld4_sc1(WSP(u32, 0) + SY_BLK(3, h >> 1, tid)), f1 = ld4_sc1(WSP(u32, 0) + SY_BLK(4, h >> 1, tid)), f2 = ld4_sc1(WSP(u32, 0) + SY_BLK(5, h >> 1, tid));
        if (__all(tid >= qb * 8 + 8 || ((tid < qb * 8 || f0 >= FULL) && f1 >= FULL && f2 >= FULL)) || ++it >= SPIN_CAP) break;
        __builtin_amdgcn_s_sleep(8);
      }
    }
    acquire_all(wv);
    fa::attn_block(WSP(char, O_QA) + FR_OFF(qb * 256, h * 128), WSP(char, O_KA) + h * 8192, WSP(char, O_VA) + h * 8192,
                   WSP(char, O_CA) + OB_OFF(qb * 256, h * 128), qb * 256, smem, wv, qh, SY_OQ(xq) - SY_QHEAD, pre);
    if (tid == 0) __hip_atomic_fetch_add(hot + SY_CC(qb), 1u, __ATOMIC_RELAXED, __HIP_MEMORY_SCOPE_AGENT);
    t = T_A + take_task(s_task, pre, wv, T_A, T_S - T_A, t);
  }
  int rest = 64 | (1 << xq);
#pragma unroll 1
  while (rest) {
    const int x = __builtin_ctz(rest);
    rest &= rest - 1;
    if (x == 6) {
      CLAIMS();
      t = take_task(s_task, pre, wv, T_S, 72, -1);
      while ((t & 0xFFFF) < 72) {
        t &= 0xFFFF;
        small_out_task(p, t & 3, t >> 2, smem, wv, hot, pre);
        t = take_task(s_task, pre, wv, T_S, 72, T_S + t);
      }
      rest = t >> 16;
      continue;
    }
    const int part = x & 1, cg = x >> 1, n = part ? NOB : NOA;
    u32* const qh = hot + SY_OQ(x);
    if (t >= 0x10000 && rest == 64) {
      t -= T_A + 0x10000;
    } else {
      CLAIM();
      t = take_task(s_task, pre, wv, T_O + x * 32, n, -1);
    }
    while (t < n) {
      const int nb = part ? NBB : NAB, big = t < nb, e = big ? t * 2 : t + nb;
      const int rb = part ? e : ((OORD >> ((e >> 2) * 4)) & 7) * 4 + (e & 3);
      if (tid < 2) {
        const u32* w = tid ? hot + SY_CW : part ? CSP((rb + big) * 2 + 1) : hot + SY_CC(rb >> 2);
        int it = 0;
        while (__any(ld4_sc1(w) < (tid ? 64u : part ? 1u : 32u)) && ++it < SPIN_CAP) __builtin_amdgcn_s_sleep(WPS);
      }
      acquire_all(wv);
      const char *pa = WSP(char, part ? O_OB : O_CA) + rb * 2 * 262144, *pw = WSP(char, O_WOF) + part * WOB + cg * 12 * 131072;
      u16* const pc = p.out + (size_t)rb * 64 * 1152 + part * 576 + cg * 192;
      if (big) out_tile<8>(pa, pw, pc, smem, wv, hot, qh, pre);
      else out_tile<4>(pa, pw, pc, smem, wv, hot, qh, pre);
      t = take_task(s_task, pre, wv, T_O + x * 32, n, T_O + x * 32 + t);
    }
    if (t == 0x20000) rest = 0;
  }
  if (xcc == 0) TLS(3); else if (xcc == 1) TLS(4); else TLS(5);
}

#undef tid
__global__ NOPK void __launch_bounds__(512, 2) mega(Params p) {
  __shared__ __attribute__((aligned(16))) char smem[LDS_ALL];
  mega_body(p, smem);
}
extern "C" void kernel_launch(void* const* d_in, const int* in_sizes, int n_in, void* d_out, int out_size, void* d_ws,
                              size_t ws_size, hipStream_t stream) {
  Params p;
  p.Ax = (const u16*)d_in[0]; p.Ac = (const u16*)d_in[1]; p.Awq = (const u16*)d_in[2]; p.Awk = (const u16*)d_in[3];
  p.Awv = (const u16*)d_in[4]; p.Awo = (const u16*)d_in[5]; p.Bx = (const u16*)d_in[6]; p.Bm = (const u16*)d_in[7];
  p.BWq = (const u16*)d_in[8]; p.BWk = (const u16*)d_in[9]; p.BWv = (const u16*)d_in[10]; p.BWo = (const u16*)d_in[11];
  p.out = (u16*)d_out;
  p.ws = (char*)d_ws;
  mega<<<256, 512, 0, stream>>>(p);
}
```

```cpp
#include <hip/hip_runtime.h>
#include <hip/hip_bf16.h>
#include <stdint.h>
#include <type_traits>

typedef unsigned short u16;
typedef unsigned int u32;
typedef unsigned long long u64;
typedef __attribute__((ext_vector_type(8))) short bf8;
typedef __attribute__((ext_vector_type(4))) short bf4;
typedef __attribute__((ext_vector_type(4))) float f4;
typedef __attribute__((ext_vector_type(2))) float f2v;
typedef __attribute__((ext_vector_type(2))) __bf16 bf2v;
typedef __attribute__((ext_vector_type(4))) unsigned u32x4;
typedef __attribute__((ext_vector_type(2))) unsigned u32x2;
typedef float f32x16 __attribute__((ext_vector_type(16)));
typedef __attribute__((ext_vector_type(8))) unsigned u32x8;
typedef __attribute__((ext_vector_type(8))) __bf16 bh8;
typedef __attribute__((ext_vector_type(16))) __bf16 bh16;
#define LDSP __attribute__((address_space(3)))

__device__ __forceinline__ u32 pk2(float a, float b) {
  f2v x = {a, b};
  bf2v y = __builtin_convertvector(x, bf2v);
  return __builtin_bit_cast(u32, y);
}
__device__ __forceinline__ u32 umin(u32 a, u32 b) { return a < b ? a : b; }
__device__ __forceinline__ u32 umax(u32 a, u32 b) { return a > b ? a : b; }
__device__ __forceinline__ float rbf(float f) { return __uint_as_float(pk2(f, f) << 16); }
#define MFMA(a, b, c) __builtin_amdgcn_mfma_f32_16x16x32_bf16(a, b, c, 0, 0, 0)
#define MFMA16(a, b, c) __builtin_amdgcn_mfma_f32_16x16x16bf16_1k(a, b, c, 0, 0, 0)
#define SMF(a, b, c, idx) __builtin_amdgcn_smfmac_f32_16x16x64_bf16(__builtin_bit_cast(bh8, a), __builtin_bit_cast(bh16, b), c, idx, 0, 0)
__device__ __forceinline__ u32x8 ilv(bf4 a, bf4 b, bf4 c, bf4 d) {
  const u32x2 x = __builtin_bit_cast(u32x2, a), y = __builtin_bit_cast(u32x2, b), z = __builtin_bit_cast(u32x2, c), w = __builtin_bit_cast(u32x2, d);
  return (u32x8){x[0], x[1], y[0], y[1], z[0], z[1], w[0], w[1]};
}
__device__ __forceinline__ u32x8 cat16(bf8 a, bf8 b) {
  const u32x4 x = __builtin_bit_cast(u32x4, a), y = __builtin_bit_cast(u32x4, b);
  return (u32x8){x[0], x[1], x[2], x[3], y[0], y[1], y[2], y[3]};
}
__device__ __forceinline__ float swadd(float a, float b) {
  auto x = __builtin_amdgcn_permlane32_swap(__float_as_uint(a), __float_as_uint(b), false, false);
  return __uint_as_float(x[0]) + __uint_as_float(x[1]);
}
__device__ __forceinline__ bf4 trrd(const char* p) {
  return __builtin_amdgcn_ds_read_tr16_b64_v4i16((LDSP bf4*)(p));
}
__device__ __forceinline__ bf8 cat8(bf4 a, bf4 b) { return __builtin_shufflevector(a, b, 0, 1, 2, 3, 4, 5, 6, 7); }
__device__ __forceinline__ void st8_sc1(void* p, u64 v) { __hip_atomic_store((u64*)p, v, __ATOMIC_RELAXED, __HIP_MEMORY_SCOPE_AGENT); }
__device__ __forceinline__ u32 ld4_sc1(const u32* p) { return __hip_atomic_load(p, __ATOMIC_RELAXED, __HIP_MEMORY_SCOPE_AGENT); }
__device__ __forceinline__ void st4_sc1(u32* p, u32 v) { __hip_atomic_store(p, v, __ATOMIC_RELAXED, __HIP_MEMORY_SCOPE_AGENT); }
#define VMW0() asm volatile("s_waitcnt vmcnt(0)" ::: "memory")
#define CFENCE() asm volatile("" ::: "memory")
#define VMW0C() do { __builtin_amdgcn_s_waitcnt(0x0F70); asm volatile("" ::: "memory"); } while (0)
#define SPIN_CAP (1 << 22)
#define XPS 4
#define SPS 4
#define WPS 8
__device__ __forceinline__ int tid_now(int wv) {
  int l; asm volatile("v_mbcnt_lo_u32_b32 %0, -1, 0\n\tv_mbcnt_hi_u32_b32 %0, -1, %0" : "=v"(l));
  return wv * 64 + l;
}
#define TIDX tid_now(wv)
#define CLAIM() do { if (TIDX == 0) pre = (int)__hip_atomic_fetch_add(qh, 1u, __ATOMIC_RELAXED, __HIP_MEMORY_SCOPE_AGENT); } while (0)

#define TLS(i)
struct Params {
  const u16 *Ax, *Ac, *Awq, *Awk, *Awv, *Awo, *Bx, *Bm, *BWq, *BWk, *BWv, *BWo;
  u16* out;
  char* ws;
};
#define SY_QHEAD 0
#define SY_CC(rb) (64 + (rb) * 16)
#define SY_CW (64 + 8 * 16)
#define SY_OQ(x) (64 + 9 * 16 + (x) * 16)
#define SY_B0 (64 + 80 * 16)
#define SY_INIT(c) (SY_B0 + 64 + (c))
#define SY_VOTE(i) (SY_B0 + 128 + (i))
#define SY_JOB(x) (SY_B0 + 256 + (x) * 16)
#define SY_ARR (SY_B0 + 384)
#define SY_WX (SY_ARR + 32)
#define SY_BLK(mi, cb, g) (SY_ARR + 64 + ((mi) * 16 + (cb)) * 64 + (g))
#define FULL 12u
#define SY_WORDS (SY_ARR + 64 + 6 * 16 * 64)
#define SY_BEAT(r) (SY_WORDS + (r) * 32)

#define ACB (2048 * 4096 * 2)
#define O_PAD 0
#define O_QA (SY_BEAT(8) * 4 + O_PAD)
#define O_KA (O_QA + ACB)
#define O_VA (O_KA + ACB)
#define O_CA (O_VA + ACB)
#define O_QB (O_CA + ACB)
#define O_KB (O_QB + ACB)
#define O_VB (O_KB + ACB)
#define O_OB (O_VB + ACB)
#define O_WOF (O_OB + ACB)
#define WOB (576 * 4096 * 2)
#define O_WX (O_WOF + 2 * WOB)
#define O_XW (O_WX + 524288)
#define X_HS 0x4000
#define X_O(b) ((b) * 0x80000 + ((0x140 >> ((b) * 4)) & 15) * 4096)
#define X_MS X_HS
#define X_M (0x185000 ^ X_MS)
#define X_ANN (0x185000 + 7 * 32768)
#define X_CAL 0x40000
#define HFL 0x2000
#define CFL 0x1000
#define X_HOT(xp) ((0xC4000 ^ HFL) ^ ((xp) & 0x7000))
#define X_CS (HFL ^ CFL ^ 0x8000)
#define CSP(s) ((const u32*)(const GLB u32*)((uintptr_t)hot ^ X_CS) + (s) * 16)
#define X_END 0x200000
#define OB_OFF(r, c) (((r) >> 5) * 262144 + ((c) >> 4) * 1024 + ((r) & 31) * 32 + ((c) & 15) * 2)
#define FR_OFF(r, c) (((r) >> 5) * 262144 + ((c) >> 3) * 512 + ((r) & 31) * 16 + ((c) & 7) * 2)
#define GLB __attribute__((address_space(1)))
#define WSP(T, off) ((T*)((GLB char*)p.ws + (off)))
#define XWIN(p) (((uintptr_t)(p).ws + O_XW + 0x1FFFFFu) & ~(uintptr_t)0x1FFFFFu)

#define HOTP(xp) ((u32*)(GLB u32*)(XWIN(p) + X_HOT(xp)))

#define XS0 48
#define NAB 8
#define NBB 10
#define NOA (32 - NAB)
#define NOB (XS0 / 2 - NBB)
#define PEEK 0
#define OXIT 1
#define LDS_TOTAL (131072 + 8192 + 16384)
#define LDS_ALL 163840

#define SB0() __builtin_amdgcn_sched_barrier(0)
#define PJ_OOB 0x7F000000
#define RSRC(ptr, bytes) __builtin_amdgcn_make_buffer_rsrc((void*)(ptr), 0, (bytes), 0x00020000)
__device__ __forceinline__ bf8 ldsc1(__amdgpu_buffer_rsrc_t r, int off) {
  return __builtin_bit_cast(bf8, __builtin_amdgcn_raw_buffer_load_b128(r, off, 0, 16));
}
__device__ __forceinline__ u32x4 pair16(u32 a0, u32 a1, u32 b0, u32 b1) {
  auto r0 = __builtin_amdgcn_permlane16_swap(a0, b0, false, false);
  auto r1 = __builtin_amdgcn_permlane16_swap(a1, b1, false, false);
  return (u32x4){r0[0], r1[0], r0[1], r1[1]};
}
__device__ __forceinline__ int fsw(int k) { return (k & 3) | (((k >> 3) & 1) << 2); }

#define AADD(w, v) __hip_atomic_fetch_add(WSP(u32, 0) + (w), v, __ATOMIC_RELAXED, __HIP_MEMORY_SCOPE_AGENT)
#define AOR(w, v) __hip_atomic_fetch_or(WSP(u32, 0) + (w), v, __ATOMIC_RELAXED, __HIP_MEMORY_SCOPE_AGENT)
#define C_B0 (LDS_ALL - 2048)
#define BEAT_LD(job) ld4_sc1(WSP(u32, 0) + SY_BEAT((job) & 7))
#define BEAT_ON(it) do { if (((it) & 31) == 31 && (TIDX & 63) < 8) AADD(SY_BEAT(TIDX & 63), 1u); } while (0)
__device__ __forceinline__ u32 confirm(const Params& p, const char* smem, const int job, u32 b1, const int wv) {
  asm volatile("" :: "v"(b1) : "memory");
  const u32 b0 = *(const u32*)(smem + C_B0);
  if (__builtin_expect(__any(b1 == b0), 0)) {
    int it = 0;
    while (__any(BEAT_LD(job) == b0) && ++it < SPIN_CAP) __builtin_amdgcn_s_sleep(2);
  }
  u32 old = 0u;
  if (TIDX == 0) old = AOR(SY_JOB(job >> 5) + TIDX, 1u << (job & 31));
  if (TIDX < 2) AADD(TIDX ? SY_BEAT(job & 7) : SY_ARR, 1u);
  return old;
}

typedef __attribute__((ext_vector_type(2))) unsigned long long u64x2;
__device__ __forceinline__ f4 zero4() {
  u64 a, b; asm volatile("v_mov_b64 %0, 0\n\tv_mov_b64 %1, 0\n\ts_nop 1" : "=v"(a), "=v"(b));
  return __builtin_bit_cast(f4, (u64x2){a, b});
}
__device__ __forceinline__ u32x4 ones4() {
  u64 a, b; asm volatile("v_mov_b64 %0, -1\n\tv_mov_b64 %1, -1" : "=v"(a), "=v"(b));
  return __builtin_bit_cast(u32x4, (u64x2){a, b});
}
#define C_PARK 131072
#define PJ_PAD 0
__device__ __forceinline__ void proj_slot(const Params& p, int x, int j, char* smem, const int wv, int& pre, int job) {
  const int tid = TIDX, lane = tid & 63, wid = wv;
  const int l15 = lane & 15, lq = lane >> 4, q4 = l15 >> 2, p4 = lane & 3, wr = wid >> 2, wc = wid & 3;
  const __amdgpu_buffer_rsrc_t rc = RSRC(p.ws, O_XW);
  const int vB = ((wid * 4 + lq) * 4096 + (((l15 >> 1) ^ (lq | (((wid >> 1) & 1) << 2))) * 16 + (lane & 1) * 8)) * 2, vB1 = vB + 32 * 8192;
  int ao = (wr * 64 + l15) * 128 + ((lq ^ (l15 & 7)) << 4);
  int bo = (int)(uintptr_t)smem + 65536 + (lq * 8 + q4) * 256 + (((wc * 2) ^ (q4 | ((lq & 1) << 2))) << 5) + p4 * 8;
  const int vD = lane * 16;
#define PJ_ORD 0x1312
  const int ord = (j & 3) == 3 ? PJ_ORD : 0x1312;
#define PJ_NEXT ((ord >> (u * 4)) & 3)
  const int cb = (x & 1) * 8 + (j >> 2);
  __amdgpu_buffer_rsrc_t rsA, rsB;
  const u16 *pA0 = p.Bx, *pB0 = (x < 2 ? p.BWk : x < 4 ? p.BWv : p.BWq) + cb * 256;
  const u16 *pA1 = x < 2 ? p.Ax : p.Ac, *pB1 = (x < 2 ? p.Awq : x < 4 ? p.Awk : p.Awv) + cb * 256;
  asm volatile("" : "+s"(pA0), "+s"(pB0), "+s"(pA1), "+s"(pB1));
  int k2, vA, vA1, sa, sb, nvA;
#define PJ_K2(u) ((u) < 2 || x < 2 ? 1152 : 1024)
#define PJ_PREP(u) do { int ln_ = vD; asm volatile("" : "+v"(ln_)); \
    nvA = (wid * 8 + (ln_ >> 7)) * PJ_K2(u) + ((ln_ ^ (ln_ >> 3)) & 0x70); \
    asm volatile("" : "+v"(nvA)); } while (0)
#define PJ_UNIT(u) do { k2 = PJ_K2(u); rsA = RSRC((u) < 2 ? pA0 : pA1, 2048 * k2); rsB = RSRC((u) < 2 ? pB0 : pB1, k2 * 4096 - cb * 512); vA = nvA; vA1 = nvA + 64 * k2; \
    sa = ((j & 3) + ((u) & 1) * 4) * 256 * k2 - 128; sb = 0; } while (0)
#define PJ_DEST(u, rt) do { const int mi_ = ((u) >> 1) * 3 + (x >> 1), g_ = ((rt) + ((u) & 1) * 4) * 8; \
    dco = O_QA + (mi_ == 2 ? 4 : (mi_ + 5) & 7) * ACB + cb * 16384 + wc * 2048 + (g_ + wr * 2) * 262144; \
    dfl = SY_BLK(mi_, cb, g_) * 4; } while (0)
#define PJ_STAGE(dst, rs, v0, v1, so) do { const int so_ = (so); \
    __builtin_amdgcn_raw_ptr_buffer_load_lds(rs, (LDSP void*)(smem + (dst) + wid * 1024), 16, v0, so_, 0, 0); \
    __builtin_amdgcn_raw_ptr_buffer_load_lds(rs, (LDSP void*)(smem + (dst) + wid * 1024 + 8192), 16, v1, so_, 0, 0); } while (0)
#define PJ_SA(dst, so) PJ_STAGE(dst, rsA, vA, vA1, so)
#define PJ_SB(dst, so) PJ_STAGE(dst, rsB, vB, vB1, so)
#define PJ_LDA(off) do { _Pragma("unroll") for (int m = 0; m < 4; m++) _Pragma("unroll") for (int k = 0; k < 2; k++) \
    At[m][k] = *(const bf8*)(smem + (ao ^ (k * 64)) + (off) + m * 2048); } while (0)
#define PJ_TR(dst, a, off) asm volatile("ds_read_b64_tr_b16 %0, %1 offset:%2" : "=&v"(dst) : "v"(a), "i"(off) : "memory")
#define PJ_LDB(D, h) do { const int b1_ = bo ^ 32; _Pragma("unroll") for (int n = 0; n < 2; n++) _Pragma("unroll") for (int k = 0; k < 2; k++) { \
    bf4 lo_, hi_; PJ_TR(lo_, n ? b1_ : bo, (h) * 16384 + k * 8192); PJ_TR(hi_, n ? b1_ : bo, (h) * 16384 + k * 8192 + 1024); D[n][k] = cat8(lo_, hi_); } } while (0)
#define PJ_CV(ai, bj, q) ((u32x4){pk2(acc[ai][bj][(q) & 2][(q) & 1][0], acc[ai][bj][(q) & 2][(q) & 1][1]), pk2(acc[ai][bj][(q) & 2][(q) & 1][2], acc[ai][bj][(q) & 2][(q) & 1][3]), \
                                  pk2(acc[ai][bj][((q) & 2) + 1][(q) & 1][0], acc[ai][bj][((q) & 2) + 1][(q) & 1][1]), pk2(acc[ai][bj][((q) & 2) + 1][(q) & 1][2], acc[ai][bj][((q) & 2) + 1][(q) & 1][3])})
#define PJ_SW(v) pair16(v[0], v[1], v[2], v[3])
#define PJ_MMA(Z, ai, bj, D, F) do { PJ_SCHED; __builtin_amdgcn_s_setprio(1); \
    _Pragma("unroll") for (int k = 0; k < 2; k++) _Pragma("unroll") for (int m = 0; m < 4; m++) _Pragma("unroll") for (int n = 0; n < 2; n++) { \
      if (k == 0 && (Z)) asm volatile("v_mfma_f32_16x16x32_bf16 %0, %1, %2, 0" : "+v"(acc[ai][bj][m][n]) : "v"(D[n][k]), "v"(At[m][k])); \
      else asm volatile("v_mfma_f32_16x16x32_bf16 %0, %1, %2, %0" : "+v"(acc[ai][bj][m][n]) : "v"(D[n][k]), "v"(At[m][k])); \
      F(k * 8 + m * 2 + n); } \
    __builtin_amdgcn_s_setprio(0); if (PJ_PAD) __builtin_amdgcn_s_sleep(PJ_PAD); PJ_SCHED; } while (0)
#define PJ_SWI(a, b) asm volatile("v_permlane16_swap_b32 %0, %1" : "+v"(a), "+v"(b))
#define PJ_CVI(d, s, e) asm volatile("v_cvt_pk_bf16_f32 %0, %1, %2" : "=v"(d) : "v"(s[e]), "v"(s[(e) + 1]))
#define PJ_PACK(P, ai, bj, i) do { const int q_ = (i) >> 2; \
    if (((i) & 3) < 2) { PJ_CVI(P[q_][(i) & 1], acc[ai][bj][q_ & 2][q_ & 1], ((i) & 1) * 2); PJ_CVI(P[q_][2 + ((i) & 1)], acc[ai][bj][(q_ & 2) + 1][q_ & 1], ((i) & 1) * 2); } \
    else PJ_SWI(P[q_][(i) & 1], P[q_][2 + ((i) & 1)]); } while (0)
#define PJ_WV(n) asm volatile("s_waitcnt vmcnt(" #n ")" ::: "memory")
#define PJ_WL(n) asm volatile("s_waitcnt lgkmcnt(" #n ")" ::: "memory")
#define PJ_WL0 do { __builtin_amdgcn_s_waitcnt(0xC07F); asm volatile("" ::: "memory"); } while (0)
#define PJ_BAR __builtin_amdgcn_s_barrier()
#define PJ_SCHED __builtin_amdgcn_sched_barrier(0)
#define PJ_NOP do { PJ_SCHED; asm volatile("s_nop 7"); PJ_SCHED; } while (0)
#define PJ_DS(v, ai, bj, q) do { PJ_SCHED; __builtin_amdgcn_raw_buffer_store_b128(v, rc, vD + ((q) & 1) * 1024, dco + ((ai) * 4 + ((q) >> 1)) * 262144 + (bj) * 8192, 16); \
    asm volatile("s_nop 1"); PJ_SCHED; } while (0)
#define PJ_PARK(q) (*(u32x4*)(smem + C_PARK + wid * 4096 + vD + (q) * 1024))
#define PJ_PUB(ai, n) do { if (wid == 4) __builtin_amdgcn_raw_buffer_store_b32(FULL, rc, vD < (n) * 16 ? vD >> 2 : PJ_OOB, dfl + (ai) * 16, 16); } while (0)
#define PJ_ST()
#define PJ_TOUCH(ptr, bytes, vo, so) __builtin_amdgcn_raw_ptr_buffer_load_lds(RSRC(ptr, bytes), (LDSP void*)(smem + C_PARK + wid * 4096), 4, vo, so, 0, 0)
#define PJ_SL 0x1E
#define PJ_PT 0
  static_assert(XS0 >= 46 && PJ_SL == 0x1E && PJ_PT == 0, "");
  int emp = 128;
  u32 old = 0u;
#define PJ_T1(n) do { \
      int vt_ = vD; asm volatile("" : "+v"(vt_)); vt_ <<= 3; \
      const int i_ = cb * 8 + wid + (n) * 128; \
      const int nw_ = x < 2 ? 576 : 512, na_ = x < 2 ? 288 : x < 4 ? 256 : 144, no_ = (x >> 1) * 96 + 96; \
      const u64 w_ = (u64)(pB1 - cb * 256), a_ = x < 4 ? (u64)pA1 : (u64)(pA0 + 1024 * 576), o_ = (u64)p.Awo + (x < 2 ? 0 : x < 4 ? 96 : 288) * 8192; \
      PJ_TOUCH(i_ < nw_ ? w_ : i_ < nw_ + na_ ? a_ : o_, i_ < nw_ + na_ ? WOB : no_ * 8192, vt_, (i_ < nw_ ? i_ : i_ < nw_ + na_ ? i_ - nw_ : i_ - nw_ - na_) * 8192); } while (0)
#define PJ_TA 3
#define PJ_WARM(k) do { \
    if (u == 0 && ((j & 3) == 2 || ((k) < PJ_TA && (j & 3) == 1))) { \
      PJ_T1((k) + ((j & 1) ? 0 : PJ_TA)); \
      if ((k) < 4 - PJ_TA && !(j & 1)) PJ_T1((k) + PJ_TA + 4); \
    } else { \
      const int e_ = emp + (u * 4 + (k)) * 3; \
      __builtin_amdgcn_raw_buffer_store_b128(ones4(), rc, vD, e_ < 128 ? O_OB + (e_ >> 1) * 262144 + cb * 16384 + (e_ & 1) * 8192 + wid * 1024 : PJ_OOB, 16); \
    } } while (0)
#define PJ_F0(i)
#define PJ_F1(i) do { if (S == 8) PJ_PACK(pa, 0, 0, i); } while (0)
#define PJ_F2(i) do { if (S == 8) PJ_PACK(pb, 0, 1, i); if (S == 0) PJ_PACK(pb, 1, 1, i); } while (0)
#define PJ_F3(i) do { if (S == 8) PJ_PACK(pa, 1, 0, i); if (S == 5 && (i) == 0) { PJ_SCHED; PJ_PREP(PJ_NEXT); PJ_SCHED; } } while (0)
#define PJ_STEP(SS) do { constexpr int S = SS; PJ_ST(); \
    PJ_LDB(B0, 0); PJ_SCHED; PJ_LDA(0); PJ_SA((bsel ^ 32768) + 16384, sa + 128 * k2); \
    PJ_WL(8); PJ_BAR; PJ_WL0; PJ_MMA(S == 0, 0, 0, B0, PJ_F0); PJ_BAR; PJ_SCHED; \
    if (S == 7) PJ_UNIT(PJ_NEXT); \
    PJ_LDB(B1, 1); PJ_SB(65536 + bsel, sb); \
    if (S == 0) { PJ_DS(pb[2], 0, 1, 2); PJ_DS(pb[3], 0, 1, 3); } \
    if ((PJ_SL >> S) & 1) PJ_WARM(PJ_PT + __builtin_popcount(PJ_SL & ((1 << S) - 1))); \
    PJ_BAR; PJ_WL0; PJ_MMA(S == 0, 0, 1, B1, PJ_F1); PJ_BAR; \
    PJ_LDA(16384); sa += 128; PJ_SA(bsel, sa); \
    if (S == 8) { PJ_SCHED; PJ_DEST(u, j & 3); _Pragma("unroll") for (int q = 0; q < 4; q++) PJ_PARK(q) = pa[q]; PJ_SCHED; } \
    PJ_BAR; PJ_WL0; PJ_MMA(S == 0, 1, 0, B0, PJ_F2); PJ_BAR; PJ_SCHED; \
    { u32x4 c0_, c1_; \
      if (S < 2) { c0_ = PJ_PARK(2 * S); c1_ = PJ_PARK(2 * S + 1); PJ_SCHED; } \
      PJ_SB(65536 + bsel + 16384, sb + 256); sb += 64 * 8192; \
      if (S == 8) { PJ_DS(pb[0], 0, 1, 0); PJ_DS(pb[1], 0, 1, 1); } \
      else if (S < 2) { PJ_DS(c0_, 0, 0, (2 * S) & 3); PJ_DS(c1_, 0, 0, (2 * S + 1) & 3); } \
      else if (S < 4) { PJ_DS(pa[(2 * S) & 3], 1, 0, (2 * S) & 3); PJ_DS(pa[(2 * S + 1) & 3], 1, 0, (2 * S + 1) & 3); } \
      else if (S < 6) { PJ_DS(pb[(2 * S) & 3], 1, 1, (2 * S) & 3); PJ_DS(pb[(2 * S + 1) & 3], 1, 1, (2 * S + 1) & 3); } } \
      \
    asm volatile("s_waitcnt vmcnt(%0)" :: "i"((S == 0 ? 10 : S < 6 || S == 8 ? 8 : 6) + ((PJ_SL >> S) & 1)) : "memory"); \
    PJ_BAR; \
    if (S == 2) PJ_PUB(0, 4); \
    if (S == 7) PJ_PUB(1, 4); \
    PJ_MMA(S == 0, 1, 1, B1, PJ_F3); PJ_BAR; \
    bsel ^= 32768; ao ^= 32768; bo ^= 32768; } while (0)
  f4 acc[2][2][4][2];
  bf8 At[4][2], B0[2][2], B1[2][2];
  u32x4 pa[4], pb[4];
  int bsel = 0, dco = PJ_OOB, dfl = PJ_OOB, u = 0;
  auto quick = [&](auto NBC, const int hf, const int ch) __attribute__((always_inline)) {
    constexpr int NB = decltype(NBC)::value, NS = 5 - NB;
#define PJ_FS(t) do { PJ_SA(((t) % NS) * 16384, hf * 128 * 1152 + (t) * 128); \
      _Pragma("unroll") for (int h_ = 0; h_ < NB; h_++) PJ_SB(NS * 16384 + (((t) % NS) * NB + h_) * 16384, (t) * 64 * 8192 + (ch + h_) * 256); PJ_SCHED; } while (0)
#pragma unroll
    for (int t = 0; t < NS - 1; t++) PJ_FS(t);
    bo += NS * 16384 - 65536;
#pragma unroll
    for (int t = 0; t < 9; t++) {
      PJ_ST();
      if (t == 8) PJ_WV(0); else if (NB == 2) PJ_WV(6); else if (t == 7) PJ_WV(4); else PJ_WV(8);
      PJ_BAR; PJ_SCHED;
      if (wr && t + NS - 1 < 9) PJ_FS(t + NS - 1);
      PJ_LDB(B0, 0); PJ_LDA((t % NS) * 16384);
      if (NB == 2) PJ_LDB(B1, 1);
      PJ_SCHED;
      bo += t == 8 ? 65536 - NS * 16384 - (8 % NS) * NB * 16384 : (t % NS == NS - 1 ? -(NS - 1) * NB * 16384 : NB * 16384);
      PJ_WL0;
      PJ_MMA(t == 0, 0, 0, B0, PJ_F0);
      if (NB == 2) PJ_MMA(t == 0, 0, 1, B1, PJ_F0);
      if (!wr && t + NS - 1 < 9) PJ_FS(t + NS - 1);
    }
    PJ_NOP;
    u32 b1;
    if (NB == 1 && job >= 0 && wid == 0) b1 = BEAT_LD(job);
#pragma unroll
    for (int q = 0; q < 4 * NB; q++) { const u32x4 v = PJ_CV(0, q >> 2, q & 3); PJ_DS(PJ_SW(v), hf, ch + (q >> 2), q & 3); }
    if (NB == 2 && tid == 0) { *(u32*)(smem + C_B0 + 4) = old; PJ_WL0; }
    PJ_WV(0); PJ_BAR;
    if (NB == 1 && job >= 0 && wid == 0) old = confirm(p, smem, job, b1, wv);
    if (tid < 4) __hip_atomic_fetch_or(WSP(u32, dfl) + hf * 4 + tid, NB == 2 ? FULL : 4u << ch, __ATOMIC_RELAXED, __HIP_MEMORY_SCOPE_AGENT);
  };
  if (((j + 1) & 3) < 2) {
    PJ_PREP(0); PJ_UNIT(0); PJ_DEST(0, 0);
    quick(std::integral_constant<int, 1>{}, 0, j & 1);
    if ((j & 3) == 0) {
      quick(std::integral_constant<int, 2>{}, 1, 0);
      emp = (__builtin_amdgcn_readfirstlane(*(const u32*)(smem + C_B0 + 4)) >> (job & 31)) & 1 ? 129 : XS0 * 2 - 12 + (x >> 1);
    }
    if ((j & 3) == 0 || PJ_ORD != 0x1312) u = 2;
    dco = PJ_OOB; dfl = PJ_OOB; job = -1;
  }
#pragma unroll
  for (int a = 0; a < 16; a++) asm volatile("" : "=v"(acc[a >> 3][(a >> 2) & 1][a & 3][0]), "=v"(acc[a >> 3][(a >> 2) & 1][a & 3][1]));
#pragma unroll
  for (int a = 0; a < 4; a++) asm volatile("" : "=v"(pa[a]), "=v"(pb[a]));
  PJ_PREP(u); PJ_UNIT(u);
  const int cs = x < 4 && (j & 3) == 2 ? (x * 8 + (j >> 2)) * 128 : -1;
#pragma unroll
  for (int i = 0; i < 2; i++) pa[i] = __builtin_amdgcn_raw_buffer_load_b128(RSRC(p.BWo, WOB), (tid >> 3) * 1152 + (tid & 7) * 16, cs >= 0 ? (cs + i * 64) * 1152 + 1024 : PJ_OOB, 0);
  PJ_SCHED;
  PJ_SB(65536, 0); PJ_SA(0, sa + 128);
  PJ_SB(65536 + 16384, 256); PJ_SA(16384, sa + 128 + 128 * k2);
  PJ_SCHED;
#pragma unroll
  for (int i = 0; i < 2; i++) __builtin_amdgcn_raw_buffer_store_b128(pa[i], rc, (tid & 7) * 65536 + (tid >> 3) * 16, cs >= 0 ? O_WX + (cs + i * 64) * 16 : PJ_OOB, 16);
  PJ_SCHED;
  if (wr == 1) PJ_BAR;
  PJ_WV(6); PJ_BAR;
  u32 b1;
  if (job >= 0 && wid == 0) b1 = BEAT_LD(job);
  PJ_SCHED;
  PJ_SB(65536 + 32768, 64 * 8192); PJ_SA(32768, sa + 256);
  PJ_SB(65536 + 32768 + 16384, 64 * 8192 + 256);
  PJ_WV(6);
  if (job >= 0 && wid == 0) confirm(p, smem, job, b1, wv);
  PJ_BAR;
  if (tid == 256 && cs >= 0) AOR(SY_WX, 1u << (cs >> 7));
#pragma unroll
  for (int i = 0; i < PJ_PT; i++) PJ_WARM(i);
  sa += 256; sb = 128 * 8192;
#pragma unroll 1
  for (;; u = PJ_NEXT) {
    PJ_STEP(0); PJ_STEP(1); PJ_STEP(2); PJ_STEP(3); PJ_STEP(4); PJ_STEP(5);
    int more = k2 - 1024; asm volatile("" : "+s"(more));
    if (more) PJ_STEP(6);
    PJ_STEP(7); PJ_STEP(8);
    if (u == 1) break;
  }
  PJ_ST();
  PJ_NOP;
  const u32 left = __builtin_amdgcn_raw_buffer_load_b32(rc, wid ? PJ_OOB : vD < 128 ? SY_JOB(0) * 4 + vD * 4 : SY_INIT(0) * 4, 0, 16);
#define FSL 0
#define FWN 15
#define PJ_FD(v, ai, bj, q) do { PJ_DS(v, ai, bj, q); if (FSL) __builtin_amdgcn_s_sleep(FSL); if (FWN < 15) asm volatile("s_waitcnt vmcnt(%0)" :: "i"(FWN) : "memory"); } while (0)
  PJ_FD(pb[2], 0, 1, 2); PJ_FD(pb[3], 0, 1, 3);
#pragma unroll
  for (int q = 0; q < 4; q++) pb[q] = PJ_PARK(q);
#pragma unroll
  for (int q = 0; q < 4; q++) PJ_FD(pa[q], 1, 0, q);
#pragma unroll
  for (int q = 0; q < 4; q++) PJ_FD(pb[q], 0, 0, q);
#pragma unroll
  for (int q = 0; q < 4; q++) { const u32x4 v = PJ_CV(1, 1, q); PJ_FD(PJ_SW(v), 1, 1, q); }
  pre = -1;
  const int xp = __builtin_amdgcn_readlane(left, 8);
  if (__all(left >= (vD < 128 ? ~0u : 1u)) && vD == 0) pre = (int)__hip_atomic_fetch_add(HOTP(xp) + SY_QHEAD, 1u, __ATOMIC_RELAXED, __HIP_MEMORY_SCOPE_AGENT) + (xp & 0x7000);
  PJ_WV(0);
  if (wr == 0) PJ_BAR;
  PJ_BAR;
  if (emp != 129) PJ_PUB(0, 8);
  PJ_ST();
}

namespace fa {
constexpr float SCALE = 0.08838834764831845f;
constexpr int NW = 8, QBLK = 32, KVBLK = 64, QB = NW * QBLK, D = 128;
constexpr int SHM_V = KVBLK * D * 2, SHM_K = KVBLK * D * 2;
#define SBAR() __builtin_amdgcn_sched_barrier(0)
#define FMX3(a, b, c) __builtin_elementwise_maximum(__builtin_elementwise_maximum(a, b), c)
#define AT_PAD 0
#define EBAR 15
#define LSUM 1
#define LSP 0x80
#define EPR 0
#define DPOS 0x0C080400
#define MX0 -1
#define ATHR 24.f
__device__ __forceinline__ void attn_block(const char* __restrict__ Qp, const char* __restrict__ Kh, const char* __restrict__ Vh,
                                           char* __restrict__ Op, int P0, char* lds, const int wv, u32* qh, const int oq, int& pre) {
  const int tid = TIDX, wid = wv, lane = tid & 63, l15 = lane & 15, lq = lane >> 4, grp = wid >> 2;
  const int NT = (P0 + QB) / KVBLK;
  const int rb = wid ^ 4;
  const int TL = NT - 4 + (rb >> 1);
  const int rem = 2 * (NT - TL) - 2 - grp;
  float l0 = 0.f, l1 = 0.f;
  f4 ls[2] = {};
  u32x4 one = {0x3F803F80u, 0x3F803F80u, 0x3F803F80u, 0x3F803F80u};
  f4 o[8][2] = {}, sc[4][2], nm[2] = {};
  int ko = lq * 512 + l15 * 16;
  int vo = 4 * SHM_K + ((lane >> 1) & 1) * 512 + (((lane >> 2) ^ ((lane & 2) * 4)) << 4) + (lane & 1) * 8;
  const __amdgpu_buffer_rsrc_t rk = RSRC(Kh, (2 * NT - 1) * 262144 + 8192), rv = RSRC(Vh, (2 * NT - 1) * 262144 + 8192);
  const int gk = tid * 16, gv = gk ^ ((tid & 32) << 2);
#define DMA(rs, go, dst, so) __builtin_amdgcn_raw_ptr_buffer_load_lds(rs, (LDSP void*)(lds + (dst) + wid * 1024), 16, go, so, 0, 0)
#define ISSUE_K(t) do { const int s_ = (t) * 524288, d_ = ((t) & 3) * SHM_K; DMA(rk, gk, d_, s_); DMA(rk, gk, d_ + 8192, s_ + 262144); } while (0)
#define ISSUE_V(t) do { const int s_ = (t) * 524288, d_ = 4 * SHM_K + ((t) & 3) * SHM_V; DMA(rv, gv, d_, s_); DMA(rv, gv, d_ + 8192, s_ + 262144); } while (0)
#define PHASE(n) do { SBAR(); asm volatile("s_waitcnt vmcnt(" #n ")" ::: "memory"); __builtin_amdgcn_s_barrier(); asm volatile("" ::: "memory"); SBAR(); } while (0)
#define LDK(i) kf[(i) % 6] = *(const bf8*)(lds + ko + ((i) >> 2) * 2048 + ((i) & 1) * 256 + ((i) & 2) * 4096)
#define LDV0(i) vl[(i) & 3] = trrd(lds + vo + ((i) & 7) * 1024 + ((i) >> 3) * 8192)
#define LDV1(i) vh[(i) & 3] = trrd(lds + vo + ((i) & 7) * 1024 + ((i) >> 3) * 8192 + 256)
#define LDV(i) do { LDV0(i); LDV1(i); } while (0)
#define AST(i)
#define AP(i)
  AP(0);
#define QK(NEXT) do { _Pragma("unroll") for (int i = 0; i < 16; ++i) { \
      _Pragma("unroll") for (int h = 0; h < 2; ++h) { \
        SBAR(); \
        if (i > 3) sc[i & 3][h] = MFMA(kf[i % 6], qr[i >> 2][h], sc[i & 3][h]); \
        else asm volatile("v_mfma_f32_16x16x32_bf16 %0, %1, %2, %3" : "+v"(sc[i][h]) : "v"(kf[i]), "v"(qr[0][h]), "v"(nm[h])); \
        SBAR(); \
        if (i + 4 < 16) { if (h == 0) LDK(i + 4); } else if (NEXT) { if (h == 0) LDV0(i - 12); else LDV1(i - 12); } } } } while (0)
#define SCX(k) sc[(k) >> 3][((k) >> 2) & 1][(k) & 3]
#define MAXS(i) do { \
      if ((i) == 0) mx = FMX3(SCX(0), SCX(1), SCX(2)); else if ((i) == 1) my = FMX3(SCX(3), SCX(4), SCX(5)); \
      else if ((i) < 15) { if ((i) & 1) my = FMX3(my, SCX(2 * (i) + 2), SCX(2 * (i) + 3)); else mx = FMX3(mx, SCX(2 * (i) + 2), SCX(2 * (i) + 3)); } \
      else { mx = __builtin_elementwise_maximum(mx, my); asm volatile("" : "+v"(mx)); } } while (0)
  float mx, my;
  constexpr float C2 = 1.4426950408889634f * SCALE;
  bf8 qr[4][2], kf[6], pa[2][2];
  bf4 vl[4], vh[4];
#pragma unroll
  for (int d0 = 0; d0 < 8; ++d0) qr[d0 >> 1][d0 & 1] = *(const bf8*)(Qp + rb * 262144 + (d0 >> 1) * 2048 + (d0 & 1) * 256 + ko);
  SBAR();
  ISSUE_K(0); ISSUE_K(1); ISSUE_V(0); ISSUE_K(2); ISSUE_V(1);
  SBAR();
#pragma unroll
  for (int i = 0; i < 8; ++i) asm volatile("" : "=v"(sc[i >> 1][i & 1]), "+v"(o[i][0]), "+v"(o[i][1]));
  asm volatile("" : "+v"(nm[0]), "+v"(nm[1]));
  if (LSUM) asm volatile("" : "+v"(ls[0]), "+v"(ls[1]), "+v"(one));
  SBAR();
#pragma unroll
  for (int d0 = 0; d0 < 8; ++d0) {
    u32x4 q = __builtin_bit_cast(u32x4, qr[d0 >> 1][d0 & 1]);
#pragma unroll
    for (int i = 0; i < 4; i++) q[i] = pk2(__uint_as_float(q[i] << 16) * C2, __uint_as_float(q[i] & 0xFFFF0000u) * C2);
    qr[d0 >> 1][d0 & 1] = __builtin_bit_cast(bf8, q);
    asm volatile("" : "+v"(qr[d0 >> 1][d0 & 1]));
  }
  AP(8);
  PHASE(8);
  AP(1);
  if (grp) PHASE(6);
  LDK(0); LDK(1); LDK(2); LDK(3);
  QK(0);
  mx = 0.f;
  AP(2);
  PHASE(4);
  AP(3);
  auto tile = [&](const int t, auto LASTC) __attribute__((always_inline)) {
    constexpr bool LAST = decltype(LASTC)::value;
    AST(0);
#define ISSUE1(i) do { if ((i) < 2) DMA(rk, gk, ((t + 3) & 3) * SHM_K + (i) * 8192, (t + 3) * 524288 + (i) * 262144); \
                       else DMA(rv, gv, 4 * SHM_K + ((t + 2) & 3) * SHM_V + ((i) & 1) * 8192, (t + 2) * 524288 + ((i) & 1) * 262144); SBAR(); } while (0)
#pragma unroll
    for (int i = 0; i < 4; ++i) if (((DPOS >> (i * 8)) & 255) == 0) ISSUE1(i);
    SBAR();
    if constexpr (LAST) {
      const int dq = l15 - 4 * lq;
      if (rb & 1) {
#pragma unroll
        for (int r = 0; r < 4; ++r) { if (dq < r) { sc[2][0][r] = -__builtin_inff(); sc[3][1][r] = -__builtin_inff(); } sc[3][0][r] = -__builtin_inff(); SBAR(); }
      } else {
#pragma unroll
        for (int r = 0; r < 4; ++r) {
          if (dq < r) { sc[0][0][r] = -__builtin_inff(); sc[1][1][r] = -__builtin_inff(); }
          sc[1][0][r] = sc[2][0][r] = sc[2][1][r] = sc[3][0][r] = sc[3][1][r] = -__builtin_inff(); SBAR();
        }
      }
    }
    if (LAST || MX0 < 0) {
#pragma unroll
      for (int i = 0; i < 16; ++i) MAXS(i);
    }
    if (__builtin_expect(t == 0 || __any(mx > ATHR), 0)) {
#pragma unroll
      for (int h = 0; h < 2; ++h) {
        float m = FMX3(FMX3(sc[0][h][0], sc[0][h][1], sc[0][h][2]), sc[0][h][3], t ? 0.f : sc[0][h][3]);
#pragma unroll
        for (int k = 1; k < 4; ++k) m = FMX3(FMX3(m, sc[k][h][0], sc[k][h][1]), sc[k][h][2], sc[k][h][3]);
        { auto rr = __builtin_amdgcn_permlane16_swap(__float_as_uint(m), __float_as_uint(m), false, false);
          m = __builtin_elementwise_maximum(__uint_as_float(rr[0]), __uint_as_float(rr[1])); }
        { auto rr = __builtin_amdgcn_permlane32_swap(__float_as_uint(m), __float_as_uint(m), false, false);
          m = __builtin_elementwise_maximum(__uint_as_float(rr[0]), __uint_as_float(rr[1])); }
        const float alpha = t ? __builtin_amdgcn_exp2f(-m) : 1.f, nn = nm[h][0] - m;
#pragma unroll
        for (int r = 0; r < 4; ++r) {
          nm[h][r] = nn;
#pragma unroll
          for (int k = 0; k < 4; ++k) sc[k][h][r] -= m;
#pragma unroll
          for (int d = 0; d < 8; ++d) o[d][h][r] *= alpha;
        }
        if (LSUM) ls[h][0] *= alpha; else if (h) l1 *= alpha; else l0 *= alpha;
        asm volatile("" : "+v"(nm[h]));
      }
    }
    AST(1);
    float x[32];
    u32 pw[16];
#define SCP(r, e) sc[(r) >> 2][(r) & 1][((r) & 2) + (e)]
#pragma unroll
    for (int r = 0; r < 18; ++r) {
      SBAR();
      if (r < 16) { x[2 * r] = __builtin_amdgcn_exp2f(SCP(r, 0)); x[2 * r + 1] = __builtin_amdgcn_exp2f(SCP(r, 1)); }
      if (r >= 2) {
        if (LSUM) {} else if (r & 1) { l0 += x[2 * r - 5]; l1 += x[2 * r - 4]; } else { if (r > 2) l1 += x[2 * r - 5]; l0 += x[2 * r - 4]; }
        pw[r - 2] = pk2(x[2 * r - 4], x[2 * r - 3]); }
      SBAR();
#pragma unroll
      for (int i = 0; i < 4; ++i) if (((DPOS >> (i * 8)) & 255) == r + 1) ISSUE1(i);
    }
    SBAR();
    if (!LSUM) l1 += x[31];
#pragma unroll
    for (int i = 0; i < 4; ++i) pa[i >> 1][i & 1] = __builtin_bit_cast(bf8, (u32x4){pw[(i >> 1) * 8 + (i & 1)], pw[(i >> 1) * 8 + (i & 1) + 2], pw[(i >> 1) * 8 + (i & 1) + 4], pw[(i >> 1) * 8 + (i & 1) + 6]});
    asm volatile("" : "+v"(pa[0][0]), "+v"(pa[0][1]), "+v"(pa[1][0]), "+v"(pa[1][1]));
    if (!LSUM) asm volatile("" : "+v"(l0), "+v"(l1));
    ko ^= (t & 1) ? 3 * SHM_K : SHM_K;
    SBAR();
    AST(3);
    if constexpr (!LAST) { LDK(0); LDK(1); LDK(2); LDK(3); } else { LDV(0); LDV(1); LDV(2); LDV(3); }
    AST(4);
    PHASE(4);
    AST(5);
    __builtin_amdgcn_s_setprio(1);
    if constexpr (!LAST) QK(1);
    AST(6);
#pragma unroll
    for (int i = 0; i < 16; ++i) {
      SBAR();
      if (LSUM && (i == (LSP & 15) || i == (LSP >> 4))) {
#pragma unroll
        for (int h = 0; h < 2; ++h) { asm volatile("v_mfma_f32_16x16x32_bf16 %0, %1, %2, %0" : "+v"(ls[h]) : "v"(one), "v"(pa[i == (LSP >> 4)][h])); SBAR(); }
      }
      o[i & 7][0] = MFMA(cat8(vl[i & 3], vh[i & 3]), pa[i >> 3][0], o[i & 7][0]);
      SBAR();
      if (i > 0 && i + 3 < 16) LDV1(i + 3);
      if constexpr (!LAST) if (MX0 >= 0 && i >= MX0 && i < MX0 + 8) MAXS(2 * (i - MX0));
      SBAR();
      o[i & 7][1] = MFMA(cat8(vl[i & 3], vh[i & 3]), pa[i >> 3][1], o[i & 7][1]);
      SBAR();
      if (i + 4 < 16) LDV0(i + 4);
      if constexpr (!LAST) if (MX0 >= 0 && i >= MX0 && i < MX0 + 8) MAXS(2 * (i - MX0) + 1);
      if (i == EBAR) AST(2);
      if (EPR && i == EBAR) __builtin_amdgcn_s_setprio(EPR);
      if (i == EBAR && (!LAST || rem)) PHASE(4);
    }
    __builtin_amdgcn_s_setprio(0);
    vo ^= (t & 1) ? 3 * SHM_V : SHM_V;
    AST(7);
  };
#pragma unroll 1
  for (int t = 0; t < TL; t++) {
    tile(t, std::false_type{});
  }
  AP(4);
  const u32 peek = __builtin_amdgcn_raw_buffer_load_b32(RSRC(qh, wid ? 0 : 4), 0, 0, 16);
  tile(TL, std::true_type{});
  AP(5);
  ISSUE_V(TL + 3);
  SBAR();
  {
    { auto rr = __builtin_amdgcn_permlane16_swap(__float_as_uint(l0), __float_as_uint(l0), false, false); l0 = __uint_as_float(rr[0]) + __uint_as_float(rr[1]); }
    { auto rr = __builtin_amdgcn_permlane16_swap(__float_as_uint(l1), __float_as_uint(l1), false, false); l1 = __uint_as_float(rr[0]) + __uint_as_float(rr[1]); }
    const float i0 = __builtin_amdgcn_rcpf(LSUM ? ls[0][0] : swadd(l0, l0)), i1 = __builtin_amdgcn_rcpf(LSUM ? ls[1][0] : swadd(l1, l1));
    const __amdgpu_buffer_rsrc_t ro = RSRC(Op + rb * 262144, 8192);
#pragma unroll
    for (int i = 0; i < 8; ++i) {
      __builtin_amdgcn_raw_buffer_store_b128(pair16(pk2(o[i][0][0] * i0, o[i][0][1] * i0), pk2(o[i][0][2] * i0, o[i][0][3] * i0), pk2(o[i][1][0] * i1, o[i][1][1] * i1), pk2(o[i][1][2] * i1, o[i][1][3] * i1)),
                                             ro, OB_OFF((lq & 1) * 16 + l15, (lq >> 1) * 8) + (i & 3) * 1024, (i >> 2) * 4096, 16);
      if (i == 3 && rem > 1) PHASE(6);
    }
  }
  AP(6);
  if (rem > 2) PHASE(10);
  if (rem > 3) { PHASE(8); PHASE(8); }
  if (TIDX == 0) {
    const bool out = PEEK && peek >= 320u;
    pre = (int)__hip_atomic_fetch_add(qh + (out ? oq : 0), 1u, __ATOMIC_RELAXED, __HIP_MEMORY_SCOPE_AGENT) + (out ? 0x10000 : 0);
  }
  VMW0();
  AP(7);
  __syncthreads();
}
#undef DMA
#undef ISSUE_K
#undef ISSUE_V
#undef PHASE
#undef LDK
#undef LDV
#undef QK
#undef MAXS
}

__device__ __forceinline__ void wof_task(const Params& p, int kc, char* smem, const int wv, u32* qh, int& pre) {
  const int tid = TIDX, lane = tid & 63, wid = wv;
  const int l15 = lane & 15, lq = lane >> 4, q4 = l15 >> 2, p4 = lane & 3;
  const __amdgpu_buffer_rsrc_t rw = RSRC(WSP(u16, O_WOF), 2 * WOB);
  bf8 st[2][9];
#pragma unroll
  for (int m = 0; m < 2; m++)
#pragma unroll
    for (int i = 0; i < 9; i++) st[m][i] = *(const bf8*)((m ? p.BWo : p.Awo) + (size_t)kc * 64 * 576 + (tid + 512 * i) * 8);
  CLAIM();
#pragma unroll
  for (int m = 0; m < 2; m++) {
#pragma unroll
    for (int i = 0; i < 9; i++) {
      const int id = tid + 512 * i, row = id / 72, ch = id % 72;
      *(bf8*)(smem + row * 1280 + (((ch >> 1) ^ fsw(row)) << 5) + ((ch & 1) << 4)) = st[m][i];
    }
    asm volatile("s_waitcnt lgkmcnt(0)" ::: "memory"); __builtin_amdgcn_s_barrier(); asm volatile("" ::: "memory");
#pragma unroll
    for (int i = 0; i < 9; i++) {
      const int f = wid * 9 + i, nt = f >> 1, k2 = f & 1;
      const int r0 = k2 * 32 + lq * 8 + q4, r1 = r0 + 4;
      bf4 lo = trrd(smem + r0 * 1280 + ((nt ^ fsw(r0)) << 5) + p4 * 8);
      bf4 hi = trrd(smem + r1 * 1280 + ((nt ^ fsw(r1)) << 5) + p4 * 8);
      __builtin_amdgcn_raw_buffer_store_b128(__builtin_bit_cast(u32x4, cat8(lo, hi)), rw, ((nt * 128 + kc * 2 + k2) * 64 + lane) * 16, m * WOB, 16);
    }
    if (m == 0) { asm volatile("s_waitcnt lgkmcnt(0)" ::: "memory"); __builtin_amdgcn_s_barrier(); asm volatile("" ::: "memory"); }
  }
  VMW0();
  __syncthreads();
}

#define OT_T(i)
#define OTI 2
#define OTD 0x1511
#define OTI4 2
template <int MT>
__device__ __forceinline__ void out_tile(const char* A, const char* W, u16* C, char* smem, const int wv, u32* hot, u32* qh, int& pre) {
  constexpr int U = 32 / MT, SC = U / 2, DP = MT / 4, NCH = 4 * MT, CW = 32768 / MT;
  const int tid = TIDX, lane = tid & 63, wid = wv;
  const int l15 = lane & 15, lq = lane >> 4, kh = wid >> 2, wc = wid & 3;
#define OTX 0
  const __amdgpu_buffer_rsrc_t ra = RSRC(A, (OTX & 1) ? 0 : MT / 2 * 262144), rw = RSRC(W + wc * 3 * 131072 + kh * 1024, (OTX & 2) ? 0 : 3 * 131072 - kh * 1024);
  const int va = tid * 16, vw = lane * 16;
  int ao = kh * 2048 + (lq >> 1) * 1024 + l15 * 32 + (lq & 1) * 16;
  f4 acc[MT][3];
  bf8 wf[U][3], af[2][MT];
#define OT_DMA(i, buf, so) __builtin_amdgcn_raw_ptr_buffer_load_lds(ra, (LDSP void*)(smem + (buf) + (i) * 8192 + wid * 1024), 16, va, \
                                                                    (so) + (MT == 4 ? ((i) >> 1) * 262144 + ((i) & 1) * 8192 : (i) * 262144), 0, 0)
#define OT_LDA(d, base, off) do { _Pragma("unroll") for (int m = 0; m < MT; m++) af[d][m] = *(const bf8*)(smem + (base) + (off) + (m >> 1) * 2 * CW + (m & 1) * 512); } while (0)
#define OT_LD1(d, m, base, off) af[d][m] = *(const bf8*)(smem + (base) + (off) + ((m) >> 1) * 2 * CW + ((m) & 1) * 512)
#define OT_VM(n) __builtin_amdgcn_s_waitcnt(0x0F70 | ((n) & 15) | (((n) >> 4) << 14))
#define OT_PAIR(buf, so) do { _Pragma("unroll") for (int i = 0; i < 8; i++) OT_DMA(i & 3, (buf) + (i >> 2) * 32768, (so) + (i >> 2) * 2 * CW); SB0(); } while (0)
#define OT_PEEK() do { if (wid == 0) asm volatile("s_mov_b32 m0, %2\n\ts_nop 0\n\tbuffer_load_dword %0, %1, 0 offen offset:%3 sc1 lds" \
                                                  :: "v"(vw * 4), "s"(RSRC(hot, SY_OQ(7) * 4)), "i"(131072), "i"(SY_OQ(0) * 4) : "memory", "m0"); } while (0)
  if constexpr (MT == 4) {
  OT_PAIR(0, 0);
#pragma unroll
  for (int u = 0; u < U; u++) {
    if (u == U - 1) OT_PAIR(65536, 4 * CW);
#pragma unroll
    for (int j = 0; j < 3; j++) wf[u][j] = __builtin_bit_cast(bf8, __builtin_amdgcn_raw_buffer_load_b128(rw, vw, u * 2048 + j * 131072, 0));
    SB0();
  }
#pragma unroll
  for (int m = 0; m < MT; m++)
#pragma unroll
    for (int j = 0; j < 3; j++) acc[m][j] = (f4){0, 0, 0, 0};
  asm volatile("s_waitcnt vmcnt(%0)" :: "i"(U * 3 + 8) : "memory"); __builtin_amdgcn_s_barrier(); asm volatile("" ::: "memory");
  OT_LDA(0, ao, 0);
  OT_T(2);
  int bs = 0;
#pragma unroll 1
  for (int c = 0; c < NCH; c += 2) {
    const int sw = c < NCH - 2 ? (c + 2) * CW : PJ_OOB, sd = c < NCH - 4 ? (c + 4) * 2 * CW : PJ_OOB;
#pragma unroll
    for (int u = 0; u < U; u++) {
      SB0();
      if (u < U - 1) OT_VM(3 * (U - 1) + 8); else OT_VM(3 * (U - 1));
      if (u == U - 1) {
        asm volatile("s_waitcnt lgkmcnt(0)" ::: "memory"); __builtin_amdgcn_s_barrier(); asm volatile("" ::: "memory");
        ao ^= 65536;
      }
      const int nxt = u == U - 1 ? 0 : ((u + 1) / SC) * 32768 + ((u + 1) % SC) * 4096;
      if (!OTI4) OT_LDA((u + 1) & 1, ao, nxt);
      SB0();
      if (u == U - 1) OT_PAIR(bs, sd);
#pragma unroll
      for (int j = 0; j < 3; j++) {
#pragma unroll
        for (int m = 0; m < MT; m++) {
          const int n = j * MT + m;
          acc[m][j] = MFMA(wf[u][j], af[u & 1][m], acc[m][j]);
          if (OTI4 && n % (OTI4 ? OTI4 : 1) == 0 && n / (OTI4 ? OTI4 : 1) < MT) { SB0(); OT_LD1((u + 1) & 1, n / (OTI4 ? OTI4 : 1), ao, nxt); SB0(); }
        }
        SB0();
        wf[u][j] = __builtin_bit_cast(bf8, __builtin_amdgcn_raw_buffer_load_b128(rw, vw, sw + u * 2048 + j * 131072, 0));
        SB0();
      }
    }
    bs ^= 65536;
    if (c == NCH - 6) OT_PEEK();
  }
  } else {
#pragma unroll
  for (int i = 0; i < 4; i++) OT_DMA(i, 0, 0);
  SB0();
#pragma unroll
  for (int u = 0; u < U; u++) {
#pragma unroll
    for (int d = 0; d < DP; d++) OT_DMA((u % SC) * DP + d, 32768 + (u / SC) * 32768, (1 + u / SC) * 2 * CW);
#pragma unroll
    for (int j = 0; j < 3; j++) wf[u][j] = __builtin_bit_cast(bf8, __builtin_amdgcn_raw_buffer_load_b128(rw, vw, u * 2048 + j * 131072, 0));
    SB0();
  }
#pragma unroll
  for (int m = 0; m < MT; m++)
#pragma unroll
    for (int j = 0; j < 3; j++) acc[m][j] = (f4){0, 0, 0, 0};
  asm volatile("s_waitcnt vmcnt(%0)" :: "i"(U * (DP + 3)) : "memory"); __builtin_amdgcn_s_barrier(); asm volatile("" ::: "memory");
  OT_LDA(0, ao, 0);
  OT_T(2);
  int bs = 0;
#pragma unroll 1
  for (int c = 0; c < NCH; c += 2) {
    const int sw = c < NCH - 2 ? (c + 2) * CW : PJ_OOB;
#pragma unroll
    for (int u = 0; u < U; u++) {
      SB0();
      __builtin_amdgcn_s_waitcnt(MT == 4 ? 0x4F7C : 0x0F7F);
      if (u % SC == SC - 1 && !(OTX & 32)) { asm volatile("s_waitcnt lgkmcnt(0)" ::: "memory"); __builtin_amdgcn_s_barrier(); asm volatile("" ::: "memory"); }
      if (u == U - 1) ao ^= 65536;
      const int nxt = u == U - 1 ? 0 : ((u + 1) / SC) * 32768 + ((u + 1) % SC) * 4096;
      if (!OTI) OT_LDA((u + 1) & 1, ao, nxt);
      SB0();
      const int cn = c + 3 + u / SC;
#pragma unroll
      for (int j = 0; j < 3; j++) {
#pragma unroll
        for (int m = 0; m < MT; m++) {
          const int n = j * MT + m;
          acc[m][j] = MFMA(wf[u][j], af[u & 1][m], acc[m][j]);
          if (OTI && n % (OTI ? OTI : 1) == 0 && n / (OTI ? OTI : 1) < MT) { SB0(); OT_LD1((u + 1) & 1, n / (OTI ? OTI : 1), ao, nxt); SB0(); }
          if ((n == (OTD & 255) || n == (OTD >> 8)) && !(OTX & 4)) { SB0(); OT_DMA((u % SC) * DP + (n == (OTD >> 8)), u < SC ? (bs ^ 65536) + 32768 : bs, cn < NCH ? cn * 2 * CW : PJ_OOB); SB0(); }
        }
        SB0();
        if (!(OTX & 8)) wf[u][j] = __builtin_bit_cast(bf8, __builtin_amdgcn_raw_buffer_load_b128(rw, vw, sw + u * 2048 + j * 131072, 0));
        SB0();
      }
    }
    bs ^= 65536;
    if (c == NCH - 6) OT_PEEK();
  }
  }
  OT_T(3);
  VMW0C();
  if (wid == 0) {
    const u32 left = *(const u32*)(smem + 131072 + SY_OQ(0) * 4 + (lane < 6 ? lane : 6) * 4);
    pre = 0x20000;
    if (!OXIT || __any(left < (lane > 5 ? 72u : (lane & 1) ? NOB : NOA))) CLAIM();
  }
  asm volatile("s_waitcnt lgkmcnt(0)" ::: "memory"); __builtin_amdgcn_s_barrier(); asm volatile("" ::: "memory");
#pragma unroll
  for (int m = 0; m < MT / 2; m++)
#pragma unroll
    for (int j = 0; j < 3; j++) *(f4*)(smem + (m * 3 + j) * 8192 + va) = kh ? acc[m][j] : acc[MT / 2 + m][j];
  __syncthreads();
#pragma unroll
  for (int m = 0; m < MT / 2; m++) {
    f4 t[3];
#pragma unroll
    for (int j = 0; j < 3; j++) { const f4 o = *(const f4*)(smem + (m * 3 + j) * 8192 + (va ^ 4096)); t[j] = kh ? o + acc[MT / 2 + m][j] : acc[m][j] + o; }
    u16* c = C + (size_t)(kh * MT * 8 + m * 16 + l15) * 1152 + wc * 48;
    *(u32x4*)(c + (lq & 1) * 16 + (lq >> 1) * 8) = pair16(pk2(t[0][0], t[0][1]), pk2(t[0][2], t[0][3]), pk2(t[1][0], t[1][1]), pk2(t[1][2], t[1][3]));
    *(u32x2*)(c + 32 + lq * 4) = (u32x2){pk2(t[2][0], t[2][1]), pk2(t[2][2], t[2][3])};
  }
  __syncthreads();
#undef OT_DMA
#undef OT_LDA
}

#define CLAIMS() do { const int l_ = TIDX; if (l_ < 64) { \
    const u32 v_ = ld4_sc1(hot + SY_OQ(l_ < 6 ? l_ : 0)); \
    if (l_ == 0) pre = (int)__hip_atomic_fetch_add(hot + SY_OQ(6), 1u, __ATOMIC_RELAXED, __HIP_MEMORY_SCOPE_AGENT); \
    pre |= (int)((u32)__ballot(l_ < 6 && v_ < ((l_ & 1) ? NOB : NOA)) << 16); } } while (0)
#define CLS 51
#define KDUE 112
#define KDL 84
#define KEAR 80
#define TPS 2
__device__ __forceinline__ void small_out_task(const Params& p, int rq, int cp, char* smem, const int wv, u32* hot, int& pre) {
  const int tid = TIDX, lane = tid & 63, wid = wv;
  const int l15 = lane & 15, lq = lane >> 4;
  const __amdgpu_buffer_rsrc_t rw = RSRC(WSP(char, O_WOF) + WOB, WOB), ro = RSRC(WSP(u16, O_OB), 2048 * 4096 * 2);
  if (tid == 0) {
    int it = 0;
    while (ld4_sc1(hot + SY_CW) < 64u && ++it < SPIN_CAP) __builtin_amdgcn_s_sleep(8);
  }
  __syncthreads();
  bf8 wf[8][4];
#pragma unroll
  for (int k = 0; k < 8; k++)
#pragma unroll
    for (int j = 0; j < 4; j++)
      wf[k][j] = __builtin_bit_cast(bf8, __builtin_amdgcn_raw_buffer_load_b128(rw, (l15 >> 3) * 1024 + (lq * 16 + (l15 & 7)) * 16 + (j & 1) * 128,
                                                                              ((cp * 2 + (j >> 1)) * 128 + wid * 16 + k * 2) * 1024, 16));
  const int va = OB_OFF(rq * 8 + (l15 & 7), wid * 512 + (l15 >> 3) * 32 + lq * 8);
  const int wo = (lq >> 1) == (l15 >> 3) ? (wid * 2 + (lq >> 1)) * 272 + (l15 & 7) * 32 + (lq & 1) * 16 : 4 * 4352 + lane * 16;
  const int rd = (lane >> 4) * 4352 + (lane & 7) * 544 + wid * 32 + ((lane >> 3) & 1) * 16;
  u32 left = 0u, due = 0u, last = 0u, per = 240u * 64u;
  bool have = false;
  auto step = [&](u32x4 (&af)[8], u32x4 (&nf)[8], const int s) __attribute__((always_inline)) {
    if (due == 0u) {
      if (tid < 64) {
        u32 v;
        int it = 0;
        while (v = ld4_sc1(CSP(s - 2 - (lane & 1))), __any(v == 0u) && ++it < SPIN_CAP) __builtin_amdgcn_s_sleep(SPS);
        *(u32*)(smem + 40960 + (lane & 1) * 4) = v;
      }
      asm volatile("s_waitcnt lgkmcnt(0)" ::: "memory"); __builtin_amdgcn_s_barrier(); asm volatile("" ::: "memory");
      due = __builtin_amdgcn_readfirstlane(*(const u32*)(smem + 40960));
      last = __builtin_amdgcn_readfirstlane(*(const u32*)(smem + 40964));
    }
    per = umin(per, due - last);
    last = due;
    int n = 400, late;
    while (late = (int)(((u32)wall_clock64() << 6) - due - per) - (s == 63 ? KDL : KDUE) * 64, late < 0 && --n > 0) {}
    if (s == 63) TLS(1);
    const u32 v = __builtin_amdgcn_raw_buffer_load_b32(RSRC(CSP(0), wid ? 0 : 4096), 0, (s - 1) * 64, 16);
    if (s == CLS) left = __builtin_amdgcn_raw_buffer_load_b32(RSRC(hot, wid ? 0 : 4096), SY_OQ(lane < 6 ? lane : 6) * 4, 0, 16);
    SB0();
    auto rows = [&](u32x4 (&f)[8], const int so) __attribute__((always_inline)) {
#pragma unroll
      for (int k = 0; k < 8; k++) f[k] = __builtin_amdgcn_raw_buffer_load_b128(ro, va, so + k * 4096, 16);
      SB0();
    };
    auto bad = [&]() __attribute__((always_inline)) {
      u32 t = 0u;
#pragma unroll
      for (int k = 0; k < 8; k++) t = umax(umax(t, af[k][0]), af[k][2]);
      return __any(t == ~0u);
    };
    if (!have) rows(af, s * 262144);
    have = late >= (int)per - KEAR * 64 && s < 63;
    if (KEAR && have) while ((int)(((u32)wall_clock64() << 6) - due - 2 * per) < (s == 62 ? KDL : KDUE) * 64 && --n > 0) {}
    rows(nf, have ? (s + 1) * 262144 : PJ_OOB);
    if (wid == 0) *(u32*)(smem + 40968 + (s & 1) * 4) = v;
    if (__builtin_expect(bad(), 0)) {
      int it = 0;
      do { __builtin_amdgcn_s_sleep(TPS); CFENCE(); rows(af, s * 262144); } while (bad() && ++it < SPIN_CAP);
    }
    SB0();
    f4 acc[4];
#pragma unroll
    for (int k = 0; k < 8; k++)
#pragma unroll
      for (int j = 0; j < 4; j++) acc[j] = MFMA(wf[k][j], __builtin_bit_cast(bf8, af[k]), k ? acc[j] : ((f4){0, 0, 0, 0}));
    char* const sm = smem + (s & 1) * 20480;
#pragma unroll
    for (int j = 0; j < 4; j++) *(f4*)(sm + wo + ((lq >> 1) == (l15 >> 3) ? j * 4352 : 0)) = acc[j];
    asm volatile("s_waitcnt lgkmcnt(0)" ::: "memory"); __builtin_amdgcn_s_barrier(); asm volatile("" ::: "memory");
    if (s == 63) TLS(2);
    due = __builtin_amdgcn_readfirstlane(*(const u32*)(smem + 40968 + (s & 1) * 4));
    f4 t = *(const f4*)(sm + rd) + *(const f4*)(sm + rd + 272);
#pragma unroll
    for (int r = 0; r < 4; r++) {
      t[r] += __uint_as_float(__builtin_amdgcn_mov_dpp(__float_as_uint(t[r]), 0xB1, 0xf, 0xf, true));
      t[r] += __uint_as_float(__builtin_amdgcn_mov_dpp(__float_as_uint(t[r]), 0x4E, 0xf, 0xf, true));
      t[r] += __uint_as_float(__builtin_amdgcn_mov_dpp(__float_as_uint(t[r]), 0x141, 0xf, 0xf, true));
    }
    __builtin_amdgcn_raw_buffer_store_b64((u32x2){pk2(t[0], t[1]), pk2(t[2], t[3])}, RSRC(p.out, 2048 * 2304), (lane & 7) == 0 ? lane : PJ_OOB,
                                          (s * 32 + rq * 8 + wid) * 2304 + 1152 + cp * 64, 0);
  };
  u32x4 fa[8], fb[8];
#pragma unroll
  for (int k = 0; k < 8; k++) asm volatile("" : "=v"(fa[k]));
#pragma unroll 1
  for (int s = XS0; s < 64; s += 2) { step(fa, fb, s); step(fb, fa, s + 1); }
  pre = 72;
  if (__any(left < (lane > 5 ? 72u : (lane & 1) ? NOB : NOA))) CLAIMS();
}
#define PSTAMP()
#define STAMP(i)
#define YEND()
#define ANS 16
#define ANX(slot) (X_ANN ^ xp ^ ((slot) * X_HS))
#define KSL 1
#define DSH 2
#define K1 14
#define K3 8
#define DRL 0
#define CSL 5
#define CSC 0
#define KA 0
#define KB 0
#define KS0 0
#define KMASK 0x00FFFE000FE00000ull
#define KXT(k) ((k) ? (int)((KMASK >> s) & 1) * (k) : 0)
#define DOMAX 90
#define DO0 80
#define DH0 54
#define DOCAP 6
#define C_IMG 0
#define C_QT 9216
#define C_KT (C_QT + 3072)
#define C_P (C_KT + 4096)
#define C_PX (C_P + 768)
#define C_KX (C_PX + 2560)
#define C_VX (C_KX + 8192)
#define C_QX (C_VX + 8192)
#define C_XP 161024
#define CALR 1
#define GPS 6
#define C_WL 49920
__device__ __forceinline__ float ror8(float v) {
  return __uint_as_float(__builtin_amdgcn_mov_dpp(__float_as_uint(v), 0x128, 0xf, 0xf, true));
}
__device__ __forceinline__ float rb1(float f) { return __uint_as_float(pk2(0.f, f)); }
#define FMX(a, b) __builtin_elementwise_maximum(a, b)
__device__ __forceinline__ float tmax(float mx) {
  mx = FMX(mx, ror8(mx));
  { auto t = __builtin_amdgcn_permlane16_swap(__float_as_uint(mx), __float_as_uint(mx), false, false);
    mx = FMX(__uint_as_float(t[0]), __uint_as_float(t[1])); }
  { auto t = __builtin_amdgcn_permlane32_swap(__float_as_uint(mx), __float_as_uint(mx), false, false);
    mx = FMX(__uint_as_float(t[0]), __uint_as_float(t[1])); }
  return mx;
}
__device__ __forceinline__ int hmax(int x) {
  x = max(x, __builtin_amdgcn_mov_dpp(x, 0x128, 0xf, 0xf, true));
  x = max(x, __builtin_amdgcn_mov_dpp(x, 0x124, 0xf, 0xf, true));
  x = max(x, __builtin_amdgcn_mov_dpp(x, 0x4E, 0xf, 0xf, true));
  x = max(x, __builtin_amdgcn_mov_dpp(x, 0xB1, 0xf, 0xf, true));
  { auto t = __builtin_amdgcn_permlane16_swap((u32)x, (u32)x, false, false); x = max((int)t[0], (int)t[1]); }
  return x;
}
__device__ __forceinline__ int hsum(int x) {
  x += __builtin_amdgcn_mov_dpp(x, 0x128, 0xf, 0xf, true);
  x += __builtin_amdgcn_mov_dpp(x, 0x124, 0xf, 0xf, true);
  x += __builtin_amdgcn_mov_dpp(x, 0x4E, 0xf, 0xf, true);
  x += __builtin_amdgcn_mov_dpp(x, 0xB1, 0xf, 0xf, true);
  { auto t = __builtin_amdgcn_permlane16_swap((u32)x, (u32)x, false, false); x = (int)t[0] + (int)t[1]; }
  return x;
}
#define OEP 2
#define CCUT 64u
#define BAR() do { asm volatile("s_waitcnt lgkmcnt(0)" ::: "memory"); __builtin_amdgcn_s_barrier(); asm volatile("" ::: "memory"); } while (0)
#define BARC() do { asm volatile("" ::: "memory"); __builtin_amdgcn_s_waitcnt(0xC07F); __builtin_amdgcn_s_barrier(); asm volatile("" ::: "memory"); } while (0)
#define BARN() do { asm volatile("" ::: "memory"); __builtin_amdgcn_s_barrier(); asm volatile("" ::: "memory"); } while (0)
#define IDX() const int tid = TIDX, lane = tid & 63, l15 = lane & 15, lq = lane >> 4, q4 = l15 >> 2, p4 = lane & 3, hi8 = l15 >> 3; \
    (void)q4; (void)p4; (void)hi8; (void)lq
#define AD_LD(HEAD) __builtin_amdgcn_raw_buffer_load_b32(rsX, (TIDX & (HEAD ? 63 : 31)) * ANS, ANX(s & 1), 16)
#define AD_GET(HEAD, K, CAP) do { int n_ = 0; u32 an = AD_LD(HEAD); \
    while ((u32)__ballot((an & 63u) != (u32)s) != 0u && ++n_ < (CAP)) { __builtin_amdgcn_s_sleep(1); CFENCE(); an = AD_LD(HEAD); } \
    const u32 r_ = (u32)__builtin_amdgcn_readfirstlane((int)an); \
    const int m_ = hmax((TIDX & 32) ? (int)umin(an, DOMAX * 64u) : (int)(an - r_)); \
    dl = ((r_ + (u32)__builtin_amdgcn_readlane(m_, 0)) & ~63u) + (u32)(K) * 64u + (HEAD ? (u32)__builtin_amdgcn_readlane(m_, 32) : 0u); \
    if (!HEAD && !CSL && c == CSC && s > 0 && n_ < (CAP)) __builtin_amdgcn_raw_buffer_store_b32(dl | 1u, rsX, 0, (X_HOT(xp) ^ X_CS) + (s - 1) * 64, 16); } while (0)
#define AD_SPIN() do { int n_ = 160; while ((int)(((u32)wall_clock64() << 6) - dl) < 0 && --n_ > 0) {} } while (0)
#define STG_AUX 0
#define OSTG_AUX 0
#define STG_D 2
#define STG_POLL 7
#define STS(c, i)

#define NXA 1
#define NXE 0
#define ADE 1
#define NXH 1
#define NXV 1
#define NXB 4
#define NXS 3
#define PKA 18
#define PK_(w0, d, a, b, w1) ".long " #w0 " + " #d ", " w1 " + " #a " + " #b " * 512\n\t"
#define PKADD(d, a, b) PK_(0xD3B24000, d, a, b, "0x18020100")
#define PKMUL(d, a, b) PK_(0xD3B14000, d, a, b, "0x18020100")
#define PKFMS(d, a, b, c) PK_(0xD3B04400, d, a, b, "0x9C020100 + " #c " * 262144")
#define CV2(d, a) "v_cvt_pk_bf16_f32 " d ", v[" #a "], v[" #a "+1]\n\t"
#define VBP 0xA9A
#define C_Z 45824
#define SMALL 0x1p-64f
#define EK 0
#define EKN 2
#define EKL 1
#define EKA 1
#define BJ 2
#define KSP 0
#define SP1 2
#define SP2 0
#define KP 3
#define GX 1
#define GXO (GX == 1 ? 4096 : 1024)
#define ZE 0x0D
#define KD 5
#define QSL 0
#define DJ 9
#define VSL 0
#define VSL1 4
#define KV0 1
#define PVP 0
#define IPR 3
#define DSLOT 0x9876E1CBAD0ull
#define F_K (47360 - C_KT)
#define F_Q (47872 - 768 - C_QT)
#define HAL 2
#define OAL 2
#define ALIGN(n) asm volatile(".p2align %0" :: "i"(n))
#define QXP 4
#define RORD 0
#define PRIO(n) __builtin_amdgcn_s_setprio(n)
#define LSL 6
__device__ __forceinline__ int chain_head(const Params& p, char* smem, const int h, const int wv, const bool spec) {
  STS(h, 1);
  TLS(12);
  const int wid = wv;
  u32x8 w[2][9], wx[3];
  const __amdgpu_buffer_rsrc_t rsX = RSRC(XWIN(p), X_END);
  const __amdgpu_buffer_rsrc_t rsB = RSRC(WSP(u16, O_QB), 4 * ACB);
  const int cb = h >> 1;
  const int nvt = (wid & 3) == 0 ? 0 : (wid < 3 ? 2 : 1);
  const int vt = wid < 3 ? wid * 2 + 2 : (wid == 3 ? 0 : wid - 4);
  const int wl = C_WL + (nvt == 1 ? vt * 12288 : 49152 + (nvt == 2 ? wid - 1 : wid >> 2) * 30720);
  int xp;
  {
    IDX();
    u32x4 st[54], nk, nv, nq;
    u32 fl, fk;
    const int vw = ((tid >> 4) * 4096 + h * 128 + (tid & 15) * 8) * 2;
#define NTL 64
#define AH 1
    u32 tc[9], b1, old = 0u;
#define TEC 0
    const u16 *pq = p.BWq, *pk = p.BWk, *pv = p.BWv;
    asm volatile("" : "+s"(pq), "+s"(pk), "+s"(pv));
    const bool te = wid < 3 && (h & 3) == TEC;
    const int tm = te ? wid : (h & 3) - 1;
    const u64 pt = tm == 1 ? (u64)pk : tm == 2 ? (u64)pv : (u64)pq;
    const __amdgpu_buffer_rsrc_t rt = RSRC(pt, te || (tm && h != 31 && wid == 7) ? 576 * 4096 * 2 : 0);
    const int tv = lane < NTL ? lane * 8192 : PJ_OOB, ts_ = te ? 8064 : (h >> 2) * 1024 + 384;
    const __amdgpu_buffer_rsrc_t rq = RSRC(pq, 576 * 4096 * 2), rk = RSRC(pk, 576 * 4096 * 2), rv = RSRC(pv, 576 * 4096 * 2);
    auto req = [&](int kc) {
#pragma unroll
      for (int i = 0; i < 6; i++) {
        st[kc * 6 + i] = __builtin_amdgcn_raw_buffer_load_b128(i < 2 ? rq : (i < 4 ? rk : rv), vw, (kc * 64 + (i & 1) * 32) * 8192, STG_AUX);
        if (NTL && kc + AH < 9 && i == (kc ? 0 : 4)) { SB0(); tc[kc] = __builtin_amdgcn_raw_buffer_load_b32(rt, tv, te && (h >> 2) != kc ? PJ_OOB : (kc + AH) * 64 * 8192 + ts_, 0); }
      }
      SB0();
    };
#pragma unroll
    for (int kc = 0; kc < STG_D; kc++) { if (kc == 0) TLS(13); req(kc); __builtin_amdgcn_s_barrier(); SB0(); }
    TLS(14);
    auto cell = [](int row, int slot) { return (((row * 2 + slot) ^ ((((row >> 2) ^ (row >> 4)) & 1) << 2) ^ ((row >> 5) & 1)) << 5); };
    auto put = [&](int unit, int row, const u32x4& v) { *(u32x4*)(smem + unit * 4160 + cell(row, (tid >> 1) & 1) + (tid & 1) * 16) = v; };
    auto frag = [&](int tile) {
      bf4 tt[4];
#pragma unroll
      for (int kk = 0; kk < 4; kk++) {
        const int r0 = (kk >> 1) * 32 + (lq >> 1) * 16 + (q4 >> 1) * 8 + (lq & 1) * 4 + (kk & 1) * 2 + (q4 & 1);
        tt[kk] = trrd(smem + (tile >> 1) * 4160 + cell(r0, tile & 1) + p4 * 8);
      }
      return ilv(tt[0], tt[1], tt[2], tt[3]);
    };
    auto park = [&](int slot, const u32x8& f) {
      char* d = smem + wl + slot * 2048 + lane * 16;
      *(u32x4*)d = (u32x4){f[0], f[1], f[2], f[3]};
      *(u32x4*)(d + 1024) = (u32x4){f[4], f[5], f[6], f[7]};
    };
    const __amdgpu_buffer_rsrc_t rs0 = RSRC(p.ws, wid ? 0 : O_QA);
    auto poll = [&]() {
      fl = __builtin_amdgcn_raw_buffer_load_b32(rs0, (lane < 32 ? SY_INIT(lane) : SY_ARR) * 4, 0, 16);
      fk = __builtin_amdgcn_raw_buffer_load_b32(rs0, SY_BLK(lane < 12 ? lane >> 2 : 0, cb, lane & 3) * 4, 0, 16);
    };
    auto rows = [&](int vo) {
      nq = __builtin_amdgcn_raw_buffer_load_b128(rsB, vo, h * 8192, 16);
      nk = __builtin_amdgcn_raw_buffer_load_b128(rsB, vo, h * 8192 + ACB, 16);
      nv = __builtin_amdgcn_raw_buffer_load_b128(rsB, vo, h * 8192 + 2 * ACB, 16);
    };
    bool early = false;
    int lost = 0;
#pragma unroll
    for (int kc = 0; kc < 9; kc++) {
      if (kc == STG_D - 1) { if (spec && wid == 0) old = confirm(p, smem, h, b1, wv); SB0(); }
      if (kc == 10 - STG_D) { early = __all(fk >= FULL); CFENCE(); rows(early ? tid * 16 : PJ_OOB); poll(); SB0(); }
#pragma unroll
      for (int i = 0; i < 6; i++) put((i >> 1) * 4 + ((tid & 15) >> 2), (i & 1) * 32 + (tid >> 4), st[kc * 6 + i]);
      if (NTL && kc + AH < 9) asm volatile("" :: "v"(tc[kc]));
      if (kc == 2 * STG_D - 1) lost = (__builtin_amdgcn_readfirstlane(old) >> h) & 1;
      BAR();
      if (kc == 0) TLS(15);
      if (kc == 0) { b1 = __builtin_amdgcn_raw_buffer_load_b32(rs0, 0, SY_BEAT(h & 7) * 4, 16); SB0(); }
      if (kc + STG_D == 8) { fk = ld4_sc1(WSP(u32, 0) + SY_BLK(lane < 12 ? lane >> 2 : 0, cb, lane & 3)); SB0(); }
      if (kc + STG_D < 9) req(kc + STG_D);
      w[0][kc] = frag(wid * 2);
      w[1][kc] = frag(nvt == 2 && kc < 4 ? 16 + vt : wid * 2 + 1);
      const u32x8 tp = frag(nvt == 0 ? 21 + (wid >> 1) : (nvt == 2 && kc < 4 ? wid * 2 + 1 : 16 + vt));
      if (kc >= 4 && kc < 7) { wx[kc - 4] = tp; if (nvt == 0) park(6 + kc, tp); }
      else park(nvt == 0 ? 6 + kc : (kc >= 7 ? kc - 7 : 2 + kc), tp);
      BAR();
      if (kc == 0 || kc == 8) STS(h, kc + 3);
    }
    TLS(6);
    if (wid == 0) {
      int it = 0;
      while (!lost && !__all(fl >= (lane < 32 ? 1u : 256u) && fk >= FULL) && ++it < SPIN_CAP) { BEAT_ON(it); __builtin_amdgcn_s_sleep(GPS); CFENCE(); poll(); }
      if (lane == 0) *(int*)(smem + C_XP) = lost ? -1 : fl & 0x7800;
    }
    BAR();
    if (!early) rows(tid * 16);
    xp = __builtin_amdgcn_readfirstlane(*(const int*)(smem + C_XP));
    if (xp < 0) return xp;
    TLS(7);
    SB0();
    *(u32x4*)(smem + C_QX + tid * 16) = nq;
    *(u32x4*)(smem + C_KX + tid * 16) = nk;
    *(u32x4*)(smem + C_VX + ((tid * 16) ^ ((tid & 32) << 2))) = nv;
  }
  STS(h, 14);
  constexpr float CE = 0.08838834764831845f * 1.4426950408889634f;
  auto run = [&](auto RC) __attribute__((always_inline)) {
  constexpr int R = decltype(RC)::value;
  constexpr int NV = R < 2 ? 0 : (R < 4 ? 1 : 2);
  constexpr bool QW = !(R & 1);
  int known = 0;
  bf8 pre[4][2], vx[NV ? NV : 1];
  int sidx, mo, io, eo, gvo, wvo = 0, so = 0, po = 0, qo = 0, ko = 0, fo = 0, fq = 0, fk = 0;
  float mk[4], mm = 0.f;
  f2v cw = {CE, CE};
#define LDW(dst, slot) do { dst[0] = *(const bf8*)(smem + wvo + (slot) * 2048); dst[1] = *(const bf8*)(smem + wvo + (slot) * 2048 + 1024); } while (0)
  {
    IDX();
    if constexpr (R == 1) {
      po = (tid * 16) ^ ((tid & 32) << 2);
      so = (lane >> 5) * 131072 + (lane & 31) * 16;
      asm volatile("" : "+v"(po), "+v"(so));
    } else if constexpr (R == 0) {
      qo = C_QT + lq * 384 + q4 * 32 + p4 * 8;
      po = C_P + l15 * 16 + lq * 4;
      fo = lq * 512 + l15 * 16;
#pragma unroll
      for (int r = 0; r < 4; r++) { mk[r] = lq * 4 + r <= l15 ? 0.f : -__builtin_inff(); asm volatile("" : "+v"(mk[r])); }
      mm = (l15 & 2) ? -__builtin_inff() : 0.f;
      asm volatile("" : "+v"(mm), "+{v[230:231]}"(cw));
      fq = C_QT + F_Q + (lq ? lq : 1) * 768 + lq * 8; fk = C_KT + F_K + lq * 776;
      asm volatile("" : "+v"(qo), "+v"(po), "+v"(fo), "+v"(fq), "+v"(fk));
      *(u32*)(smem + fq) = 0u; *(u32*)(smem + fk) = 0u;
    } else {
      wvo = wl + lane * 16;
      po = C_P + (lq * 4 + q4) * 16 + (p4 & 1) * 8;
      so = ((l15 >> 2) & 1) * 131072 + (((vt & 3) + (NV == 2 ? hi8 : 0)) * 8 + ((l15 >> 1) & 1) * 4 + (lq >> 1) * 2 + (l15 & 1)) * 16 + (lq & 1) * 8;
      if (NV == 1 && hi8) so = PJ_OOB;
      asm volatile("" : "+v"(wvo), "+v"(po), "+v"(so));
      if constexpr (NV == 2) {
        qo = po ^ ((p4 & 2) << 4); ko = (p4 & 2) ? po : C_Z; po = (p4 & 2) ? C_Z : po;
        *(u64*)(smem + C_Z + hi8 * 256) = 0ull;
        asm volatile("" : "+v"(ko), "+v"(po), "+v"(qo));
      }
#pragma unroll
      for (int j = 0; j < 4; j++) LDW(pre[j], NV == 2 ? 2 + j : j);
    }
    eo = C_QT + wid * 768 + ((l15 >> 2) * 2 + (l15 & 1)) * 32 + lq * 8 + ((l15 >> 1) & 1) * 4;
    sidx = (l15 & 2) ? 0xEEEE : 0x4444;
    mo = C_IMG + lane * 16;
    io = C_IMG + ((tid & ~30) | ((tid & 10) << 1) | ((tid & 20) >> 1)) * 16;
    gvo = tid * 16;
    asm volatile("" : "+v"(sidx), "+v"(mo), "+v"(io), "+v"(eo));
    if constexpr (R < 2) asm volatile("" : "+v"(gvo));
  }
  f4 a0, a1;
  u32x4 vm = {0u, 0u, 0u, 0u};
  if constexpr (NV > 0) asm volatile("" : "+v"(vm));
  u32 ts = 0;
  u64 tq = 0;
  int dur = DH0 * 64;
  const int ob = ((h >> 3) * 32768 + (h & 7) * 512) ^ xp ^ (NV == 1 ? X_HS : 0);
  const int ib = X_M ^ xp ^ ((wid >> 2) * 32768 + (wid & 3) * 1024);
  int b3 = 0;
  PRIO(QW ? 2 : 1);
#pragma unroll 1
  for (int s = 0;; s++) {
    int mb = ib + b3 * 65536, mbx = X_M ^ xp ^ (196608 + b3 * 1024);
    if constexpr (R == GX) asm volatile("" : "+s"(mb), "+s"(mbx));
    u32x4 g, gx;
    if constexpr (R != 1) ALIGN(HAL);
    BAR();
    STAMP(13);
    if constexpr (R == 1 && NXE) {
      asm volatile("buffer_load_dwordx4 %0, %2, %3, %4 offen sc1\n\tbuffer_load_dwordx4 %1, %2, %3, %5 offen sc1"
                   : "=&v"(g), "=&v"(gx) : "v"(mo), "s"(rsX), "s"(mb), "s"(mbx) : "memory");
    } else {
      g = __builtin_amdgcn_raw_buffer_load_b128(rsX, mo, mb, 16);
      if constexpr (R == GX) gx = __builtin_amdgcn_raw_buffer_load_b128(rsX, mo, mbx, 16);
    }
    SB0();
    if constexpr (R == 0) {
#pragma unroll
      for (int j = 0; j < 4; j++) { const char* kx_ = smem + C_KX + fo + j * 2048;
        pre[j >> 1][j & 1] = *(const bf8*)kx_; pre[2 + (j >> 1)][j & 1] = *(const bf8*)(kx_ + 256); }
    } else if constexpr (R > 1) {
      IDX();
#pragma unroll
      for (int d = 0; d < NV; d++) { const char* vb_ = smem + C_VX + (vt + d) * 1024 + (p4 >> 1) * 512 + (((lq * 4 + q4) ^ ((p4 >> 1) * 8)) << 4) + (p4 & 1) * 8;
        vx[d] = cat8(trrd(vb_), trrd(vb_ + 256)); }
    }
    SB0();
    a0 = zero4(); a1 = zero4();
    f4 c0, c1, a2;
#define A2INI() do { u64 z_; float m0_, m1_; asm volatile("v_mov_b32 %0, %3\n\tv_mov_b32 %1, %3\n\tv_mov_b64 %2, 0\n\ts_nop 1" : "=&v"(m0_), "=&v"(m1_), "=v"(z_) : "v"(mm)); \
                     const f2v zz_ = __builtin_bit_cast(f2v, z_); a2 = (f4){m0_, m1_, zz_[0], zz_[1]}; } while (0)
    if constexpr ((ZE >> R) & 1) {
      c1 = zero4();
      if constexpr (NV != 1) c0 = zero4();
      if constexpr (R == 0) { A2INI(); asm volatile("" : "+v"(a2)); }
    }
    int so_ = X_O(b3) ^ ob, an_ = ANX(s & 1) + h * ANS;
    int nb = s < 63 ? (s + 1) * 262144 + h * 8192 : PJ_OOB;
    asm volatile("" : "+s"(so_), "+s"(an_), "+s"(nb));
    u32x4 nx[2];
#define NXL1(g, i) __builtin_amdgcn_raw_ptr_buffer_load_lds(rsB, (LDSP void*)(smem + C_KX + (g) * 4096), 16, (g) < 2 ? gvo : po, \
                                                             nb + (1 + ((g) >> 1)) * ACB + ((g) & 1) * 4096 - 4096, (i) * 1024, 16)
#define NXL(g) do { NXL1(g, 0); NXL1(g, 1); NXL1(g, 2); NXL1(g, 3); } while (0)
    SB0();
    if (NXE) {
      BARC();
      if constexpr (R == 1) {
        if (NXE > 1) __builtin_amdgcn_s_sleep(NXE - 1);
#pragma unroll
        for (int i = 0; i < 4; i++)
          asm volatile("s_mov_b32 m0, %3\n\ts_nop 0\n\tbuffer_load_dwordx4 %0, %1, %2 offen sc1 lds\n\tbuffer_load_dwordx4 %0, %1, %2 offen offset:1024 sc1 lds\n\t"
                       "buffer_load_dwordx4 %0, %1, %2 offen offset:2048 sc1 lds\n\tbuffer_load_dwordx4 %0, %1, %2 offen offset:3072 sc1 lds"
                       :: "v"(i < 2 ? gvo : po), "s"(rsB), "s"(nb + (1 + (i >> 1)) * ACB + (i & 1) * 4096 - 4096), "i"(C_KX + i * 4096) : "memory", "m0");
      }
      SB0();
    }
#define LDM(j) *(const bf8*)(smem + mo + (j) * 1024)
    constexpr int NB = R == 1 ? 9 : 5;
    bf8 b[NB];
    if constexpr (!QW) {
      PRIO(IPR); BARN();
      if (((EK >> R) & 1) && !EKL) {
#pragma unroll
        for (int j = 0; j < EKN; j++) { b[j] = LDM(j); SB0(); }
      }
    }
    {
      if constexpr (R == 1 && NXE) asm volatile("s_waitcnt vmcnt(17)" : "+v"(g));
      else __builtin_amdgcn_s_waitcnt(R == GX ? 0x0F71 : 0x0F70);
      CFENCE();
      *(u32x4*)(smem + io) = g;
      SB0();
      u32 top = umax(umax(umax(g[0], g[1]), g[2]), g[3]);
      if constexpr (R == GX) {
        asm volatile("" : "+v"(top));
        SB0();
        if constexpr (R == 1 && NXE) asm volatile("s_waitcnt vmcnt(16)" : "+v"(gx));
        *(u32x4*)(smem + GXO + io) = gx;
        SB0();
        top = umax(umax(umax(umax(top, gx[0]), gx[1]), gx[2]), gx[3]);
      }
#define G_BAD() (umax(umax(umax(g[0], g[1]), umax(g[2], g[3])), R != GX ? 0u : umax(umax(gx[0], gx[1]), umax(gx[2], gx[3]))) == ~0u)
      if (__builtin_expect(__any(top == ~0u), 0)) {
        int it = 0;
        do {
          CFENCE();
          if (G_BAD()) {
            g = __builtin_amdgcn_raw_buffer_load_b128(rsX, mo, mb, 16);
            if constexpr (R == GX) gx = __builtin_amdgcn_raw_buffer_load_b128(rsX, mo, mbx, 16);
          }
          VMW0C();
        } while (__any(G_BAD()) && ++it < SPIN_CAP);
        *(u32x4*)(smem + io) = g;
        if constexpr (R == GX) *(u32x4*)(smem + GXO + io) = gx;
      }
    }
    SB0();
    STAMP(15);
    if constexpr (!QW) PRIO(1);
    if (((EK >> R) & 1) && EKL) {
#pragma unroll
      for (int j = 0; j < EKN; j++) { b[j] = LDM(j); SB0(); }
      CFENCE(); __builtin_amdgcn_s_waitcnt(0xC07F | (EKN << 8)); BARN();
    } else
    BARC();
    STAMP(0);
#define ANNC() do { ts = (u32)tq; __builtin_amdgcn_raw_buffer_store_b32((((ts << 6) + (u32)dur) & ~63u) | (u32)s, rsX, 0, an_, 16); SB0(); } while (0)
    if constexpr (R == 1) {
      STAMP(6);
      if (EK & 2) asm volatile("s_memrealtime %0" : "=s"(tq));
      else { asm volatile("s_memrealtime %0\n\ts_waitcnt lgkmcnt(0)" : "=s"(tq)); ANNC(); }
      SB0();
#pragma unroll
      for (int j = (EK & 2) ? EKN : 0; j < 9; j++) { b[j] = LDM(j); SB0(); }
#pragma unroll
      for (int i = 0; i < NXA; i++) { NXL(i); SB0(); }
    } else {
      if (!QW && KSP) __builtin_amdgcn_s_sleep(KSP);
#pragma unroll
      for (int j = ((EK >> R) & 1) ? EKN : 0; j < 4; j++) { b[j] = LDM(j); SB0(); }
    }
#pragma unroll
    for (int j = 0; j < 9; j++) {
      if constexpr (QW) {
        if (j == BJ) {
          BARN();
#pragma unroll
          for (int i = 4; i < 4 + BJ; i++) {
            b[i % 5] = LDM(i); SB0();
          }
        }
        if (j >= BJ && j + 4 < 9) b[(j + 4) % 5] = LDM(j + 4);
      } else if (R != 1 && j + 4 < 9) b[(j + 4) % 5] = LDM(j + 4);
      SB0();
      a0 = SMF(b[j % NB], w[0][j], a0, sidx);
      if (NV < 2 || j >= 4) a1 = SMF(b[j % NB], w[1][j], a1, sidx);
      else a1 = SMF(b[j % 5], cat16(pre[j & 3][0], pre[j & 3][1]), a1, sidx);
      SB0();
      if (R == 1 && (EK & 2) && j == EKA) { asm volatile("s_waitcnt lgkmcnt(0)" : "+s"(tq)); ANNC(); }
      if (j == 0) STAMP(10);
      if (j == 4) STAMP(11);
      if (j == 8) STAMP(12);
      if (NV == 2 && j >= DJ && j < DJ + 4) LDW(pre[j - DJ], (DSLOT >> ((j - DJ) * 4)) & 15);
    }
    {
      {
        const u32 e0 = pk2(a0[0] + a0[2], a0[1] + a0[3]), e1 = pk2(a1[0] + a1[2], a1[1] + a1[3]);
        asm volatile("ds_write2_b32 %0, %1, %2 offset1:%3" :: "v"(eo), "v"(e0), "v"(e1), "i"(96) : "memory");
      }
      if constexpr (R != 0) asm volatile("ds_write_b32 %0, %1 offset:%2" :: "v"(eo), "v"(sidx), "i"(QW ? F_Q : F_K) : "memory");
    }
    SB0();
    STAMP(1);
    if constexpr (QW) PRIO(R ? 0 : SP1);
    else if (KV0) PRIO(0);
    if constexpr ((ZE >> R) & 1) { a0 = c0; a1 = c1; }
    else if constexpr (R != 1) { a1 = zero4(); if constexpr (NV != 1) a0 = zero4(); if constexpr (R == 0) A2INI(); }
    if constexpr (NV == 0) {
      u32 dl = 0;
      const int c = 1;
      bf8 km[2];
      if constexpr (R == 0) {
        if (QSL) __builtin_amdgcn_s_sleep(QSL);
        bf8 qa[2];
        u32 fl;
        do {
          CFENCE();
          fl = *(const volatile LDSP u32*)(smem + fq);
          CFENCE(); SB0();
#pragma unroll
          for (int i = 0; i < 2; i++) { qa[i] = cat8(trrd(smem + qo + i * 1536), trrd(smem + qo + i * 1536 + 128)); SB0(); }
        } while (__builtin_expect(__any(fl == 0u), 0));
        STAMP(2);
        auto rdk = [&]() __attribute__((always_inline)) {
          CFENCE(); SB0();
          fl = *(const volatile LDSP u32*)(smem + fk);
          CFENCE(); SB0();
#pragma unroll
          for (int i = 0; i < 2; i++) { km[i] = cat8(trrd(smem + qo + 3072 + i * 1536), trrd(smem + qo + 3200 + i * 1536)); SB0(); }
        };
        auto mulk = [&]() __attribute__((always_inline)) {
#pragma unroll
          for (int i = 0; i < 2; i++) { a2 = MFMA(qa[i], km[i], a2); if (!i) asm volatile("" : "+{v[240:243]}"(a2)); SB0(); }
        };
#pragma unroll
        for (int i = 0; i < 2; i++) {
          a0 = SMF(qa[i], cat16(pre[i][0], pre[i][1]), a0, sidx); asm volatile("" : "+v"(a0)); SB0();
          a1 = SMF(qa[i], cat16(pre[2 + i][0], pre[2 + i][1]), a1, sidx); if (!i) asm volatile("" : "+v"(a1)); SB0();
        }
#define EX2(d) "v_exp_f32 v[" #d "], v[" #d "]\n\tv_exp_f32 v[" #d "+1], v[" #d "+1]\n\t"
#define DP2(OP, d, N) OP " v[" #d "], v[" #d "], v[" #d "] row_ror:" #N " row_mask:0xf bank_mask:0xf\n\t" OP " v[" #d "+1], v[" #d "+1], v[" #d "+1] row_ror:" #N " row_mask:0xf bank_mask:0xf\n\t"
#define ROW2(OP, d) DP2(OP, d, 8) "s_nop 0\n\t" DP2(OP, d, 4) "s_nop 0\n\t" DP2(OP, d, 2) "s_nop 0\n\t" DP2(OP, d, 1)
#define S_F PKFMS(234, 232, 230, 244) PKFMS(238, 236, 230, 244)
#define S_E EX2(234) EX2(238)
#define S_IO "+{v[232:235]}"(a0), "+{v[236:239]}"(a1)
        f4 z;
#define S_0 "s_setprio %[pr]\n\ts_nop 5\n\t" PKADD(232, 232, 234) PKADD(236, 236, 238) "s_nop 0\n\t" \
            "v_mul_f32_dpp v244, v232, v230 row_newbcast:0 row_mask:0xf bank_mask:0xf\n\tv_mul_f32_dpp v245, v233, v230 row_newbcast:0 row_mask:0xf bank_mask:0xf\n\t" S_F
        if (KP == 3) asm volatile(S_0 S_E "s_setprio 2" : S_IO, "=&{v[244:247]}"(z) : "{v[230:231]}"(cw), [pr] "i"(SP2));
        if (KP == 2) asm volatile(S_0 EX2(234) "s_setprio 2" : S_IO, "=&{v[244:247]}"(z) : "{v[230:231]}"(cw), [pr] "i"(SP2));
        if (KP == 1) asm volatile(S_0 "s_setprio 2" : S_IO, "=&{v[244:247]}"(z) : "{v[230:231]}"(cw), [pr] "i"(SP2));
        STAMP(3);
        if (KD) asm volatile("s_nop %0" :: "i"(KD - 1));
        SB0();
        rdk();
#define S_X PKADD(246, 234, 238)
#define S_M PKFMS(242, 240, 230, 244) EX2(242) "s_nop 0\n\t" PKADD(246, 246, 242) "s_nop 1\n\t" ROW2("v_add_f32_dpp", 246) "v_rcp_f32 v246, v246\n\tv_rcp_f32 v247, v247\n\ts_nop 0\n\t" \
            PKMUL(234, 234, 246) PKMUL(238, 238, 246) PKMUL(242, 242, 246) \
            CV2("v234", 234) CV2("v238", 238) CV2("v242", 242) "ds_write2st64_b32 %[po], v234, v238 offset1:1\n\tds_write_b32 %[po], v242 offset:512\n\tv_min_f32 v246, v246, v247"
        if (KP == 3) asm volatile(S_X : S_IO, "+{v[244:247]}"(z));
        if (KP == 2) asm volatile(EX2(238) "s_nop 0\n\t" S_X : S_IO, "+{v[244:247]}"(z));
        if (KP == 1) asm volatile(S_E "s_nop 0\n\t" S_X : S_IO, "+{v[244:247]}"(z));
        SB0();
        if (__builtin_expect(__any(fl == 0u), 0)) {
          asm volatile("s_nop 0");
          do {
            rdk();
          } while (__any(fl == 0u));
        }
        mulk();
        STAMP(4);
#define S_IO2 S_IO, "+{v[240:243]}"(a2), "+{v[244:247]}"(z) : "{v[230:231]}"(cw), [po] "v"(po) : "memory"
        asm volatile("s_nop 7\n\tv_add_f32_dpp v240, v242, v240 quad_perm:[2,3,0,1] row_mask:0xf bank_mask:0xf\n\tv_add_f32_dpp v241, v243, v241 quad_perm:[2,3,0,1] row_mask:0xf bank_mask:0xf\n\t" S_M : S_IO2);
        SB0();
        if (__builtin_expect(__any(z[2] < SMALL), 0)) {
          asm volatile("v_max3_f32 v244, v232, v236, v240\n\tv_max3_f32 v245, v233, v237, v241\n\ts_nop 0\n\t" ROW2("v_max_f32_dpp", 244) PKMUL(244, 244, 230) S_F S_E "s_nop 0\n\t" S_X S_M : S_IO2);
        }
        SB0();
      } else {
#pragma unroll
        for (int i = NXA; i < (NXV ? NXH : NXB) && !NXE; i++) NXL(i);
        SB0();
        const int rb = X_O(b3 == 0 ? 2 : b3 - 1) ^ ob;
        const u32x4 e = ones4();
        __builtin_amdgcn_raw_buffer_store_b128(e, rsX, so, rb, 16);
        __builtin_amdgcn_raw_buffer_store_b128(e, rsX, so, rb ^ X_HS, 16);
        PSTAMP();
      }
      STAMP(5);
      BARC();
      if constexpr (R == 0) STAMP(6);
      if constexpr (R == 1) {
        const u32 tp = (u32)wall_clock64(); dur += ((int)umin((tp - ts) << 6, 200u * 64u) - dur) >> DSH;
        SB0();
        if (ADE && s < 63) AD_GET(true, K1 + K3 + KXT(KA + KB) + (s ? 0 : KS0), 4096);
        if (NXB < 4) {
          __builtin_amdgcn_s_sleep(NXS);
#pragma unroll
          for (int i = NXB; i < 4; i++) NXL(i);
          BARN();
        }
        ALIGN(HAL);
        VMW0C();
        STAMP(8);
        if (NXB == 4) BARN();
        if (s == 63) break;
      } else {
        *(u32*)(smem + fq) = 0u; *(u32*)(smem + fk) = 0u;
        bf8 qx[2][4];
#pragma unroll
        for (int ks = 0; ks < 8; ks++) { qx[ks >> 2][ks & 3] = *(const bf8*)(smem + C_QX + (ks >> 2) * 256 + fo + (ks & 3) * 2048); if (ks < QXP) SB0(); }
#pragma unroll
        for (int NT = 1; NT <= 2; NT++) {
          f4 sc[2], sm = zero4();
          const bf8* qf = qx[NT - 1];
#pragma unroll
          for (int ks = 0; ks < 4; ks++) {
#pragma unroll
            for (int j = 0; j < NT; j++) sc[j] = MFMA(pre[2 * j + (ks >> 1)][ks & 1], qf[ks], ks ? sc[j] : (j == NT - 1 ? ((f4){mk[0], mk[1], mk[2], mk[3]}) : ((f4){0, 0, 0, 0})));
            if (ks & 1) sm = SMF(km[ks >> 1], cat16(qf[ks - 1], qf[ks]), sm, sidx);
          }
          sm[0] += sm[2]; sm[1] += sm[3];
          float mx = FMX3(FMX3(sc[0][0], sc[0][1], sc[0][2]), sc[0][3], FMX(sm[0], sm[1]));
          if (NT == 2) mx = FMX3(FMX3(mx, sc[1][0], sc[1][1]), sc[1][2], sc[1][3]);
          { auto t = __builtin_amdgcn_permlane16_swap(__float_as_uint(mx), __float_as_uint(mx), false, false);
            mx = FMX(__uint_as_float(t[0]), __uint_as_float(t[1])); }
          { auto t = __builtin_amdgcn_permlane32_swap(__float_as_uint(mx), __float_as_uint(mx), false, false);
            mx = FMX(__uint_as_float(t[0]), __uint_as_float(t[1])); }
          mx *= -CE;
#pragma unroll
          for (int j = 0; j < NT; j++)
#pragma unroll
            for (int r = 0; r < 4; r++) sc[j][r] = __builtin_amdgcn_exp2f(__builtin_fmaf(sc[j][r], CE, mx));
          sm[0] = __builtin_amdgcn_exp2f(__builtin_fmaf(sm[0], CE, mx)); sm[1] = __builtin_amdgcn_exp2f(__builtin_fmaf(sm[1], CE, mx));
          float sum = ((sc[0][0] + sc[0][1]) + (sc[0][2] + sc[0][3])) + (sm[0] + sm[1]);
          if (NT == 2) sum += (sc[1][0] + sc[1][1]) + (sc[1][2] + sc[1][3]);
          { auto t = __builtin_amdgcn_permlane16_swap(__float_as_uint(sum), __float_as_uint(sum), false, false);
            sum = __uint_as_float(t[0]) + __uint_as_float(t[1]); }
          sum = swadd(sum, sum);
          const float inv = __builtin_amdgcn_rcpf(sum);
          *(u32x4*)(smem + C_PX + (NT - 1) * 1024 + gvo) = (u32x4){pk2(sc[0][0] * inv, sc[0][1] * inv), pk2(sc[0][2] * inv, sc[0][3] * inv),
                                                                  NT == 2 ? pk2(sc[1][0] * inv, sc[1][1] * inv) : 0u, NT == 2 ? pk2(sc[1][2] * inv, sc[1][3] * inv) : 0u};
          *(u32*)(smem + C_PX + 2048 + (NT - 1) * 256 + (gvo >> 2)) = pk2(sm[0] * inv, sm[1] * inv);
        }
        STAMP(8);
        BAR();
        if (s == 63) break;
        if (known < (s < 61 ? s + 3 : 64)) {
          int n = 0;
          while (true) {
            u32 bf[3];
#pragma unroll
            for (int i = 0; i < 3; i++) bf[i] = ld4_sc1(WSP(u32, 0) + SY_BLK(i, cb, gvo >> 4));
            const u64 miss = __ballot(!(bf[0] >= FULL && bf[1] >= FULL && bf[2] >= FULL));
            known = miss ? __builtin_ctzll(miss) : 64;
            if (known >= (s < 61 ? s + 3 : 64) || ++n >= SPIN_CAP) break;
            __builtin_amdgcn_s_sleep(KSL);
          }
        }
      }
      STAMP(9);
      YEND();
      if constexpr (R == 1) { if (!ADE) AD_GET(true, K1 + K3, 4096); AD_SPIN(); }
    } else {
      if (NV == 2 && DJ > 5) {
#pragma unroll
        for (int j = 0; j < 4; j++) LDW(pre[j], (DSLOT >> (j * 4)) & 15);
        SB0();
      }
      if (QW && (NV == 2 ? VSL : VSL1)) __builtin_amdgcn_s_sleep(NV == 2 ? VSL : VSL1);
      bf4 p0, p1, p2, p3, m0, m1;
      auto getp = [&]() __attribute__((always_inline)) {
        if (QW && PVP) PRIO(2);
        if (!QW && KV0) PRIO(1);
        SB0();
        STAMP(5);
        BARN();
        p0 = trrd(smem + po); p1 = trrd(smem + po + 256);
        SB0();
        if constexpr (NV == 2) { p2 = trrd(smem + ko); p3 = trrd(smem + ko + 256); }
        SB0();
        m1 = trrd(smem + (NV == 2 ? qo : po) + 512);
        SB0();
      };
      constexpr int VB = (VBP >> (R * 4 - 8)) & 15;
      if constexpr (NV == 2) {
        int u = 0;
#pragma unroll
        for (int i = 0; i < 9; i++) {
          const int bs = (0x31042 >> ((i % 5) * 4)) & 7;
#define RING(a) do { a = SMF(b[bs], cat16(pre[u & 3][0], pre[u & 3][1]), a, sidx); SB0(); \
                     if (u < 7) LDW(pre[u & 3], (DSLOT >> (u * 4 + 16)) & 15); u++; } while (0)
          SB0();
          if (i == VB) getp();
          if (i == 0 || i == 4) RING(a0);
          else a0 = SMF(b[bs], i < 4 ? wx[i - 1] : w[1][i - 5], a0, sidx);
          SB0();
          RING(a1);
          if (i < 4) b[bs] = LDM(i);
        }
      } else {
#pragma unroll
        for (int i = 0; i < 9; i++) {
          SB0();
          if (i == VB) getp();
          if (i < 3) a1 = SMF(b[(i + 4) % 5], wx[i], a1, sidx);
          else a1 = SMF(b[(i + 4) % 5], cat16(pre[(i + 1) & 3][0], pre[(i + 1) & 3][1]), a1, sidx);
          SB0();
          if (i < 4) b[(i + 4) % 5] = LDM(i);
          if (i == 3 || i == 4) LDW(pre[(i + 1) & 3], 1 + i);
        }
      }
      if (VB == 9) getp();
      if constexpr (NV == 2) { vm[0] = pk2(a0[0] + a0[2], a0[1] + a0[3]); SB0(); }
      vm[NV - 1] = pk2(a1[0] + a1[2], a1[1] + a1[3]);
      asm volatile("" : "+v"(vm));
      SB0();
      if (VB > 9) getp();
      {
        const u32x8 pp = ilv(m0, m1, p0, p1);
        const bf8 pq = cat8(p2, p3);
        f4 o = MFMA(vx[0], __builtin_bit_cast(bf8, __builtin_shufflevector(pp, pp, 4, 5, 6, 7)), ((f4){0, 0, 0, 0})); asm volatile("" : "+v"(o)); SB0();
        if constexpr (NV == 2) { o = MFMA(vx[1], pq, o); asm volatile("" : "+v"(o)); SB0(); }
        o = MFMA(__builtin_bit_cast(bf8, vm), __builtin_bit_cast(bf8, __builtin_shufflevector(pp, pp, 2, 3, 4, 5)), o);
        asm volatile("" : "+v"(o));
        asm volatile("" :: "v"(pp));
        if constexpr (NV == 2) asm volatile("" :: "v"(pq));
        SB0();
        __builtin_amdgcn_raw_buffer_store_b64((u32x2){umin(pk2(o[0], o[1]), 0xFFFFFFFEu), pk2(o[2], o[3])}, rsX, so, so_, 16);
        SB0();
      }
      STAMP(7);
      if (QW && !PVP) PRIO(2);
      int xo, lo, pa = 0;
      {
        IDX();
        if constexpr (NV == 1) {
          __builtin_amdgcn_s_sleep(LSL);
          if (NXV && vt >= NXH) {
            asm volatile("s_mov_b32 m0, %3\n\ts_nop 0\n\tbuffer_load_dwordx4 %0, %1, %2 offen sc1 lds\n\tbuffer_load_dwordx4 %0, %1, %2 offen offset:1024 sc1 lds\n\t"
                         "buffer_load_dwordx4 %0, %1, %2 offen offset:2048 sc1 lds\n\tbuffer_load_dwordx4 %0, %1, %2 offen offset:3072 sc1 lds"
                         :: "v"((lane * 16) ^ ((lane & (vt * 16) & 32) << 2)), "s"(rsB), "s"(nb + (1 + (vt >> 1)) * ACB + (vt & 1) * 4096), "s"(C_KX + vt * 4096) : "memory", "m0");
            SB0();
          }
#pragma unroll
          for (int i = 0; i < 2; i++) nx[i] = __builtin_amdgcn_raw_buffer_load_b128(rsB, lane * 16 + i * 1024, nb + vt * 2048, 16);
          pa = C_QX + vt * 2048 + lane * 16;
          asm volatile("" : "+v"(pa));
        }
        xo = OB_OFF((lq & 1) * 16 + l15, (lq >> 1) * 8);
        lo = lane * 4;
        asm volatile("" : "+v"(xo), "+v"(lo));
#pragma unroll
        for (int j = 0; j < 4; j++) LDW(pre[j], NV == 2 ? 2 + j : j);
      }
      SB0();
      BARN();
      STAMP(8);
      {
        f4 o[2][NV];
#pragma unroll
        for (int it = 0; it < 2; it++) {
          const bf8 pf = *(const bf8*)(smem + C_PX + it * 1024 + lo * 4);
          const u32 pm = *(const u32*)(smem + C_PX + 2048 + it * 256 + lo);
#pragma unroll
          for (int d = 0; d < NV; d++) {
            o[it][d] = MFMA(vx[d], pf, ((f4){0, 0, 0, 0}));
            o[it][d] = MFMA(__builtin_bit_cast(bf8, vm), __builtin_bit_cast(bf8, d ? (u32x4){0u, pm, 0u, 0u} : (u32x4){pm, 0u, 0u, 0u}), o[it][d]);
          }
        }
#pragma unroll
        for (int d = 0; d < NV; d++)
          __builtin_amdgcn_raw_buffer_store_b128(pair16(umin(pk2(o[0][d][0], o[0][d][1]), 0xFFFFFFFEu), pk2(o[0][d][2], o[0][d][3]),
                                                        umin(pk2(o[1][d][0], o[1][d][1]), 0xFFFFFFFEu), pk2(o[1][d][2], o[1][d][3])), rsB,
              xo, 3 * ACB + OB_OFF(s * 32, h * 128 + (vt + d) * 16), 16);
        SB0();
        if (s == 63) break;
        if constexpr (NV == 1) { *(u32x4*)(smem + pa) = nx[0]; *(u32x4*)(smem + pa + 1024) = nx[1]; }
      }
      STAMP(9);
      YEND();
    }
    b3 = b3 == 2 ? 0 : b3 + 1;
    STAMP(14);
  }
  };
  if (wid == 0) run(std::integral_constant<int, 0>{});
  else if (wid == 4) run(std::integral_constant<int, 1>{});
  else if (wid > 4) run(std::integral_constant<int, 3>{});
  else if (wid == 3) run(std::integral_constant<int, 2>{});
  else run(std::integral_constant<int, 4>{});
  PRIO(0);
  TLS(0);
  return xp;
}

__device__ __forceinline__ int chain_out(const Params& p, char* smem, const int c, const int wv, const bool spec) {
  STS(32 + c, 1);
  constexpr int R = 5; (void)R;
  const int wid = wv, tq = c & 1, cg = c >> 1;
  u32x8 w[4][5];
  const __amdgpu_buffer_rsrc_t rsX = RSRC(XWIN(p), X_END);
  int xp = 0, mo, wo, ro;
  const int ms = wid < 4 ? 65536 : 1024;
#define OST 0
  const int r1 = tq & OST, r2 = cg & (OST >> 1);
  {
    IDX();
    mo = wid < 4 ? (cg >> 3) * 32768 + (cg & 7) * 512 + tq * 256 + wid * 64 : 196608 + (cg >> 2) * 512 + tq * 256 + (cg & 3) * 64;
    if (wid > 4 || (wid == 4 && cg >= 8)) mo = PJ_OOB;
    mo ^= X_M;
    u32 m0[2], old = 0u;
    if (wid == 0) {
#pragma unroll
      for (int i = 0; i < 2; i++) m0[i] = *(const GLB u32*)(p.Bm + (tq * 4 + q4) * 576 + (i ? 512 + (cg & 7) * 8 : cg * 32 + lq * 8) + p4 * 2);
      const int cal = X_CAL + c * 128;
      __builtin_amdgcn_raw_buffer_store_b32(0u, rsX, lane < 8 ? lane * 4096 : PJ_OOB, cal, 16);
      const u32 b1 = BEAT_LD(c);
      asm volatile("" :: "v"(m0[0]), "v"(m0[1]));
      VMW0();
      if (spec) old = confirm(p, smem, 32 + c, b1, wv);
      TLS(8);
      u32 dv = ~0u;
      int side = 0;
      u32 t0 = 0u, dt = ~0u;
      int at = cal;
      const int vz = lane ? PJ_OOB : 0;
#pragma unroll 1
      for (int i = 1; i < 4 * CALR + 4; i++) {
        asm volatile("buffer_store_dword %0, %0, %1, %2 offen sc1" :: "v"(vz), "s"(rsX), "s"(at + side) : "memory");
        SB0();
        dv = lane == (i > 4 ? (i - 1) & 3 : 4) ? umin(dv, dt) : dv;
        at = cal + ((0x54105410200ull >> (i * 4)) & 15) * 4096;
        u32 lim = i == 3 ? dt : 0u;
        asm volatile("" : "+s"(at), "+s"(lim), "+v"(dv));
        SB0();
        u64 t1;
        asm volatile("s_waitcnt vmcnt(0)\n\ts_memtime %0\n\ts_waitcnt lgkmcnt(0)" : "=s"(t1) :: "memory");
        dt = (u32)t1 - t0; t0 = (u32)t1;
        side = dt < lim ? 8192 : side;
      }
      dv = lane == 3 ? umin(dv, dt) : dv;
      static_assert(CALR <= 2, "");
      u32 d[4];
#pragma unroll
      for (int k = 0; k < 4; k++) d[k] = __builtin_amdgcn_readlane(dv, k);
      const u32 dm = umin(umin(d[0], d[1]), umin(d[2], d[3]));
      if (lane == 0 && !((old >> c) & 1)) st4_sc1(WSP(u32, 0) + SY_VOTE(c), (umin((dm >> 4) + 1u, 63u) << 25) + (side << 11) + (umin(d[0] - dm, CCUT) >> 1) + (umin(d[1] - dm, CCUT) >> 1 << 6) + (umin(d[2] - dm, CCUT) >> 1 << 12) + (umin(d[3] - dm, CCUT) >> 1 << 18));
      TLS(11);
    }
    if (wid) BARN();
    SB0();
    const __amdgpu_buffer_rsrc_t rw = RSRC(p.BWo, 576 * 4096 * 2);
    u32x4 st[4][8];
    u32 fl;
    bool early = false;
    const int va = ((lane >> 2) * 576 + cg * 32 + p4 * 8) * 2;
#pragma unroll
    for (int i = 0; i < 4; i++) {
#pragma unroll
      for (int k = 0; k < 8; k++) st[i][k] = __builtin_amdgcn_raw_buffer_load_b128(rw, va, ((((i & 1) ^ r1) * 16 + wid * 2 + ((k >> 2) ^ r2)) * 128 + (i >> 1) * 64 + (k & 3) * 16) * 1152, OSTG_AUX);
      SB0();
    }
    if (wid == 0) {
      BARN();
      u32 fv;
      int it = 0;
      while (fv = ld4_sc1(WSP(u32, 0) + SY_VOTE(lane & 31)), xp = -((__builtin_amdgcn_readfirstlane(old) >> c) & 1), !(xp || __all(fv != 0u)) && ++it < SPIN_CAP) {
        BEAT_ON(it); __builtin_amdgcn_s_sleep(2);
      }
      const bool up = __builtin_amdgcn_readfirstlane(hmax(-(int)(fv >> 24))) & 1;
      if ((bool)((fv >> 24) & 1u) != up) fv = 0u;
      const int s02 = __builtin_amdgcn_readfirstlane(hsum((fv & 63u) + ((fv << 4) & 0x3F0000u))), s13 = __builtin_amdgcn_readfirstlane(hsum(((fv >> 6) & 63u) + ((fv >> 2) & 0x3F0000u)));
      const int bk = min(min((s02 & 0xFFFF) * 8, (s13 & 0xFFFF) * 8 + 1), min((s02 >> 16) * 8 + 4, (s13 >> 16) * 8 + 5));
      xp |= ((bk & 7) + (up ? 2 : 0)) * 4096;
      if (xp >= 0) {
#pragma unroll
        for (int i = 0; i < 3; i++) __builtin_amdgcn_raw_buffer_store_b32(i ? ~0u : umin(m0[0], 0xFFFFFFFEu), rsX, lane * 4, mo ^ xp ^ (i * 65536), 16);
        __builtin_amdgcn_raw_buffer_store_b32(lq ? ~0u : umin(m0[1], 0xFFFFFFFEu), rsX, lq < 3 && cg < 8 ? lq * 1024 + l15 * 4 : PJ_OOB,
                                              (196608 + (cg >> 2) * 512 + tq * 256 + (cg & 3) * 64) ^ X_M ^ xp, 16);
#pragma unroll
        for (int i = 0; i < 6; i++)
          __builtin_amdgcn_raw_buffer_store_b128(ones4(), rsX, (lane >> 5) * 131072 + (lane & 31) * 16, X_O(i >> 1) ^ ((c >> 3) * 32768 + (c & 7) * 512) ^ xp ^ ((i & 1) * X_HS), 16);
        if (c == 0) {
#pragma unroll
          for (int i = 0; i < 6; i++) __builtin_amdgcn_raw_buffer_store_b128((u32x4){0u, 0u, 0u, 0u}, rsX, lane * 16 + (i & 3) * 1024, X_HOT(xp) ^ (i < 4 ? X_CS : 0), 16);
        }
        __builtin_amdgcn_raw_buffer_store_b32(lane & 2 ? DO0 * 64u : ~0u, rsX, lane < 4 ? ANX(lane & 1) + ((lane & 2) * 16 + c) * ANS : PJ_OOB, 0, 16);
        VMW0();
        if (lane == 0) st4_sc1(WSP(u32, 0) + SY_INIT(c), (u32)xp + 1u);
        TLS(10);
      }
    }
    SB0();
    fl = ld4_sc1(WSP(u32, 0) + SY_WX);
    SB0();
    auto wx = [&](int vo) {
#pragma unroll
      for (int k = 0; k < 8; k++)
        st[0][k] = __builtin_amdgcn_raw_buffer_load_b128(RSRC(WSP(char, O_WX), 524288), vo, cg * 65536 + ((((k & 2) ^ (r1 * 2)) * 8 + wid * 2 + (k & 1)) * 128 + (k >> 2) * 64) * 16, 16);
    };
    auto poll = [&]() { fl = __builtin_amdgcn_raw_buffer_load_b32(RSRC(p.ws, wid ? 0 : O_QA), (lane < 32 ? SY_INIT(lane) : lane < 48 ? SY_ARR : SY_WX) * 4, 0, 16); };
    char* sc = smem + wid * 10240;
    auto frag = [&](const char* b, int rs) {
      bf4 tt[4];
#pragma unroll
      for (int kk = 0; kk < 4; kk++) {
        const int r0 = ((kk >> 1) * 2 + (lq >> 1)) * 32 + (p4 >> 1) * 16 + (q4 >> 1) * 8 + (lq & 1) * 4 + (kk & 1) * 2 + (q4 & 1);
        tt[kk] = trrd(b + r0 * rs + (p4 & 1) * 8);
      }
      return ilv(tt[0], tt[1], tt[2], tt[3]);
    };
#pragma unroll
    for (int i = 0; i < 4; i++) {
#pragma unroll
      for (int k = 0; k < 8; k++) *(u32x4*)(sc + ((k ^ (r2 * 4)) * 16 + (lane >> 2)) * 80 + p4 * 16) = st[i][k];
      __builtin_amdgcn_fence(__ATOMIC_RELEASE, "wavefront");
      __builtin_amdgcn_wave_barrier();
#pragma unroll
      for (int t = 0; t < 4; t++) w[i][t] = frag(sc + t * 16, 80);
      asm volatile("s_waitcnt lgkmcnt(0)" ::: "memory");
      __builtin_amdgcn_wave_barrier();
      if (i == OEP) { early = __all(fl == ~0u); CFENCE(); poll(); wx(early ? lane * 16 : PJ_OOB); }
      SB0();
    }
    if (wid == 0) {
      int it = 0;
      while (xp >= 0 && !__all(fl >= (lane < 32 ? 1u : lane < 48 ? 256u : ~0u)) && ++it < SPIN_CAP) { BEAT_ON(it); __builtin_amdgcn_s_sleep(GPS); CFENCE(); poll(); }
      *(int*)(smem + C_XP) = xp;
    }
    asm volatile("s_waitcnt lgkmcnt(0)" ::: "memory");
    BARN();
    xp = __builtin_amdgcn_readfirstlane(*(const int*)(smem + C_XP));
    if (xp < 0) return xp;
    {
      if (!early) wx(lane * 16);
#pragma unroll
      for (int k = 0; k < 8; k++) *(u32x4*)(sc + k * 1024 + lane * 16) = st[0][k];
      __builtin_amdgcn_fence(__ATOMIC_RELEASE, "wavefront");
      __builtin_amdgcn_wave_barrier();
#pragma unroll
      for (int i = 0; i < 4; i++) w[i][4] = frag(sc + i * 2048, 16);
    }
    mo = __builtin_amdgcn_readfirstlane(mo ^ xp);
    const int row = (lq & 1) * 4 + ((l15 >> 1) & 3);
    wo = ((lq >> 1) == hi8 ? row * 128 : 1024) + (((wid * 2 + hi8) ^ (row * 2)) << 3) + (l15 & 1) * 4;
    ro = wid > 4 ? 0 : wid * 2560 + (lane >> 5) * 1280 + ((lane >> 2) & 7) * 128 + (lane & 8) * 8 + p4 * 16;
  }
  __syncthreads();
  STS(32 + c, 14);
  TLS(9);
  int go, sidx, po;
  { IDX(); go = lane * 16; sidx = (l15 & 2) ? 0xEEEE : 0x4444;
    po = p4 == 0 ? (lq & 1) * 32 + (lq >> 1) * 16 + q4 * 4 : PJ_OOB; }
  int ro1 = ro ^ 64;
  asm volatile("" : "+v"(ro), "+v"(ro1), "+v"(wo));
#define PUTK(t, r, e, nop) asm volatile(nop PKADD(r, r, (r + 2)) "ds_write2st64_b32 %1, v[" #r "], v[" #r "+1] offset0:%2 offset1:%3" \
                                  : "+{v[" #r ":" #e "]}"(d[t]) : "v"(wo), "i"((t) * 10), "i"((t) * 10 + 5) : "memory")
#define PUTP(t) do { if (PKA & 8) { if (t == 0) PUTK(0, 236, 239, "s_nop 5\n\t"); if (t == 1) PUTK(1, 240, 243, ""); if (t == 2) PUTK(2, 244, 247, ""); \
                                    if (t == 3) PUTK(3, 248, 251, "s_nop 2\n\t"); if (t == 4) PUTK(4, 252, 255, "s_nop 0\n\t"); } \
    else asm volatile("ds_write2st64_b32 %0, %1, %2 offset0:%3 offset1:%4" :: "v"(wo), "v"(d[t][0] + d[t][2]), "v"(d[t][1] + d[t][3]), "i"((t) * 10), "i"((t) * 10 + 5) : "memory"); } while (0)
#define APS 18
#define ONOP 7
  int b3 = 0, dur = DO0 * 64;
#pragma unroll 1
  for (int s = 0; s < 63; s++) {
    f4 d[5];
    int gb[4], pb = mo ^ ((b3 == 2 ? 0 : b3 + 1) * ms);
#pragma unroll
    for (int i = 0; i < 4; i++) { gb[i] = X_O(b3) ^ xp ^ (tq * 131072 + (((i & 1) ^ r1) * 2 + (wid >> 2)) * 32768 + (wid & 3) * 1024 + (i < 2 ? X_HS : 0)); asm volatile("" : "+s"(gb[i])); }
    asm volatile("" : "+s"(pb));
    u32x4 g[4];
    u32 bad;
    auto mult = [&]() __attribute__((always_inline)) {
      CFENCE();
#pragma unroll
      for (int i = 0; i < 4; i++) {
        g[i] = __builtin_amdgcn_raw_buffer_load_b128(rsX, go, gb[i], 16);
        SB0();
        if (ONOP >= 0 && i < 3) asm volatile("s_nop %0" :: "i"(ONOP >= 0 ? ONOP : 0));
        SB0();
      }
      bad = 0u;
#pragma unroll
      for (int i = 0; i < 4; i++) {
        SB0();
#pragma unroll
        for (int t = 0; t < 5; t++) {
          d[t] = SMF(g[i], w[i][t], d[t], sidx);
          if (t == 0) bad = umax(umax(bad, g[i][0]), g[i][2]);
          if (i == 3 && t >= 2) { SB0(); PUTP(t - 2); SB0(); }
        }
      }
      SB0();
#pragma unroll
      for (int t = 3; t < 5; t++) PUTP(t);
      SB0();
    };
#pragma unroll
    for (int t = 0; t < 5; t++) d[t] = zero4();
    SB0();
    u32 dl = 0;
    ALIGN(OAL);
    if (wid == 7) { __builtin_amdgcn_s_sleep(APS); STAMP(4); AD_GET(false, K1 + KXT(KA), SPIN_CAP); STAMP(5); AD_SPIN(); }
    STAMP(15);
    BARN();
    STAMP(0);
    mult();
    if (__builtin_expect(__any(bad == ~0u), 0)) {
      int it = 0;
      do {
#pragma unroll
        for (int t = 0; t < 5; t++) d[t] = zero4();
        mult();
      } while (__any(bad == ~0u) && ++it < SPIN_CAP);
    }
    STAMP(1);
    BARC();
    STAMP(2);
#define OALL 1
    f4 t0, t1;
    if (OALL) { t0 = *(const f4*)(smem + ro); t1 = *(const f4*)(smem + ro1); SB0(); }
    if (__builtin_expect(wid < 5, 1)) {
      if (!OALL) { t0 = *(const f4*)(smem + ro); t1 = *(const f4*)(smem + ro1); }
      if (PKA & 16) {
#define QP2(q) "v_add_f32_dpp v228, v228, v228 quad_perm:" q " row_mask:0xf bank_mask:0xf\n\tv_add_f32_dpp v229, v229, v229 quad_perm:" q " row_mask:0xf bank_mask:0xf\n\t"
        asm volatile(PKADD(228, 228, 230) PKADD(232, 232, 234) PKADD(228, 228, 232) "s_nop 1\n\t"
                     QP2("[1,0,3,2]") "s_nop 0\n\t" QP2("[2,3,0,1]") CV2("v228", 228) "v_min_u32 v228, -2, v228\n\tbuffer_store_dword v228, %2, %3, %4 offen sc1"
                     : "+{v[228:231]}"(t0), "+{v[232:235]}"(t1) : "v"(po), "s"(rsX), "s"(pb) : "memory");
      } else {
      float a = (t0[0] + t0[2]) + (t1[0] + t1[2]), b = (t0[1] + t0[3]) + (t1[1] + t1[3]);
      a += __uint_as_float(__builtin_amdgcn_mov_dpp(__float_as_uint(a), 0xB1, 0xf, 0xf, true));
      b += __uint_as_float(__builtin_amdgcn_mov_dpp(__float_as_uint(b), 0xB1, 0xf, 0xf, true));
      a += __uint_as_float(__builtin_amdgcn_mov_dpp(__float_as_uint(a), 0x4E, 0xf, 0xf, true));
      b += __uint_as_float(__builtin_amdgcn_mov_dpp(__float_as_uint(b), 0x4E, 0xf, 0xf, true));
      __builtin_amdgcn_raw_buffer_store_b32(umin(pk2(a, b), 0xFFFFFFFEu), rsX, po, pb, 16);
      }
      SB0();
      STAMP(3);
      __builtin_amdgcn_s_sleep(4);
      __builtin_amdgcn_raw_buffer_store_b32(~0u, rsX, po, mo ^ (b3 * ms), 16);
    } else if (wid == 7) {
      dur += (min((int)(((u32)wall_clock64() << 6) - dl), dur + DOCAP * 64) - dur) >> DSH;
      if (DRL) __builtin_amdgcn_s_sleep(DRL);
      __builtin_amdgcn_raw_buffer_store_b32((u32)dur, rsX, 0, ANX(~s & 1) + (32 + c) * ANS, 16);
      if (CSL && c == CSC && s > 0) { if (CSL > DRL) __builtin_amdgcn_s_sleep(CSL > DRL ? CSL - DRL : 0); __builtin_amdgcn_raw_buffer_store_b32(dl | 1u, rsX, 0, (X_HOT(xp) ^ X_CS) + (s - 1) * 64, 16); }
    }
    VMW0C();
    YEND();
    b3 = b3 == 2 ? 0 : b3 + 1;
  }
  __syncthreads();
  return xp;
}

__device__ __forceinline__ void wait_ge(const u32* w, u32 val, const int wv) {
  if (TIDX == 0) {
    int it = 0;
    while (ld4_sc1(w) < val && ++it < SPIN_CAP) __builtin_amdgcn_s_sleep(WPS);
  }
}
__device__ __forceinline__ void acquire_all(const int wv) {
  if (TIDX == 0) {
    __builtin_amdgcn_fence(__ATOMIC_ACQUIRE, "agent");
    VMW0();
  }
  __syncthreads();
}

#define T_KV 0
#define T_Q 0
#define T_A 192
#define T_W (T_A + 64)
#define T_S (T_W + 256)
#define T_L (T_S + 72)
#define T_O T_L
#define T_CHAIN (1 << 20)
#define CODE_KB 84
#define OWARM 2
#define BXW 1
#define AORD 0x01234567u
#define OORD 0x67014523u

__device__ __forceinline__ int next_task(const Params& p, int* s_task, const int wv, const int pt, const int pre) {
  __syncthreads();
  if (wv == 0) {
    int t = T_A + pre;
    if (__builtin_amdgcn_readfirstlane(pre) < 0) {
      t = -1;
      int xp = 0;
      for (int it = 0; it < SPIN_CAP; it++) {
        const int l = TIDX & 15;
        const u32 v = ld4_sc1(WSP(u32, 0) + (l < 8 ? SY_JOB(l) : l == 8 ? SY_ARR : SY_INIT(0)));
        const u32 open = (u32)__ballot(v != ~0u) & 255u;
        xp = __builtin_amdgcn_readlane(v, 9);
        if (!open && xp) break;
        if (!open || __builtin_amdgcn_readlane(v, 8) < 256u) { BEAT_ON(it); __builtin_amdgcn_s_sleep(8); continue; }
        const int x = __builtin_ctz(open), k = __builtin_ctz(~(u32)__builtin_amdgcn_readlane(v, x));
        u32 old = 0u;
        if (TIDX == 0) old = AOR(SY_JOB(x), 1u << k);
        if (!((__builtin_amdgcn_readfirstlane(old) >> k) & 1)) { t = x * 32 + k + (x < 2 ? T_CHAIN : -64); break; }
      }
      if (t < 0 && TIDX == 0) t = T_A + (int)__hip_atomic_fetch_add(HOTP(xp) + SY_QHEAD, 1u, __ATOMIC_RELAXED, __HIP_MEMORY_SCOPE_AGENT) + (xp & 0x7000);
    }
    if (TIDX == 0) *s_task = t;
  }
  __syncthreads();
  return __builtin_amdgcn_readfirstlane(*s_task);
}

__device__ __forceinline__ int take_task(int* s_task, const int pre, const int wv, const int pb, const int pl, const int pt) {
  if (TIDX == 0) {
    *s_task = pre;
  }
  __syncthreads();
  const int t = __builtin_amdgcn_readfirstlane(*s_task);
  __syncthreads();
  return t;
}
#if defined(__HIP_DEVICE_COMPILE__) && !defined(KEEP_PK)
#define NOPK __attribute__((target("no-packed-fp32-ops")))
#else
#define NOPK
#endif
__device__ __forceinline__ void mega_body(const Params& p0, char* smem) {
  int* const s_task = (int*)(smem + LDS_TOTAL);
  int wv = __builtin_amdgcn_readfirstlane(threadIdx.x >> 6), t, xs;
#define tid TIDX
  {
    const Params& p = p0;
    u64 pc0; asm volatile("s_getpc_b64 %0" : "=s"(pc0));
#define ZB 1
    const int xcc = (int)(__builtin_amdgcn_s_getreg(20 | (3 << 11)) & 7u), xq = xcc >= 2 ? xcc - 2 : xcc, idx = (blockIdx.x >> 3) & 31;
    t = xq * 32 + (xcc < 2 ? T_CHAIN + idx : (idx + 31) & 31);
    xs = xcc + 8;
#define WARM(ptr, bytes, vo, so, sc) asm volatile("s_mov_b32 m0, %3\n\ts_nop 0\n\tbuffer_load_dword %0, %1, %2 offen" sc " lds" \
                                                   :: "v"(vo), "s"(RSRC(ptr, bytes)), "s"(so), "s"(C_B0 + wv * 256) : "memory", "m0")
    if (wv == 0) WARM(p.ws, O_QA, 0, SY_BEAT(t & 7) * 4, " sc1");
    if (blockIdx.x == ZB) {
#pragma unroll
      for (int i = 0; i < 4; i++) __builtin_amdgcn_raw_buffer_store_b128((u32x4){0u, 0u, 0u, 0u}, RSRC(p.ws, SY_WORDS * 4), tid * 16 + i * 8192, 0, 16);
      VMW0();
      BARN();
      if (tid < 8) AADD(SY_BEAT(tid), 1u);
    }
    if (wv) {
      const int g = idx * 7 + wv - 1, vo = (tid & 63) * 64;
      if (g < CODE_KB / 4) WARM(pc0, CODE_KB * 1024, vo, g * 4096, "");
      if (xcc > 1) {
        if (OWARM == 1 && g >= 32) WARM(p.BWo, WOB, vo, (xq * 192 + g - 32) * 4096, "");
        if (OWARM == 2 && g >= 128) WARM(p.BWo, WOB, vo * 2, (xq * 96 + g - 128) * 8192, "");
        if (BXW) WARM(p.Bx, 256 * 1152, vo, (223 - g) * 4096, "");
      }
    }
  }
  u64 pp = (u64)__builtin_amdgcn_kernarg_segment_ptr();
  SB0();
  asm volatile(".Lmega_again:" : "+{s[40:41]}"(pp), "+{s42}"(wv), "+{s43}"(t), "+{s44}"(xs) :: "memory");
  SB0();
  const Params& p = *(const Params*)(const __attribute__((address_space(4))) Params*)pp;
  const int xcc = xs & 7, xq = xcc >= 2 ? xcc - 2 : xcc;
  const bool spec = xs > 7;
  int pre = -1;
  if (t < T_A) {
    const int x = t >> 5, j = (t & 7) * 4 + ((0x9C >> ((t >> 2) & 6)) & 3);
    proj_slot(p, x, j, smem, wv, pre, spec ? t + 64 : -1);
    VMW0C();
  } else {
    int own;
    if (t < T_CHAIN + 32) { own = chain_head(p, smem, t - T_CHAIN, wv, spec); VMW0C(); }
    else { own = chain_out(p, smem, t - T_CHAIN - 32, wv, spec); VMW0C(); asm volatile("s_nop 0"); }
    if (own >= 0) {
      const int l = tid & 7;
      const u32 v = ld4_sc1(HOTP(own) + (l < 7 ? SY_OQ(l) : SY_QHEAD));
      if (tid == 0) *s_task = 0;
      __syncthreads();
      if (v < (l == 7 ? T_S - T_A : l == 6 ? 72 : (l & 1) ? NOB : NOA)) *s_task = 1;
      __syncthreads();
      if (__builtin_amdgcn_readfirstlane(*s_task) == 0) {
        if (xcc == 0) TLS(3); else TLS(4);
        return;
      }
    }
  }
  t = next_task(p, s_task, wv, t, pre);
  if (t < T_A || t >= T_CHAIN) {
    asm volatile("s_waitcnt vmcnt(0) lgkmcnt(0)\n\ts_barrier\n\ts_branch .Lmega_again" :: "{s[40:41]}"(pp), "{s42}"(wv), "{s43}"(t), "{s44}"(xcc) : "memory");
    return;
  }
  u32* const hot = HOTP(t);
  u32* const qh = hot + SY_QHEAD;
  t &= ~0x7000;
#define APL 8
#define WPOS 0
  while (t < T_S) {
    if ((u32)(t - T_A - WPOS) < 64u) {
      wof_task(p, t - T_A - WPOS, smem, wv, qh, pre);
      if (tid == 0) __hip_atomic_fetch_add(hot + SY_CW, 1u, __ATOMIC_RELAXED, __HIP_MEMORY_SCOPE_AGENT);
      t = T_A + take_task(s_task, pre, wv, T_A, T_S - T_A, t);
      continue;
    }
    const int qb = (AORD >> (((t - (t < T_A + WPOS ? T_A : T_W)) >> 5) * 4)) & 7, h = t & 31;
    if (tid < 64) {
      int it = 0;
      while (true) {
        const u32 f0 = ld4_sc1(WSP(u32, 0) + SY_BLK(3, h >> 1, tid)), f1 = ld4_sc1(WSP(u32, 0) + SY_BLK(4, h >> 1, tid)), f2 = ld4_sc1(WSP(u32, 0) + SY_BLK(5, h >> 1, tid));
        if (__all(tid >= qb * 8 + 8 || ((tid < qb * 8 || f0 >= FULL) && f1 >= FULL && f2 >= FULL)) || ++it >= SPIN_CAP) break;
        __builtin_amdgcn_s_sleep(APL);
      }
    }
    acquire_all(wv);
    fa::attn_block(WSP(char, O_QA) + FR_OFF(qb * 256, h * 128), WSP(char, O_KA) + h * 8192, WSP(char, O_VA) + h * 8192,
                   WSP(char, O_CA) + OB_OFF(qb * 256, h * 128), qb * 256, smem, wv, qh, SY_OQ(xq) - SY_QHEAD, pre);
    if (tid == 0) __hip_atomic_fetch_add(hot + SY_CC(qb), 1u, __ATOMIC_RELAXED, __HIP_MEMORY_SCOPE_AGENT);
    t = T_A + take_task(s_task, pre, wv, T_A, T_S - T_A, t);
  }
  int rest = 64 | (1 << xq);
#pragma unroll 1
  while (rest) {
    const int x = __builtin_ctz(rest);
    rest &= rest - 1;
    if (x == 6) {
      CLAIMS();
      t = take_task(s_task, pre, wv, T_S, 72, -1);
      while ((t & 0xFFFF) < 72) {
        t &= 0xFFFF;
        small_out_task(p, t & 3, t >> 2, smem, wv, hot, pre);
        t = take_task(s_task, pre, wv, T_S, 72, T_S + t);
      }
      rest = t >> 16;
      continue;
    }
    const int part = x & 1, cg = x >> 1, n = part ? NOB : NOA;
    u32* const qh = hot + SY_OQ(x);
    if (t >= 0x10000 && rest == 64) {
      t -= T_A + 0x10000;
    } else {
      CLAIM();
      t = take_task(s_task, pre, wv, T_O + x * 32, n, -1);
    }
    while (t < n) {
      const int nb = part ? NBB : NAB, big = t < nb, e = big ? t * 2 : t + nb;
      const int rb = part ? e : ((OORD >> ((e >> 2) * 4)) & 7) * 4 + (e & 3);
      if (tid < 2) {
        const u32* w = tid ? hot + SY_CW : part ? CSP((rb + big) * 2 + 1) : hot + SY_CC(rb >> 2);
        int it = 0;
        while (__any(ld4_sc1(w) < (tid ? 64u : part ? 1u : 32u)) && ++it < SPIN_CAP) __builtin_amdgcn_s_sleep(WPS);
      }
      acquire_all(wv);
      const char *pa = WSP(char, part ? O_OB : O_CA) + rb * 2 * 262144, *pw = WSP(char, O_WOF) + part * WOB + cg * 12 * 131072;
      u16* const pc = p.out + (size_t)rb * 64 * 1152 + part * 576 + cg * 192;
      if (big) out_tile<8>(pa, pw, pc, smem, wv, hot, qh, pre);
      else out_tile<4>(pa, pw, pc, smem, wv, hot, qh, pre);
      t = take_task(s_task, pre, wv, T_O + x * 32, n, T_O + x * 32 + t);
    }
    if (t == 0x20000) rest = 0;
  }
  if (xcc == 0) TLS(3); else if (xcc == 1) TLS(4); else TLS(5);
}

#undef tid
__global__ NOPK void __launch_bounds__(512, 2) mega(Params p) {
  __shared__ __attribute__((aligned(16))) char smem[LDS_ALL];
  mega_body(p, smem);
}
extern "C" void kernel_launch(void* const* d_in, const int* in_sizes, int n_in, void* d_out, int out_size, void* d_ws,
                              size_t ws_size, hipStream_t stream) {
  Params p;
  p.Ax = (const u16*)d_in[0]; p.Ac = (const u16*)d_in[1]; p.Awq = (const u16*)d_in[2]; p.Awk = (const u16*)d_in[3];
  p.Awv = (const u16*)d_in[4]; p.Awo = (const u16*)d_in[5]; p.Bx = (const u16*)d_in[6]; p.Bm = (const u16*)d_in[7];
  p.BWq = (const u16*)d_in[8]; p.BWk = (const u16*)d_in[9]; p.BWv = (const u16*)d_in[10]; p.BWo = (const u16*)d_in[11];
  p.out = (u16*)d_out;
  p.ws = (char*)d_ws;
  mega<<<256, 512, 0, stream>>>(p);
}
```

```cpp
#include <hip/hip_runtime.h>
#include <hip/hip_bf16.h>
#include <stdint.h>
#include <type_traits>

typedef unsigned short u16;
typedef unsigned int u32;
typedef unsigned long long u64;
typedef __attribute__((ext_vector_type(8))) short bf8;
typedef __attribute__((ext_vector_type(4))) short bf4;
typedef __attribute__((ext_vector_type(4))) float f4;
typedef __attribute__((ext_vector_type(2))) float f2v;
typedef __attribute__((ext_vector_type(2))) __bf16 bf2v;
typedef __attribute__((ext_vector_type(4))) unsigned u32x4;
typedef __attribute__((ext_vector_type(2))) unsigned u32x2;
typedef float f32x16 __attribute__((ext_vector_type(16)));
typedef __attribute__((ext_vector_type(8))) unsigned u32x8;
typedef __attribute__((ext_vector_type(8))) __bf16 bh8;
typedef __attribute__((ext_vector_type(16))) __bf16 bh16;
#define LDSP __attribute__((address_space(3)))

__device__ __forceinline__ u32 pk2(float a, float b) {
  f2v x = {a, b};
  bf2v y = __builtin_convertvector(x, bf2v);
  return __builtin_bit_cast(u32, y);
}
__device__ __forceinline__ u32 umin(u32 a, u32 b) { return a < b ? a : b; }
__device__ __forceinline__ u32 umax(u32 a, u32 b) { return a > b ? a : b; }
__device__ __forceinline__ float rbf(float f) { return __uint_as_float(pk2(f, f) << 16); }
#define MFMA(a, b, c) __builtin_amdgcn_mfma_f32_16x16x32_bf16(a, b, c, 0, 0, 0)
#define MFMA16(a, b, c) __builtin_amdgcn_mfma_f32_16x16x16bf16_1k(a, b, c, 0, 0, 0)
#define SMF(a, b, c, idx) __builtin_amdgcn_smfmac_f32_16x16x64_bf16(__builtin_bit_cast(bh8, a), __builtin_bit_cast(bh16, b), c, idx, 0, 0)
__device__ __forceinline__ u32x8 ilv(bf4 a, bf4 b, bf4 c, bf4 d) {
  const u32x2 x = __builtin_bit_cast(u32x2, a), y = __builtin_bit_cast(u32x2, b), z = __builtin_bit_cast(u32x2, c), w = __builtin_bit_cast(u32x2, d);
  return (u32x8){x[0], x[1], y[0], y[1], z[0], z[1], w[0], w[1]};
}
__device__ __forceinline__ u32x8 cat16(bf8 a, bf8 b) {
  const u32x4 x = __builtin_bit_cast(u32x4, a), y = __builtin_bit_cast(u32x4, b);
  return (u32x8){x[0], x[1], x[2], x[3], y[0], y[1], y[2], y[3]};
}
__device__ __forceinline__ float swadd(float a, float b) {
  auto x = __builtin_amdgcn_permlane32_swap(__float_as_uint(a), __float_as_uint(b), false, false);
  return __uint_as_float(x[0]) + __uint_as_float(x[1]);
}
__device__ __forceinline__ bf4 trrd(const char* p) {
  return __builtin_amdgcn_ds_read_tr16_b64_v4i16((LDSP bf4*)(p));
}
__device__ __forceinline__ bf8 cat8(bf4 a, bf4 b) { return __builtin_shufflevector(a, b, 0, 1, 2, 3, 4, 5, 6, 7); }
__device__ __forceinline__ void st8_sc1(void* p, u64 v) { __hip_atomic_store((u64*)p, v, __ATOMIC_RELAXED, __HIP_MEMORY_SCOPE_AGENT); }
__device__ __forceinline__ u32 ld4_sc1(const u32* p) { return __hip_atomic_load(p, __ATOMIC_RELAXED, __HIP_MEMORY_SCOPE_AGENT); }
__device__ __forceinline__ void st4_sc1(u32* p, u32 v) { __hip_atomic_store(p, v, __ATOMIC_RELAXED, __HIP_MEMORY_SCOPE_AGENT); }
#define VMW0() asm volatile("s_waitcnt vmcnt(0)" ::: "memory")
#define CFENCE() asm volatile("" ::: "memory")
#define VMW0C() do { __builtin_amdgcn_s_waitcnt(0x0F70); asm volatile("" ::: "memory"); } while (0)
#define SPIN_CAP (1 << 22)
#define XPS 4
#define SPS 4
#define WPS 8
__device__ __forceinline__ int tid_now(int wv) {
  int l; asm volatile("v_mbcnt_lo_u32_b32 %0, -1, 0\n\tv_mbcnt_hi_u32_b32 %0, -1, %0" : "=v"(l));
  return wv * 64 + l;
}
#define TIDX tid_now(wv)
#define CLAIM() do { if (TIDX == 0) pre = (int)__hip_atomic_fetch_add(qh, 1u, __ATOMIC_RELAXED, __HIP_MEMORY_SCOPE_AGENT); } while (0)

#define TLS(i)
struct Params {
  const u16 *Ax, *Ac, *Awq, *Awk, *Awv, *Awo, *Bx, *Bm, *BWq, *BWk, *BWv, *BWo;
  u16* out;
  char* ws;
};
#define SY_QHEAD 0
#define SY_CC(rb) (64 + (rb) * 16)
#define SY_CW (64 + 8 * 16)
#define SY_OQ(x) (64 + 9 * 16 + (x) * 16)
#define SY_B0 (64 + 80 * 16)
#define SY_INIT(c) (SY_B0 + 64 + (c))
#define SY_VOTE(i) (SY_B0 + 128 + (i))
#define SY_JOB(x) (SY_B0 + 256 + (x) * 16)
#define SY_ARR (SY_B0 + 384)
#define SY_WX (SY_ARR + 32)
#define SY_BLK(mi, cb, g) (SY_ARR + 64 + ((mi) * 16 + (cb)) * 64 + (g))
#define FULL 12u
#define SY_WORDS (SY_ARR + 64 + 6 * 16 * 64)
#define SY_BEAT(r) (SY_WORDS + (r) * 32)

#define ACB (2048 * 4096 * 2)
#define O_PAD 0
#define O_QA (SY_BEAT(8) * 4 + O_PAD)
#define O_KA (O_QA + ACB)
#define O_VA (O_KA + ACB)
#define O_CA (O_VA + ACB)
#define O_QB (O_CA + ACB)
#define O_KB (O_QB + ACB)
#define O_VB (O_KB + ACB)
#define O_OB (O_VB + ACB)
#define O_WOF (O_OB + ACB)
#define WOB (576 * 4096 * 2)
#define O_WX (O_WOF + 2 * WOB)
#define O_XW (O_WX + 524288)
#define X_HS 0x4000
#define X_O(b) ((b) * 0x80000 + ((0x140 >> ((b) * 4)) & 15) * 4096)
#define X_MS X_HS
#define X_M (0x185000 ^ X_MS)
#define X_ANN (0x185000 + 7 * 32768)
#define X_CAL 0x40000
#define HFL 0x2000
#define CFL 0x1000
#define X_HOT(xp) ((0xC4000 ^ HFL) ^ ((xp) & 0x7000))
#define X_CS (HFL ^ CFL ^ 0x8000)
#define CSP(s) ((const u32*)(const GLB u32*)((uintptr_t)hot ^ X_CS) + (s) * 16)
#define X_END 0x200000
#define OB_OFF(r, c) (((r) >> 5) * 262144 + ((c) >> 4) * 1024 + ((r) & 31) * 32 + ((c) & 15) * 2)
#define FR_OFF(r, c) (((r) >> 5) * 262144 + ((c) >> 3) * 512 + ((r) & 31) * 16 + ((c) & 7) * 2)
#define GLB __attribute__((address_space(1)))
#define WSP(T, off) ((T*)((GLB char*)p.ws + (off)))
#define XWIN(p) (((uintptr_t)(p).ws + O_XW + 0x1FFFFFu) & ~(uintptr_t)0x1FFFFFu)

#define HOTP(xp) ((u32*)(GLB u32*)(XWIN(p) + X_HOT(xp)))

#define XS0 48
#define NAB 8
#define NBB 10
#define NOA (32 - NAB)
#define NOB (XS0 / 2 - NBB)
#define PEEK 0
#define OXIT 1
#define LDS_TOTAL (131072 + 8192 + 16384)
#define LDS_ALL 163840

#define SB0() __builtin_amdgcn_sched_barrier(0)
#define PJ_OOB 0x7F000000
#define RSRC(ptr, bytes) __builtin_amdgcn_make_buffer_rsrc((void*)(ptr), 0, (bytes), 0x00020000)
__device__ __forceinline__ bf8 ldsc1(__amdgpu_buffer_rsrc_t r, int off) {
  return __builtin_bit_cast(bf8, __builtin_amdgcn_raw_buffer_load_b128(r, off, 0, 16));
}
__device__ __forceinline__ u32x4 pair16(u32 a0, u32 a1, u32 b0, u32 b1) {
  auto r0 = __builtin_amdgcn_permlane16_swap(a0, b0, false, false);
  auto r1 = __builtin_amdgcn_permlane16_swap(a1, b1, false, false);
  return (u32x4){r0[0], r1[0], r0[1], r1[1]};
}
__device__ __forceinline__ int fsw(int k) { return (k & 3) | (((k >> 3) & 1) << 2); }

#define AADD(w, v) __hip_atomic_fetch_add(WSP(u32, 0) + (w), v, __ATOMIC_RELAXED, __HIP_MEMORY_SCOPE_AGENT)
#define AOR(w, v) __hip_atomic_fetch_or(WSP(u32, 0) + (w), v, __ATOMIC_RELAXED, __HIP_MEMORY_SCOPE_AGENT)
#define C_B0 (LDS_ALL - 2048)
#define BEAT_LD(job) ld4_sc1(WSP(u32, 0) + SY_BEAT((job) & 7))
#define BEAT_ON(it) do { if (((it) & 31) == 31 && (TIDX & 63) < 8) AADD(SY_BEAT(TIDX & 63), 1u); } while (0)
__device__ __forceinline__ u32 confirm(const Params& p, const char* smem, const int job, u32 b1, const int wv) {
  asm volatile("" :: "v"(b1) : "memory");
  const u32 b0 = *(const u32*)(smem + C_B0);
  if (__builtin_expect(__any(b1 == b0), 0)) {
    int it = 0;
    while (__any(BEAT_LD(job) == b0) && ++it < SPIN_CAP) __builtin_amdgcn_s_sleep(2);
  }
  u32 old = 0u;
  if (TIDX == 0) old = AOR(SY_JOB(job >> 5) + TIDX, 1u << (job & 31));
  if (TIDX < 2) AADD(TIDX ? SY_BEAT(job & 7) : SY_ARR, 1u);
  return old;
}

typedef __attribute__((ext_vector_type(2))) unsigned long long u64x2;
__device__ __forceinline__ f4 zero4() {
  u64 a, b; asm volatile("v_mov_b64 %0, 0\n\tv_mov_b64 %1, 0\n\ts_nop 1" : "=v"(a), "=v"(b));
  return __builtin_bit_cast(f4, (u64x2){a, b});
}
__device__ __forceinline__ u32x4 ones4() {
  u64 a, b; asm volatile("v_mov_b64 %0, -1\n\tv_mov_b64 %1, -1" : "=v"(a), "=v"(b));
  return __builtin_bit_cast(u32x4, (u64x2){a, b});
}
#define C_PARK 131072
#define PJ_PAD 0
__device__ __forceinline__ void proj_slot(const Params& p, int x, int j, char* smem, const int wv, int& pre, int job) {
  const int tid = TIDX, lane = tid & 63, wid = wv;
  const int l15 = lane & 15, lq = lane >> 4, q4 = l15 >> 2, p4 = lane & 3, wr = wid >> 2, wc = wid & 3;
  const __amdgpu_buffer_rsrc_t rc = RSRC(p.ws, O_XW);
  const int vB = ((wid * 4 + lq) * 4096 + (((l15 >> 1) ^ (lq | (((wid >> 1) & 1) << 2))) * 16 + (lane & 1) * 8)) * 2, vB1 = vB + 32 * 8192;
  int ao = (wr * 64 + l15) * 128 + ((lq ^ (l15 & 7)) << 4);
  int bo = (int)(uintptr_t)smem + 65536 + (lq * 8 + q4) * 256 + (((wc * 2) ^ (q4 | ((lq & 1) << 2))) << 5) + p4 * 8;
  const int vD = lane * 16;
#define PJ_ORD 0x1312
  const int ord = (j & 3) == 3 ? PJ_ORD : 0x1312;
#define PJ_NEXT ((ord >> (u * 4)) & 3)
  const int cb = (x & 1) * 8 + (j >> 2);
  __amdgpu_buffer_rsrc_t rsA, rsB;
  const u16 *pA0 = p.Bx, *pB0 = (x < 2 ? p.BWk : x < 4 ? p.BWv : p.BWq) + cb * 256;
  const u16 *pA1 = x < 2 ? p.Ax : p.Ac, *pB1 = (x < 2 ? p.Awq : x < 4 ? p.Awk : p.Awv) + cb * 256;
  asm volatile("" : "+s"(pA0), "+s"(pB0), "+s"(pA1), "+s"(pB1));
  int k2, vA, vA1, sa, sb, nvA;
#define PJ_K2(u) ((u) < 2 || x < 2 ? 1152 : 1024)
#define PJ_PREP(u) do { int ln_ = vD; asm volatile("" : "+v"(ln_)); \
    nvA = (wid * 8 + (ln_ >> 7)) * PJ_K2(u) + ((ln_ ^ (ln_ >> 3)) & 0x70); \
    asm volatile("" : "+v"(nvA)); } while (0)
#define PJ_UNIT(u) do { k2 = PJ_K2(u); rsA = RSRC((u) < 2 ? pA0 : pA1, 2048 * k2); rsB = RSRC((u) < 2 ? pB0 : pB1, k2 * 4096 - cb * 512); vA = nvA; vA1 = nvA + 64 * k2; \
    sa = ((j & 3) + ((u) & 1) * 4) * 256 * k2 - 128; sb = 0; } while (0)
#define PJ_DEST(u, rt) do { const int mi_ = ((u) >> 1) * 3 + (x >> 1), g_ = ((rt) + ((u) & 1) * 4) * 8; \
    dco = O_QA + (mi_ == 2 ? 4 : (mi_ + 5) & 7) * ACB + cb * 16384 + wc * 2048 + (g_ + wr * 2) * 262144; \
    dfl = SY_BLK(mi_, cb, g_) * 4; } while (0)
#define PJ_STAGE(dst, rs, v0, v1, so) do { const int so_ = (so); \
    __builtin_amdgcn_raw_ptr_buffer_load_lds(rs, (LDSP void*)(smem + (dst) + wid * 1024), 16, v0, so_, 0, 0); \
    __builtin_amdgcn_raw_ptr_buffer_load_lds(rs, (LDSP void*)(smem + (dst) + wid * 1024 + 8192), 16, v1, so_, 0, 0); } while (0)
#define PJ_SA(dst, so) PJ_STAGE(dst, rsA, vA, vA1, so)
#define PJ_SB(dst, so) PJ_STAGE(dst, rsB, vB, vB1, so)
#define PJ_LDA(off) do { _Pragma("unroll") for (int m = 0; m < 4; m++) _Pragma("unroll") for (int k = 0; k < 2; k++) \
    At[m][k] = *(const bf8*)(smem + (ao ^ (k * 64)) + (off) + m * 2048); } while (0)
#define PJ_TR(dst, a, off) asm volatile("ds_read_b64_tr_b16 %0, %1 offset:%2" : "=&v"(dst) : "v"(a), "i"(off) : "memory")
#define PJ_LDB(D, h) do { const int b1_ = bo ^ 32; _Pragma("unroll") for (int n = 0; n < 2; n++) _Pragma("unroll") for (int k = 0; k < 2; k++) { \
    bf4 lo_, hi_; PJ_TR(lo_, n ? b1_ : bo, (h) * 16384 + k * 8192); PJ_TR(hi_, n ? b1_ : bo, (h) * 16384 + k * 8192 + 1024); D[n][k] = cat8(lo_, hi_); } } while (0)
#define PJ_CV(ai, bj, q) ((u32x4){pk2(acc[ai][bj][(q) & 2][(q) & 1][0], acc[ai][bj][(q) & 2][(q) & 1][1]), pk2(acc[ai][bj][(q) & 2][(q) & 1][2], acc[ai][bj][(q) & 2][(q) & 1][3]), \
                                  pk2(acc[ai][bj][((q) & 2) + 1][(q) & 1][0], acc[ai][bj][((q) & 2) + 1][(q) & 1][1]), pk2(acc[ai][bj][((q) & 2) + 1][(q) & 1][2], acc[ai][bj][((q) & 2) + 1][(q) & 1][3])})
#define PJ_SW(v) pair16(v[0], v[1], v[2], v[3])
#define PJ_MMA(Z, ai, bj, D, F) do { PJ_SCHED; __builtin_amdgcn_s_setprio(1); \
    _Pragma("unroll") for (int k = 0; k < 2; k++) _Pragma("unroll") for (int m = 0; m < 4; m++) _Pragma("unroll") for (int n = 0; n < 2; n++) { \
      if (k == 0 && (Z)) asm volatile("v_mfma_f32_16x16x32_bf16 %0, %1, %2, 0" : "+v"(acc[ai][bj][m][n]) : "v"(D[n][k]), "v"(At[m][k])); \
      else asm volatile("v_mfma_f32_16x16x32_bf16 %0, %1, %2, %0" : "+v"(acc[ai][bj][m][n]) : "v"(D[n][k]), "v"(At[m][k])); \
      F(k * 8 + m * 2 + n); } \
    __builtin_amdgcn_s_setprio(0); if (PJ_PAD) __builtin_amdgcn_s_sleep(PJ_PAD); PJ_SCHED; } while (0)
#define PJ_SWI(a, b) asm volatile("v_permlane16_swap_b32 %0, %1" : "+v"(a), "+v"(b))
#define PJ_CVI(d, s, e) asm volatile("v_cvt_pk_bf16_f32 %0, %1, %2" : "=v"(d) : "v"(s[e]), "v"(s[(e) + 1]))
#define PJ_PACK(P, ai, bj, i) do { const int q_ = (i) >> 2; \
    if (((i) & 3) < 2) { PJ_CVI(P[q_][(i) & 1], acc[ai][bj][q_ & 2][q_ & 1], ((i) & 1) * 2); PJ_CVI(P[q_][2 + ((i) & 1)], acc[ai][bj][(q_ & 2) + 1][q_ & 1], ((i) & 1) * 2); } \
    else PJ_SWI(P[q_][(i) & 1], P[q_][2 + ((i) & 1)]); } while (0)
#define PJ_WV(n) asm volatile("s_waitcnt vmcnt(" #n ")" ::: "memory")
#define PJ_WL(n) asm volatile("s_waitcnt lgkmcnt(" #n ")" ::: "memory")
#define PJ_WL0 do { __builtin_amdgcn_s_waitcnt(0xC07F); asm volatile("" ::: "memory"); } while (0)
#define PJ_BAR __builtin_amdgcn_s_barrier()
#define PJ_SCHED __builtin_amdgcn_sched_barrier(0)
#define PJ_NOP do { PJ_SCHED; asm volatile("s_nop 7"); PJ_SCHED; } while (0)
#define PJ_DS(v, ai, bj, q) do { PJ_SCHED; __builtin_amdgcn_raw_buffer_store_b128(v, rc, vD + ((q) & 1) * 1024, dco + ((ai) * 4 + ((q) >> 1)) * 262144 + (bj) * 8192, 16); \
    asm volatile("s_nop 1"); PJ_SCHED; } while (0)
#define PJ_PARK(q) (*(u32x4*)(smem + C_PARK + wid * 4096 + vD + (q) * 1024))
#define PJ_PUB(ai, n) do { if (wid == 4) __builtin_amdgcn_raw_buffer_store_b32(FULL, rc, vD < (n) * 16 ? vD >> 2 : PJ_OOB, dfl + (ai) * 16, 16); } while (0)
#define PJ_ST()
#define PJ_TOUCH(ptr, bytes, vo, so) __builtin_amdgcn_raw_ptr_buffer_load_lds(RSRC(ptr, bytes), (LDSP void*)(smem + C_PARK + wid * 4096), 4, vo, so, 0, 0)
#define PJ_SL 0x1E
#define PJ_PT 0
  static_assert(XS0 >= 46 && PJ_SL == 0x1E && PJ_PT == 0, "");
  int emp = 128;
  u32 old = 0u;
#define PJ_T1(n) do { \
      int vt_ = vD; asm volatile("" : "+v"(vt_)); vt_ <<= 3; \
      const int i_ = cb * 8 + wid + (n) * 128; \
      const int nw_ = x < 2 ? 576 : 512, na_ = x < 2 ? 288 : x < 4 ? 256 : 144, no_ = (x >> 1) * 96 + 96; \
      const u64 w_ = (u64)(pB1 - cb * 256), a_ = x < 4 ? (u64)pA1 : (u64)(pA0 + 1024 * 576), o_ = (u64)p.Awo + (x < 2 ? 0 : x < 4 ? 96 : 288) * 8192; \
      PJ_TOUCH(i_ < nw_ ? w_ : i_ < nw_ + na_ ? a_ : o_, i_ < nw_ + na_ ? WOB : no_ * 8192, vt_, (i_ < nw_ ? i_ : i_ < nw_ + na_ ? i_ - nw_ : i_ - nw_ - na_) * 8192); } while (0)
#define PJ_TA 3
#define PJ_WARM(k) do { \
    if (u == 0 && ((j & 3) == 2 || ((k) < PJ_TA && (j & 3) == 1))) { \
      PJ_T1((k) + ((j & 1) ? 0 : PJ_TA)); \
      if ((k) < 4 - PJ_TA && !(j & 1)) PJ_T1((k) + PJ_TA + 4); \
    } else { \
      const int e_ = emp + (u * 4 + (k)) * 3; \
      __builtin_amdgcn_raw_buffer_store_b128(ones4(), rc, vD, e_ < 128 ? O_OB + (e_ >> 1) * 262144 + cb * 16384 + (e_ & 1) * 8192 + wid * 1024 : PJ_OOB, 16); \
    } } while (0)
#define PJ_F0(i)
#define PJ_F1(i) do { if (S == 8) PJ_PACK(pa, 0, 0, i); } while (0)
#define PJ_F2(i) do { if (S == 8) PJ_PACK(pb, 0, 1, i); if (S == 0) PJ_PACK(pb, 1, 1, i); } while (0)
#define PJ_F3(i) do { if (S == 8) PJ_PACK(pa, 1, 0, i); if (S == 5 && (i) == 0) { PJ_SCHED; PJ_PREP(PJ_NEXT); PJ_SCHED; } } while (0)
#define PJ_STEP(SS) do { constexpr int S = SS; PJ_ST(); \
    PJ_LDB(B0, 0); PJ_SCHED; PJ_LDA(0); PJ_SA((bsel ^ 32768) + 16384, sa + 128 * k2); \
    PJ_WL(8); PJ_BAR; PJ_WL0; PJ_MMA(S == 0, 0, 0, B0, PJ_F0); PJ_BAR; PJ_SCHED; \
    if (S == 7) PJ_UNIT(PJ_NEXT); \
    PJ_LDB(B1, 1); PJ_SB(65536 + bsel, sb); \
    if (S == 0) { PJ_DS(pb[2], 0, 1, 2); PJ_DS(pb[3], 0, 1, 3); } \
    if ((PJ_SL >> S) & 1) PJ_WARM(PJ_PT + __builtin_popcount(PJ_SL & ((1 << S) - 1))); \
    PJ_BAR; PJ_WL0; PJ_MMA(S == 0, 0, 1, B1, PJ_F1); PJ_BAR; \
    PJ_LDA(16384); sa += 128; PJ_SA(bsel, sa); \
    if (S == 8) { PJ_SCHED; PJ_DEST(u, j & 3); _Pragma("unroll") for (int q = 0; q < 4; q++) PJ_PARK(q) = pa[q]; PJ_SCHED; } \
    PJ_BAR; PJ_WL0; PJ_MMA(S == 0, 1, 0, B0, PJ_F2); PJ_BAR; PJ_SCHED; \
    { u32x4 c0_, c1_; \
      if (S < 2) { c0_ = PJ_PARK(2 * S); c1_ = PJ_PARK(2 * S + 1); PJ_SCHED; } \
      PJ_SB(65536 + bsel + 16384, sb + 256); sb += 64 * 8192; \
      if (S == 8) { PJ_DS(pb[0], 0, 1, 0); PJ_DS(pb[1], 0, 1, 1); } \
      else if (S < 2) { PJ_DS(c0_, 0, 0, (2 * S) & 3); PJ_DS(c1_, 0, 0, (2 * S + 1) & 3); } \
      else if (S < 4) { PJ_DS(pa[(2 * S) & 3], 1, 0, (2 * S) & 3); PJ_DS(pa[(2 * S + 1) & 3], 1, 0, (2 * S + 1) & 3); } \
      else if (S < 6) { PJ_DS(pb[(2 * S) & 3], 1, 1, (2 * S) & 3); PJ_DS(pb[(2 * S + 1) & 3], 1, 1, (2 * S + 1) & 3); } } \
      \
    asm volatile("s_waitcnt vmcnt(%0)" :: "i"((S == 0 ? 10 : S < 6 || S == 8 ? 8 : 6) + ((PJ_SL >> S) & 1)) : "memory"); \
    PJ_BAR; \
    if (S == 2) PJ_PUB(0, 4); \
    if (S == 7) PJ_PUB(1, 4); \
    PJ_MMA(S == 0, 1, 1, B1, PJ_F3); PJ_BAR; \
    bsel ^= 32768; ao ^= 32768; bo ^= 32768; } while (0)
  f4 acc[2][2][4][2];
  bf8 At[4][2], B0[2][2], B1[2][2];
  u32x4 pa[4], pb[4];
  int bsel = 0, dco = PJ_OOB, dfl = PJ_OOB, u = 0;
  auto quick = [&](auto NBC, const int hf, const int ch) __attribute__((always_inline)) {
    constexpr int NB = decltype(NBC)::value, NS = 5 - NB;
#define PJ_FS(t) do { PJ_SA(((t) % NS) * 16384, hf * 128 * 1152 + (t) * 128); \
      _Pragma("unroll") for (int h_ = 0; h_ < NB; h_++) PJ_SB(NS * 16384 + (((t) % NS) * NB + h_) * 16384, (t) * 64 * 8192 + (ch + h_) * 256); PJ_SCHED; } while (0)
#pragma unroll
    for (int t = 0; t < NS - 1; t++) PJ_FS(t);
    bo += NS * 16384 - 65536;
#pragma unroll
    for (int t = 0; t < 9; t++) {
      PJ_ST();
      if (t == 8) PJ_WV(0); else if (NB == 2) PJ_WV(6); else if (t == 7) PJ_WV(4); else PJ_WV(8);
      PJ_BAR; PJ_SCHED;
      if (wr && t + NS - 1 < 9) PJ_FS(t + NS - 1);
      PJ_LDB(B0, 0); PJ_LDA((t % NS) * 16384);
      if (NB == 2) PJ_LDB(B1, 1);
      PJ_SCHED;
      bo += t == 8 ? 65536 - NS * 16384 - (8 % NS) * NB * 16384 : (t % NS == NS - 1 ? -(NS - 1) * NB * 16384 : NB * 16384);
      PJ_WL0;
      PJ_MMA(t == 0, 0, 0, B0, PJ_F0);
      if (NB == 2) PJ_MMA(t == 0, 0, 1, B1, PJ_F0);
      if (!wr && t + NS - 1 < 9) PJ_FS(t + NS - 1);
    }
    PJ_NOP;
    u32 b1;
    if (NB == 1 && job >= 0 && wid == 0) b1 = BEAT_LD(job);
#pragma unroll
    for (int q = 0; q < 4 * NB; q++) { const u32x4 v = PJ_CV(0, q >> 2, q & 3); PJ_DS(PJ_SW(v), hf, ch + (q >> 2), q & 3); }
    if (NB == 2 && tid == 0) { *(u32*)(smem + C_B0 + 4) = old; PJ_WL0; }
    PJ_WV(0); PJ_BAR;
    if (NB == 1 && job >= 0 && wid == 0) old = confirm(p, smem, job, b1, wv);
    if (tid < 4) __hip_atomic_fetch_or(WSP(u32, dfl) + hf * 4 + tid, NB == 2 ? FULL : 4u << ch, __ATOMIC_RELAXED, __HIP_MEMORY_SCOPE_AGENT);
  };
  if (((j + 1) & 3) < 2) {
    PJ_PREP(0); PJ_UNIT(0); PJ_DEST(0, 0);
    quick(std::integral_constant<int, 1>{}, 0, j & 1);
    if ((j & 3) == 0) {
      quick(std::integral_constant<int, 2>{}, 1, 0);
      emp = (__builtin_amdgcn_readfirstlane(*(const u32*)(smem + C_B0 + 4)) >> (job & 31)) & 1 ? 129 : XS0 * 2 - 12 + (x >> 1);
    }
    if ((j & 3) == 0 || PJ_ORD != 0x1312) u = 2;
    dco = PJ_OOB; dfl = PJ_OOB; job = -1;
  }
#pragma unroll
  for (int a = 0; a < 16; a++) asm volatile("" : "=v"(acc[a >> 3][(a >> 2) & 1][a & 3][0]), "=v"(acc[a >> 3][(a >> 2) & 1][a & 3][1]));
#pragma unroll
  for (int a = 0; a < 4; a++) asm volatile("" : "=v"(pa[a]), "=v"(pb[a]));
  PJ_PREP(u); PJ_UNIT(u);
  const int cs = x < 4 && (j & 3) == 2 ? (x * 8 + (j >> 2)) * 128 : -1;
#pragma unroll
  for (int i = 0; i < 2; i++) pa[i] = __builtin_amdgcn_raw_buffer_load_b128(RSRC(p.BWo, WOB), (tid >> 3) * 1152 + (tid & 7) * 16, cs >= 0 ? (cs + i * 64) * 1152 + 1024 : PJ_OOB, 0);
  PJ_SCHED;
  PJ_SB(65536, 0); PJ_SA(0, sa + 128);
  PJ_SB(65536 + 16384, 256); PJ_SA(16384, sa + 128 + 128 * k2);
  PJ_SCHED;
#pragma unroll
  for (int i = 0; i < 2; i++) __builtin_amdgcn_raw_buffer_store_b128(pa[i], rc, (tid & 7) * 65536 + (tid >> 3) * 16, cs >= 0 ? O_WX + (cs + i * 64) * 16 : PJ_OOB, 16);
  PJ_SCHED;
  if (wr == 1) PJ_BAR;
  PJ_WV(6); PJ_BAR;
  u32 b1;
  if (job >= 0 && wid == 0) b1 = BEAT_LD(job);
  PJ_SCHED;
  PJ_SB(65536 + 32768, 64 * 8192); PJ_SA(32768, sa + 256);
  PJ_SB(65536 + 32768 + 16384, 64 * 8192 + 256);
  PJ_WV(6);
  if (job >= 0 && wid == 0) confirm(p, smem, job, b1, wv);
  PJ_BAR;
  if (tid == 256 && cs >= 0) AOR(SY_WX, 1u << (cs >> 7));
#pragma unroll
  for (int i = 0; i < PJ_PT; i++) PJ_WARM(i);
  sa += 256; sb = 128 * 8192;
#pragma unroll 1
  for (;; u = PJ_NEXT) {
    PJ_STEP(0); PJ_STEP(1); PJ_STEP(2); PJ_STEP(3); PJ_STEP(4); PJ_STEP(5);
    int more = k2 - 1024; asm volatile("" : "+s"(more));
    if (more) PJ_STEP(6);
    PJ_STEP(7); PJ_STEP(8);
    if (u == 1) break;
  }
  PJ_ST();
  PJ_NOP;
  const u32 left = __builtin_amdgcn_raw_buffer_load_b32(rc, wid ? PJ_OOB : vD < 128 ? SY_JOB(0) * 4 + vD * 4 : SY_INIT(0) * 4, 0, 16);
#define FSL 0
#define FWN 15
#define PJ_FD(v, ai, bj, q) do { PJ_DS(v, ai, bj, q); if (FSL) __builtin_amdgcn_s_sleep(FSL); if (FWN < 15) asm volatile("s_waitcnt vmcnt(%0)" :: "i"(FWN) : "memory"); } while (0)
  PJ_FD(pb[2], 0, 1, 2); PJ_FD(pb[3], 0, 1, 3);
#pragma unroll
  for (int q = 0; q < 4; q++) pb[q] = PJ_PARK(q);
#pragma unroll
  for (int q = 0; q < 4; q++) PJ_FD(pa[q], 1, 0, q);
#pragma unroll
  for (int q = 0; q < 4; q++) PJ_FD(pb[q], 0, 0, q);
#pragma unroll
  for (int q = 0; q < 4; q++) { const u32x4 v = PJ_CV(1, 1, q); PJ_FD(PJ_SW(v), 1, 1, q); }
  pre = -1;
  const int xp = __builtin_amdgcn_readlane(left, 8);
  if (__all(left >= (vD < 128 ? ~0u : 1u)) && vD == 0) pre = (int)__hip_atomic_fetch_add(HOTP(xp) + SY_QHEAD, 1u, __ATOMIC_RELAXED, __HIP_MEMORY_SCOPE_AGENT) + (xp & 0x7000);
  PJ_WV(0);
  if (wr == 0) PJ_BAR;
  PJ_BAR;
  if (emp != 129) PJ_PUB(0, 8);
  PJ_ST();
}

namespace fa {
constexpr float SCALE = 0.08838834764831845f;
constexpr int NW = 8, QBLK = 32, KVBLK = 64, QB = NW * QBLK, D = 128;
constexpr int SHM_V = KVBLK * D * 2, SHM_K = KVBLK * D * 2;
#define SBAR() __builtin_amdgcn_sched_barrier(0)
#define FMX3(a, b, c) __builtin_elementwise_maximum(__builtin_elementwise_maximum(a, b), c)
#define AT_PAD 0
#define EBAR 15
#define LSUM 1
#define LSP 0x80
#define EPR 0
#define DPOS 0x0C080400
#define MX0 -1
#define ATHR 24.f
__device__ __forceinline__ void attn_block(const char* __restrict__ Qp, const char* __restrict__ Kh, const char* __restrict__ Vh,
                                           char* __restrict__ Op, int P0, char* lds, const int wv, u32* qh, const int oq, int& pre) {
  const int tid = TIDX, wid = wv, lane = tid & 63, l15 = lane & 15, lq = lane >> 4, grp = wid >> 2;
  const int NT = (P0 + QB) / KVBLK;
  const int rb = wid ^ 4;
  const int TL = NT - 4 + (rb >> 1);
  const int rem = 2 * (NT - TL) - 2 - grp;
  float l0 = 0.f, l1 = 0.f;
  f4 ls[2] = {};
  u32x4 one = {0x3F803F80u, 0x3F803F80u, 0x3F803F80u, 0x3F803F80u};
  f4 o[8][2] = {}, sc[4][2], nm[2] = {};
  int ko = lq * 512 + l15 * 16;
  int vo = 4 * SHM_K + ((lane >> 1) & 1) * 512 + (((lane >> 2) ^ ((lane & 2) * 4)) << 4) + (lane & 1) * 8;
  const __amdgpu_buffer_rsrc_t rk = RSRC(Kh, (2 * NT - 1) * 262144 + 8192), rv = RSRC(Vh, (2 * NT - 1) * 262144 + 8192);
  const int gk = tid * 16, gv = gk ^ ((tid & 32) << 2);
#define DMA(rs, go, dst, so) __builtin_amdgcn_raw_ptr_buffer_load_lds(rs, (LDSP void*)(lds + (dst) + wid * 1024), 16, go, so, 0, 0)
#define ISSUE_K(t) do { const int s_ = (t) * 524288, d_ = ((t) & 3) * SHM_K; DMA(rk, gk, d_, s_); DMA(rk, gk, d_ + 8192, s_ + 262144); } while (0)
#define ISSUE_V(t) do { const int s_ = (t) * 524288, d_ = 4 * SHM_K + ((t) & 3) * SHM_V; DMA(rv, gv, d_, s_); DMA(rv, gv, d_ + 8192, s_ + 262144); } while (0)
#define PHASE(n) do { SBAR(); asm volatile("s_waitcnt vmcnt(" #n ")" ::: "memory"); __builtin_amdgcn_s_barrier(); asm volatile("" ::: "memory"); SBAR(); } while (0)
#define LDK(i) kf[(i) % 6] = *(const bf8*)(lds + ko + ((i) >> 2) * 2048 + ((i) & 1) * 256 + ((i) & 2) * 4096)
#define LDV0(i) vl[(i) & 3] = trrd(lds + vo + ((i) & 7) * 1024 + ((i) >> 3) * 8192)
#define LDV1(i) vh[(i) & 3] = trrd(lds + vo + ((i) & 7) * 1024 + ((i) >> 3) * 8192 + 256)
#define LDV(i) do { LDV0(i); LDV1(i); } while (0)
#define AST(i)
#define AP(i)
  AP(0);
#define QK(NEXT) do { _Pragma("unroll") for (int i = 0; i < 16; ++i) { \
      _Pragma("unroll") for (int h = 0; h < 2; ++h) { \
        SBAR(); \
        if (i > 3) sc[i & 3][h] = MFMA(kf[i % 6], qr[i >> 2][h], sc[i & 3][h]); \
        else asm volatile("v_mfma_f32_16x16x32_bf16 %0, %1, %2, %3" : "+v"(sc[i][h]) : "v"(kf[i]), "v"(qr[0][h]), "v"(nm[h])); \
        SBAR(); \
        if (i + 4 < 16) { if (h == 0) LDK(i + 4); } else if (NEXT) { if (h == 0) LDV0(i - 12); else LDV1(i - 12); } } } } while (0)
#define SCX(k) sc[(k) >> 3][((k) >> 2) & 1][(k) & 3]
#define MAXS(i) do { \
      if ((i) == 0) mx = FMX3(SCX(0), SCX(1), SCX(2)); else if ((i) == 1) my = FMX3(SCX(3), SCX(4), SCX(5)); \
      else if ((i) < 15) { if ((i) & 1) my = FMX3(my, SCX(2 * (i) + 2), SCX(2 * (i) + 3)); else mx = FMX3(mx, SCX(2 * (i) + 2), SCX(2 * (i) + 3)); } \
      else { mx = __builtin_elementwise_maximum(mx, my); asm volatile("" : "+v"(mx)); } } while (0)
  float mx, my;
  constexpr float C2 = 1.4426950408889634f * SCALE;
  bf8 qr[4][2], kf[6], pa[2][2];
  bf4 vl[4], vh[4];
#pragma unroll
  for (int d0 = 0; d0 < 8; ++d0) qr[d0 >> 1][d0 & 1] = *(const bf8*)(Qp + rb * 262144 + (d0 >> 1) * 2048 + (d0 & 1) * 256 + ko);
  SBAR();
  ISSUE_K(0); ISSUE_K(1); ISSUE_V(0); ISSUE_K(2); ISSUE_V(1);
  SBAR();
#pragma unroll
  for (int i = 0; i < 8; ++i) asm volatile("" : "=v"(sc[i >> 1][i & 1]), "+v"(o[i][0]), "+v"(o[i][1]));
  asm volatile("" : "+v"(nm[0]), "+v"(nm[1]));
  if (LSUM) asm volatile("" : "+v"(ls[0]), "+v"(ls[1]), "+v"(one));
  SBAR();
#pragma unroll
  for (int d0 = 0; d0 < 8; ++d0) {
    u32x4 q = __builtin_bit_cast(u32x4, qr[d0 >> 1][d0 & 1]);
#pragma unroll
    for (int i = 0; i < 4; i++) q[i] = pk2(__uint_as_float(q[i] << 16) * C2, __uint_as_float(q[i] & 0xFFFF0000u) * C2);
    qr[d0 >> 1][d0 & 1] = __builtin_bit_cast(bf8, q);
    asm volatile("" : "+v"(qr[d0 >> 1][d0 & 1]));
  }
  AP(8);
  PHASE(8);
  AP(1);
  if (grp) PHASE(6);
  LDK(0); LDK(1); LDK(2); LDK(3);
  QK(0);
  mx = 0.f;
  AP(2);
  PHASE(4);
  AP(3);
  auto tile = [&](const int t, auto LASTC) __attribute__((always_inline)) {
    constexpr bool LAST = decltype(LASTC)::value;
    AST(0);
#define ISSUE1(i) do { if ((i) < 2) DMA(rk, gk, ((t + 3) & 3) * SHM_K + (i) * 8192, (t + 3) * 524288 + (i) * 262144); \
                       else DMA(rv, gv, 4 * SHM_K + ((t + 2) & 3) * SHM_V + ((i) & 1) * 8192, (t + 2) * 524288 + ((i) & 1) * 262144); SBAR(); } while (0)
#pragma unroll
    for (int i = 0; i < 4; ++i) if (((DPOS >> (i * 8)) & 255) == 0) ISSUE1(i);
    SBAR();
    if constexpr (LAST) {
      const int dq = l15 - 4 * lq;
      if (rb & 1) {
#pragma unroll
        for (int r = 0; r < 4; ++r) { if (dq < r) { sc[2][0][r] = -__builtin_inff(); sc[3][1][r] = -__builtin_inff(); } sc[3][0][r] = -__builtin_inff(); SBAR(); }
      } else {
#pragma unroll
        for (int r = 0; r < 4; ++r) {
          if (dq < r) { sc[0][0][r] = -__builtin_inff(); sc[1][1][r] = -__builtin_inff(); }
          sc[1][0][r] = sc[2][0][r] = sc[2][1][r] = sc[3][0][r] = sc[3][1][r] = -__builtin_inff(); SBAR();
        }
      }
    }
    if (LAST || MX0 < 0) {
#pragma unroll
      for (int i = 0; i < 16; ++i) MAXS(i);
    }
    if (__builtin_expect(t == 0 || __any(mx > ATHR), 0)) {
#pragma unroll
      for (int h = 0; h < 2; ++h) {
        float m = FMX3(FMX3(sc[0][h][0], sc[0][h][1], sc[0][h][2]), sc[0][h][3], t ? 0.f : sc[0][h][3]);
#pragma unroll
        for (int k = 1; k < 4; ++k) m = FMX3(FMX3(m, sc[k][h][0], sc[k][h][1]), sc[k][h][2], sc[k][h][3]);
        { auto rr = __builtin_amdgcn_permlane16_swap(__float_as_uint(m), __float_as_uint(m), false, false);
          m = __builtin_elementwise_maximum(__uint_as_float(rr[0]), __uint_as_float(rr[1])); }
        { auto rr = __builtin_amdgcn_permlane32_swap(__float_as_uint(m), __float_as_uint(m), false, false);
          m = __builtin_elementwise_maximum(__uint_as_float(rr[0]), __uint_as_float(rr[1])); }
        const float alpha = t ? __builtin_amdgcn_exp2f(-m) : 1.f, nn = nm[h][0] - m;
#pragma unroll
        for (int r = 0; r < 4; ++r) {
          nm[h][r] = nn;
#pragma unroll
          for (int k = 0; k < 4; ++k) sc[k][h][r] -= m;
#pragma unroll
          for (int d = 0; d < 8; ++d) o[d][h][r] *= alpha;
        }
        if (LSUM) ls[h][0] *= alpha; else if (h) l1 *= alpha; else l0 *= alpha;
        asm volatile("" : "+v"(nm[h]));
      }
    }
    AST(1);
    float x[32];
    u32 pw[16];
#define SCP(r, e) sc[(r) >> 2][(r) & 1][((r) & 2) + (e)]
#pragma unroll
    for (int r = 0; r < 18; ++r) {
      SBAR();
      if (r < 16) { x[2 * r] = __builtin_amdgcn_exp2f(SCP(r, 0)); x[2 * r + 1] = __builtin_amdgcn_exp2f(SCP(r, 1)); }
      if (r >= 2) {
        if (LSUM) {} else if (r & 1) { l0 += x[2 * r - 5]; l1 += x[2 * r - 4]; } else { if (r > 2) l1 += x[2 * r - 5]; l0 += x[2 * r - 4]; }
        pw[r - 2] = pk2(x[2 * r - 4], x[2 * r - 3]); }
      SBAR();
#pragma unroll
      for (int i = 0; i < 4; ++i) if (((DPOS >> (i * 8)) & 255) == r + 1) ISSUE1(i);
    }
    SBAR();
    if (!LSUM) l1 += x[31];
#pragma unroll
    for (int i = 0; i < 4; ++i) pa[i >> 1][i & 1] = __builtin_bit_cast(bf8, (u32x4){pw[(i >> 1) * 8 + (i & 1)], pw[(i >> 1) * 8 + (i & 1) + 2], pw[(i >> 1) * 8 + (i & 1) + 4], pw[(i >> 1) * 8 + (i & 1) + 6]});
    asm volatile("" : "+v"(pa[0][0]), "+v"(pa[0][1]), "+v"(pa[1][0]), "+v"(pa[1][1]));
    if (!LSUM) asm volatile("" : "+v"(l0), "+v"(l1));
    ko ^= (t & 1) ? 3 * SHM_K : SHM_K;
    SBAR();
    AST(3);
    if constexpr (!LAST) { LDK(0); LDK(1); LDK(2); LDK(3); } else { LDV(0); LDV(1); LDV(2); LDV(3); }
    AST(4);
    PHASE(4);
    AST(5);
    __builtin_amdgcn_s_setprio(1);
    if constexpr (!LAST) QK(1);
    AST(6);
#pragma unroll
    for (int i = 0; i < 16; ++i) {
      SBAR();
      if (LSUM && (i == (LSP & 15) || i == (LSP >> 4))) {
#pragma unroll
        for (int h = 0; h < 2; ++h) { asm volatile("v_mfma_f32_16x16x32_bf16 %0, %1, %2, %0" : "+v"(ls[h]) : "v"(one), "v"(pa[i == (LSP >> 4)][h])); SBAR(); }
      }
      o[i & 7][0] = MFMA(cat8(vl[i & 3], vh[i & 3]), pa[i >> 3][0], o[i & 7][0]);
      SBAR();
      if (i > 0 && i + 3 < 16) LDV1(i + 3);
      if constexpr (!LAST) if (MX0 >= 0 && i >= MX0 && i < MX0 + 8) MAXS(2 * (i - MX0));
      SBAR();
      o[i & 7][1] = MFMA(cat8(vl[i & 3], vh[i & 3]), pa[i >> 3][1], o[i & 7][1]);
      SBAR();
      if (i + 4 < 16) LDV0(i + 4);
      if constexpr (!LAST) if (MX0 >= 0 && i >= MX0 && i < MX0 + 8) MAXS(2 * (i - MX0) + 1);
      if (i == EBAR) AST(2);
      if (EPR && i == EBAR) __builtin_amdgcn_s_setprio(EPR);
      if (i == EBAR && (!LAST || rem)) PHASE(4);
    }
    __builtin_amdgcn_s_setprio(0);
    vo ^= (t & 1) ? 3 * SHM_V : SHM_V;
    AST(7);
  };
#pragma unroll 1
  for (int t = 0; t < TL; t++) {
    tile(t, std::false_type{});
  }
  AP(4);
  const u32 peek = __builtin_amdgcn_raw_buffer_load_b32(RSRC(qh, wid ? 0 : 4), 0, 0, 16);
  tile(TL, std::true_type{});
  AP(5);
  ISSUE_V(TL + 3);
  SBAR();
  {
    { auto rr = __builtin_amdgcn_permlane16_swap(__float_as_uint(l0), __float_as_uint(l0), false, false); l0 = __uint_as_float(rr[0]) + __uint_as_float(rr[1]); }
    { auto rr = __builtin_amdgcn_permlane16_swap(__float_as_uint(l1), __float_as_uint(l1), false, false); l1 = __uint_as_float(rr[0]) + __uint_as_float(rr[1]); }
    const float i0 = __builtin_amdgcn_rcpf(LSUM ? ls[0][0] : swadd(l0, l0)), i1 = __builtin_amdgcn_rcpf(LSUM ? ls[1][0] : swadd(l1, l1));
    const __amdgpu_buffer_rsrc_t ro = RSRC(Op + rb * 262144, 8192);
#pragma unroll
    for (int i = 0; i < 8; ++i) {
      __builtin_amdgcn_raw_buffer_store_b128(pair16(pk2(o[i][0][0] * i0, o[i][0][1] * i0), pk2(o[i][0][2] * i0, o[i][0][3] * i0), pk2(o[i][1][0] * i1, o[i][1][1] * i1), pk2(o[i][1][2] * i1, o[i][1][3] * i1)),
                                             ro, OB_OFF((lq & 1) * 16 + l15, (lq >> 1) * 8) + (i & 3) * 1024, (i >> 2) * 4096, 16);
      if (i == 3 && rem > 1) PHASE(6);
    }
  }
  AP(6);
  if (rem > 2) PHASE(10);
  if (rem > 3) { PHASE(8); PHASE(8); }
  if (TIDX == 0) {
    const bool out = PEEK && peek >= 320u;
    pre = (int)__hip_atomic_fetch_add(qh + (out ? oq : 0), 1u, __ATOMIC_RELAXED, __HIP_MEMORY_SCOPE_AGENT) + (out ? 0x10000 : 0);
  }
  VMW0();
  AP(7);
  __syncthreads();
}
#undef DMA
#undef ISSUE_K
#undef ISSUE_V
#undef PHASE
#undef LDK
#undef LDV
#undef QK
#undef MAXS
}

__device__ __forceinline__ void wof_task(const Params& p, int kc, char* smem, const int wv, u32* qh, int& pre) {
  const int tid = TIDX, lane = tid & 63, wid = wv;
  const int l15 = lane & 15, lq = lane >> 4, q4 = l15 >> 2, p4 = lane & 3;
  const __amdgpu_buffer_rsrc_t rw = RSRC(WSP(u16, O_WOF), 2 * WOB);
  bf8 st[2][9];
#pragma unroll
  for (int m = 0; m < 2; m++)
#pragma unroll
    for (int i = 0; i < 9; i++) st[m][i] = *(const bf8*)((m ? p.BWo : p.Awo) + (size_t)kc * 64 * 576 + (tid + 512 * i) * 8);
  CLAIM();
#pragma unroll
  for (int m = 0; m < 2; m++) {
#pragma unroll
    for (int i = 0; i < 9; i++) {
      const int id = tid + 512 * i, row = id / 72, ch = id % 72;
      *(bf8*)(smem + row * 1280 + (((ch >> 1) ^ fsw(row)) << 5) + ((ch & 1) << 4)) = st[m][i];
    }
    asm volatile("s_waitcnt lgkmcnt(0)" ::: "memory"); __builtin_amdgcn_s_barrier(); asm volatile("" ::: "memory");
#pragma unroll
    for (int i = 0; i < 9; i++) {
      const int f = wid * 9 + i, nt = f >> 1, k2 = f & 1;
      const int r0 = k2 * 32 + lq * 8 + q4, r1 = r0 + 4;
      bf4 lo = trrd(smem + r0 * 1280 + ((nt ^ fsw(r0)) << 5) + p4 * 8);
      bf4 hi = trrd(smem + r1 * 1280 + ((nt ^ fsw(r1)) << 5) + p4 * 8);
      __builtin_amdgcn_raw_buffer_store_b128(__builtin_bit_cast(u32x4, cat8(lo, hi)), rw, ((nt * 128 + kc * 2 + k2) * 64 + lane) * 16, m * WOB, 16);
    }
    if (m == 0) { asm volatile("s_waitcnt lgkmcnt(0)" ::: "memory"); __builtin_amdgcn_s_barrier(); asm volatile("" ::: "memory"); }
  }
  VMW0();
  __syncthreads();
}

#define OT_T(i)
#define OTI 2
#define OTD 0x1511
#define OTI4 2
#define OSC 18
#define SSC 16
#define OTR 1
template <int MT>
__device__ __forceinline__ void out_tile(const char* A, const char* W, u16* C, char* smem, const int wv, u32* hot, u32* qh, int& pre) {
  constexpr int U = 32 / MT, SC = U / 2, DP = MT / 4, NCH = 4 * MT, CW = 32768 / MT;
  const int tid = TIDX, lane = tid & 63, wid = wv;
  const int l15 = lane & 15, lq = lane >> 4, kh = wid >> 2, wc = wid & 3;
#define OTX 0
  const __amdgpu_buffer_rsrc_t ra = RSRC(A, (OTX & 1) ? 0 : MT / 2 * 262144), rw = RSRC(W + wc * 3 * 131072 + kh * 1024, (OTX & 2) ? 0 : 3 * 131072 - kh * 1024);
  const int va = tid * 16, vw = lane * 16;
  int ao = kh * 2048 + (lq >> 1) * 1024 + l15 * 32 + (lq & 1) * 16;
  f4 acc[MT][3];
  bf8 wf[U][3], af[2][MT];
#define OT_DMA(i, buf, so) __builtin_amdgcn_raw_ptr_buffer_load_lds(ra, (LDSP void*)(smem + (buf) + (i) * 8192 + wid * 1024), 16, va, \
                                                                    (so) + (MT == 4 ? ((i) >> 1) * 262144 + ((i) & 1) * 8192 : (i) * 262144), 0, 0)
#define OT_LDA(d, base, off) do { _Pragma("unroll") for (int m = 0; m < MT; m++) af[d][m] = *(const bf8*)(smem + (base) + (off) + (m >> 1) * 2 * CW + (m & 1) * 512); } while (0)
#define OT_LD1(d, m, base, off) af[d][m] = *(const bf8*)(smem + (base) + (off) + ((m) >> 1) * 2 * CW + ((m) & 1) * 512)
#define OT_VM(n) __builtin_amdgcn_s_waitcnt(0x0F70 | ((n) & 15) | (((n) >> 4) << 14))
#define OT_PAIR(buf, so) do { _Pragma("unroll") for (int i = 0; i < 8; i++) OT_DMA(i & 3, (buf) + (i >> 2) * 32768, (so) + (i >> 2) * 2 * CW); SB0(); } while (0)
#define OT_PEEK() do { if (wid == 0) asm volatile("s_mov_b32 m0, %2\n\ts_nop 0\n\tbuffer_load_dword %0, %1, 0 offen offset:%3 sc1 lds" \
                                                  :: "v"(vw * 4), "s"(RSRC(hot, SY_OQ(7) * 4)), "i"(131072), "i"(SY_OQ(0) * 4) : "memory", "m0"); } while (0)
  if constexpr (MT == 4) {
  OT_PAIR(0, 0);
#pragma unroll
  for (int u = 0; u < U; u++) {
    if (u == U - 1) OT_PAIR(65536, 4 * CW);
#pragma unroll
    for (int j = 0; j < 3; j++) wf[u][j] = __builtin_bit_cast(bf8, __builtin_amdgcn_raw_buffer_load_b128(rw, vw, u * 2048 + j * 131072, 0));
    SB0();
  }
#pragma unroll
  for (int m = 0; m < MT; m++)
#pragma unroll
    for (int j = 0; j < 3; j++) acc[m][j] = (f4){0, 0, 0, 0};
  asm volatile("s_waitcnt vmcnt(%0)" :: "i"(U * 3 + 8) : "memory"); __builtin_amdgcn_s_barrier(); asm volatile("" ::: "memory");
  OT_LDA(0, ao, 0);
  OT_T(2);
  int bs = 0;
#pragma unroll 1
  for (int c = 0; c < NCH; c += 2) {
    const int sw = c < NCH - 2 ? (c + 2) * CW : PJ_OOB, sd = c < NCH - 4 ? (c + 4) * 2 * CW : PJ_OOB;
#pragma unroll
    for (int u = 0; u < U; u++) {
      SB0();
      if (u < U - 1) OT_VM(3 * (U - 1) + 8); else OT_VM(3 * (U - 1));
      if (u == U - 1) {
        asm volatile("s_waitcnt lgkmcnt(0)" ::: "memory"); __builtin_amdgcn_s_barrier(); asm volatile("" ::: "memory");
        ao ^= 65536;
      }
      const int nxt = u == U - 1 ? 0 : ((u + 1) / SC) * 32768 + ((u + 1) % SC) * 4096;
      if (!OTI4) OT_LDA((u + 1) & 1, ao, nxt);
      SB0();
      if (u == U - 1) OT_PAIR(bs, sd);
#pragma unroll
      for (int j = 0; j < 3; j++) {
#pragma unroll
        for (int m = 0; m < MT; m++) {
          const int n = j * MT + m;
          acc[m][j] = MFMA(wf[u][j], af[u & 1][m], acc[m][j]);
          if (OTI4 && n % (OTI4 ? OTI4 : 1) == 0 && n / (OTI4 ? OTI4 : 1) < MT) { SB0(); OT_LD1((u + 1) & 1, n / (OTI4 ? OTI4 : 1), ao, nxt); SB0(); }
        }
        SB0();
        wf[u][j] = __builtin_bit_cast(bf8, __builtin_amdgcn_raw_buffer_load_b128(rw, vw, sw + u * 2048 + j * 131072, 0));
        SB0();
      }
    }
    bs ^= 65536;
    if (c == NCH - 6) OT_PEEK();
  }
  } else {
#pragma unroll
  for (int i = 0; i < 4; i++) OT_DMA(i, 0, 0);
  SB0();
#pragma unroll
  for (int u = 0; u < U; u++) {
#pragma unroll
    for (int d = 0; d < DP; d++) OT_DMA((u % SC) * DP + d, 32768 + (u / SC) * 32768, (1 + u / SC) * 2 * CW);
#pragma unroll
    for (int j = 0; j < 3; j++) wf[u][j] = __builtin_bit_cast(bf8, __builtin_amdgcn_raw_buffer_load_b128(rw, vw, u * 2048 + j * 131072, 0));
    SB0();
  }
#pragma unroll
  for (int m = 0; m < MT; m++)
#pragma unroll
    for (int j = 0; j < 3; j++) acc[m][j] = (f4){0, 0, 0, 0};
  asm volatile("s_waitcnt vmcnt(%0)" :: "i"(U * (DP + 3)) : "memory"); __builtin_amdgcn_s_barrier(); asm volatile("" ::: "memory");
  OT_LDA(0, ao, 0);
  OT_T(2);
  int bs = 0;
#pragma unroll 1
  for (int c = 0; c < NCH; c += 2) {
    const int sw = c < NCH - 2 ? (c + 2) * CW : PJ_OOB;
#pragma unroll
    for (int u = 0; u < U; u++) {
      SB0();
      __builtin_amdgcn_s_waitcnt(MT == 4 ? 0x4F7C : 0x0F7F);
      if (u % SC == SC - 1 && !(OTX & 32)) { asm volatile("s_waitcnt lgkmcnt(0)" ::: "memory"); __builtin_amdgcn_s_barrier(); asm volatile("" ::: "memory"); }
      if (u == U - 1) ao ^= 65536;
      const int nxt = u == U - 1 ? 0 : ((u + 1) / SC) * 32768 + ((u + 1) % SC) * 4096;
      if (!OTI) OT_LDA((u + 1) & 1, ao, nxt);
      SB0();
      const int cn = c + 3 + u / SC;
#pragma unroll
      for (int j = 0; j < 3; j++) {
#pragma unroll
        for (int m = 0; m < MT; m++) {
          const int n = j * MT + m;
          acc[m][j] = MFMA(wf[u][j], af[u & 1][m], acc[m][j]);
          if (OTI && n % (OTI ? OTI : 1) == 0 && n / (OTI ? OTI : 1) < MT) { SB0(); OT_LD1((u + 1) & 1, n / (OTI ? OTI : 1), ao, nxt); SB0(); }
          if ((n == (OTD & 255) || n == (OTD >> 8)) && !(OTX & 4)) { SB0(); OT_DMA((u % SC) * DP + (n == (OTD >> 8)), u < SC ? (bs ^ 65536) + 32768 : bs, cn < NCH ? cn * 2 * CW : PJ_OOB); SB0(); }
        }
        SB0();
        if (!(OTX & 8)) wf[u][j] = __builtin_bit_cast(bf8, __builtin_amdgcn_raw_buffer_load_b128(rw, vw, sw + u * 2048 + j * 131072, 0));
        SB0();
      }
    }
    bs ^= 65536;
    if (c == NCH - 6) OT_PEEK();
  }
  }
  OT_T(3);
  VMW0C();
  if (wid == 0) {
    const u32 left = *(const u32*)(smem + 131072 + SY_OQ(0) * 4 + (lane < 6 ? lane : 6) * 4);
    pre = 0x20000;
    if (!OXIT || __any(left < (lane > 5 ? 72u : (lane & 1) ? NOB : NOA))) CLAIM();
  }
  asm volatile("s_waitcnt lgkmcnt(0)" ::: "memory"); __builtin_amdgcn_s_barrier(); asm volatile("" ::: "memory");
  const __amdgpu_buffer_rsrc_t rc = RSRC(C, (MT * 16 - 1) * 2304 + 384);
#pragma unroll
  for (int m = 0; m < MT / 2; m++)
#pragma unroll
    for (int j = 0; j < 3; j++) *(f4*)(smem + (m * 3 + j) * 8192 + va) = kh ? acc[m][j] : acc[MT / 2 + m][j];
  __syncthreads();
#pragma unroll
  for (int m = 0; m < MT / 2; m++) {
    f4 t[3];
#pragma unroll
    for (int j = 0; j < 3; j++) { const f4 o = *(const f4*)(smem + (m * 3 + j) * 8192 + (va ^ 4096)); t[j] = kh ? o + acc[MT / 2 + m][j] : acc[m][j] + o; }
    const u32x4 v4 = pair16(pk2(t[0][0], t[0][1]), pk2(t[0][2], t[0][3]), pk2(t[1][0], t[1][1]), pk2(t[1][2], t[1][3]));
    const u32x2 v2 = {pk2(t[2][0], t[2][1]), pk2(t[2][2], t[2][3])};
    if (OTR) {
      char* d = smem + 98304 + (kh * MT * 8 + m * 16 + l15) * 400 + wc * 96;
      *(u32x4*)(d + (lq & 1) * 32 + (lq >> 1) * 16) = v4;
      *(u32x2*)(d + 64 + lq * 8) = v2;
    } else {
      const int co = (kh * MT * 8 + l15) * 2304 + wc * 96;
      __builtin_amdgcn_raw_buffer_store_b128(v4, rc, co + (lq & 1) * 32 + (lq >> 1) * 16, m * 16 * 2304, OSC);
      __builtin_amdgcn_raw_buffer_store_b64(v2, rc, co + 64 + lq * 8, m * 16 * 2304, OSC);
    }
  }
  if (OTR) {
    __syncthreads();
#pragma unroll
    for (int i = 0; i < MT * 3 / 4; i++) {
      const int n = i * 512 + tid, r = n / 24, q = (n - r * 24) * 16;
      __builtin_amdgcn_raw_buffer_store_b128(*(const u32x4*)(smem + 98304 + r * 400 + q), rc, r * 2304 + q, 0, OSC);
    }
  }
  __syncthreads();
#undef OT_DMA
#undef OT_LDA
}

#define CLAIMS() do { const int l_ = TIDX; if (l_ < 64) { \
    const u32 v_ = ld4_sc1(hot + SY_OQ(l_ < 6 ? l_ : 0)); \
    if (l_ == 0) pre = (int)__hip_atomic_fetch_add(hot + SY_OQ(6), 1u, __ATOMIC_RELAXED, __HIP_MEMORY_SCOPE_AGENT); \
    pre |= (int)((u32)__ballot(l_ < 6 && v_ < ((l_ & 1) ? NOB : NOA)) << 16); } } while (0)
#define CLS 51
#define KDUE 112
#define KDL 84
#define KEAR 80
#define TPS 2
__device__ __forceinline__ void small_out_task(const Params& p, int rq, int cp, char* smem, const int wv, u32* hot, int& pre) {
  const int tid = TIDX, lane = tid & 63, wid = wv;
  const int l15 = lane & 15, lq = lane >> 4;
  const __amdgpu_buffer_rsrc_t rw = RSRC(WSP(char, O_WOF) + WOB, WOB), ro = RSRC(WSP(u16, O_OB), 2048 * 4096 * 2);
  if (tid == 0) {
    int it = 0;
    while (ld4_sc1(hot + SY_CW) < 64u && ++it < SPIN_CAP) __builtin_amdgcn_s_sleep(8);
  }
  __syncthreads();
  bf8 wf[8][4];
#pragma unroll
  for (int k = 0; k < 8; k++)
#pragma unroll
    for (int j = 0; j < 4; j++)
      wf[k][j] = __builtin_bit_cast(bf8, __builtin_amdgcn_raw_buffer_load_b128(rw, (l15 >> 3) * 1024 + (lq * 16 + (l15 & 7)) * 16 + (j & 1) * 128,
                                                                              ((cp * 2 + (j >> 1)) * 128 + wid * 16 + k * 2) * 1024, 16));
  const int va = OB_OFF(rq * 8 + (l15 & 7), wid * 512 + (l15 >> 3) * 32 + lq * 8);
  const int wo = (lq >> 1) == (l15 >> 3) ? (wid * 2 + (lq >> 1)) * 272 + (l15 & 7) * 32 + (lq & 1) * 16 : 4 * 4352 + lane * 16;
  const int rd = (lane >> 4) * 4352 + (lane & 7) * 544 + wid * 32 + ((lane >> 3) & 1) * 16;
  u32 left = 0u, due = 0u, last = 0u, per = 240u * 64u;
  bool have = false;
  auto step = [&](u32x4 (&af)[8], u32x4 (&nf)[8], const int s) __attribute__((always_inline)) {
    if (due == 0u) {
      if (tid < 64) {
        u32 v;
        int it = 0;
        while (v = ld4_sc1(CSP(s - 2 - (lane & 1))), __any(v == 0u) && ++it < SPIN_CAP) __builtin_amdgcn_s_sleep(SPS);
        *(u32*)(smem + 40960 + (lane & 1) * 4) = v;
      }
      asm volatile("s_waitcnt lgkmcnt(0)" ::: "memory"); __builtin_amdgcn_s_barrier(); asm volatile("" ::: "memory");
      due = __builtin_amdgcn_readfirstlane(*(const u32*)(smem + 40960));
      last = __builtin_amdgcn_readfirstlane(*(const u32*)(smem + 40964));
    }
    per = umin(per, due - last);
    last = due;
    int n = 400, late;
    while (late = (int)(((u32)wall_clock64() << 6) - due - per) - (s == 63 ? KDL : KDUE) * 64, late < 0 && --n > 0) {}
    if (s == 63) TLS(1);
    const u32 v = __builtin_amdgcn_raw_buffer_load_b32(RSRC(CSP(0), wid ? 0 : 4096), 0, (s - 1) * 64, 16);
    if (s == CLS) left = __builtin_amdgcn_raw_buffer_load_b32(RSRC(hot, wid ? 0 : 4096), SY_OQ(lane < 6 ? lane : 6) * 4, 0, 16);
    SB0();
    auto rows = [&](u32x4 (&f)[8], const int so) __attribute__((always_inline)) {
#pragma unroll
      for (int k = 0; k < 8; k++) f[k] = __builtin_amdgcn_raw_buffer_load_b128(ro, va, so + k * 4096, 16);
      SB0();
    };
    auto bad = [&]() __attribute__((always_inline)) {
      u32 t = 0u;
#pragma unroll
      for (int k = 0; k < 8; k++) t = umax(umax(t, af[k][0]), af[k][2]);
      return __any(t == ~0u);
    };
    if (!have) rows(af, s * 262144);
    have = late >= (int)per - KEAR * 64 && s < 63;
    if (KEAR && have) while ((int)(((u32)wall_clock64() << 6) - due - 2 * per) < (s == 62 ? KDL : KDUE) * 64 && --n > 0) {}
    rows(nf, have ? (s + 1) * 262144 : PJ_OOB);
    if (wid == 0) *(u32*)(smem + 40968 + (s & 1) * 4) = v;
    if (__builtin_expect(bad(), 0)) {
      int it = 0;
      do { __builtin_amdgcn_s_sleep(TPS); CFENCE(); rows(af, s * 262144); } while (bad() && ++it < SPIN_CAP);
    }
    SB0();
    f4 acc[4];
#pragma unroll
    for (int k = 0; k < 8; k++)
#pragma unroll
      for (int j = 0; j < 4; j++) acc[j] = MFMA(wf[k][j], __builtin_bit_cast(bf8, af[k]), k ? acc[j] : ((f4){0, 0, 0, 0}));
    char* const sm = smem + (s & 1) * 20480;
#pragma unroll
    for (int j = 0; j < 4; j++) *(f4*)(sm + wo + ((lq >> 1) == (l15 >> 3) ? j * 4352 : 0)) = acc[j];
    asm volatile("s_waitcnt lgkmcnt(0)" ::: "memory"); __builtin_amdgcn_s_barrier(); asm volatile("" ::: "memory");
    if (s == 63) TLS(2);
    due = __builtin_amdgcn_readfirstlane(*(const u32*)(smem + 40968 + (s & 1) * 4));
    f4 t = *(const f4*)(sm + rd) + *(const f4*)(sm + rd + 272);
#pragma unroll
    for (int r = 0; r < 4; r++) {
      t[r] += __uint_as_float(__builtin_amdgcn_mov_dpp(__float_as_uint(t[r]), 0xB1, 0xf, 0xf, true));
      t[r] += __uint_as_float(__builtin_amdgcn_mov_dpp(__float_as_uint(t[r]), 0x4E, 0xf, 0xf, true));
      t[r] += __uint_as_float(__builtin_amdgcn_mov_dpp(__float_as_uint(t[r]), 0x141, 0xf, 0xf, true));
    }
    __builtin_amdgcn_raw_buffer_store_b64((u32x2){pk2(t[0], t[1]), pk2(t[2], t[3])}, RSRC(p.out, 2048 * 2304), (lane & 7) == 0 ? lane : PJ_OOB,
                                          (s * 32 + rq * 8 + wid) * 2304 + 1152 + cp * 64, SSC);
  };
  u32x4 fa[8], fb[8];
#pragma unroll
  for (int k = 0; k < 8; k++) asm volatile("" : "=v"(fa[k]));
#pragma unroll 1
  for (int s = XS0; s < 64; s += 2) { step(fa, fb, s); step(fb, fa, s + 1); }
  pre = 72;
  if (__any(left < (lane > 5 ? 72u : (lane & 1) ? NOB : NOA))) CLAIMS();
}
#define PSTAMP()
#define STAMP(i)
#define YEND()
#define ANS 16
#define ANX(slot) (X_ANN ^ xp ^ ((slot) * X_HS))
#define KSL 1
#define DSH 2
#define K1 14
#define K3 8
#define DRL 0
#define CSL 5
#define CSC 0
#define KA 0
#define KB 0
#define KS0 0
#define KMASK 0x00FFFE000FE00000ull
#define KXT(k) ((k) ? (int)((KMASK >> s) & 1) * (k) : 0)
#define DOMAX 90
#define DO0 80
#define DH0 54
#define DOCAP 6
#define C_IMG 0
#define C_QT 9216
#define C_KT (C_QT + 3072)
#define C_P (C_KT + 4096)
#define C_PX (C_P + 768)
#define C_KX (C_PX + 2560)
#define C_VX (C_KX + 8192)
#define C_QX (C_VX + 8192)
#define C_XP 161024
#define CALR 1
#define GPS 6
#define C_WL 49920
__device__ __forceinline__ float ror8(float v) {
  return __uint_as_float(__builtin_amdgcn_mov_dpp(__float_as_uint(v), 0x128, 0xf, 0xf, true));
}
__device__ __forceinline__ float rb1(float f) { return __uint_as_float(pk2(0.f, f)); }
#define FMX(a, b) __builtin_elementwise_maximum(a, b)
__device__ __forceinline__ float tmax(float mx) {
  mx = FMX(mx, ror8(mx));
  { auto t = __builtin_amdgcn_permlane16_swap(__float_as_uint(mx), __float_as_uint(mx), false, false);
    mx = FMX(__uint_as_float(t[0]), __uint_as_float(t[1])); }
  { auto t = __builtin_amdgcn_permlane32_swap(__float_as_uint(mx), __float_as_uint(mx), false, false);
    mx = FMX(__uint_as_float(t[0]), __uint_as_float(t[1])); }
  return mx;
}
__device__ __forceinline__ int hmax(int x) {
  x = max(x, __builtin_amdgcn_mov_dpp(x, 0x128, 0xf, 0xf, true));
  x = max(x, __builtin_amdgcn_mov_dpp(x, 0x124, 0xf, 0xf, true));
  x = max(x, __builtin_amdgcn_mov_dpp(x, 0x4E, 0xf, 0xf, true));
  x = max(x, __builtin_amdgcn_mov_dpp(x, 0xB1, 0xf, 0xf, true));
  { auto t = __builtin_amdgcn_permlane16_swap((u32)x, (u32)x, false, false); x = max((int)t[0], (int)t[1]); }
  return x;
}
__device__ __forceinline__ int hsum(int x) {
  x += __builtin_amdgcn_mov_dpp(x, 0x128, 0xf, 0xf, true);
  x += __builtin_amdgcn_mov_dpp(x, 0x124, 0xf, 0xf, true);
  x += __builtin_amdgcn_mov_dpp(x, 0x4E, 0xf, 0xf, true);
  x += __builtin_amdgcn_mov_dpp(x, 0xB1, 0xf, 0xf, true);
  { auto t = __builtin_amdgcn_permlane16_swap((u32)x, (u32)x, false, false); x = (int)t[0] + (int)t[1]; }
  return x;
}
#define OEP 2
#define CCUT 64u
#define BAR() do { asm volatile("s_waitcnt lgkmcnt(0)" ::: "memory"); __builtin_amdgcn_s_barrier(); asm volatile("" ::: "memory"); } while (0)
#define BARC() do { asm volatile("" ::: "memory"); __builtin_amdgcn_s_waitcnt(0xC07F); __builtin_amdgcn_s_barrier(); asm volatile("" ::: "memory"); } while (0)
#define BARN() do { asm volatile("" ::: "memory"); __builtin_amdgcn_s_barrier(); asm volatile("" ::: "memory"); } while (0)
#define IDX() const int tid = TIDX, lane = tid & 63, l15 = lane & 15, lq = lane >> 4, q4 = l15 >> 2, p4 = lane & 3, hi8 = l15 >> 3; \
    (void)q4; (void)p4; (void)hi8; (void)lq
#define AD_LD(HEAD) __builtin_amdgcn_raw_buffer_load_b32(rsX, (TIDX & (HEAD ? 63 : 31)) * ANS, ANX(s & 1), 16)
#define AD_GET(HEAD, K, CAP) do { int n_ = 0; u32 an = AD_LD(HEAD); \
    while ((u32)__ballot((an & 63u) != (u32)s) != 0u && ++n_ < (CAP)) { __builtin_amdgcn_s_sleep(1); CFENCE(); an = AD_LD(HEAD); } \
    const u32 r_ = (u32)__builtin_amdgcn_readfirstlane((int)an); \
    const int m_ = hmax((TIDX & 32) ? (int)umin(an, DOMAX * 64u) : (int)(an - r_)); \
    dl = ((r_ + (u32)__builtin_amdgcn_readlane(m_, 0)) & ~63u) + (u32)(K) * 64u + (HEAD ? (u32)__builtin_amdgcn_readlane(m_, 32) : 0u); \
    if (!HEAD && !CSL && c == CSC && s > 0 && n_ < (CAP)) __builtin_amdgcn_raw_buffer_store_b32(dl | 1u, rsX, 0, (X_HOT(xp) ^ X_CS) + (s - 1) * 64, 16); } while (0)
#define AD_SPIN() do { int n_ = 160; while ((int)(((u32)wall_clock64() << 6) - dl) < 0 && --n_ > 0) {} } while (0)
#define STG_AUX 0
#define OSTG_AUX 0
#define STG_D 2
#define STG_POLL 7
#define STS(c, i)

#define NXA 1
#define NXE 0
#define ADE 1
#define NXH 1
#define NXV 1
#define NXB 4
#define NXS 3
#define PKA 18
#define PK_(w0, d, a, b, w1) ".long " #w0 " + " #d ", " w1 " + " #a " + " #b " * 512\n\t"
#define PKADD(d, a, b) PK_(0xD3B24000, d, a, b, "0x18020100")
#define PKMUL(d, a, b) PK_(0xD3B14000, d, a, b, "0x18020100")
#define PKFMS(d, a, b, c) PK_(0xD3B04400, d, a, b, "0x9C020100 + " #c " * 262144")
#define CV2(d, a) "v_cvt_pk_bf16_f32 " d ", v[" #a "], v[" #a "+1]\n\t"
#define VBP 0xA9A
#define C_Z 45824
#define SMALL 0x1p-64f
#define EK 0
#define EKN 2
#define EKL 1
#define EKA 1
#define BJ 2
#define KSP 0
#define SP1 2
#define SP2 0
#define KP 3
#define GX 1
#define GXO (GX == 1 ? 4096 : 1024)
#define ZE 0x0D
#define KD 5
#define QSL 0
#define DJ 9
#define VSL 0
#define VSL1 4
#define KV0 1
#define PVP 0
#define IPR 3
#define DSLOT 0x9876E1CBAD0ull
#define F_K (47360 - C_KT)
#define F_Q (47872 - 768 - C_QT)
#define HAL 2
#define OAL 2
#define ALIGN(n) asm volatile(".p2align %0" :: "i"(n))
#define QXP 4
#define RORD 0
#define PRIO(n) __builtin_amdgcn_s_setprio(n)
#define LSL 6
__device__ __forceinline__ int chain_head(const Params& p, char* smem, const int h, const int wv, const bool spec) {
  STS(h, 1);
  TLS(12);
  const int wid = wv;
  u32x8 w[2][9], wx[3];
  const __amdgpu_buffer_rsrc_t rsX = RSRC(XWIN(p), X_END);
  const __amdgpu_buffer_rsrc_t rsB = RSRC(WSP(u16, O_QB), 4 * ACB);
  const int cb = h >> 1;
  const int nvt = (wid & 3) == 0 ? 0 : (wid < 3 ? 2 : 1);
  const int vt = wid < 3 ? wid * 2 + 2 : (wid == 3 ? 0 : wid - 4);
  const int wl = C_WL + (nvt == 1 ? vt * 12288 : 49152 + (nvt == 2 ? wid - 1 : wid >> 2) * 30720);
  int xp;
  {
    IDX();
    u32x4 st[54], nk, nv, nq;
    u32 fl, fk;
    const int vw = ((tid >> 4) * 4096 + h * 128 + (tid & 15) * 8) * 2;
#define NTL 64
#define AH 1
    u32 tc[9], b1, old = 0u;
#define TEC 0
    const u16 *pq = p.BWq, *pk = p.BWk, *pv = p.BWv;
    asm volatile("" : "+s"(pq), "+s"(pk), "+s"(pv));
    const bool te = wid < 3 && (h & 3) == TEC;
    const int tm = te ? wid : (h & 3) - 1;
    const u64 pt = tm == 1 ? (u64)pk : tm == 2 ? (u64)pv : (u64)pq;
    const __amdgpu_buffer_rsrc_t rt = RSRC(pt, te || (tm && h != 31 && wid == 7) ? 576 * 4096 * 2 : 0);
    const int tv = lane < NTL ? lane * 8192 : PJ_OOB, ts_ = te ? 8064 : (h >> 2) * 1024 + 384;
    const __amdgpu_buffer_rsrc_t rq = RSRC(pq, 576 * 4096 * 2), rk = RSRC(pk, 576 * 4096 * 2), rv = RSRC(pv, 576 * 4096 * 2);
    auto req = [&](int kc) {
#pragma unroll
      for (int i = 0; i < 6; i++) {
        st[kc * 6 + i] = __builtin_amdgcn_raw_buffer_load_b128(i < 2 ? rq : (i < 4 ? rk : rv), vw, (kc * 64 + (i & 1) * 32) * 8192, STG_AUX);
        if (NTL && kc + AH < 9 && i == (kc ? 0 : 4)) { SB0(); tc[kc] = __builtin_amdgcn_raw_buffer_load_b32(rt, tv, te && (h >> 2) != kc ? PJ_OOB : (kc + AH) * 64 * 8192 + ts_, 0); }
      }
      SB0();
    };
#pragma unroll
    for (int kc = 0; kc < STG_D; kc++) { if (kc == 0) TLS(13); req(kc); __builtin_amdgcn_s_barrier(); SB0(); }
    TLS(14);
    auto cell = [](int row, int slot) { return (((row * 2 + slot) ^ ((((row >> 2) ^ (row >> 4)) & 1) << 2) ^ ((row >> 5) & 1)) << 5); };
    auto put = [&](int unit, int row, const u32x4& v) { *(u32x4*)(smem + unit * 4160 + cell(row, (tid >> 1) & 1) + (tid & 1) * 16) = v; };
    auto frag = [&](int tile) {
      bf4 tt[4];
#pragma unroll
      for (int kk = 0; kk < 4; kk++) {
        const int r0 = (kk >> 1) * 32 + (lq >> 1) * 16 + (q4 >> 1) * 8 + (lq & 1) * 4 + (kk & 1) * 2 + (q4 & 1);
        tt[kk] = trrd(smem + (tile >> 1) * 4160 + cell(r0, tile & 1) + p4 * 8);
      }
      return ilv(tt[0], tt[1], tt[2], tt[3]);
    };
    auto park = [&](int slot, const u32x8& f) {
      char* d = smem + wl + slot * 2048 + lane * 16;
      *(u32x4*)d = (u32x4){f[0], f[1], f[2], f[3]};
      *(u32x4*)(d + 1024) = (u32x4){f[4], f[5], f[6], f[7]};
    };
    const __amdgpu_buffer_rsrc_t rs0 = RSRC(p.ws, wid ? 0 : O_QA);
    auto poll = [&]() {
      fl = __builtin_amdgcn_raw_buffer_load_b32(rs0, (lane < 32 ? SY_INIT(lane) : SY_ARR) * 4, 0, 16);
      fk = __builtin_amdgcn_raw_buffer_load_b32(rs0, SY_BLK(lane < 12 ? lane >> 2 : 0, cb, lane & 3) * 4, 0, 16);
    };
    auto rows = [&](int vo) {
      nq = __builtin_amdgcn_raw_buffer_load_b128(rsB, vo, h * 8192, 16);
      nk = __builtin_amdgcn_raw_buffer_load_b128(rsB, vo, h * 8192 + ACB, 16);
      nv = __builtin_amdgcn_raw_buffer_load_b128(rsB, vo, h * 8192 + 2 * ACB, 16);
    };
    bool early = false;
    int lost = 0;
#pragma unroll
    for (int kc = 0; kc < 9; kc++) {
      if (kc == STG_D - 1) { if (spec && wid == 0) old = confirm(p, smem, h, b1, wv); SB0(); }
      if (kc == 10 - STG_D) { early = __all(fk >= FULL); CFENCE(); rows(early ? tid * 16 : PJ_OOB); poll(); SB0(); }
#pragma unroll
      for (int i = 0; i < 6; i++) put((i >> 1) * 4 + ((tid & 15) >> 2), (i & 1) * 32 + (tid >> 4), st[kc * 6 + i]);
      if (NTL && kc + AH < 9) asm volatile("" :: "v"(tc[kc]));
      if (kc == 2 * STG_D - 1) lost = (__builtin_amdgcn_readfirstlane(old) >> h) & 1;
      BAR();
      if (kc == 0) TLS(15);
      if (kc == 0) { b1 = __builtin_amdgcn_raw_buffer_load_b32(rs0, 0, SY_BEAT(h & 7) * 4, 16); SB0(); }
      if (kc + STG_D == 8) { fk = ld4_sc1(WSP(u32, 0) + SY_BLK(lane < 12 ? lane >> 2 : 0, cb, lane & 3)); SB0(); }
      if (kc + STG_D < 9) req(kc + STG_D);
      w[0][kc] = frag(wid * 2);
      w[1][kc] = frag(nvt == 2 && kc < 4 ? 16 + vt : wid * 2 + 1);
      const u32x8 tp = frag(nvt == 0 ? 21 + (wid >> 1) : (nvt == 2 && kc < 4 ? wid * 2 + 1 : 16 + vt));
      if (kc >= 4 && kc < 7) { wx[kc - 4] = tp; if (nvt == 0) park(6 + kc, tp); }
      else park(nvt == 0 ? 6 + kc : (kc >= 7 ? kc - 7 : 2 + kc), tp);
      BAR();
      if (kc == 0 || kc == 8) STS(h, kc + 3);
    }
    TLS(6);
    if (wid == 0) {
      int it = 0;
      while (!lost && !__all(fl >= (lane < 32 ? 1u : 256u) && fk >= FULL) && ++it < SPIN_CAP) { BEAT_ON(it); __builtin_amdgcn_s_sleep(GPS); CFENCE(); poll(); }
      if (lane == 0) *(int*)(smem + C_XP) = lost ? -1 : fl & 0x7800;
    }
    BAR();
    if (!early) rows(tid * 16);
    xp = __builtin_amdgcn_readfirstlane(*(const int*)(smem + C_XP));
    if (xp < 0) return xp;
    TLS(7);
    SB0();
    *(u32x4*)(smem + C_QX + tid * 16) = nq;
    *(u32x4*)(smem + C_KX + tid * 16) = nk;
    *(u32x4*)(smem + C_VX + ((tid * 16) ^ ((tid & 32) << 2))) = nv;
  }
  STS(h, 14);
  constexpr float CE = 0.08838834764831845f * 1.4426950408889634f;
  auto run = [&](auto RC) __attribute__((always_inline)) {
  constexpr int R = decltype(RC)::value;
  constexpr int NV = R < 2 ? 0 : (R < 4 ? 1 : 2);
  constexpr bool QW = !(R & 1);
  int known = 0;
  bf8 pre[4][2], vx[NV ? NV : 1];
  int sidx, mo, io, eo, gvo, wvo = 0, so = 0, po = 0, qo = 0, ko = 0, fo = 0, fq = 0, fk = 0;
  float mk[4], mm = 0.f;
  f2v cw = {CE, CE};
#define LDW(dst, slot) do { dst[0] = *(const bf8*)(smem + wvo + (slot) * 2048); dst[1] = *(const bf8*)(smem + wvo + (slot) * 2048 + 1024); } while (0)
  {
    IDX();
    if constexpr (R == 1) {
      po = (tid * 16) ^ ((tid & 32) << 2);
      so = (lane >> 5) * 131072 + (lane & 31) * 16;
      asm volatile("" : "+v"(po), "+v"(so));
    } else if constexpr (R == 0) {
      qo = C_QT + lq * 384 + q4 * 32 + p4 * 8;
      po = C_P + l15 * 16 + lq * 4;
      fo = lq * 512 + l15 * 16;
#pragma unroll
      for (int r = 0; r < 4; r++) { mk[r] = lq * 4 + r <= l15 ? 0.f : -__builtin_inff(); asm volatile("" : "+v"(mk[r])); }
      mm = (l15 & 2) ? -__builtin_inff() : 0.f;
      asm volatile("" : "+v"(mm), "+{v[230:231]}"(cw));
      fq = C_QT + F_Q + (lq ? lq : 1) * 768 + lq * 8; fk = C_KT + F_K + lq * 776;
      asm volatile("" : "+v"(qo), "+v"(po), "+v"(fo), "+v"(fq), "+v"(fk));
      *(u32*)(smem + fq) = 0u; *(u32*)(smem + fk) = 0u;
    } else {
      wvo = wl + lane * 16;
      po = C_P + (lq * 4 + q4) * 16 + (p4 & 1) * 8;
      so = ((l15 >> 2) & 1) * 131072 + (((vt & 3) + (NV == 2 ? hi8 : 0)) * 8 + ((l15 >> 1) & 1) * 4 + (lq >> 1) * 2 + (l15 & 1)) * 16 + (lq & 1) * 8;
      if (NV == 1 && hi8) so = PJ_OOB;
      asm volatile("" : "+v"(wvo), "+v"(po), "+v"(so));
      if constexpr (NV == 2) {
        qo = po ^ ((p4 & 2) << 4); ko = (p4 & 2) ? po : C_Z; po = (p4 & 2) ? C_Z : po;
        *(u64*)(smem + C_Z + hi8 * 256) = 0ull;
        asm volatile("" : "+v"(ko), "+v"(po), "+v"(qo));
      }
#pragma unroll
      for (int j = 0; j < 4; j++) LDW(pre[j], NV == 2 ? 2 + j : j);
    }
    eo = C_QT + wid * 768 + ((l15 >> 2) * 2 + (l15 & 1)) * 32 + lq * 8 + ((l15 >> 1) & 1) * 4;
    sidx = (l15 & 2) ? 0xEEEE : 0x4444;
    mo = C_IMG + lane * 16;
    io = C_IMG + ((tid & ~30) | ((tid & 10) << 1) | ((tid & 20) >> 1)) * 16;
    gvo = tid * 16;
    asm volatile("" : "+v"(sidx), "+v"(mo), "+v"(io), "+v"(eo));
    if constexpr (R < 2) asm volatile("" : "+v"(gvo));
  }
  f4 a0, a1;
  u32x4 vm = {0u, 0u, 0u, 0u};
  if constexpr (NV > 0) asm volatile("" : "+v"(vm));
  u32 ts = 0;
  u64 tq = 0;
  int dur = DH0 * 64;
  const int ob = ((h >> 3) * 32768 + (h & 7) * 512) ^ xp ^ (NV == 1 ? X_HS : 0);
  const int ib = X_M ^ xp ^ ((wid >> 2) * 32768 + (wid & 3) * 1024);
  int b3 = 0;
  PRIO(QW ? 2 : 1);
#pragma unroll 1
  for (int s = 0;; s++) {
    int mb = ib + b3 * 65536, mbx = X_M ^ xp ^ (196608 + b3 * 1024);
    if constexpr (R == GX) asm volatile("" : "+s"(mb), "+s"(mbx));
    u32x4 g, gx;
    if constexpr (R != 1) ALIGN(HAL);
    BAR();
    STAMP(13);
    if constexpr (R == 1 && NXE) {
      asm volatile("buffer_load_dwordx4 %0, %2, %3, %4 offen sc1\n\tbuffer_load_dwordx4 %1, %2, %3, %5 offen sc1"
                   : "=&v"(g), "=&v"(gx) : "v"(mo), "s"(rsX), "s"(mb), "s"(mbx) : "memory");
    } else {
      g = __builtin_amdgcn_raw_buffer_load_b128(rsX, mo, mb, 16);
      if constexpr (R == GX) gx = __builtin_amdgcn_raw_buffer_load_b128(rsX, mo, mbx, 16);
    }
    SB0();
    if constexpr (R == 0) {
#pragma unroll
      for (int j = 0; j < 4; j++) { const char* kx_ = smem + C_KX + fo + j * 2048;
        pre[j >> 1][j & 1] = *(const bf8*)kx_; pre[2 + (j >> 1)][j & 1] = *(const bf8*)(kx_ + 256); }
    } else if constexpr (R > 1) {
      IDX();
#pragma unroll
      for (int d = 0; d < NV; d++) { const char* vb_ = smem + C_VX + (vt + d) * 1024 + (p4 >> 1) * 512 + (((lq * 4 + q4) ^ ((p4 >> 1) * 8)) << 4) + (p4 & 1) * 8;
        vx[d] = cat8(trrd(vb_), trrd(vb_ + 256)); }
    }
    SB0();
    a0 = zero4(); a1 = zero4();
    f4 c0, c1, a2;
#define A2INI() do { u64 z_; float m0_, m1_; asm volatile("v_mov_b32 %0, %3\n\tv_mov_b32 %1, %3\n\tv_mov_b64 %2, 0\n\ts_nop 1" : "=&v"(m0_), "=&v"(m1_), "=v"(z_) : "v"(mm)); \
                     const f2v zz_ = __builtin_bit_cast(f2v, z_); a2 = (f4){m0_, m1_, zz_[0], zz_[1]}; } while (0)
    if constexpr ((ZE >> R) & 1) {
      c1 = zero4();
      if constexpr (NV != 1) c0 = zero4();
      if constexpr (R == 0) { A2INI(); asm volatile("" : "+v"(a2)); }
    }
    int so_ = X_O(b3) ^ ob, an_ = ANX(s & 1) + h * ANS;
    int nb = s < 63 ? (s + 1) * 262144 + h * 8192 : PJ_OOB;
    asm volatile("" : "+s"(so_), "+s"(an_), "+s"(nb));
    u32x4 nx[2];
#define NXL1(g, i) __builtin_amdgcn_raw_ptr_buffer_load_lds(rsB, (LDSP void*)(smem + C_KX + (g) * 4096), 16, (g) < 2 ? gvo : po, \
                                                             nb + (1 + ((g) >> 1)) * ACB + ((g) & 1) * 4096 - 4096, (i) * 1024, 16)
#define NXL(g) do { NXL1(g, 0); NXL1(g, 1); NXL1(g, 2); NXL1(g, 3); } while (0)
    SB0();
    if (NXE) {
      BARC();
      if constexpr (R == 1) {
        if (NXE > 1) __builtin_amdgcn_s_sleep(NXE - 1);
#pragma unroll
        for (int i = 0; i < 4; i++)
          asm volatile("s_mov_b32 m0, %3\n\ts_nop 0\n\tbuffer_load_dwordx4 %0, %1, %2 offen sc1 lds\n\tbuffer_load_dwordx4 %0, %1, %2 offen offset:1024 sc1 lds\n\t"
                       "buffer_load_dwordx4 %0, %1, %2 offen offset:2048 sc1 lds\n\tbuffer_load_dwordx4 %0, %1, %2 offen offset:3072 sc1 lds"
                       :: "v"(i < 2 ? gvo : po), "s"(rsB), "s"(nb + (1 + (i >> 1)) * ACB + (i & 1) * 4096 - 4096), "i"(C_KX + i * 4096) : "memory", "m0");
      }
      SB0();
    }
#define LDM(j) *(const bf8*)(smem + mo + (j) * 1024)
    constexpr int NB = R == 1 ? 9 : 5;
    bf8 b[NB];
    if constexpr (!QW) {
      PRIO(IPR); BARN();
      if (((EK >> R) & 1) && !EKL) {
#pragma unroll
        for (int j = 0; j < EKN; j++) { b[j] = LDM(j); SB0(); }
      }
    }
    {
      if constexpr (R == 1 && NXE) asm volatile("s_waitcnt vmcnt(17)" : "+v"(g));
      else __builtin_amdgcn_s_waitcnt(R == GX ? 0x0F71 : 0x0F70);
      CFENCE();
      *(u32x4*)(smem + io) = g;
      SB0();
      u32 top = umax(umax(umax(g[0], g[1]), g[2]), g[3]);
      if constexpr (R == GX) {
        asm volatile("" : "+v"(top));
        SB0();
        if constexpr (R == 1 && NXE) asm volatile("s_waitcnt vmcnt(16)" : "+v"(gx));
        *(u32x4*)(smem + GXO + io) = gx;
        SB0();
        top = umax(umax(umax(umax(top, gx[0]), gx[1]), gx[2]), gx[3]);
      }
#define G_BAD() (umax(umax(umax(g[0], g[1]), umax(g[2], g[3])), R != GX ? 0u : umax(umax(gx[0], gx[1]), umax(gx[2], gx[3]))) == ~0u)
      if (__builtin_expect(__any(top == ~0u), 0)) {
        int it = 0;
        do {
          CFENCE();
          if (G_BAD()) {
            g = __builtin_amdgcn_raw_buffer_load_b128(rsX, mo, mb, 16);
            if constexpr (R == GX) gx = __builtin_amdgcn_raw_buffer_load_b128(rsX, mo, mbx, 16);
          }
          VMW0C();
        } while (__any(G_BAD()) && ++it < SPIN_CAP);
        *(u32x4*)(smem + io) = g;
        if constexpr (R == GX) *(u32x4*)(smem + GXO + io) = gx;
      }
    }
    SB0();
    STAMP(15);
    if constexpr (!QW) PRIO(1);
    if (((EK >> R) & 1) && EKL) {
#pragma unroll
      for (int j = 0; j < EKN; j++) { b[j] = LDM(j); SB0(); }
      CFENCE(); __builtin_amdgcn_s_waitcnt(0xC07F | (EKN << 8)); BARN();
    } else
    BARC();
    STAMP(0);
#define ANNC() do { ts = (u32)tq; __builtin_amdgcn_raw_buffer_store_b32((((ts << 6) + (u32)dur) & ~63u) | (u32)s, rsX, 0, an_, 16); SB0(); } while (0)
    if constexpr (R == 1) {
      STAMP(6);
      if (EK & 2) asm volatile("s_memrealtime %0" : "=s"(tq));
      else { asm volatile("s_memrealtime %0\n\ts_waitcnt lgkmcnt(0)" : "=s"(tq)); ANNC(); }
      SB0();
#pragma unroll
      for (int j = (EK & 2) ? EKN : 0; j < 9; j++) { b[j] = LDM(j); SB0(); }
#pragma unroll
      for (int i = 0; i < NXA; i++) { NXL(i); SB0(); }
    } else {
      if (!QW && KSP) __builtin_amdgcn_s_sleep(KSP);
#pragma unroll
      for (int j = ((EK >> R) & 1) ? EKN : 0; j < 4; j++) { b[j] = LDM(j); SB0(); }
    }
#pragma unroll
    for (int j = 0; j < 9; j++) {
      if constexpr (QW) {
        if (j == BJ) {
          BARN();
#pragma unroll
          for (int i = 4; i < 4 + BJ; i++) {
            b[i % 5] = LDM(i); SB0();
          }
        }
        if (j >= BJ && j + 4 < 9) b[(j + 4) % 5] = LDM(j + 4);
      } else if (R != 1 && j + 4 < 9) b[(j + 4) % 5] = LDM(j + 4);
      SB0();
      a0 = SMF(b[j % NB], w[0][j], a0, sidx);
      if (NV < 2 || j >= 4) a1 = SMF(b[j % NB], w[1][j], a1, sidx);
      else a1 = SMF(b[j % 5], cat16(pre[j & 3][0], pre[j & 3][1]), a1, sidx);
      SB0();
      if (R == 1 && (EK & 2) && j == EKA) { asm volatile("s_waitcnt lgkmcnt(0)" : "+s"(tq)); ANNC(); }
      if (j == 0) STAMP(10);
      if (j == 4) STAMP(11);
      if (j == 8) STAMP(12);
      if (NV == 2 && j >= DJ && j < DJ + 4) LDW(pre[j - DJ], (DSLOT >> ((j - DJ) * 4)) & 15);
    }
    {
      {
        const u32 e0 = pk2(a0[0] + a0[2], a0[1] + a0[3]), e1 = pk2(a1[0] + a1[2], a1[1] + a1[3]);
        asm volatile("ds_write2_b32 %0, %1, %2 offset1:%3" :: "v"(eo), "v"(e0), "v"(e1), "i"(96) : "memory");
      }
      if constexpr (R != 0) asm volatile("ds_write_b32 %0, %1 offset:%2" :: "v"(eo), "v"(sidx), "i"(QW ? F_Q : F_K) : "memory");
    }
    SB0();
    STAMP(1);
    if constexpr (QW) PRIO(R ? 0 : SP1);
    else if (KV0) PRIO(0);
    if constexpr ((ZE >> R) & 1) { a0 = c0; a1 = c1; }
    else if constexpr (R != 1) { a1 = zero4(); if constexpr (NV != 1) a0 = zero4(); if constexpr (R == 0) A2INI(); }
    if constexpr (NV == 0) {
      u32 dl = 0;
      const int c = 1;
      bf8 km[2];
      if constexpr (R == 0) {
        if (QSL) __builtin_amdgcn_s_sleep(QSL);
        bf8 qa[2];
        u32 fl;
        do {
          CFENCE();
          fl = *(const volatile LDSP u32*)(smem + fq);
          CFENCE(); SB0();
#pragma unroll
          for (int i = 0; i < 2; i++) { qa[i] = cat8(trrd(smem + qo + i * 1536), trrd(smem + qo + i * 1536 + 128)); SB0(); }
        } while (__builtin_expect(__any(fl == 0u), 0));
        STAMP(2);
        auto rdk = [&]() __attribute__((always_inline)) {
          CFENCE(); SB0();
          fl = *(const volatile LDSP u32*)(smem + fk);
          CFENCE(); SB0();
#pragma unroll
          for (int i = 0; i < 2; i++) { km[i] = cat8(trrd(smem + qo + 3072 + i * 1536), trrd(smem + qo + 3200 + i * 1536)); SB0(); }
        };
        auto mulk = [&]() __attribute__((always_inline)) {
#pragma unroll
          for (int i = 0; i < 2; i++) { a2 = MFMA(qa[i], km[i], a2); if (!i) asm volatile("" : "+{v[240:243]}"(a2)); SB0(); }
        };
#pragma unroll
        for (int i = 0; i < 2; i++) {
          a0 = SMF(qa[i], cat16(pre[i][0], pre[i][1]), a0, sidx); asm volatile("" : "+v"(a0)); SB0();
          a1 = SMF(qa[i], cat16(pre[2 + i][0], pre[2 + i][1]), a1, sidx); if (!i) asm volatile("" : "+v"(a1)); SB0();
        }
#define EX2(d) "v_exp_f32 v[" #d "], v[" #d "]\n\tv_exp_f32 v[" #d "+1], v[" #d "+1]\n\t"
#define DP2(OP, d, N) OP " v[" #d "], v[" #d "], v[" #d "] row_ror:" #N " row_mask:0xf bank_mask:0xf\n\t" OP " v[" #d "+1], v[" #d "+1], v[" #d "+1] row_ror:" #N " row_mask:0xf bank_mask:0xf\n\t"
#define ROW2(OP, d) DP2(OP, d, 8) "s_nop 0\n\t" DP2(OP, d, 4) "s_nop 0\n\t" DP2(OP, d, 2) "s_nop 0\n\t" DP2(OP, d, 1)
#define S_F PKFMS(234, 232, 230, 244) PKFMS(238, 236, 230, 244)
#define S_E EX2(234) EX2(238)
#define S_IO "+{v[232:235]}"(a0), "+{v[236:239]}"(a1)
        f4 z;
#define S_0 "s_setprio %[pr]\n\ts_nop 5\n\t" PKADD(232, 232, 234) PKADD(236, 236, 238) "s_nop 0\n\t" \
            "v_mul_f32_dpp v244, v232, v230 row_newbcast:0 row_mask:0xf bank_mask:0xf\n\tv_mul_f32_dpp v245, v233, v230 row_newbcast:0 row_mask:0xf bank_mask:0xf\n\t" S_F
        if (KP == 3) asm volatile(S_0 S_E "s_setprio 2" : S_IO, "=&{v[244:247]}"(z) : "{v[230:231]}"(cw), [pr] "i"(SP2));
        if (KP == 2) asm volatile(S_0 EX2(234) "s_setprio 2" : S_IO, "=&{v[244:247]}"(z) : "{v[230:231]}"(cw), [pr] "i"(SP2));
        if (KP == 1) asm volatile(S_0 "s_setprio 2" : S_IO, "=&{v[244:247]}"(z) : "{v[230:231]}"(cw), [pr] "i"(SP2));
        STAMP(3);
        if (KD) asm volatile("s_nop %0" :: "i"(KD - 1));
        SB0();
        rdk();
#define S_X PKADD(246, 234, 238)
#define S_M PKFMS(242, 240, 230, 244) EX2(242) "s_nop 0\n\t" PKADD(246, 246, 242) "s_nop 1\n\t" ROW2("v_add_f32_dpp", 246) "v_rcp_f32 v246, v246\n\tv_rcp_f32 v247, v247\n\ts_nop 0\n\t" \
            PKMUL(234, 234, 246) PKMUL(238, 238, 246) PKMUL(242, 242, 246) \
            CV2("v234", 234) CV2("v238", 238) CV2("v242", 242) "ds_write2st64_b32 %[po], v234, v238 offset1:1\n\tds_write_b32 %[po], v242 offset:512\n\tv_min_f32 v246, v246, v247"
        if (KP == 3) asm volatile(S_X : S_IO, "+{v[244:247]}"(z));
        if (KP == 2) asm volatile(EX2(238) "s_nop 0\n\t" S_X : S_IO, "+{v[244:247]}"(z));
        if (KP == 1) asm volatile(S_E "s_nop 0\n\t" S_X : S_IO, "+{v[244:247]}"(z));
        SB0();
        if (__builtin_expect(__any(fl == 0u), 0)) {
          asm volatile("s_nop 0");
          do {
            rdk();
          } while (__any(fl == 0u));
        }
        mulk();
        STAMP(4);
#define S_IO2 S_IO, "+{v[240:243]}"(a2), "+{v[244:247]}"(z) : "{v[230:231]}"(cw), [po] "v"(po) : "memory"
        asm volatile("s_nop 7\n\tv_add_f32_dpp v240, v242, v240 quad_perm:[2,3,0,1] row_mask:0xf bank_mask:0xf\n\tv_add_f32_dpp v241, v243, v241 quad_perm:[2,3,0,1] row_mask:0xf bank_mask:0xf\n\t" S_M : S_IO2);
        SB0();
        if (__builtin_expect(__any(z[2] < SMALL), 0)) {
          asm volatile("v_max3_f32 v244, v232, v236, v240\n\tv_max3_f32 v245, v233, v237, v241\n\ts_nop 0\n\t" ROW2("v_max_f32_dpp", 244) PKMUL(244, 244, 230) S_F S_E "s_nop 0\n\t" S_X S_M : S_IO2);
        }
        SB0();
      } else {
#pragma unroll
        for (int i = NXA; i < (NXV ? NXH : NXB) && !NXE; i++) NXL(i);
        SB0();
        const int rb = X_O(b3 == 0 ? 2 : b3 - 1) ^ ob;
        const u32x4 e = ones4();
        __builtin_amdgcn_raw_buffer_store_b128(e, rsX, so, rb, 16);
        __builtin_amdgcn_raw_buffer_store_b128(e, rsX, so, rb ^ X_HS, 16);
        PSTAMP();
      }
      STAMP(5);
      BARC();
      if constexpr (R == 0) STAMP(6);
      if constexpr (R == 1) {
        const u32 tp = (u32)wall_clock64(); dur += ((int)umin((tp - ts) << 6, 200u * 64u) - dur) >> DSH;
        SB0();
        if (ADE && s < 63) AD_GET(true, K1 + K3 + KXT(KA + KB) + (s ? 0 : KS0), 4096);
        if (NXB < 4) {
          __builtin_amdgcn_s_sleep(NXS);
#pragma unroll
          for (int i = NXB; i < 4; i++) NXL(i);
          BARN();
        }
        ALIGN(HAL);
        VMW0C();
        STAMP(8);
        if (NXB == 4) BARN();
        if (s == 63) break;
      } else {
        *(u32*)(smem + fq) = 0u; *(u32*)(smem + fk) = 0u;
        bf8 qx[2][4];
#pragma unroll
        for (int ks = 0; ks < 8; ks++) { qx[ks >> 2][ks & 3] = *(const bf8*)(smem + C_QX + (ks >> 2) * 256 + fo + (ks & 3) * 2048); if (ks < QXP) SB0(); }
#pragma unroll
        for (int NT = 1; NT <= 2; NT++) {
          f4 sc[2], sm = zero4();
          const bf8* qf = qx[NT - 1];
#pragma unroll
          for (int ks = 0; ks < 4; ks++) {
#pragma unroll
            for (int j = 0; j < NT; j++) sc[j] = MFMA(pre[2 * j + (ks >> 1)][ks & 1], qf[ks], ks ? sc[j] : (j == NT - 1 ? ((f4){mk[0], mk[1], mk[2], mk[3]}) : ((f4){0, 0, 0, 0})));
            if (ks & 1) sm = SMF(km[ks >> 1], cat16(qf[ks - 1], qf[ks]), sm, sidx);
          }
          sm[0] += sm[2]; sm[1] += sm[3];
          float mx = FMX3(FMX3(sc[0][0], sc[0][1], sc[0][2]), sc[0][3], FMX(sm[0], sm[1]));
          if (NT == 2) mx = FMX3(FMX3(mx, sc[1][0], sc[1][1]), sc[1][2], sc[1][3]);
          { auto t = __builtin_amdgcn_permlane16_swap(__float_as_uint(mx), __float_as_uint(mx), false, false);
            mx = FMX(__uint_as_float(t[0]), __uint_as_float(t[1])); }
          { auto t = __builtin_amdgcn_permlane32_swap(__float_as_uint(mx), __float_as_uint(mx), false, false);
            mx = FMX(__uint_as_float(t[0]), __uint_as_float(t[1])); }
          mx *= -CE;
#pragma unroll
          for (int j = 0; j < NT; j++)
#pragma unroll
            for (int r = 0; r < 4; r++) sc[j][r] = __builtin_amdgcn_exp2f(__builtin_fmaf(sc[j][r], CE, mx));
          sm[0] = __builtin_amdgcn_exp2f(__builtin_fmaf(sm[0], CE, mx)); sm[1] = __builtin_amdgcn_exp2f(__builtin_fmaf(sm[1], CE, mx));
          float sum = ((sc[0][0] + sc[0][1]) + (sc[0][2] + sc[0][3])) + (sm[0] + sm[1]);
          if (NT == 2) sum += (sc[1][0] + sc[1][1]) + (sc[1][2] + sc[1][3]);
          { auto t = __builtin_amdgcn_permlane16_swap(__float_as_uint(sum), __float_as_uint(sum), false, false);
            sum = __uint_as_float(t[0]) + __uint_as_float(t[1]); }
          sum = swadd(sum, sum);
          const float inv = __builtin_amdgcn_rcpf(sum);
          *(u32x4*)(smem + C_PX + (NT - 1) * 1024 + gvo) = (u32x4){pk2(sc[0][0] * inv, sc[0][1] * inv), pk2(sc[0][2] * inv, sc[0][3] * inv),
                                                                  NT == 2 ? pk2(sc[1][0] * inv, sc[1][1] * inv) : 0u, NT == 2 ? pk2(sc[1][2] * inv, sc[1][3] * inv) : 0u};
          *(u32*)(smem + C_PX + 2048 + (NT - 1) * 256 + (gvo >> 2)) = pk2(sm[0] * inv, sm[1] * inv);
        }
        STAMP(8);
        BAR();
        if (s == 63) break;
        if (known < (s < 61 ? s + 3 : 64)) {
          int n = 0;
          while (true) {
            u32 bf[3];
#pragma unroll
            for (int i = 0; i < 3; i++) bf[i] = ld4_sc1(WSP(u32, 0) + SY_BLK(i, cb, gvo >> 4));
            const u64 miss = __ballot(!(bf[0] >= FULL && bf[1] >= FULL && bf[2] >= FULL));
            known = miss ? __builtin_ctzll(miss) : 64;
            if (known >= (s < 61 ? s + 3 : 64) || ++n >= SPIN_CAP) break;
            __builtin_amdgcn_s_sleep(KSL);
          }
        }
      }
      STAMP(9);
      YEND();
      if constexpr (R == 1) { if (!ADE) AD_GET(true, K1 + K3, 4096); AD_SPIN(); }
    } else {
      if (NV == 2 && DJ > 5) {
#pragma unroll
        for (int j = 0; j < 4; j++) LDW(pre[j], (DSLOT >> (j * 4)) & 15);
        SB0();
      }
      if (QW && (NV == 2 ? VSL : VSL1)) __builtin_amdgcn_s_sleep(NV == 2 ? VSL : VSL1);
      bf4 p0, p1, p2, p3, m0, m1;
      auto getp = [&]() __attribute__((always_inline)) {
        if (QW && PVP) PRIO(2);
        if (!QW && KV0) PRIO(1);
        SB0();
        STAMP(5);
        BARN();
        p0 = trrd(smem + po); p1 = trrd(smem + po + 256);
        SB0();
        if constexpr (NV == 2) { p2 = trrd(smem + ko); p3 = trrd(smem + ko + 256); }
        SB0();
        m1 = trrd(smem + (NV == 2 ? qo : po) + 512);
        SB0();
      };
      constexpr int VB = (VBP >> (R * 4 - 8)) & 15;
      if constexpr (NV == 2) {
        int u = 0;
#pragma unroll
        for (int i = 0; i < 9; i++) {
          const int bs = (0x31042 >> ((i % 5) * 4)) & 7;
#define RING(a) do { a = SMF(b[bs], cat16(pre[u & 3][0], pre[u & 3][1]), a, sidx); SB0(); \
                     if (u < 7) LDW(pre[u & 3], (DSLOT >> (u * 4 + 16)) & 15); u++; } while (0)
          SB0();
          if (i == VB) getp();
          if (i == 0 || i == 4) RING(a0);
          else a0 = SMF(b[bs], i < 4 ? wx[i - 1] : w[1][i - 5], a0, sidx);
          SB0();
          RING(a1);
          if (i < 4) b[bs] = LDM(i);
        }
      } else {
#pragma unroll
        for (int i = 0; i < 9; i++) {
          SB0();
          if (i == VB) getp();
          if (i < 3) a1 = SMF(b[(i + 4) % 5], wx[i], a1, sidx);
          else a1 = SMF(b[(i + 4) % 5], cat16(pre[(i + 1) & 3][0], pre[(i + 1) & 3][1]), a1, sidx);
          SB0();
          if (i < 4) b[(i + 4) % 5] = LDM(i);
          if (i == 3 || i == 4) LDW(pre[(i + 1) & 3], 1 + i);
        }
      }
      if (VB == 9) getp();
      if constexpr (NV == 2) { vm[0] = pk2(a0[0] + a0[2], a0[1] + a0[3]); SB0(); }
      vm[NV - 1] = pk2(a1[0] + a1[2], a1[1] + a1[3]);
      asm volatile("" : "+v"(vm));
      SB0();
      if (VB > 9) getp();
      {
        const u32x8 pp = ilv(m0, m1, p0, p1);
        const bf8 pq = cat8(p2, p3);
        f4 o = MFMA(vx[0], __builtin_bit_cast(bf8, __builtin_shufflevector(pp, pp, 4, 5, 6, 7)), ((f4){0, 0, 0, 0})); asm volatile("" : "+v"(o)); SB0();
        if constexpr (NV == 2) { o = MFMA(vx[1], pq, o); asm volatile("" : "+v"(o)); SB0(); }
        o = MFMA(__builtin_bit_cast(bf8, vm), __builtin_bit_cast(bf8, __builtin_shufflevector(pp, pp, 2, 3, 4, 5)), o);
        asm volatile("" : "+v"(o));
        asm volatile("" :: "v"(pp));
        if constexpr (NV == 2) asm volatile("" :: "v"(pq));
        SB0();
        __builtin_amdgcn_raw_buffer_store_b64((u32x2){umin(pk2(o[0], o[1]), 0xFFFFFFFEu), pk2(o[2], o[3])}, rsX, so, so_, 16);
        SB0();
      }
      STAMP(7);
      if (QW && !PVP) PRIO(2);
      int xo, lo, pa = 0;
      {
        IDX();
        if constexpr (NV == 1) {
          __builtin_amdgcn_s_sleep(LSL);
          if (NXV && vt >= NXH) {
            asm volatile("s_mov_b32 m0, %3\n\ts_nop 0\n\tbuffer_load_dwordx4 %0, %1, %2 offen sc1 lds\n\tbuffer_load_dwordx4 %0, %1, %2 offen offset:1024 sc1 lds\n\t"
                         "buffer_load_dwordx4 %0, %1, %2 offen offset:2048 sc1 lds\n\tbuffer_load_dwordx4 %0, %1, %2 offen offset:3072 sc1 lds"
                         :: "v"((lane * 16) ^ ((lane & (vt * 16) & 32) << 2)), "s"(rsB), "s"(nb + (1 + (vt >> 1)) * ACB + (vt & 1) * 4096), "s"(C_KX + vt * 4096) : "memory", "m0");
            SB0();
          }
#pragma unroll
          for (int i = 0; i < 2; i++) nx[i] = __builtin_amdgcn_raw_buffer_load_b128(rsB, lane * 16 + i * 1024, nb + vt * 2048, 16);
          pa = C_QX + vt * 2048 + lane * 16;
          asm volatile("" : "+v"(pa));
        }
        xo = OB_OFF((lq & 1) * 16 + l15, (lq >> 1) * 8);
        lo = lane * 4;
        asm volatile("" : "+v"(xo), "+v"(lo));
#pragma unroll
        for (int j = 0; j < 4; j++) LDW(pre[j], NV == 2 ? 2 + j : j);
      }
      SB0();
      BARN();
      STAMP(8);
      {
        f4 o[2][NV];
#pragma unroll
        for (int it = 0; it < 2; it++) {
          const bf8 pf = *(const bf8*)(smem + C_PX + it * 1024 + lo * 4);
          const u32 pm = *(const u32*)(smem + C_PX + 2048 + it * 256 + lo);
#pragma unroll
          for (int d = 0; d < NV; d++) {
            o[it][d] = MFMA(vx[d], pf, ((f4){0, 0, 0, 0}));
            o[it][d] = MFMA(__builtin_bit_cast(bf8, vm), __builtin_bit_cast(bf8, d ? (u32x4){0u, pm, 0u, 0u} : (u32x4){pm, 0u, 0u, 0u}), o[it][d]);
          }
        }
#pragma unroll
        for (int d = 0; d < NV; d++)
          __builtin_amdgcn_raw_buffer_store_b128(pair16(umin(pk2(o[0][d][0], o[0][d][1]), 0xFFFFFFFEu), pk2(o[0][d][2], o[0][d][3]),
                                                        umin(pk2(o[1][d][0], o[1][d][1]), 0xFFFFFFFEu), pk2(o[1][d][2], o[1][d][3])), rsB,
              xo, 3 * ACB + OB_OFF(s * 32, h * 128 + (vt + d) * 16), 16);
        SB0();
        if (s == 63) break;
        if constexpr (NV == 1) { *(u32x4*)(smem + pa) = nx[0]; *(u32x4*)(smem + pa + 1024) = nx[1]; }
      }
      STAMP(9);
      YEND();
    }
    b3 = b3 == 2 ? 0 : b3 + 1;
    STAMP(14);
  }
  };
  if (wid == 0) run(std::integral_constant<int, 0>{});
  else if (wid == 4) run(std::integral_constant<int, 1>{});
  else if (wid > 4) run(std::integral_constant<int, 3>{});
  else if (wid == 3) run(std::integral_constant<int, 2>{});
  else run(std::integral_constant<int, 4>{});
  PRIO(0);
  TLS(0);
  return xp;
}

__device__ __forceinline__ int chain_out(const Params& p, char* smem, const int c, const int wv, const bool spec) {
  STS(32 + c, 1);
  constexpr int R = 5; (void)R;
  const int wid = wv, tq = c & 1, cg = c >> 1;
  u32x8 w[4][5];
  const __amdgpu_buffer_rsrc_t rsX = RSRC(XWIN(p), X_END);
  int xp = 0, mo, wo, ro;
  const int ms = wid < 4 ? 65536 : 1024;
#define OST 0
  const int r1 = tq & OST, r2 = cg & (OST >> 1);
  {
    IDX();
    mo = wid < 4 ? (cg >> 3) * 32768 + (cg & 7) * 512 + tq * 256 + wid * 64 : 196608 + (cg >> 2) * 512 + tq * 256 + (cg & 3) * 64;
    if (wid > 4 || (wid == 4 && cg >= 8)) mo = PJ_OOB;
    mo ^= X_M;
    u32 m0[2], old = 0u;
    if (wid == 0) {
#pragma unroll
      for (int i = 0; i < 2; i++) m0[i] = *(const GLB u32*)(p.Bm + (tq * 4 + q4) * 576 + (i ? 512 + (cg & 7) * 8 : cg * 32 + lq * 8) + p4 * 2);
      const int cal = X_CAL + c * 128;
      __builtin_amdgcn_raw_buffer_store_b32(0u, rsX, lane < 8 ? lane * 4096 : PJ_OOB, cal, 16);
      const u32 b1 = BEAT_LD(c);
      asm volatile("" :: "v"(m0[0]), "v"(m0[1]));
      VMW0();
      if (spec) old = confirm(p, smem, 32 + c, b1, wv);
      TLS(8);
      u32 dv = ~0u;
      int side = 0;
      u32 t0 = 0u, dt = ~0u;
      int at = cal;
      const int vz = lane ? PJ_OOB : 0;
#pragma unroll 1
      for (int i = 1; i < 4 * CALR + 4; i++) {
        asm volatile("buffer_store_dword %0, %0, %1, %2 offen sc1" :: "v"(vz), "s"(rsX), "s"(at + side) : "memory");
        SB0();
        dv = lane == (i > 4 ? (i - 1) & 3 : 4) ? umin(dv, dt) : dv;
        at = cal + ((0x54105410200ull >> (i * 4)) & 15) * 4096;
        u32 lim = i == 3 ? dt : 0u;
        asm volatile("" : "+s"(at), "+s"(lim), "+v"(dv));
        SB0();
        u64 t1;
        asm volatile("s_waitcnt vmcnt(0)\n\ts_memtime %0\n\ts_waitcnt lgkmcnt(0)" : "=s"(t1) :: "memory");
        dt = (u32)t1 - t0; t0 = (u32)t1;
        side = dt < lim ? 8192 : side;
      }
      dv = lane == 3 ? umin(dv, dt) : dv;
      static_assert(CALR <= 2, "");
      u32 d[4];
#pragma unroll
      for (int k = 0; k < 4; k++) d[k] = __builtin_amdgcn_readlane(dv, k);
      const u32 dm = umin(umin(d[0], d[1]), umin(d[2], d[3]));
      if (lane == 0 && !((old >> c) & 1)) st4_sc1(WSP(u32, 0) + SY_VOTE(c), (umin((dm >> 4) + 1u, 63u) << 25) + (side << 11) + (umin(d[0] - dm, CCUT) >> 1) + (umin(d[1] - dm, CCUT) >> 1 << 6) + (umin(d[2] - dm, CCUT) >> 1 << 12) + (umin(d[3] - dm, CCUT) >> 1 << 18));
      TLS(11);
    }
    if (wid) BARN();
    SB0();
    const __amdgpu_buffer_rsrc_t rw = RSRC(p.BWo, 576 * 4096 * 2);
    u32x4 st[4][8];
    u32 fl;
    bool early = false;
    const int va = ((lane >> 2) * 576 + cg * 32 + p4 * 8) * 2;
#pragma unroll
    for (int i = 0; i < 4; i++) {
#pragma unroll
      for (int k = 0; k < 8; k++) st[i][k] = __builtin_amdgcn_raw_buffer_load_b128(rw, va, ((((i & 1) ^ r1) * 16 + wid * 2 + ((k >> 2) ^ r2)) * 128 + (i >> 1) * 64 + (k & 3) * 16) * 1152, OSTG_AUX);
      SB0();
    }
    if (wid == 0) {
      BARN();
      u32 fv;
      int it = 0;
      while (fv = ld4_sc1(WSP(u32, 0) + SY_VOTE(lane & 31)), xp = -((__builtin_amdgcn_readfirstlane(old) >> c) & 1), !(xp || __all(fv != 0u)) && ++it < SPIN_CAP) {
        BEAT_ON(it); __builtin_amdgcn_s_sleep(2);
      }
      const bool up = __builtin_amdgcn_readfirstlane(hmax(-(int)(fv >> 24))) & 1;
      if ((bool)((fv >> 24) & 1u) != up) fv = 0u;
      const int s02 = __builtin_amdgcn_readfirstlane(hsum((fv & 63u) + ((fv << 4) & 0x3F0000u))), s13 = __builtin_amdgcn_readfirstlane(hsum(((fv >> 6) & 63u) + ((fv >> 2) & 0x3F0000u)));
      const int bk = min(min((s02 & 0xFFFF) * 8, (s13 & 0xFFFF) * 8 + 1), min((s02 >> 16) * 8 + 4, (s13 >> 16) * 8 + 5));
      xp |= ((bk & 7) + (up ? 2 : 0)) * 4096;
      if (xp >= 0) {
#pragma unroll
        for (int i = 0; i < 3; i++) __builtin_amdgcn_raw_buffer_store_b32(i ? ~0u : umin(m0[0], 0xFFFFFFFEu), rsX, lane * 4, mo ^ xp ^ (i * 65536), 16);
        __builtin_amdgcn_raw_buffer_store_b32(lq ? ~0u : umin(m0[1], 0xFFFFFFFEu), rsX, lq < 3 && cg < 8 ? lq * 1024 + l15 * 4 : PJ_OOB,
                                              (196608 + (cg >> 2) * 512 + tq * 256 + (cg & 3) * 64) ^ X_M ^ xp, 16);
#pragma unroll
        for (int i = 0; i < 6; i++)
          __builtin_amdgcn_raw_buffer_store_b128(ones4(), rsX, (lane >> 5) * 131072 + (lane & 31) * 16, X_O(i >> 1) ^ ((c >> 3) * 32768 + (c & 7) * 512) ^ xp ^ ((i & 1) * X_HS), 16);
        if (c == 0) {
#pragma unroll
          for (int i = 0; i < 6; i++) __builtin_amdgcn_raw_buffer_store_b128((u32x4){0u, 0u, 0u, 0u}, rsX, lane * 16 + (i & 3) * 1024, X_HOT(xp) ^ (i < 4 ? X_CS : 0), 16);
        }
        __builtin_amdgcn_raw_buffer_store_b32(lane & 2 ? DO0 * 64u : ~0u, rsX, lane < 4 ? ANX(lane & 1) + ((lane & 2) * 16 + c) * ANS : PJ_OOB, 0, 16);
        VMW0();
        if (lane == 0) st4_sc1(WSP(u32, 0) + SY_INIT(c), (u32)xp + 1u);
        TLS(10);
      }
    }
    SB0();
    fl = ld4_sc1(WSP(u32, 0) + SY_WX);
    SB0();
    auto wx = [&](int vo) {
#pragma unroll
      for (int k = 0; k < 8; k++)
        st[0][k] = __builtin_amdgcn_raw_buffer_load_b128(RSRC(WSP(char, O_WX), 524288), vo, cg * 65536 + ((((k & 2) ^ (r1 * 2)) * 8 + wid * 2 + (k & 1)) * 128 + (k >> 2) * 64) * 16, 16);
    };
    auto poll = [&]() { fl = __builtin_amdgcn_raw_buffer_load_b32(RSRC(p.ws, wid ? 0 : O_QA), (lane < 32 ? SY_INIT(lane) : lane < 48 ? SY_ARR : SY_WX) * 4, 0, 16); };
    char* sc = smem + wid * 10240;
    auto frag = [&](const char* b, int rs) {
      bf4 tt[4];
#pragma unroll
      for (int kk = 0; kk < 4; kk++) {
        const int r0 = ((kk >> 1) * 2 + (lq >> 1)) * 32 + (p4 >> 1) * 16 + (q4 >> 1) * 8 + (lq & 1) * 4 + (kk & 1) * 2 + (q4 & 1);
        tt[kk] = trrd(b + r0 * rs + (p4 & 1) * 8);
      }
      return ilv(tt[0], tt[1], tt[2], tt[3]);
    };
#pragma unroll
    for (int i = 0; i < 4; i++) {
#pragma unroll
      for (int k = 0; k < 8; k++) *(u32x4*)(sc + ((k ^ (r2 * 4)) * 16 + (lane >> 2)) * 80 + p4 * 16) = st[i][k];
      __builtin_amdgcn_fence(__ATOMIC_RELEASE, "wavefront");
      __builtin_amdgcn_wave_barrier();
#pragma unroll
      for (int t = 0; t < 4; t++) w[i][t] = frag(sc + t * 16, 80);
      asm volatile("s_waitcnt lgkmcnt(0)" ::: "memory");
      __builtin_amdgcn_wave_barrier();
      if (i == OEP) { early = __all(fl == ~0u); CFENCE(); poll(); wx(early ? lane * 16 : PJ_OOB); }
      SB0();
    }
    if (wid == 0) {
      int it = 0;
      while (xp >= 0 && !__all(fl >= (lane < 32 ? 1u : lane < 48 ? 256u : ~0u)) && ++it < SPIN_CAP) { BEAT_ON(it); __builtin_amdgcn_s_sleep(GPS); CFENCE(); poll(); }
      *(int*)(smem + C_XP) = xp;
    }
    asm volatile("s_waitcnt lgkmcnt(0)" ::: "memory");
    BARN();
    xp = __builtin_amdgcn_readfirstlane(*(const int*)(smem + C_XP));
    if (xp < 0) return xp;
    {
      if (!early) wx(lane * 16);
#pragma unroll
      for (int k = 0; k < 8; k++) *(u32x4*)(sc + k * 1024 + lane * 16) = st[0][k];
      __builtin_amdgcn_fence(__ATOMIC_RELEASE, "wavefront");
      __builtin_amdgcn_wave_barrier();
#pragma unroll
      for (int i = 0; i < 4; i++) w[i][4] = frag(sc + i * 2048, 16);
    }
    mo = __builtin_amdgcn_readfirstlane(mo ^ xp);
    const int row = (lq & 1) * 4 + ((l15 >> 1) & 3);
    wo = ((lq >> 1) == hi8 ? row * 128 : 1024) + (((wid * 2 + hi8) ^ (row * 2)) << 3) + (l15 & 1) * 4;
    ro = wid > 4 ? 0 : wid * 2560 + (lane >> 5) * 1280 + ((lane >> 2) & 7) * 128 + (lane & 8) * 8 + p4 * 16;
  }
  __syncthreads();
  STS(32 + c, 14);
  TLS(9);
  int go, sidx, po;
  { IDX(); go = lane * 16; sidx = (l15 & 2) ? 0xEEEE : 0x4444;
    po = p4 == 0 ? (lq & 1) * 32 + (lq >> 1) * 16 + q4 * 4 : PJ_OOB; }
  int ro1 = ro ^ 64;
  asm volatile("" : "+v"(ro), "+v"(ro1), "+v"(wo));
#define PUTK(t, r, e, nop) asm volatile(nop PKADD(r, r, (r + 2)) "ds_write2st64_b32 %1, v[" #r "], v[" #r "+1] offset0:%2 offset1:%3" \
                                  : "+{v[" #r ":" #e "]}"(d[t]) : "v"(wo), "i"((t) * 10), "i"((t) * 10 + 5) : "memory")
#define PUTP(t) do { if (PKA & 8) { if (t == 0) PUTK(0, 236, 239, "s_nop 5\n\t"); if (t == 1) PUTK(1, 240, 243, ""); if (t == 2) PUTK(2, 244, 247, ""); \
                                    if (t == 3) PUTK(3, 248, 251, "s_nop 2\n\t"); if (t == 4) PUTK(4, 252, 255, "s_nop 0\n\t"); } \
    else asm volatile("ds_write2st64_b32 %0, %1, %2 offset0:%3 offset1:%4" :: "v"(wo), "v"(d[t][0] + d[t][2]), "v"(d[t][1] + d[t][3]), "i"((t) * 10), "i"((t) * 10 + 5) : "memory"); } while (0)
#define APS 18
#define ONOP 7
  int b3 = 0, dur = DO0 * 64;
#pragma unroll 1
  for (int s = 0; s < 63; s++) {
    f4 d[5];
    int gb[4], pb = mo ^ ((b3 == 2 ? 0 : b3 + 1) * ms);
#pragma unroll
    for (int i = 0; i < 4; i++) { gb[i] = X_O(b3) ^ xp ^ (tq * 131072 + (((i & 1) ^ r1) * 2 + (wid >> 2)) * 32768 + (wid & 3) * 1024 + (i < 2 ? X_HS : 0)); asm volatile("" : "+s"(gb[i])); }
    asm volatile("" : "+s"(pb));
    u32x4 g[4];
    u32 bad;
    auto mult = [&]() __attribute__((always_inline)) {
      CFENCE();
#pragma unroll
      for (int i = 0; i < 4; i++) {
        g[i] = __builtin_amdgcn_raw_buffer_load_b128(rsX, go, gb[i], 16);
        SB0();
        if (ONOP >= 0 && i < 3) asm volatile("s_nop %0" :: "i"(ONOP >= 0 ? ONOP : 0));
        SB0();
      }
      bad = 0u;
#pragma unroll
      for (int i = 0; i < 4; i++) {
        SB0();
#pragma unroll
        for (int t = 0; t < 5; t++) {
          d[t] = SMF(g[i], w[i][t], d[t], sidx);
          if (t == 0) bad = umax(umax(bad, g[i][0]), g[i][2]);
          if (i == 3 && t >= 2) { SB0(); PUTP(t - 2); SB0(); }
        }
      }
      SB0();
#pragma unroll
      for (int t = 3; t < 5; t++) PUTP(t);
      SB0();
    };
#pragma unroll
    for (int t = 0; t < 5; t++) d[t] = zero4();
    SB0();
    u32 dl = 0;
    ALIGN(OAL);
    if (wid == 7) { __builtin_amdgcn_s_sleep(APS); STAMP(4); AD_GET(false, K1 + KXT(KA), SPIN_CAP); STAMP(5); AD_SPIN(); }
    STAMP(15);
    BARN();
    STAMP(0);
    mult();
    if (__builtin_expect(__any(bad == ~0u), 0)) {
      int it = 0;
      do {
#pragma unroll
        for (int t = 0; t < 5; t++) d[t] = zero4();
        mult();
      } while (__any(bad == ~0u) && ++it < SPIN_CAP);
    }
    STAMP(1);
    BARC();
    STAMP(2);
#define OALL 1
    f4 t0, t1;
    if (OALL) { t0 = *(const f4*)(smem + ro); t1 = *(const f4*)(smem + ro1); SB0(); }
    if (__builtin_expect(wid < 5, 1)) {
      if (!OALL) { t0 = *(const f4*)(smem + ro); t1 = *(const f4*)(smem + ro1); }
      if (PKA & 16) {
#define QP2(q) "v_add_f32_dpp v228, v228, v228 quad_perm:" q " row_mask:0xf bank_mask:0xf\n\tv_add_f32_dpp v229, v229, v229 quad_perm:" q " row_mask:0xf bank_mask:0xf\n\t"
        asm volatile(PKADD(228, 228, 230) PKADD(232, 232, 234) PKADD(228, 228, 232) "s_nop 1\n\t"
                     QP2("[1,0,3,2]") "s_nop 0\n\t" QP2("[2,3,0,1]") CV2("v228", 228) "v_min_u32 v228, -2, v228\n\tbuffer_store_dword v228, %2, %3, %4 offen sc1"
                     : "+{v[228:231]}"(t0), "+{v[232:235]}"(t1) : "v"(po), "s"(rsX), "s"(pb) : "memory");
      } else {
      float a = (t0[0] + t0[2]) + (t1[0] + t1[2]), b = (t0[1] + t0[3]) + (t1[1] + t1[3]);
      a += __uint_as_float(__builtin_amdgcn_mov_dpp(__float_as_uint(a), 0xB1, 0xf, 0xf, true));
      b += __uint_as_float(__builtin_amdgcn_mov_dpp(__float_as_uint(b), 0xB1, 0xf, 0xf, true));
      a += __uint_as_float(__builtin_amdgcn_mov_dpp(__float_as_uint(a), 0x4E, 0xf, 0xf, true));
      b += __uint_as_float(__builtin_amdgcn_mov_dpp(__float_as_uint(b), 0x4E, 0xf, 0xf, true));
      __builtin_amdgcn_raw_buffer_store_b32(umin(pk2(a, b), 0xFFFFFFFEu), rsX, po, pb, 16);
      }
      SB0();
      STAMP(3);
      __builtin_amdgcn_s_sleep(4);
      __builtin_amdgcn_raw_buffer_store_b32(~0u, rsX, po, mo ^ (b3 * ms), 16);
    } else if (wid == 7) {
      dur += (min((int)(((u32)wall_clock64() << 6) - dl), dur + DOCAP * 64) - dur) >> DSH;
      if (DRL) __builtin_amdgcn_s_sleep(DRL);
      __builtin_amdgcn_raw_buffer_store_b32((u32)dur, rsX, 0, ANX(~s & 1) + (32 + c) * ANS, 16);
      if (CSL && c == CSC && s > 0) { if (CSL > DRL) __builtin_amdgcn_s_sleep(CSL > DRL ? CSL - DRL : 0); __builtin_amdgcn_raw_buffer_store_b32(dl | 1u, rsX, 0, (X_HOT(xp) ^ X_CS) + (s - 1) * 64, 16); }
    }
    VMW0C();
    YEND();
    b3 = b3 == 2 ? 0 : b3 + 1;
  }
  __syncthreads();
  return xp;
}

__device__ __forceinline__ void wait_ge(const u32* w, u32 val, const int wv) {
  if (TIDX == 0) {
    int it = 0;
    while (ld4_sc1(w) < val && ++it < SPIN_CAP) __builtin_amdgcn_s_sleep(WPS);
  }
}
__device__ __forceinline__ void acquire_all(const int wv) {
  if (TIDX == 0) {
    __builtin_amdgcn_fence(__ATOMIC_ACQUIRE, "agent");
    VMW0();
  }
  __syncthreads();
}

#define T_KV 0
#define T_Q 0
#define T_A 192
#define T_W (T_A + 64)
#define T_S (T_W + 256)
#define T_L (T_S + 72)
#define T_O T_L
#define T_CHAIN (1 << 20)
#define CODE_KB 84
#define OWARM 2
#define BXW 1
#define AORD 0x01234567u
#define OORD 0x67014523u

__device__ __forceinline__ int next_task(const Params& p, int* s_task, const int wv, const int pt, const int pre) {
  __syncthreads();
  if (wv == 0) {
    int t = T_A + pre;
    if (__builtin_amdgcn_readfirstlane(pre) < 0) {
      t = -1;
      int xp = 0;
      for (int it = 0; it < SPIN_CAP; it++) {
        const int l = TIDX & 15;
        const u32 v = ld4_sc1(WSP(u32, 0) + (l < 8 ? SY_JOB(l) : l == 8 ? SY_ARR : SY_INIT(0)));
        const u32 open = (u32)__ballot(v != ~0u) & 255u;
        xp = __builtin_amdgcn_readlane(v, 9);
        if (!open && xp) break;
        if (!open || __builtin_amdgcn_readlane(v, 8) < 256u) { BEAT_ON(it); __builtin_amdgcn_s_sleep(8); continue; }
        const int x = __builtin_ctz(open), k = __builtin_ctz(~(u32)__builtin_amdgcn_readlane(v, x));
        u32 old = 0u;
        if (TIDX == 0) old = AOR(SY_JOB(x), 1u << k);
        if (!((__builtin_amdgcn_readfirstlane(old) >> k) & 1)) { t = x * 32 + k + (x < 2 ? T_CHAIN : -64); break; }
      }
      if (t < 0 && TIDX == 0) t = T_A + (int)__hip_atomic_fetch_add(HOTP(xp) + SY_QHEAD, 1u, __ATOMIC_RELAXED, __HIP_MEMORY_SCOPE_AGENT) + (xp & 0x7000);
    }
    if (TIDX == 0) *s_task = t;
  }
  __syncthreads();
  return __builtin_amdgcn_readfirstlane(*s_task);
}

__device__ __forceinline__ int take_task(int* s_task, const int pre, const int wv, const int pb, const int pl, const int pt) {
  if (TIDX == 0) {
    *s_task = pre;
  }
  __syncthreads();
  const int t = __builtin_amdgcn_readfirstlane(*s_task);
  __syncthreads();
  return t;
}
#if defined(__HIP_DEVICE_COMPILE__) && !defined(KEEP_PK)
#define NOPK __attribute__((target("no-packed-fp32-ops")))
#else
#define NOPK
#endif
__device__ __forceinline__ void mega_body(const Params& p0, char* smem) {
  int* const s_task = (int*)(smem + LDS_TOTAL);
  int wv = __builtin_amdgcn_readfirstlane(threadIdx.x >> 6), t, xs;
#define tid TIDX
  {
    const Params& p = p0;
    u64 pc0; asm volatile("s_getpc_b64 %0" : "=s"(pc0));
#define ZB 1
    const int xcc = (int)(__builtin_amdgcn_s_getreg(20 | (3 << 11)) & 7u), xq = xcc >= 2 ? xcc - 2 : xcc, idx = (blockIdx.x >> 3) & 31;
    t = xq * 32 + (xcc < 2 ? T_CHAIN + idx : (idx + 31) & 31);
    xs = xcc + 8;
#define WARM(ptr, bytes, vo, so, sc) asm volatile("s_mov_b32 m0, %3\n\ts_nop 0\n\tbuffer_load_dword %0, %1, %2 offen" sc " lds" \
                                                   :: "v"(vo), "s"(RSRC(ptr, bytes)), "s"(so), "s"(C_B0 + wv * 256) : "memory", "m0")
    if (wv == 0) WARM(p.ws, O_QA, 0, SY_BEAT(t & 7) * 4, " sc1");
    if (blockIdx.x == ZB) {
#pragma unroll
      for (int i = 0; i < 4; i++) __builtin_amdgcn_raw_buffer_store_b128((u32x4){0u, 0u, 0u, 0u}, RSRC(p.ws, SY_WORDS * 4), tid * 16 + i * 8192, 0, 16);
      VMW0();
      BARN();
      if (tid < 8) AADD(SY_BEAT(tid), 1u);
    }
    if (wv) {
      const int g = idx * 7 + wv - 1, vo = (tid & 63) * 64;
      if (g < CODE_KB / 4) WARM(pc0, CODE_KB * 1024, vo, g * 4096, "");
      if (xcc > 1) {
        if (OWARM == 1 && g >= 32) WARM(p.BWo, WOB, vo, (xq * 192 + g - 32) * 4096, "");
        if (OWARM == 2 && g >= 128) WARM(p.BWo, WOB, vo * 2, (xq * 96 + g - 128) * 8192, "");
        if (BXW) WARM(p.Bx, 256 * 1152, vo, (223 - g) * 4096, "");
      }
    }
  }
  u64 pp = (u64)__builtin_amdgcn_kernarg_segment_ptr();
  SB0();
  asm volatile(".Lmega_again:" : "+{s[40:41]}"(pp), "+{s42}"(wv), "+{s43}"(t), "+{s44}"(xs) :: "memory");
  SB0();
  const Params& p = *(const Params*)(const __attribute__((address_space(4))) Params*)pp;
  const int xcc = xs & 7, xq = xcc >= 2 ? xcc - 2 : xcc;
  const bool spec = xs > 7;
  int pre = -1;
  if (t < T_A) {
    const int x = t >> 5, j = (t & 7) * 4 + ((0x9C >> ((t >> 2) & 6)) & 3);
    proj_slot(p, x, j, smem, wv, pre, spec ? t + 64 : -1);
    VMW0C();
  } else {
    int own;
    if (t < T_CHAIN + 32) { own = chain_head(p, smem, t - T_CHAIN, wv, spec); VMW0C(); }
    else { own = chain_out(p, smem, t - T_CHAIN - 32, wv, spec); VMW0C(); asm volatile("s_nop 0"); }
    if (own >= 0) {
      const int l = tid & 7;
      const u32 v = ld4_sc1(HOTP(own) + (l < 7 ? SY_OQ(l) : SY_QHEAD));
      if (tid == 0) *s_task = 0;
      __syncthreads();
      if (v < (l == 7 ? T_S - T_A : l == 6 ? 72 : (l & 1) ? NOB : NOA)) *s_task = 1;
      __syncthreads();
      if (__builtin_amdgcn_readfirstlane(*s_task) == 0) {
        if (xcc == 0) TLS(3); else TLS(4);
        return;
      }
    }
  }
  t = next_task(p, s_task, wv, t, pre);
  if (t < T_A || t >= T_CHAIN) {
    asm volatile("s_waitcnt vmcnt(0) lgkmcnt(0)\n\ts_barrier\n\ts_branch .Lmega_again" :: "{s[40:41]}"(pp), "{s42}"(wv), "{s43}"(t), "{s44}"(xcc) : "memory");
    return;
  }
  u32* const hot = HOTP(t);
  u32* const qh = hot + SY_QHEAD;
  t &= ~0x7000;
#define APL 8
#define WPOS 0
  while (t < T_S) {
    if ((u32)(t - T_A - WPOS) < 64u) {
      wof_task(p, t - T_A - WPOS, smem, wv, qh, pre);
      if (tid == 0) __hip_atomic_fetch_add(hot + SY_CW, 1u, __ATOMIC_RELAXED, __HIP_MEMORY_SCOPE_AGENT);
      t = T_A + take_task(s_task, pre, wv, T_A, T_S - T_A, t);
      continue;
    }
    const int qb = (AORD >> (((t - (t < T_A + WPOS ? T_A : T_W)) >> 5) * 4)) & 7, h = t & 31;
    if (tid < 64) {
      int it = 0;
      while (true) {
        const u32 f0 = ld4_sc1(WSP(u32, 0) + SY_BLK(3, h >> 1, tid)), f1 = ld4_sc1(WSP(u32, 0) + SY_BLK(4, h >> 1, tid)), f2 = ld4_sc1(WSP(u32, 0) + SY_BLK(5, h >> 1, tid));
        if (__all(tid >= qb * 8 + 8 || ((tid < qb * 8 || f0 >= FULL) && f1 >= FULL && f2 >= FULL)) || ++it >= SPIN_CAP) break;
        __builtin_amdgcn_s_sleep(APL);
      }
    }
    acquire_all(wv);
    fa::attn_block(WSP(char, O_QA) + FR_OFF(qb * 256, h * 128), WSP(char, O_KA) + h * 8192, WSP(char, O_VA) + h * 8192,
                   WSP(char, O_CA) + OB_OFF(qb * 256, h * 128), qb * 256, smem, wv, qh, SY_OQ(xq) - SY_QHEAD, pre);
    if (tid == 0) __hip_atomic_fetch_add(hot + SY_CC(qb), 1u, __ATOMIC_RELAXED, __HIP_MEMORY_SCOPE_AGENT);
    t = T_A + take_task(s_task, pre, wv, T_A, T_S - T_A, t);
  }
  int rest = 64 | (1 << xq);
#pragma unroll 1
  while (rest) {
    const int x = __builtin_ctz(rest);
    rest &= rest - 1;
    if (x == 6) {
      CLAIMS();
      t = take_task(s_task, pre, wv, T_S, 72, -1);
      while ((t & 0xFFFF) < 72) {
        t &= 0xFFFF;
        small_out_task(p, t & 3, t >> 2, smem, wv, hot, pre);
        t = take_task(s_task, pre, wv, T_S, 72, T_S + t);
      }
      rest = t >> 16;
      continue;
    }
    const int part = x & 1, cg = x >> 1, n = part ? NOB : NOA;
    u32* const qh = hot + SY_OQ(x);
    if (t >= 0x10000 && rest == 64) {
      t -= T_A + 0x10000;
    } else {
      CLAIM();
      t = take_task(s_task, pre, wv, T_O + x * 32, n, -1);
    }
    while (t < n) {
      const int nb = part ? NBB : NAB, big = t < nb, e = big ? t * 2 : t + nb;
      const int rb = part ? e : ((OORD >> ((e >> 2) * 4)) & 7) * 4 + (e & 3);
      if (tid < 2) {
        const u32* w = tid ? hot + SY_CW : part ? CSP((rb + big) * 2 + 1) : hot + SY_CC(rb >> 2);
        int it = 0;
        while (__any(ld4_sc1(w) < (tid ? 64u : part ? 1u : 32u)) && ++it < SPIN_CAP) __builtin_amdgcn_s_sleep(WPS);
      }
      acquire_all(wv);
      const char *pa = WSP(char, part ? O_OB : O_CA) + rb * 2 * 262144, *pw = WSP(char, O_WOF) + part * WOB + cg * 12 * 131072;
      u16* const pc = p.out + (size_t)rb * 64 * 1152 + part * 576 + cg * 192;
      if (big) out_tile<8>(pa, pw, pc, smem, wv, hot, qh, pre);
      else out_tile<4>(pa, pw, pc, smem, wv, hot, qh, pre);
      t = take_task(s_task, pre, wv, T_O + x * 32, n, T_O + x * 32 + t);
    }
    if (t == 0x20000) rest = 0;
  }
  if (xcc == 0) TLS(3); else if (xcc == 1) TLS(4); else TLS(5);
}

#undef tid
__global__ NOPK void __launch_bounds__(512, 2) mega(Params p) {
  __shared__ __attribute__((aligned(16))) char smem[LDS_ALL];
  mega_body(p, smem);
}
extern "C" void kernel_launch(void* const* d_in, const int* in_sizes, int n_in, void* d_out, int out_size, void* d_ws,
                              size_t ws_size, hipStream_t stream) {
  Params p;
  p.Ax = (const u16*)d_in[0]; p.Ac = (const u16*)d_in[1]; p.Awq = (const u16*)d_in[2]; p.Awk = (const u16*)d_in[3];
  p.Awv = (const u16*)d_in[4]; p.Awo = (const u16*)d_in[5]; p.Bx = (const u16*)d_in[6]; p.Bm = (const u16*)d_in[7];
  p.BWq = (const u16*)d_in[8]; p.BWk = (const u16*)d_in[9]; p.BWv = (const u16*)d_in[10]; p.BWo = (const u16*)d_in[11];
  p.out = (u16*)d_out;
  p.ws = (char*)d_ws;
  mega<<<256, 512, 0, stream>>>(p);
}
```
